```python
import torch

def _softmax(x, axis=-1):
    return torch.softmax(x, dim=axis)
def _gelu(x):
    return torch.nn.functional.gelu(x, approximate="tanh")
def _silu(x):
    return torch.nn.functional.silu(x)
def _sigmoid(x):
    return torch.sigmoid(x)
def _layernorm(x, axis=-1, eps=1e-5):
    m = x.mean(dim=axis, keepdim=True)
    v = x.var(dim=axis, keepdim=True, unbiased=False)
    return (x - m) / torch.sqrt(v + eps)
def _scatter_add(tgt, idx, src, axis=0):
    return tgt.clone().index_add_(axis, idx, src)
def _segment_sum(src, seg, n):
    out = torch.zeros((n,) + src.shape[1:], dtype=src.dtype)
    return out.index_add_(0, seg, src)
def _pad(x, pw):
    flat = tuple(v for pair in reversed(pw) for v in pair)
    return torch.nn.functional.pad(x, flat)

kernel_name = 'gated_mix__sliding_local_dilated__dense_activation_relu__prompt'

def setup_inputs():

    _w = (lambda a: (lambda t: t.to(torch.bfloat16) if t.is_floating_point() else t)(torch.as_tensor(a)))
    def _A_setup():
        B, S, D = 1, 2048, 1024
        return {
            "x": _w((lambda *s: torch.randn(*s, dtype=torch.bfloat16) * 0.05)(B, S, D)),
            "wqkv": _w((lambda *s: torch.randn(*s, dtype=torch.bfloat16) * 0.8)(D, 3 * D)),
            "wo": _w((lambda *s: torch.randn(*s, dtype=torch.bfloat16) * 0.05)(D, D)),
        }
    _A_in = _A_setup()

    def _B_setup():
        B, S, D, Dff = 1, 2048, 1024, 3584
        return {
            "x": _w((lambda *s: torch.randn(*s, dtype=torch.bfloat16) * 0.05)(B, S, D)),
            "w1": _w((lambda *s: torch.randn(*s, dtype=torch.bfloat16) * 0.2)(D, Dff)),
            "w2": _w((lambda *s: torch.randn(*s, dtype=torch.bfloat16) * 0.2)(Dff, D)),
        }
    _B_in = _B_setup()

    return {
        "A_x": _A_in["x"],
        "A_wqkv": _A_in["wqkv"],
        "A_wo": _A_in["wo"],
        "B_x": _B_in["x"],
        "B_w1": _B_in["w1"],
        "B_w2": _B_in["w2"],
        "gate_w": _w((lambda *s: torch.randn(*s, dtype=torch.bfloat16) * 0.05)(1024) * 16.0),
    }

def reference(A_x, A_wqkv, A_wo, B_x, B_w1, B_w2, gate_w):
    def _A_ref(x, wqkv, wo):
        B, S, D = x.shape
        H = 32; Dh = D // H
        W, dil = 8, 2
        qkv = (lambda x, *d: x.permute(*d))((x @ wqkv).reshape(B, S, 3, H, Dh), 2, 0, 3, 1, 4)
        q, k, v = qkv[0], qkv[1], qkv[2]
        scale = Dh ** -0.5
        pos = torch.arange(S)
        off = torch.arange(W) * dil
        idx = pos[:, None] - off[None, :]
        mask = idx >= 0
        idx_c = torch.where(mask, idx, 0).reshape(-1)
        kg = k[:, :, idx_c, :].reshape(B, H, S, W, Dh)
        vg = v[:, :, idx_c, :].reshape(B, H, S, W, Dh)
        logits = torch.einsum("bhsd,bhswd->bhsw", q, kg) * scale
        logits = torch.where(mask[None, None, :, :], logits, -1e9)
        attn = _softmax(logits, axis=-1)
        o = torch.einsum("bhsw,bhswd->bhsd", attn, vg)
        return (lambda x, *d: x.permute(*d))(o, 0, 2, 1, 3).reshape(B, S, D) @ wo

    def _B_ref(x, w1, w2):
        h = torch.einsum("bsd,df->bsf", x, w1)
        zero = torch.zeros_like(h)
        h = torch.maximum(h, zero)
        return torch.einsum("bsf,fd->bsd", h, w2)

    x = A_x
    g = _sigmoid((x * gate_w).sum(-1, keepdims=True))
    a = _A_ref(x, A_wqkv, A_wo)
    b = _B_ref(x, B_w1, B_w2)
    return g * a + (1 - g) * b
```

```cpp
#include <hip/hip_runtime.h>
#include <hip/hip_bf16.h>
typedef unsigned short u16;
typedef __attribute__((ext_vector_type(8))) short bf16x8;
typedef __attribute__((ext_vector_type(4))) float f32x4;
__device__ inline float bf2f(u16 x){ return __uint_as_float(((unsigned)x)<<16); }
__device__ inline u16 f2bf(float f){ unsigned u=__float_as_uint(f); u += 0x7FFF + ((u>>16)&1); return (u16)(u>>16); }

template<int MODE>
__global__ __launch_bounds__(256) void gemm(const u16* A, int lda, const u16* B, int ldb, u16* C, int ldc, int K, const float* g){
  __shared__ u16 As[64][40]; __shared__ u16 Bs[64][40];
  int tid=threadIdx.x, lane=tid&63, w=tid>>6, wr=w>>1, wc=w&1;
  int bm=blockIdx.y*64, bn=blockIdx.x*64;
  f32x4 acc[2][2]={};
  for(int k0=0;k0<K;k0+=32){
    { int r=tid>>2, c=(tid&3)*8; *(bf16x8*)&As[r][c] = *(const bf16x8*)&A[(size_t)(bm+r)*lda+k0+c]; }
    { int kr=tid>>3, c=(tid&7)*8; bf16x8 v=*(const bf16x8*)&B[(size_t)(k0+kr)*ldb+bn+c]; for(int j=0;j<8;j++) Bs[c+j][kr]=v[j]; }
    __syncthreads();
    bf16x8 a[2],b[2];
    for(int i=0;i<2;i++){ a[i]=*(bf16x8*)&As[wr*32+i*16+(lane&15)][(lane>>4)*8]; b[i]=*(bf16x8*)&Bs[wc*32+i*16+(lane&15)][(lane>>4)*8]; }
    for(int i=0;i<2;i++)for(int j=0;j<2;j++) acc[i][j]=__builtin_amdgcn_mfma_f32_16x16x32_bf16(a[i],b[j],acc[i][j],0,0,0);
    __syncthreads();
  }
  for(int i=0;i<2;i++)for(int j=0;j<2;j++)for(int r=0;r<4;r++){
    int row=bm+wr*32+i*16+(lane>>4)*4+r, col=bn+wc*32+j*16+(lane&15);
    float v=acc[i][j][r];
    if(MODE==1){ v=fmaxf(v,0.f)*(1.f-g[row]); }
    C[(size_t)row*ldc+col]=f2bf(v);
  }
}
__global__ void gate(const u16* x,const u16* gw,float* g){
  int row=blockIdx.x; int lane=threadIdx.x; float s=0;
  for(int i=lane;i<1024;i+=64) s+=bf2f(x[row*1024+i])*bf2f(gw[i]);
  for(int o=32;o;o>>=1) s+=__shfl_xor(s,o);
  if(lane==0) g[row]=1.f/(1.f+__expf(-s));
}
__global__ void attn(const u16* qkv,const float* g,u16* cat){
  int s=blockIdx.x; int t=threadIdx.x;
  float q=bf2f(qkv[(size_t)s*3072+t]); float l[8]; float m=-1e30f;
  for(int w=0;w<8;w++){ int idx=s-2*w; float v; if(idx>=0){ v=q*bf2f(qkv[(size_t)idx*3072+1024+t]); } else v=0;
    for(int o=16;o;o>>=1) v+=__shfl_xor(v,o); v*=0.17677669529663687f; if(idx<0) v=-1e9f; l[w]=v; m=fmaxf(m,v);}
  float sum=0; for(int w=0;w<8;w++){ l[w]=__expf(l[w]-m); sum+=l[w]; }
  float o=0; for(int w=0;w<8;w++){ int idx=s-2*w; if(idx<0) idx=0; o+=l[w]*bf2f(qkv[(size_t)idx*3072+2048+t]); }
  cat[(size_t)s*4608+t]=f2bf(o/sum*g[s]);
}
extern "C" void kernel_launch(void* const* d_in, const int* in_sizes, int n_in, void* d_out, int out_size, void* d_ws, size_t ws_size, hipStream_t stream){
  const u16* x=(const u16*)d_in[0]; const u16* wqkv=(const u16*)d_in[1]; const u16* wo=(const u16*)d_in[2];
  const u16* w1=(const u16*)d_in[4]; const u16* w2=(const u16*)d_in[5]; const u16* gw=(const u16*)d_in[6];
  char* ws=(char*)d_ws; float* g=(float*)ws; u16* qkv=(u16*)(ws+(1<<20)); u16* cat=(u16*)(ws+(16<<20)); u16* wcat=(u16*)(ws+(48<<20));
  hipMemcpyAsync(wcat,wo,1024*1024*2,hipMemcpyDeviceToDevice,stream);
  hipMemcpyAsync(wcat+1024*1024,w2,3584*1024*2,hipMemcpyDeviceToDevice,stream);
  gate<<<2048,64,0,stream>>>(x,gw,g);
  gemm<0><<<dim3(3072/64,32),256,0,stream>>>(x,1024,wqkv,3072,qkv,3072,1024,g);
  gemm<1><<<dim3(3584/64,32),256,0,stream>>>(x,1024,w1,3584,cat+1024,4608,1024,g);
  attn<<<2048,1024,0,stream>>>(qkv,g,cat);
  gemm<2><<<dim3(1024/64,32),256,0,stream>>>(cat,4608,wcat,1024,(u16*)d_out,1024,4608,g);
}
```

```cpp
#include <hip/hip_runtime.h>
#include <hip/hip_bf16.h>

#define KASM \
"v_accvgpr_write_b32 a252, v250\n" \
"v_accvgpr_write_b32 a253, v251\n" \
"v_accvgpr_write_b32 a254, v252\n" \
"v_accvgpr_write_b32 a255, v253\n" \
"v_lshrrev_b32 v247, 6, v249\n" \
"v_and_b32 v248, 63, v249\n" \
"v_readfirstlane_b32 s59, v247\n" \
"s_and_b32 s88, %[info], 15\n" \
"s_bfe_u32 s89, %[info], 0x40004\n" \
"s_mov_b32 s40, 0\n" \
"s_mov_b32 s41, 32768\n" \
"s_mov_b32 s42, 2048\n" \
"s_mov_b32 s43, 34816\n" \
"s_mov_b32 s44, 65536\n" \
"s_mov_b32 s45, 98304\n" \
"s_mov_b32 s46, 67584\n" \
"s_mov_b32 s47, 100352\n" \
"v_lshrrev_b32 v230, 3, v248\n" \
"v_lshlrev_b32 v230, 12, v230\n" \
"v_lshrrev_b32 v231, 4, v248\n" \
"v_and_b32 v229, 7, v248\n" \
"v_xor_b32 v231, v231, v229\n" \
"v_lshl_add_u32 v230, v231, 4, v230\n" \
"v_lshl_add_u32 v232, v247, 17, v230\n" \
"v_xor_b32 v233, 64, v232\n" \
"s_lshl_b32 s54, s59, 13\n" \
"s_add_u32 s54, s54, 32768\n" \
"s_lshl_b32 s60, s88, 19\n" \
"s_waitcnt lgkmcnt(0)\n" \
"s_mov_b64 s[20:21], s[4:5]\n" \
"s_add_u32 s20, s20, s60\n" \
"s_addc_u32 s21, s21, 0\n" \
"s_mov_b32 s22, 0x7fffffff\n" \
"s_mov_b32 s23, 0x20000\n" \
"s_lshl_b32 s61, s88, 4\n" \
"s_add_u32 s61, s61, s89\n" \
"s_lshl_b32 s61, s61, 2\n" \
"s_add_u32 s74, s61, s59\n" \
"s_bitcmp1_b32 %[info], 8\n" \
"s_cbranch_scc1 Lq_entry_%=\n" \
"Lh_entry_%=:\n" \
"s_mov_b64 s[24:25], s[10:11]\n" \
"s_mov_b32 s26, 917504\n" \
"s_mov_b32 s27, 0x20000\n" \
"s_mul_i32 s61, s74, 1792\n" \
"v_min_u32 v228, 13, v248\n" \
"v_lshlrev_b32 v228, 7, v228\n" \
"v_add_u32 v228, s61, v228\n" \
"buffer_load_dword a201, v228, s[24:27], 0 offen\n" \
"s_lshl_b32 s61, s74, 13\n" \
"v_and_b32 v228, 3, v248\n" \
"v_lshlrev_b32 v228, 11, v228\n" \
"v_bfe_u32 v227, v248, 2, 1\n" \
"v_lshl_add_u32 v228, v227, 7, v228\n" \
"v_add_u32 v228, s61, v228\n" \
"s_mov_b64 s[28:29], s[4:5]\n" \
"s_mov_b32 s30, 4194304\n" \
"s_mov_b32 s31, 0x20000\n" \
"buffer_load_dword a202, v228, s[28:31], 0 offen\n" \
"s_add_u32 m0, s54, 0\n" \
"s_nop 0\n" \
"buffer_load_dwordx4 v232, s[20:23], s40 offen lds\n" \
"s_add_u32 m0, s54, 1024\n" \
"s_nop 0\n" \
"buffer_load_dwordx4 v233, s[20:23], s41 offen lds\n" \
"s_add_u32 m0, s54, 2048\n" \
"s_nop 0\n" \
"buffer_load_dwordx4 v232, s[20:23], s42 offen lds\n" \
"s_add_u32 m0, s54, 3072\n" \
"s_nop 0\n" \
"buffer_load_dwordx4 v233, s[20:23], s43 offen lds\n" \
"s_add_u32 m0, s54, 4096\n" \
"s_nop 0\n" \
"buffer_load_dwordx4 v232, s[20:23], s44 offen lds\n" \
"s_add_u32 m0, s54, 5120\n" \
"s_nop 0\n" \
"buffer_load_dwordx4 v233, s[20:23], s45 offen lds\n" \
"s_add_u32 m0, s54, 6144\n" \
"s_nop 0\n" \
"buffer_load_dwordx4 v232, s[20:23], s46 offen lds\n" \
"s_add_u32 m0, s54, 7168\n" \
"s_nop 0\n" \
"buffer_load_dwordx4 v233, s[20:23], s47 offen lds\n" \
"s_xor_b32 s54, s54, 32768\n" \
"s_add_u32 s20, s20, 128\n" \
"s_addc_u32 s21, s21, 0\n" \
"s_bfe_u32 s90, %[info], 0x3000c\n" \
"s_bfe_u32 s91, %[info], 0x20010\n" \
"s_bfe_u32 s92, %[info], 0x50004\n" \
"s_mul_i32 s60, s89, 384\n" \
"s_add_u32 s60, s60, 0\n" \
"s_mov_b64 s[24:25], s[10:11]\n" \
"s_add_u32 s24, s24, s60\n" \
"s_addc_u32 s25, s25, 0\n" \
"s_mov_b32 s26, 0x7fffffff\n" \
"s_mov_b32 s27, 0x20000\n" \
"s_mov_b32 s48, 0\n" \
"s_mov_b32 s49, 128\n" \
"s_mov_b32 s50, 256\n" \
"s_mov_b32 s51, 229376\n" \
"s_mov_b32 s52, 229504\n" \
"s_mov_b32 s53, 229632\n" \
"s_lshl_b32 s62, s59, 10\n" \
"s_add_u32 s62, s62, 65536\n" \
"s_add_u32 s55, s62, 0\n" \
"s_mov_b32 s63, 0\n" \
"v_lshrrev_b32 v230, 3, v248\n" \
"v_lshl_add_u32 v231, v247, 3, v230\n" \
"v_mul_u32_u24 v231, 7168, v231\n" \
"v_and_b32 v230, 2, v230\n" \
"v_and_b32 v229, 1, v247\n" \
"v_or_b32 v230, v230, v229\n" \
"v_lshlrev_b32 v230, 1, v230\n" \
"v_and_b32 v229, 7, v248\n" \
"v_xor_b32 v230, v230, v229\n" \
"v_lshl_add_u32 v234, v230, 4, v231\n" \
"s_mov_b32 s75, 458752\n" \
"s_mov_b64 s[64:65], s[18:19]\n" \
"s_mov_b32 s66, 0x2200000\n" \
"s_mov_b32 s67, 0x20000\n" \
"s_add_u32 m0, s55, 0\n" \
"s_nop 0\n" \
"buffer_load_dwordx4 v234, s[24:27], s48 offen lds\n" \
"v_mov_b64 v[224:225], 0\n" \
"v_mov_b64 v[226:227], 0\n" \
"s_lshl_b32 s60, s88, 2\n" \
"s_add_u32 s60, s60, s59\n" \
"s_lshl_b32 s61, s60, 7\n" \
"s_add_u32 s93, s61, 33751040\n" \
"s_lshl_b32 s61, s60, 8\n" \
"s_add_u32 s94, s61, 33554432\n" \
"s_lshl_b32 s61, s60, 12\n" \
"s_add_u32 s84, s61, 34078720\n" \
"v_and_b32 v250, 15, v248\n" \
"v_lshrrev_b32 v251, 4, v248\n" \
"s_add_u32 m0, s55, 4096\n" \
"s_nop 0\n" \
"buffer_load_dwordx4 v234, s[24:27], s49 offen lds\n" \
"v_lshrrev_b32 v252, 1, v250\n" \
"v_xor_b32 v252, v252, v251\n" \
"v_lshlrev_b32 v252, 4, v252\n" \
"v_lshl_add_u32 v252, v250, 7, v252\n" \
"v_lshl_add_u32 v237, v247, 13, v252\n" \
"v_add_u32 v237, 0x8000, v237\n" \
"s_add_u32 m0, s55, 8192\n" \
"s_nop 0\n" \
"buffer_load_dwordx4 v234, s[24:27], s50 offen lds\n" \
"v_mfma_f32_32x32x16_bf16 v[48:63], v[224:227], v[224:227], 0\n" \
"v_xor_b32 v238, 64, v237\n" \
"v_lshrrev_b32 v252, 2, v250\n" \
"v_and_b32 v253, 3, v248\n" \
"v_lshlrev_b32 v254, 10, v251\n" \
"v_lshl_add_u32 v254, v252, 7, v254\n" \
"s_add_u32 m0, s55, 12288\n" \
"s_nop 0\n" \
"buffer_load_dwordx4 v234, s[24:27], s51 offen lds\n" \
"v_lshl_add_u32 v254, v253, 3, v254\n" \
"v_add_u32 v254, 65536, v254\n" \
"v_and_b32 v26, 2, v252\n" \
"v_and_b32 v27, 1, v251\n" \
"v_or_b32 v26, v26, v27\n" \
"v_mfma_f32_32x32x16_bf16 v[64:79], v[224:227], v[224:227], 0\n" \
"s_add_u32 m0, s55, 16384\n" \
"s_nop 0\n" \
"buffer_load_dwordx4 v234, s[24:27], s52 offen lds\n" \
"v_lshlrev_b32 v26, 5, v26\n" \
"v_xor_b32 v28, 0, v26\n" \
"v_add_u32 v239, v254, v28\n" \
"v_xor_b32 v28, 32, v26\n" \
"v_add_u32 v240, v254, v28\n" \
"v_xor_b32 v28, 64, v26\n" \
"s_add_u32 m0, s55, 20480\n" \
"s_nop 0\n" \
"buffer_load_dwordx4 v234, s[24:27], s53 offen lds\n" \
"v_add_u32 v241, v254, v28\n" \
"v_xor_b32 v28, 96, v26\n" \
"v_add_u32 v242, v254, v28\n" \
"v_lshl_add_u32 v29, v27, 2, v252\n" \
"v_mfma_f32_32x32x16_bf16 v[80:95], v[224:227], v[224:227], 0\n" \
"v_lshrrev_b32 v30, 1, v251\n" \
"s_add_u32 s24, s24, s75\n" \
"s_addc_u32 s25, s25, 0\n" \
"s_add_u32 s55, s55, 24576\n" \
"s_add_u32 s63, s63, 1\n" \
"s_cmp_eq_u32 s63, 4\n" \
"s_cmov_b32 s55, s62\n" \
"s_cmov_b32 s63, 0\n" \
"s_add_u32 m0, s54, 0\n" \
"s_nop 0\n" \
"buffer_load_dwordx4 v232, s[20:23], s40 offen lds\n" \
"v_lshlrev_b32 v30, 10, v30\n" \
"v_lshl_add_u32 v30, v29, 7, v30\n" \
"v_lshl_add_u32 v30, v253, 3, v30\n" \
"v_lshrrev_b32 v29, 1, v29\n" \
"v_lshlrev_b32 v29, 5, v29\n" \
"v_xor_b32 v28, 0, v29\n" \
"s_add_u32 m0, s54, 1024\n" \
"s_nop 0\n" \
"buffer_load_dwordx4 v233, s[20:23], s41 offen lds\n" \
"v_add_u32 v28, v30, v28\n" \
"v_accvgpr_write_b32 a234, v28\n" \
"v_xor_b32 v28, 32, v29\n" \
"v_mfma_f32_32x32x16_bf16 v[96:111], v[224:227], v[224:227], 0\n" \
"v_add_u32 v28, v30, v28\n" \
"v_accvgpr_write_b32 a235, v28\n" \
"s_add_u32 m0, s54, 2048\n" \
"s_nop 0\n" \
"buffer_load_dwordx4 v232, s[20:23], s42 offen lds\n" \
"v_xor_b32 v28, 64, v29\n" \
"v_add_u32 v28, v30, v28\n" \
"v_accvgpr_write_b32 a236, v28\n" \
"v_xor_b32 v28, 96, v29\n" \
"v_add_u32 v28, v30, v28\n" \
"v_accvgpr_write_b32 a237, v28\n" \
"s_add_u32 m0, s54, 3072\n" \
"s_nop 0\n" \
"buffer_load_dwordx4 v233, s[20:23], s43 offen lds\n" \
"v_and_b32 v28, 7, v250\n" \
"v_lshlrev_b32 v28, 12, v28\n" \
"v_mfma_f32_32x32x16_bf16 v[112:127], v[224:227], v[224:227], 0\n" \
"v_lshrrev_b32 v29, 3, v250\n" \
"v_lshl_add_u32 v28, v29, 6, v28\n" \
"v_lshl_add_u32 v28, v27, 5, v28\n" \
"s_add_u32 m0, s54, 4096\n" \
"s_nop 0\n" \
"buffer_load_dwordx4 v232, s[20:23], s44 offen lds\n" \
"v_lshrrev_b32 v29, 1, v251\n" \
"v_lshl_add_u32 v28, v29, 4, v28\n" \
"s_lshl_b32 s60, s88, 2\n" \
"s_add_u32 s60, s60, s59\n" \
"s_lshl_b32 s60, s60, 17\n" \
"s_and_b32 s61, s91, 2\n" \
"s_lshl_b32 s61, s61, 15\n" \
"s_add_u32 s60, s60, s61\n" \
"s_lshl_b32 s61, s90, 8\n" \
"s_add_u32 s60, s60, s61\n" \
"s_and_b32 s61, s91, 1\n" \
"s_lshl_b32 s61, s61, 7\n" \
"s_add_u32 s60, s60, s61\n" \
"v_add_u32 v28, s60, v28\n" \
"v_accvgpr_write_b32 a242, v28\n" \
"s_add_u32 m0, s54, 5120\n" \
"s_nop 0\n" \
"buffer_load_dwordx4 v233, s[20:23], s45 offen lds\n" \
"v_lshlrev_b32 v28, 4, v250\n" \
"v_mfma_f32_32x32x16_bf16 v[128:143], v[224:227], v[224:227], 0\n" \
"v_accvgpr_write_b32 a241, v28\n" \
"v_lshrrev_b32 v26, 3, v248\n" \
"v_lshl_add_u32 v29, v247, 3, v26\n" \
"v_lshlrev_b32 v29, 11, v29\n" \
"s_add_u32 m0, s54, 6144\n" \
"s_nop 0\n" \
"buffer_load_dwordx4 v232, s[20:23], s46 offen lds\n" \
"v_and_b32 v26, 6, v26\n" \
"v_and_b32 v28, 7, v248\n" \
"v_xor_b32 v26, v26, v28\n" \
"v_lshl_add_u32 v29, v26, 4, v29\n" \
"v_accvgpr_write_b32 a233, v29\n" \
"v_lshlrev_b32 v28, 12, v247\n" \
"s_add_u32 m0, s54, 7168\n" \
"s_nop 0\n" \
"buffer_load_dwordx4 v233, s[20:23], s47 offen lds\n" \
"v_mfma_f32_32x32x16_bf16 v[144:159], v[224:227], v[224:227], 0\n" \
"v_lshl_add_u32 v28, v248, 4, v28\n" \
"v_accvgpr_write_b32 a232, v28\n" \
"s_bfe_u32 s60, %[info], 0x5001a\n" \
"s_sub_u32 s60, s60, 1\n" \
"s_bfe_u32 s61, %[info], 0x50014\n" \
"v_min_u32 v28, s60, v248\n" \
"v_add_u32 v28, s61, v28\n" \
"s_xor_b32 s54, s54, 32768\n" \
"s_add_u32 s20, s20, 128\n" \
"s_addc_u32 s21, s21, 0\n" \
"s_waitcnt vmcnt(8)\n" \
"s_barrier\n" \
"ds_read_b128 a[0:3], v237 offset:0\n" \
"ds_read_b128 a[4:7], v237 offset:2048\n" \
"ds_read_b128 a[8:11], v237 offset:4096\n" \
"ds_read_b128 a[12:15], v237 offset:6144\n" \
"ds_read_b64_tr_b16 a[32:33], v239 offset:0\n" \
"ds_read_b64_tr_b16 a[34:35], v239 offset:512\n" \
"s_add_u32 m0, s55, 0\n" \
"s_nop 0\n" \
"buffer_load_dwordx4 v234, s[24:27], s48 offen lds\n" \
"v_lshlrev_b32 v28, 7, v28\n" \
"v_accvgpr_write_b32 a238, v28\n" \
"v_min_u32 v28, 31, v248\n" \
"v_lshlrev_b32 v28, 7, v28\n" \
"v_accvgpr_write_b32 a239, v28\n" \
"v_mfma_f32_32x32x16_bf16 v[160:175], v[224:227], v[224:227], 0\n" \
"ds_read_b64_tr_b16 a[36:37], v240 offset:0\n" \
"ds_read_b64_tr_b16 a[38:39], v240 offset:512\n" \
"ds_read_b64_tr_b16 a[40:41], v241 offset:0\n" \
"ds_read_b64_tr_b16 a[42:43], v241 offset:512\n" \
"ds_read_b64_tr_b16 a[44:45], v242 offset:0\n" \
"ds_read_b64_tr_b16 a[46:47], v242 offset:512\n" \
"s_add_u32 m0, s55, 4096\n" \
"s_nop 0\n" \
"buffer_load_dwordx4 v234, s[24:27], s49 offen lds\n" \
"v_min_u32 v28, 2, v248\n" \
"v_add_u32 v28, 1, v28\n" \
"v_xor_b32 v28, s91, v28\n" \
"v_lshlrev_b32 v28, 7, v28\n" \
"v_accvgpr_write_b32 a240, v28\n" \
"s_lshl_b32 s60, s88, 3\n" \
"s_add_u32 s60, s60, s90\n" \
"s_lshl_b32 s98, s60, 2\n" \
"s_add_u32 s60, s98, s59\n" \
"s_lshl_b32 s60, s60, 9\n" \
"s_add_u32 s99, s60, 34340864\n" \
"ds_read_b64_tr_b16 a[48:49], v239 offset:4096\n" \
"ds_read_b64_tr_b16 a[50:51], v239 offset:4608\n" \
"ds_read_b64_tr_b16 a[52:53], v240 offset:4096\n" \
"ds_read_b64_tr_b16 a[54:55], v240 offset:4608\n" \
"ds_read_b64_tr_b16 a[56:57], v241 offset:4096\n" \
"ds_read_b64_tr_b16 a[58:59], v241 offset:4608\n" \
"s_add_u32 m0, s55, 8192\n" \
"s_nop 0\n" \
"buffer_load_dwordx4 v234, s[24:27], s50 offen lds\n" \
"s_mov_b64 s[96:97], s[8:9]\n" \
"s_and_b32 s60, s88, 1\n" \
"s_lshl_b32 s60, s60, 3\n" \
"s_add_u32 s60, s60, s90\n" \
"s_lshl_b32 s60, s60, 2\n" \
"s_add_u32 s60, s60, s59\n" \
"s_lshl_b32 s60, s60, 9\n" \
"v_lshlrev_b32 v28, 7, v248\n" \
"v_add_u32 v28, s60, v28\n" \
"v_mfma_f32_32x32x16_bf16 v[176:191], v[224:227], v[224:227], 0\n" \
"v_mov_b32 v29, 0x80000000\n" \
"ds_read_b64_tr_b16 a[60:61], v242 offset:4096\n" \
"ds_read_b64_tr_b16 a[62:63], v242 offset:4608\n" \
"ds_read_b64_tr_b16 a[64:65], v239 offset:8192\n" \
"ds_read_b64_tr_b16 a[66:67], v239 offset:8704\n" \
"ds_read_b64_tr_b16 a[68:69], v240 offset:8192\n" \
"ds_read_b64_tr_b16 a[70:71], v240 offset:8704\n" \
"s_add_u32 m0, s55, 12288\n" \
"s_nop 0\n" \
"buffer_load_dwordx4 v234, s[24:27], s51 offen lds\n" \
"v_cmp_gt_u32 vcc, 4, v248\n" \
"s_nop 1\n" \
"v_cndmask_b32 v28, v29, v28, vcc\n" \
"v_accvgpr_write_b32 a243, v28\n" \
"s_and_b32 s81, s91, 1\n" \
"s_lshl_b32 s81, s81, 7\n" \
"s_xor_b32 s80, s81, 128\n" \
"s_lshl_b32 s60, s88, 8\n" \
"s_lshl_b32 s61, s59, 6\n" \
"s_add_u32 s60, s60, s61\n" \
"s_lshl_b32 s61, s89, 2\n" \
"s_add_u32 s60, s60, s61\n" \
"s_lshl_b32 s60, s60, 11\n" \
"v_lshlrev_b32 v28, 5, v248\n" \
"v_accvgpr_write_b32 a194, v28\n" \
"ds_read_b64_tr_b16 a[72:73], v241 offset:8192\n" \
"ds_read_b64_tr_b16 a[74:75], v241 offset:8704\n" \
"ds_read_b64_tr_b16 a[76:77], v242 offset:8192\n" \
"ds_read_b64_tr_b16 a[78:79], v242 offset:8704\n" \
"ds_read_b128 a[16:19], v238 offset:0\n" \
"ds_read_b128 a[20:23], v238 offset:2048\n" \
"s_add_u32 m0, s55, 16384\n" \
"s_nop 0\n" \
"buffer_load_dwordx4 v234, s[24:27], s52 offen lds\n" \
"v_add_u32 v28, s60, v28\n" \
"v_accvgpr_write_b32 a195, v28\n" \
"v_lshrrev_b32 v28, 5, v248\n" \
"v_lshlrev_b32 v28, 2, v28\n" \
"v_lshl_add_u32 v28, v27, 4, v28\n" \
"v_accvgpr_write_b32 a193, v28\n" \
"ds_read_b128 a[24:27], v238 offset:4096\n" \
"ds_read_b128 a[28:31], v238 offset:6144\n" \
"s_add_u32 m0, s55, 20480\n" \
"s_nop 0\n" \
"buffer_load_dwordx4 v234, s[24:27], s53 offen lds\n" \
"v_mfma_f32_32x32x16_bf16 v[0:15], v[224:227], v[224:227], 0\n" \
"v_mfma_f32_32x32x16_bf16 v[16:31], v[224:227], v[224:227], 0\n" \
"v_mfma_f32_32x32x16_bf16 v[32:47], v[224:227], v[224:227], 0\n" \
"s_add_u32 s24, s24, s75\n" \
"s_addc_u32 s25, s25, 0\n" \
"s_add_u32 s55, s55, 24576\n" \
"s_add_u32 s63, s63, 1\n" \
"s_cmp_eq_u32 s63, 4\n" \
"s_cmov_b32 s55, s62\n" \
"s_cmov_b32 s63, 0\n" \
"v_mov_b32 v252, 0\n" \
"buffer_load_dwordx2 a[244:245], v252, s[64:67], s93 offen sc1\n" \
"s_lshl_b32 s60, s91, 7\n" \
"s_add_u32 s60, s60, s99\n" \
"v_mov_b32 v252, s60\n" \
"v_writelane_b32 v252, s94, 2\n" \
"s_lshl_b32 s60, s92, 7\n" \
"s_add_u32 s60, s60, s84\n" \
"v_writelane_b32 v252, s60, 1\n" \
"s_add_u32 s60, s60, 131072\n" \
"v_writelane_b32 v252, s60, 3\n" \
"s_add_u32 s60, s94, 128\n" \
"v_writelane_b32 v252, s60, 4\n" \
"buffer_load_dword a210, v252, s[64:67], 0 offen sc1\n" \
"s_mov_b32 s60, s74\n" \
"s_mov_b64 s[76:77], s[4:5]\n" \
"s_add_u32 s76, s76, 0\n" \
"s_addc_u32 s77, s77, 0\n" \
"s_mov_b32 s78, 2097152\n" \
"s_mov_b32 s79, 0x20000\n" \
"s_add_u32 s0, s10, 3211264\n" \
"s_addc_u32 s1, s11, 0\n" \
"s_add_u32 s34, s12, 0\n" \
"s_addc_u32 s35, s13, 0\n" \
"s_mov_b32 s74, 4293918720\n" \
"s_mul_i32 s61, s60, 2048\n" \
"v_lshlrev_b32 v253, 7, v248\n" \
"v_add_u32 v252, s61, v253\n" \
"v_cmp_gt_u32 vcc, 16, v248\n" \
"v_mov_b32 v254, 0x80000000\n" \
"s_nop 0\n" \
"v_cndmask_b32 v254, v254, v252, vcc\n" \
"s_mov_b32 s82, 128\n" \
"s_mov_b32 s83, 0\n" \
"s_mov_b64 s[68:69], s[12:13]\n" \
"s_mov_b32 s70, 7340032\n" \
"s_mov_b32 s71, 0x20000\n" \
"s_mov_b32 s57, -1\n" \
"s_mov_b32 s58, 0\n" \
"s_mov_b32 s95, 4294893568\n" \
"s_mov_b32 s85, 24576\n" \
"s_cmp_eq_u32 s59, 1\n" \
"s_cbranch_scc1 Lhe1_%=\n" \
"s_cmp_eq_u32 s59, 2\n" \
"s_cbranch_scc1 Lhe2_%=\n" \
"s_cmp_eq_u32 s59, 3\n" \
"s_cbranch_scc1 Lhe3_%=\n" \
"Lhe0_%=:\n" \
"s_add_u32 m0, s55, 0\n" \
"s_nop 0\n" \
"buffer_load_dwordx4 v234, s[24:27], s48 offen lds\n" \
"s_add_u32 m0, s55, 4096\n" \
"s_nop 0\n" \
"buffer_load_dwordx4 v234, s[24:27], s49 offen lds\n" \
"s_add_u32 m0, s55, 8192\n" \
"s_nop 0\n" \
"buffer_load_dwordx4 v234, s[24:27], s50 offen lds\n" \
"s_add_u32 m0, s55, 12288\n" \
"s_nop 0\n" \
"buffer_load_dwordx4 v234, s[24:27], s51 offen lds\n" \
"s_add_u32 m0, s55, 16384\n" \
"s_nop 0\n" \
"buffer_load_dwordx4 v234, s[24:27], s52 offen lds\n" \
"s_add_u32 m0, s55, 20480\n" \
"s_nop 0\n" \
"buffer_load_dwordx4 v234, s[24:27], s53 offen lds\n" \
"s_add_u32 s24, s24, s75\n" \
"s_addc_u32 s25, s25, 0\n" \
"s_add_u32 s55, s55, 24576\n" \
"s_branch Lhin0_%=\n" \
".p2align 6\n" \
"Lhm0_%=:\n" \
"v_mfma_f32_16x16x32_bf16 v[156:159], a[124:127], a[28:31], v[156:159]\n" \
"s_add_u32 s24, s24, s75\n" \
"s_addc_u32 s25, s25, 0\n" \
"v_mfma_f32_16x16x32_bf16 v[184:187], a[128:131], a[28:31], v[184:187]\n" \
"s_add_u32 s55, s55, 24576\n" \
"v_mfma_f32_16x16x32_bf16 v[188:191], a[132:135], a[28:31], v[188:191]\n" \
"ds_read_b128 a[28:31], v238 offset:6144\n" \
"Lhin0_%=:\n" \
"s_waitcnt lgkmcnt(1)\n" \
"v_mfma_f32_16x16x32_bf16 v[0:3], a[32:35], a[0:3], v[0:3]\n" \
"s_add_u32 s63, s63, 1\n" \
"ds_read_b64_tr_b16 a[88:89], v239 offset:12288\n" \
"v_mfma_f32_16x16x32_bf16 v[8:11], a[32:35], a[4:7], v[8:11]\n" \
"s_add_u32 m0, s54, 0\n" \
"ds_read_b64_tr_b16 a[90:91], v239 offset:12800\n" \
"v_mfma_f32_16x16x32_bf16 v[16:19], a[32:35], a[8:11], v[16:19]\n" \
"buffer_load_dwordx4 v232, s[20:23], s40 offen lds\n" \
"v_mfma_f32_16x16x32_bf16 v[24:27], a[32:35], a[12:15], v[24:27]\n" \
"s_cmp_eq_u32 s63, 4\n" \
"s_cmov_b32 s55, s62\n" \
"s_cmov_b32 s63, 0\n" \
"v_mfma_f32_16x16x32_bf16 v[28:31], a[36:39], a[12:15], v[28:31]\n" \
"ds_read_b64_tr_b16 a[92:93], v240 offset:12288\n" \
"v_mfma_f32_16x16x32_bf16 v[20:23], a[36:39], a[8:11], v[20:23]\n" \
"s_add_u32 s74, s74, 1048576\n" \
"ds_read_b64_tr_b16 a[94:95], v240 offset:12800\n" \
"v_mfma_f32_16x16x32_bf16 v[12:15], a[36:39], a[4:7], v[12:15]\n" \
"s_add_u32 m0, s54, 1024\n" \
"ds_read_b64_tr_b16 a[96:97], v241 offset:12288\n" \
"v_mfma_f32_16x16x32_bf16 v[4:7], a[36:39], a[0:3], v[4:7]\n" \
"buffer_load_dwordx4 v233, s[20:23], s41 offen lds\n" \
"v_mfma_f32_16x16x32_bf16 v[32:35], a[40:43], a[0:3], v[32:35]\n" \
"s_cmp_eq_u32 s58, 15\n" \
"s_cselect_b32 s26, 0, s26\n" \
"s_cselect_b32 s55, 0x30000, s55\n" \
"v_mfma_f32_16x16x32_bf16 v[40:43], a[40:43], a[4:7], v[40:43]\n" \
"s_cmp_eq_u32 s58, 3\n" \
"s_cmov_b64 s[76:77], s[0:1]\n" \
"s_cmov_b32 s78, 4128768\n" \
"s_cmov_b32 s74, 0\n" \
"v_mfma_f32_16x16x32_bf16 v[48:51], a[40:43], a[8:11], v[48:51]\n" \
"s_add_u32 s57, s57, 1\n" \
"ds_read_b64_tr_b16 a[98:99], v241 offset:12800\n" \
"v_mfma_f32_16x16x32_bf16 v[56:59], a[40:43], a[12:15], v[56:59]\n" \
"s_add_u32 m0, s54, 2048\n" \
"ds_read_b64_tr_b16 a[100:101], v242 offset:12288\n" \
"v_mfma_f32_16x16x32_bf16 v[60:63], a[44:47], a[12:15], v[60:63]\n" \
"buffer_load_dwordx4 v232, s[20:23], s42 offen lds\n" \
"v_mfma_f32_16x16x32_bf16 v[52:55], a[44:47], a[8:11], v[52:55]\n" \
"s_cmp_eq_u32 s58, 7\n" \
"s_cmov_b64 s[76:77], s[34:35]\n" \
"s_cmov_b32 s78, 4194304\n" \
"s_cmov_b32 s74, 0\n" \
"v_mfma_f32_16x16x32_bf16 v[44:47], a[44:47], a[4:7], v[44:47]\n" \
"s_cmp_eq_u32 s57, 3\n" \
"s_cselect_b32 s56, s95, s85\n" \
"s_cselect_b32 s57, -1, s57\n" \
"v_mfma_f32_16x16x32_bf16 v[36:39], a[44:47], a[0:3], v[36:39]\n" \
"ds_read_b64_tr_b16 a[102:103], v242 offset:12800\n" \
"v_mfma_f32_16x16x32_bf16 v[64:67], a[48:51], a[0:3], v[64:67]\n" \
"v_mfma_f32_16x16x32_bf16 v[72:75], a[48:51], a[4:7], v[72:75]\n" \
"s_add_u32 m0, s54, 3072\n" \
"ds_read_b64_tr_b16 a[104:105], v239 offset:16384\n" \
"v_mfma_f32_16x16x32_bf16 v[80:83], a[48:51], a[8:11], v[80:83]\n" \
"buffer_load_dwordx4 v233, s[20:23], s43 offen lds\n" \
"v_mfma_f32_16x16x32_bf16 v[88:91], a[48:51], a[12:15], v[88:91]\n" \
"ds_read_b64_tr_b16 a[106:107], v239 offset:16896\n" \
"v_mfma_f32_16x16x32_bf16 v[92:95], a[52:55], a[12:15], v[92:95]\n" \
"ds_read_b64_tr_b16 a[108:109], v240 offset:16384\n" \
"v_mfma_f32_16x16x32_bf16 v[84:87], a[52:55], a[8:11], v[84:87]\n" \
"ds_read_b64_tr_b16 a[110:111], v240 offset:16896\n" \
"v_mfma_f32_16x16x32_bf16 v[76:79], a[52:55], a[4:7], v[76:79]\n" \
"s_add_u32 m0, s54, 4096\n" \
"ds_read_b64_tr_b16 a[112:113], v241 offset:16384\n" \
"v_mfma_f32_16x16x32_bf16 v[68:71], a[52:55], a[0:3], v[68:71]\n" \
"buffer_load_dwordx4 v232, s[20:23], s44 offen lds\n" \
"v_mfma_f32_16x16x32_bf16 v[96:99], a[56:59], a[0:3], v[96:99]\n" \
"ds_read_b64_tr_b16 a[114:115], v241 offset:16896\n" \
"v_mfma_f32_16x16x32_bf16 v[104:107], a[56:59], a[4:7], v[104:107]\n" \
"ds_read_b64_tr_b16 a[116:117], v242 offset:16384\n" \
"v_mfma_f32_16x16x32_bf16 v[112:115], a[56:59], a[8:11], v[112:115]\n" \
"ds_read_b64_tr_b16 a[118:119], v242 offset:16896\n" \
"v_mfma_f32_16x16x32_bf16 v[120:123], a[56:59], a[12:15], v[120:123]\n" \
"s_add_u32 m0, s54, 5120\n" \
"v_mfma_f32_16x16x32_bf16 v[124:127], a[60:63], a[12:15], v[124:127]\n" \
"buffer_load_dwordx4 v233, s[20:23], s45 offen lds\n" \
"v_mfma_f32_16x16x32_bf16 v[116:119], a[60:63], a[8:11], v[116:119]\n" \
"ds_read_b64_tr_b16 a[120:121], v239 offset:20480\n" \
"v_mfma_f32_16x16x32_bf16 v[108:111], a[60:63], a[4:7], v[108:111]\n" \
"ds_read_b64_tr_b16 a[122:123], v239 offset:20992\n" \
"v_mfma_f32_16x16x32_bf16 v[100:103], a[60:63], a[0:3], v[100:103]\n" \
"ds_read_b64_tr_b16 a[124:125], v240 offset:20480\n" \
"v_mfma_f32_16x16x32_bf16 v[128:131], a[64:67], a[0:3], v[128:131]\n" \
"ds_read_b64_tr_b16 a[126:127], v240 offset:20992\n" \
"v_mfma_f32_16x16x32_bf16 v[136:139], a[64:67], a[4:7], v[136:139]\n" \
"s_add_u32 m0, s54, 6144\n" \
"ds_read_b64_tr_b16 a[128:129], v241 offset:20480\n" \
"v_mfma_f32_16x16x32_bf16 v[144:147], a[64:67], a[8:11], v[144:147]\n" \
"s_waitcnt lgkmcnt(15)\n" \
"buffer_load_dwordx4 v232, s[20:23], s46 offen lds\n" \
"v_mfma_f32_16x16x32_bf16 v[152:155], a[64:67], a[12:15], v[152:155]\n" \
"ds_read_b64_tr_b16 a[130:131], v241 offset:20992\n" \
"v_mfma_f32_16x16x32_bf16 v[156:159], a[68:71], a[12:15], v[156:159]\n" \
"ds_read_b64_tr_b16 a[132:133], v242 offset:20480\n" \
"v_mfma_f32_16x16x32_bf16 v[148:151], a[68:71], a[8:11], v[148:151]\n" \
"ds_read_b64_tr_b16 a[134:135], v242 offset:20992\n" \
"v_mfma_f32_16x16x32_bf16 v[140:143], a[68:71], a[4:7], v[140:143]\n" \
"s_add_u32 m0, s54, 7168\n" \
"v_mfma_f32_16x16x32_bf16 v[132:135], a[68:71], a[0:3], v[132:135]\n" \
"buffer_load_dwordx4 v233, s[20:23], s47 offen lds\n" \
"v_mfma_f32_16x16x32_bf16 v[160:163], a[72:75], a[0:3], v[160:163]\n" \
"s_add_u32 s20, s20, s82\n" \
"s_addc_u32 s21, s21, s83\n" \
"v_mfma_f32_16x16x32_bf16 v[168:171], a[72:75], a[4:7], v[168:171]\n" \
"s_xor_b32 s54, s54, 32768\n" \
"v_mfma_f32_16x16x32_bf16 v[176:179], a[72:75], a[8:11], v[176:179]\n" \
"v_mfma_f32_16x16x32_bf16 v[184:187], a[72:75], a[12:15], v[184:187]\n" \
"v_mfma_f32_16x16x32_bf16 v[188:191], a[76:79], a[12:15], v[188:191]\n" \
"v_mfma_f32_16x16x32_bf16 v[180:183], a[76:79], a[8:11], v[180:183]\n" \
"v_mfma_f32_16x16x32_bf16 v[172:175], a[76:79], a[4:7], v[172:175]\n" \
"v_mfma_f32_16x16x32_bf16 v[164:167], a[76:79], a[0:3], v[164:167]\n" \
"s_waitcnt vmcnt(15) lgkmcnt(0)\n" \
"s_barrier\n" \
"v_mfma_f32_16x16x32_bf16 v[0:3], a[88:91], a[16:19], v[0:3]\n" \
"v_xor_b32 v237, 0x8000, v237\n" \
"v_xor_b32 v238, 0x8000, v238\n" \
"v_mfma_f32_16x16x32_bf16 v[4:7], a[92:95], a[16:19], v[4:7]\n" \
"v_add_u32 v239, s56, v239\n" \
"v_add_u32 v240, s56, v240\n" \
"v_mfma_f32_16x16x32_bf16 v[32:35], a[96:99], a[16:19], v[32:35]\n" \
"v_add_u32 v241, s56, v241\n" \
"v_add_u32 v242, s56, v242\n" \
"v_mfma_f32_16x16x32_bf16 v[36:39], a[100:103], a[16:19], v[36:39]\n" \
"s_add_u32 m0, s55, 0\n" \
"ds_read_b128 a[0:3], v237 offset:0\n" \
"v_mfma_f32_16x16x32_bf16 v[64:67], a[104:107], a[16:19], v[64:67]\n" \
"buffer_load_dwordx4 v234, s[24:27], s48 offen lds\n" \
"v_mfma_f32_16x16x32_bf16 v[68:71], a[108:111], a[16:19], v[68:71]\n" \
"ds_read_b128 a[4:7], v237 offset:2048\n" \
"v_mfma_f32_16x16x32_bf16 v[96:99], a[112:115], a[16:19], v[96:99]\n" \
"ds_read_b128 a[8:11], v237 offset:4096\n" \
"v_mfma_f32_16x16x32_bf16 v[100:103], a[116:119], a[16:19], v[100:103]\n" \
"ds_read_b128 a[12:15], v237 offset:6144\n" \
"v_mfma_f32_16x16x32_bf16 v[128:131], a[120:123], a[16:19], v[128:131]\n" \
"s_add_u32 m0, s55, 4096\n" \
"ds_read_b64_tr_b16 a[32:33], v239 offset:0\n" \
"v_mfma_f32_16x16x32_bf16 v[132:135], a[124:127], a[16:19], v[132:135]\n" \
"buffer_load_dwordx4 v234, s[24:27], s49 offen lds\n" \
"v_mfma_f32_16x16x32_bf16 v[160:163], a[128:131], a[16:19], v[160:163]\n" \
"ds_read_b64_tr_b16 a[34:35], v239 offset:512\n" \
"v_mfma_f32_16x16x32_bf16 v[164:167], a[132:135], a[16:19], v[164:167]\n" \
"ds_read_b64_tr_b16 a[36:37], v240 offset:0\n" \
"v_mfma_f32_16x16x32_bf16 v[8:11], a[88:91], a[20:23], v[8:11]\n" \
"ds_read_b64_tr_b16 a[38:39], v240 offset:512\n" \
"v_mfma_f32_16x16x32_bf16 v[12:15], a[92:95], a[20:23], v[12:15]\n" \
"s_add_u32 m0, s55, 8192\n" \
"ds_read_b128 a[16:19], v238 offset:0\n" \
"v_mfma_f32_16x16x32_bf16 v[40:43], a[96:99], a[20:23], v[40:43]\n" \
"buffer_load_dwordx4 v234, s[24:27], s50 offen lds\n" \
"v_mfma_f32_16x16x32_bf16 v[44:47], a[100:103], a[20:23], v[44:47]\n" \
"ds_read_b64_tr_b16 a[40:41], v241 offset:0\n" \
"v_mfma_f32_16x16x32_bf16 v[72:75], a[104:107], a[20:23], v[72:75]\n" \
"ds_read_b64_tr_b16 a[42:43], v241 offset:512\n" \
"v_mfma_f32_16x16x32_bf16 v[76:79], a[108:111], a[20:23], v[76:79]\n" \
"ds_read_b64_tr_b16 a[44:45], v242 offset:0\n" \
"v_mfma_f32_16x16x32_bf16 v[104:107], a[112:115], a[20:23], v[104:107]\n" \
"s_add_u32 m0, s55, 12288\n" \
"ds_read_b64_tr_b16 a[46:47], v242 offset:512\n" \
"v_mfma_f32_16x16x32_bf16 v[108:111], a[116:119], a[20:23], v[108:111]\n" \
"buffer_load_dwordx4 v234, s[24:27], s51 offen lds\n" \
"v_mfma_f32_16x16x32_bf16 v[136:139], a[120:123], a[20:23], v[136:139]\n" \
"ds_read_b64_tr_b16 a[48:49], v239 offset:4096\n" \
"v_mfma_f32_16x16x32_bf16 v[140:143], a[124:127], a[20:23], v[140:143]\n" \
"ds_read_b64_tr_b16 a[50:51], v239 offset:4608\n" \
"v_mfma_f32_16x16x32_bf16 v[168:171], a[128:131], a[20:23], v[168:171]\n" \
"ds_read_b64_tr_b16 a[52:53], v240 offset:4096\n" \
"v_mfma_f32_16x16x32_bf16 v[172:175], a[132:135], a[20:23], v[172:175]\n" \
"ds_read_b64_tr_b16 a[54:55], v240 offset:4608\n" \
"v_mfma_f32_16x16x32_bf16 v[16:19], a[88:91], a[24:27], v[16:19]\n" \
"s_add_u32 m0, s55, 16384\n" \
"ds_read_b64_tr_b16 a[56:57], v241 offset:4096\n" \
"v_mfma_f32_16x16x32_bf16 v[20:23], a[92:95], a[24:27], v[20:23]\n" \
"buffer_load_dwordx4 v234, s[24:27], s52 offen lds\n" \
"v_mfma_f32_16x16x32_bf16 v[48:51], a[96:99], a[24:27], v[48:51]\n" \
"ds_read_b128 a[20:23], v238 offset:2048\n" \
"ds_read_b64_tr_b16 a[58:59], v241 offset:4608\n" \
"v_mfma_f32_16x16x32_bf16 v[52:55], a[100:103], a[24:27], v[52:55]\n" \
"ds_read_b64_tr_b16 a[60:61], v242 offset:4096\n" \
"v_mfma_f32_16x16x32_bf16 v[80:83], a[104:107], a[24:27], v[80:83]\n" \
"ds_read_b64_tr_b16 a[62:63], v242 offset:4608\n" \
"v_mfma_f32_16x16x32_bf16 v[84:87], a[108:111], a[24:27], v[84:87]\n" \
"s_add_u32 m0, s55, 20480\n" \
"ds_read_b64_tr_b16 a[64:65], v239 offset:8192\n" \
"v_mfma_f32_16x16x32_bf16 v[112:115], a[112:115], a[24:27], v[112:115]\n" \
"buffer_load_dwordx4 v234, s[24:27], s53 offen lds\n" \
"v_mfma_f32_16x16x32_bf16 v[116:119], a[116:119], a[24:27], v[116:119]\n" \
"ds_read_b64_tr_b16 a[66:67], v239 offset:8704\n" \
"v_mfma_f32_16x16x32_bf16 v[144:147], a[120:123], a[24:27], v[144:147]\n" \
"ds_read_b64_tr_b16 a[68:69], v240 offset:8192\n" \
"v_mfma_f32_16x16x32_bf16 v[148:151], a[124:127], a[24:27], v[148:151]\n" \
"ds_read_b64_tr_b16 a[70:71], v240 offset:8704\n" \
"v_mfma_f32_16x16x32_bf16 v[176:179], a[128:131], a[24:27], v[176:179]\n" \
"ds_read_b64_tr_b16 a[72:73], v241 offset:8192\n" \
"v_mfma_f32_16x16x32_bf16 v[180:183], a[132:135], a[24:27], v[180:183]\n" \
"buffer_load_dword a246, v254, s[76:79], s74 offen\n" \
"v_mfma_f32_16x16x32_bf16 v[24:27], a[88:91], a[28:31], v[24:27]\n" \
"ds_read_b64_tr_b16 a[74:75], v241 offset:8704\n" \
"v_mfma_f32_16x16x32_bf16 v[28:31], a[92:95], a[28:31], v[28:31]\n" \
"ds_read_b128 a[24:27], v238 offset:4096\n" \
"ds_read_b64_tr_b16 a[76:77], v242 offset:8192\n" \
"v_mfma_f32_16x16x32_bf16 v[56:59], a[96:99], a[28:31], v[56:59]\n" \
"ds_read_b64_tr_b16 a[78:79], v242 offset:8704\n" \
"v_mfma_f32_16x16x32_bf16 v[60:63], a[100:103], a[28:31], v[60:63]\n" \
"v_mfma_f32_16x16x32_bf16 v[88:91], a[104:107], a[28:31], v[88:91]\n" \
"v_mfma_f32_16x16x32_bf16 v[92:95], a[108:111], a[28:31], v[92:95]\n" \
"s_add_u32 s58, s58, 1\n" \
"v_mfma_f32_16x16x32_bf16 v[120:123], a[112:115], a[28:31], v[120:123]\n" \
"v_mfma_f32_16x16x32_bf16 v[124:127], a[116:119], a[28:31], v[124:127]\n" \
"v_mfma_f32_16x16x32_bf16 v[152:155], a[120:123], a[28:31], v[152:155]\n" \
"s_bitcmp1_b32 86018, s58\n" \
"s_cbranch_scc0 Lhm0_%=\n" \
"s_cmp_lt_u32 s58, 16\n" \
"s_cbranch_scc1 Lhsp0_%=\n" \
"v_mfma_f32_16x16x32_bf16 v[156:159], a[124:127], a[28:31], v[156:159]\n" \
"v_mfma_f32_16x16x32_bf16 v[184:187], a[128:131], a[28:31], v[184:187]\n" \
"v_mfma_f32_16x16x32_bf16 v[188:191], a[132:135], a[28:31], v[188:191]\n" \
"s_branch Lh_epi_%=\n" \
"Lhsp0_%=:\n" \
"s_cmp_ge_u32 s58, 12\n" \
"s_cbranch_scc1 Lhs20_%=\n" \
"s_add_u32 s24, s24, s75\n" \
"s_addc_u32 s25, s25, 0\n" \
"s_add_u32 s55, s55, 24576\n" \
"s_add_u32 s63, s63, 1\n" \
"s_cmp_eq_u32 s63, 4\n" \
"s_cmov_b32 s55, s62\n" \
"s_cmov_b32 s63, 0\n" \
"s_add_u32 m0, s55, 0\n" \
"s_nop 0\n" \
"buffer_load_dwordx4 v234, s[24:27], s48 offen lds\n" \
"s_add_u32 m0, s55, 4096\n" \
"s_nop 0\n" \
"buffer_load_dwordx4 v234, s[24:27], s49 offen lds\n" \
"s_add_u32 m0, s55, 8192\n" \
"s_nop 0\n" \
"buffer_load_dwordx4 v234, s[24:27], s50 offen lds\n" \
"s_add_u32 m0, s55, 12288\n" \
"s_nop 0\n" \
"buffer_load_dwordx4 v234, s[24:27], s51 offen lds\n" \
"s_add_u32 m0, s55, 16384\n" \
"s_nop 0\n" \
"buffer_load_dwordx4 v234, s[24:27], s52 offen lds\n" \
"s_add_u32 m0, s55, 20480\n" \
"s_nop 0\n" \
"buffer_load_dwordx4 v234, s[24:27], s53 offen lds\n" \
"s_branch Lhm0_%=\n" \
"Lhs10_%=:\n" \
"Lhs20_%=:\n" \
"v_accvgpr_read_b32 v247, a253\n" \
"s_cmp_eq_u32 s58, 14\n" \
"s_cbranch_scc1 Lhs40_%=\n" \
"v_accvgpr_read_b32 v234, a233\n" \
"v_readlane_b32 s60, v247, 0\n" \
"s_mov_b32 s75, 196608\n" \
"s_add_u32 s48, s81, 0\n" \
"s_add_u32 s49, s80, 0\n" \
"s_add_u32 s50, s81, 65536\n" \
"s_add_u32 s51, s80, 65536\n" \
"s_add_u32 s52, s81, 131072\n" \
"s_add_u32 s53, s80, 131072\n" \
"s_add_u32 s24, s68, s60\n" \
"s_addc_u32 s25, s69, 0\n" \
"s_sub_u32 s24, s24, s75\n" \
"s_subb_u32 s25, s25, 0\n" \
"s_getpc_b64 s[28:29]\n" \
"Lct1_%=:\n" \
"s_add_u32 s28, s28, Lh_epi_%=-Lct1_%=\n" \
"s_addc_u32 s29, s29, 0\n" \
"s_mov_b32 s30, Lq_entry_%=-Lh_epi_%=\n" \
"s_mov_b32 s31, 0x20000\n" \
"v_accvgpr_read_b32 v249, a243\n" \
"buffer_load_dword a207, v249, s[28:31], 0 offen\n" \
"s_branch Lhm0_%=\n" \
"Lhs40_%=:\n" \
"v_accvgpr_read_b32 v232, a233\n" \
"v_readlane_b32 s60, v247, 12\n" \
"v_readlane_b32 s82, v247, 8\n" \
"v_readlane_b32 s61, v247, 6\n" \
"s_add_u32 s40, s81, 0\n" \
"s_add_u32 s41, s81, 16384\n" \
"s_add_u32 s42, s81, 32768\n" \
"s_add_u32 s43, s81, 49152\n" \
"s_add_u32 s44, s80, 0\n" \
"s_add_u32 s45, s80, 16384\n" \
"s_add_u32 s46, s80, 32768\n" \
"s_add_u32 s47, s80, 49152\n" \
"s_add_u32 s20, s68, s60\n" \
"s_addc_u32 s21, s69, 0\n" \
"s_sub_u32 s82, s82, s60\n" \
"s_ashr_i32 s83, s82, 31\n" \
"s_add_u32 s24, s68, s61\n" \
"s_addc_u32 s25, s69, 0\n" \
"s_sub_u32 s24, s24, s75\n" \
"s_subb_u32 s25, s25, 0\n" \
"v_subrev_u32 v232, 0, v232\n" \
"v_mov_b32 v233, v232\n" \
"s_branch Lhm0_%=\n" \
"Lhe1_%=:\n" \
"s_add_u32 m0, s55, 0\n" \
"s_nop 0\n" \
"buffer_load_dwordx4 v234, s[24:27], s48 offen lds\n" \
"s_add_u32 m0, s55, 4096\n" \
"s_nop 0\n" \
"buffer_load_dwordx4 v234, s[24:27], s49 offen lds\n" \
"s_add_u32 m0, s55, 8192\n" \
"s_nop 0\n" \
"buffer_load_dwordx4 v234, s[24:27], s50 offen lds\n" \
"s_add_u32 m0, s55, 12288\n" \
"s_nop 0\n" \
"buffer_load_dwordx4 v234, s[24:27], s51 offen lds\n" \
"s_add_u32 m0, s55, 16384\n" \
"s_nop 0\n" \
"buffer_load_dwordx4 v234, s[24:27], s52 offen lds\n" \
"s_add_u32 m0, s55, 20480\n" \
"s_nop 0\n" \
"buffer_load_dwordx4 v234, s[24:27], s53 offen lds\n" \
"s_add_u32 s24, s24, s75\n" \
"s_addc_u32 s25, s25, 0\n" \
"s_add_u32 s55, s55, 24576\n" \
"s_branch Lhin1_%=\n" \
".p2align 6\n" \
"Lhm1_%=:\n" \
"v_mfma_f32_16x16x32_bf16 v[156:159], a[124:127], a[28:31], v[156:159]\n" \
"s_add_u32 s24, s24, s75\n" \
"s_addc_u32 s25, s25, 0\n" \
"v_mfma_f32_16x16x32_bf16 v[184:187], a[128:131], a[28:31], v[184:187]\n" \
"s_add_u32 s55, s55, 24576\n" \
"v_mfma_f32_16x16x32_bf16 v[188:191], a[132:135], a[28:31], v[188:191]\n" \
"ds_read_b128 a[28:31], v238 offset:6144\n" \
"Lhin1_%=:\n" \
"s_waitcnt lgkmcnt(1)\n" \
"v_mfma_f32_16x16x32_bf16 v[0:3], a[32:35], a[0:3], v[0:3]\n" \
"s_add_u32 s63, s63, 1\n" \
"ds_read_b64_tr_b16 a[88:89], v239 offset:12288\n" \
"v_mfma_f32_16x16x32_bf16 v[8:11], a[32:35], a[4:7], v[8:11]\n" \
"ds_read_b64_tr_b16 a[90:91], v239 offset:12800\n" \
"v_mfma_f32_16x16x32_bf16 v[16:19], a[32:35], a[8:11], v[16:19]\n" \
"s_add_u32 m0, s54, 0\n" \
"ds_read_b64_tr_b16 a[92:93], v240 offset:12288\n" \
"v_mfma_f32_16x16x32_bf16 v[24:27], a[32:35], a[12:15], v[24:27]\n" \
"buffer_load_dwordx4 v232, s[20:23], s40 offen lds\n" \
"v_mfma_f32_16x16x32_bf16 v[28:31], a[36:39], a[12:15], v[28:31]\n" \
"s_cmp_eq_u32 s63, 4\n" \
"s_cmov_b32 s55, s62\n" \
"s_cmov_b32 s63, 0\n" \
"v_mfma_f32_16x16x32_bf16 v[20:23], a[36:39], a[8:11], v[20:23]\n" \
"s_add_u32 s74, s74, 1048576\n" \
"ds_read_b64_tr_b16 a[94:95], v240 offset:12800\n" \
"v_mfma_f32_16x16x32_bf16 v[12:15], a[36:39], a[4:7], v[12:15]\n" \
"s_cmp_eq_u32 s58, 15\n" \
"s_cselect_b32 s26, 0, s26\n" \
"s_cselect_b32 s55, 0x30000, s55\n" \
"v_mfma_f32_16x16x32_bf16 v[4:7], a[36:39], a[0:3], v[4:7]\n" \
"s_add_u32 m0, s54, 1024\n" \
"ds_read_b64_tr_b16 a[96:97], v241 offset:12288\n" \
"v_mfma_f32_16x16x32_bf16 v[32:35], a[40:43], a[0:3], v[32:35]\n" \
"buffer_load_dwordx4 v233, s[20:23], s41 offen lds\n" \
"v_mfma_f32_16x16x32_bf16 v[40:43], a[40:43], a[4:7], v[40:43]\n" \
"s_cmp_eq_u32 s58, 3\n" \
"s_cmov_b64 s[76:77], s[0:1]\n" \
"s_cmov_b32 s78, 4128768\n" \
"s_cmov_b32 s74, 0\n" \
"v_mfma_f32_16x16x32_bf16 v[48:51], a[40:43], a[8:11], v[48:51]\n" \
"s_add_u32 s57, s57, 1\n" \
"ds_read_b64_tr_b16 a[98:99], v241 offset:12800\n" \
"v_mfma_f32_16x16x32_bf16 v[56:59], a[40:43], a[12:15], v[56:59]\n" \
"ds_read_b64_tr_b16 a[100:101], v242 offset:12288\n" \
"v_mfma_f32_16x16x32_bf16 v[60:63], a[44:47], a[12:15], v[60:63]\n" \
"s_add_u32 m0, s54, 2048\n" \
"ds_read_b64_tr_b16 a[102:103], v242 offset:12800\n" \
"v_mfma_f32_16x16x32_bf16 v[52:55], a[44:47], a[8:11], v[52:55]\n" \
"buffer_load_dwordx4 v232, s[20:23], s42 offen lds\n" \
"v_mfma_f32_16x16x32_bf16 v[44:47], a[44:47], a[4:7], v[44:47]\n" \
"s_cmp_eq_u32 s58, 7\n" \
"s_cmov_b64 s[76:77], s[34:35]\n" \
"s_cmov_b32 s78, 4194304\n" \
"s_cmov_b32 s74, 0\n" \
"v_mfma_f32_16x16x32_bf16 v[36:39], a[44:47], a[0:3], v[36:39]\n" \
"s_cmp_eq_u32 s57, 3\n" \
"s_cselect_b32 s56, s95, s85\n" \
"s_cselect_b32 s57, -1, s57\n" \
"v_mfma_f32_16x16x32_bf16 v[64:67], a[48:51], a[0:3], v[64:67]\n" \
"v_mfma_f32_16x16x32_bf16 v[72:75], a[48:51], a[4:7], v[72:75]\n" \
"ds_read_b64_tr_b16 a[104:105], v239 offset:16384\n" \
"v_mfma_f32_16x16x32_bf16 v[80:83], a[48:51], a[8:11], v[80:83]\n" \
"s_add_u32 m0, s54, 3072\n" \
"ds_read_b64_tr_b16 a[106:107], v239 offset:16896\n" \
"v_mfma_f32_16x16x32_bf16 v[88:91], a[48:51], a[12:15], v[88:91]\n" \
"buffer_load_dwordx4 v233, s[20:23], s43 offen lds\n" \
"v_mfma_f32_16x16x32_bf16 v[92:95], a[52:55], a[12:15], v[92:95]\n" \
"ds_read_b64_tr_b16 a[108:109], v240 offset:16384\n" \
"v_mfma_f32_16x16x32_bf16 v[84:87], a[52:55], a[8:11], v[84:87]\n" \
"ds_read_b64_tr_b16 a[110:111], v240 offset:16896\n" \
"v_mfma_f32_16x16x32_bf16 v[76:79], a[52:55], a[4:7], v[76:79]\n" \
"ds_read_b64_tr_b16 a[112:113], v241 offset:16384\n" \
"v_mfma_f32_16x16x32_bf16 v[68:71], a[52:55], a[0:3], v[68:71]\n" \
"s_add_u32 m0, s54, 4096\n" \
"ds_read_b64_tr_b16 a[114:115], v241 offset:16896\n" \
"v_mfma_f32_16x16x32_bf16 v[96:99], a[56:59], a[0:3], v[96:99]\n" \
"buffer_load_dwordx4 v232, s[20:23], s44 offen lds\n" \
"v_mfma_f32_16x16x32_bf16 v[104:107], a[56:59], a[4:7], v[104:107]\n" \
"ds_read_b64_tr_b16 a[116:117], v242 offset:16384\n" \
"v_mfma_f32_16x16x32_bf16 v[112:115], a[56:59], a[8:11], v[112:115]\n" \
"ds_read_b64_tr_b16 a[118:119], v242 offset:16896\n" \
"v_mfma_f32_16x16x32_bf16 v[120:123], a[56:59], a[12:15], v[120:123]\n" \
"v_mfma_f32_16x16x32_bf16 v[124:127], a[60:63], a[12:15], v[124:127]\n" \
"s_add_u32 m0, s54, 5120\n" \
"ds_read_b64_tr_b16 a[120:121], v239 offset:20480\n" \
"v_mfma_f32_16x16x32_bf16 v[116:119], a[60:63], a[8:11], v[116:119]\n" \
"buffer_load_dwordx4 v233, s[20:23], s45 offen lds\n" \
"v_mfma_f32_16x16x32_bf16 v[108:111], a[60:63], a[4:7], v[108:111]\n" \
"ds_read_b64_tr_b16 a[122:123], v239 offset:20992\n" \
"v_mfma_f32_16x16x32_bf16 v[100:103], a[60:63], a[0:3], v[100:103]\n" \
"ds_read_b64_tr_b16 a[124:125], v240 offset:20480\n" \
"v_mfma_f32_16x16x32_bf16 v[128:131], a[64:67], a[0:3], v[128:131]\n" \
"ds_read_b64_tr_b16 a[126:127], v240 offset:20992\n" \
"v_mfma_f32_16x16x32_bf16 v[136:139], a[64:67], a[4:7], v[136:139]\n" \
"ds_read_b64_tr_b16 a[128:129], v241 offset:20480\n" \
"v_mfma_f32_16x16x32_bf16 v[144:147], a[64:67], a[8:11], v[144:147]\n" \
"s_add_u32 m0, s54, 6144\n" \
"ds_read_b64_tr_b16 a[130:131], v241 offset:20992\n" \
"v_mfma_f32_16x16x32_bf16 v[152:155], a[64:67], a[12:15], v[152:155]\n" \
"s_waitcnt lgkmcnt(15)\n" \
"buffer_load_dwordx4 v232, s[20:23], s46 offen lds\n" \
"v_mfma_f32_16x16x32_bf16 v[156:159], a[68:71], a[12:15], v[156:159]\n" \
"ds_read_b64_tr_b16 a[132:133], v242 offset:20480\n" \
"v_mfma_f32_16x16x32_bf16 v[148:151], a[68:71], a[8:11], v[148:151]\n" \
"ds_read_b64_tr_b16 a[134:135], v242 offset:20992\n" \
"v_mfma_f32_16x16x32_bf16 v[140:143], a[68:71], a[4:7], v[140:143]\n" \
"v_mfma_f32_16x16x32_bf16 v[132:135], a[68:71], a[0:3], v[132:135]\n" \
"s_add_u32 m0, s54, 7168\n" \
"v_mfma_f32_16x16x32_bf16 v[160:163], a[72:75], a[0:3], v[160:163]\n" \
"buffer_load_dwordx4 v233, s[20:23], s47 offen lds\n" \
"v_mfma_f32_16x16x32_bf16 v[168:171], a[72:75], a[4:7], v[168:171]\n" \
"s_add_u32 s20, s20, s82\n" \
"s_addc_u32 s21, s21, s83\n" \
"v_mfma_f32_16x16x32_bf16 v[176:179], a[72:75], a[8:11], v[176:179]\n" \
"s_xor_b32 s54, s54, 32768\n" \
"v_mfma_f32_16x16x32_bf16 v[184:187], a[72:75], a[12:15], v[184:187]\n" \
"v_mfma_f32_16x16x32_bf16 v[188:191], a[76:79], a[12:15], v[188:191]\n" \
"v_mfma_f32_16x16x32_bf16 v[180:183], a[76:79], a[8:11], v[180:183]\n" \
"v_mfma_f32_16x16x32_bf16 v[172:175], a[76:79], a[4:7], v[172:175]\n" \
"v_mfma_f32_16x16x32_bf16 v[164:167], a[76:79], a[0:3], v[164:167]\n" \
"s_waitcnt vmcnt(15) lgkmcnt(0)\n" \
"s_barrier\n" \
"v_mfma_f32_16x16x32_bf16 v[0:3], a[88:91], a[16:19], v[0:3]\n" \
"v_xor_b32 v237, 0x8000, v237\n" \
"v_xor_b32 v238, 0x8000, v238\n" \
"v_mfma_f32_16x16x32_bf16 v[4:7], a[92:95], a[16:19], v[4:7]\n" \
"v_add_u32 v239, s56, v239\n" \
"v_add_u32 v240, s56, v240\n" \
"v_mfma_f32_16x16x32_bf16 v[32:35], a[96:99], a[16:19], v[32:35]\n" \
"v_add_u32 v241, s56, v241\n" \
"v_add_u32 v242, s56, v242\n" \
"v_mfma_f32_16x16x32_bf16 v[36:39], a[100:103], a[16:19], v[36:39]\n" \
"ds_read_b128 a[0:3], v237 offset:0\n" \
"v_mfma_f32_16x16x32_bf16 v[64:67], a[104:107], a[16:19], v[64:67]\n" \
"s_add_u32 m0, s55, 0\n" \
"ds_read_b128 a[4:7], v237 offset:2048\n" \
"v_mfma_f32_16x16x32_bf16 v[68:71], a[108:111], a[16:19], v[68:71]\n" \
"buffer_load_dwordx4 v234, s[24:27], s48 offen lds\n" \
"v_mfma_f32_16x16x32_bf16 v[96:99], a[112:115], a[16:19], v[96:99]\n" \
"ds_read_b128 a[8:11], v237 offset:4096\n" \
"v_mfma_f32_16x16x32_bf16 v[100:103], a[116:119], a[16:19], v[100:103]\n" \
"ds_read_b128 a[12:15], v237 offset:6144\n" \
"v_mfma_f32_16x16x32_bf16 v[128:131], a[120:123], a[16:19], v[128:131]\n" \
"ds_read_b64_tr_b16 a[32:33], v239 offset:0\n" \
"v_mfma_f32_16x16x32_bf16 v[132:135], a[124:127], a[16:19], v[132:135]\n" \
"s_add_u32 m0, s55, 4096\n" \
"ds_read_b64_tr_b16 a[34:35], v239 offset:512\n" \
"v_mfma_f32_16x16x32_bf16 v[160:163], a[128:131], a[16:19], v[160:163]\n" \
"buffer_load_dwordx4 v234, s[24:27], s49 offen lds\n" \
"v_mfma_f32_16x16x32_bf16 v[164:167], a[132:135], a[16:19], v[164:167]\n" \
"ds_read_b64_tr_b16 a[36:37], v240 offset:0\n" \
"v_mfma_f32_16x16x32_bf16 v[8:11], a[88:91], a[20:23], v[8:11]\n" \
"ds_read_b64_tr_b16 a[38:39], v240 offset:512\n" \
"v_mfma_f32_16x16x32_bf16 v[12:15], a[92:95], a[20:23], v[12:15]\n" \
"ds_read_b128 a[16:19], v238 offset:0\n" \
"ds_read_b64_tr_b16 a[40:41], v241 offset:0\n" \
"v_mfma_f32_16x16x32_bf16 v[40:43], a[96:99], a[20:23], v[40:43]\n" \
"s_add_u32 m0, s55, 8192\n" \
"ds_read_b64_tr_b16 a[42:43], v241 offset:512\n" \
"v_mfma_f32_16x16x32_bf16 v[44:47], a[100:103], a[20:23], v[44:47]\n" \
"buffer_load_dwordx4 v234, s[24:27], s50 offen lds\n" \
"v_mfma_f32_16x16x32_bf16 v[72:75], a[104:107], a[20:23], v[72:75]\n" \
"ds_read_b64_tr_b16 a[44:45], v242 offset:0\n" \
"v_mfma_f32_16x16x32_bf16 v[76:79], a[108:111], a[20:23], v[76:79]\n" \
"ds_read_b64_tr_b16 a[46:47], v242 offset:512\n" \
"v_mfma_f32_16x16x32_bf16 v[104:107], a[112:115], a[20:23], v[104:107]\n" \
"ds_read_b64_tr_b16 a[48:49], v239 offset:4096\n" \
"v_mfma_f32_16x16x32_bf16 v[108:111], a[116:119], a[20:23], v[108:111]\n" \
"s_add_u32 m0, s55, 12288\n" \
"ds_read_b64_tr_b16 a[50:51], v239 offset:4608\n" \
"v_mfma_f32_16x16x32_bf16 v[136:139], a[120:123], a[20:23], v[136:139]\n" \
"buffer_load_dwordx4 v234, s[24:27], s51 offen lds\n" \
"v_mfma_f32_16x16x32_bf16 v[140:143], a[124:127], a[20:23], v[140:143]\n" \
"ds_read_b64_tr_b16 a[52:53], v240 offset:4096\n" \
"v_mfma_f32_16x16x32_bf16 v[168:171], a[128:131], a[20:23], v[168:171]\n" \
"ds_read_b64_tr_b16 a[54:55], v240 offset:4608\n" \
"v_mfma_f32_16x16x32_bf16 v[172:175], a[132:135], a[20:23], v[172:175]\n" \
"ds_read_b64_tr_b16 a[56:57], v241 offset:4096\n" \
"v_mfma_f32_16x16x32_bf16 v[16:19], a[88:91], a[24:27], v[16:19]\n" \
"ds_read_b64_tr_b16 a[58:59], v241 offset:4608\n" \
"v_mfma_f32_16x16x32_bf16 v[20:23], a[92:95], a[24:27], v[20:23]\n" \
"s_add_u32 m0, s55, 16384\n" \
"ds_read_b128 a[20:23], v238 offset:2048\n" \
"v_mfma_f32_16x16x32_bf16 v[48:51], a[96:99], a[24:27], v[48:51]\n" \
"buffer_load_dwordx4 v234, s[24:27], s52 offen lds\n" \
"v_mfma_f32_16x16x32_bf16 v[52:55], a[100:103], a[24:27], v[52:55]\n" \
"ds_read_b64_tr_b16 a[60:61], v242 offset:4096\n" \
"v_mfma_f32_16x16x32_bf16 v[80:83], a[104:107], a[24:27], v[80:83]\n" \
"ds_read_b64_tr_b16 a[62:63], v242 offset:4608\n" \
"v_mfma_f32_16x16x32_bf16 v[84:87], a[108:111], a[24:27], v[84:87]\n" \
"ds_read_b64_tr_b16 a[64:65], v239 offset:8192\n" \
"v_mfma_f32_16x16x32_bf16 v[112:115], a[112:115], a[24:27], v[112:115]\n" \
"s_add_u32 m0, s55, 20480\n" \
"ds_read_b64_tr_b16 a[66:67], v239 offset:8704\n" \
"v_mfma_f32_16x16x32_bf16 v[116:119], a[116:119], a[24:27], v[116:119]\n" \
"buffer_load_dwordx4 v234, s[24:27], s53 offen lds\n" \
"v_mfma_f32_16x16x32_bf16 v[144:147], a[120:123], a[24:27], v[144:147]\n" \
"ds_read_b64_tr_b16 a[68:69], v240 offset:8192\n" \
"v_mfma_f32_16x16x32_bf16 v[148:151], a[124:127], a[24:27], v[148:151]\n" \
"ds_read_b64_tr_b16 a[70:71], v240 offset:8704\n" \
"v_mfma_f32_16x16x32_bf16 v[176:179], a[128:131], a[24:27], v[176:179]\n" \
"ds_read_b64_tr_b16 a[72:73], v241 offset:8192\n" \
"v_mfma_f32_16x16x32_bf16 v[180:183], a[132:135], a[24:27], v[180:183]\n" \
"ds_read_b64_tr_b16 a[74:75], v241 offset:8704\n" \
"v_mfma_f32_16x16x32_bf16 v[24:27], a[88:91], a[28:31], v[24:27]\n" \
"buffer_load_dword a246, v254, s[76:79], s74 offen\n" \
"v_mfma_f32_16x16x32_bf16 v[28:31], a[92:95], a[28:31], v[28:31]\n" \
"ds_read_b128 a[24:27], v238 offset:4096\n" \
"ds_read_b64_tr_b16 a[76:77], v242 offset:8192\n" \
"v_mfma_f32_16x16x32_bf16 v[56:59], a[96:99], a[28:31], v[56:59]\n" \
"ds_read_b64_tr_b16 a[78:79], v242 offset:8704\n" \
"v_mfma_f32_16x16x32_bf16 v[60:63], a[100:103], a[28:31], v[60:63]\n" \
"v_mfma_f32_16x16x32_bf16 v[88:91], a[104:107], a[28:31], v[88:91]\n" \
"v_mfma_f32_16x16x32_bf16 v[92:95], a[108:111], a[28:31], v[92:95]\n" \
"s_add_u32 s58, s58, 1\n" \
"v_mfma_f32_16x16x32_bf16 v[120:123], a[112:115], a[28:31], v[120:123]\n" \
"v_mfma_f32_16x16x32_bf16 v[124:127], a[116:119], a[28:31], v[124:127]\n" \
"v_mfma_f32_16x16x32_bf16 v[152:155], a[120:123], a[28:31], v[152:155]\n" \
"s_bitcmp1_b32 86018, s58\n" \
"s_cbranch_scc0 Lhm1_%=\n" \
"s_cmp_lt_u32 s58, 16\n" \
"s_cbranch_scc1 Lhsp1_%=\n" \
"v_mfma_f32_16x16x32_bf16 v[156:159], a[124:127], a[28:31], v[156:159]\n" \
"v_mfma_f32_16x16x32_bf16 v[184:187], a[128:131], a[28:31], v[184:187]\n" \
"v_mfma_f32_16x16x32_bf16 v[188:191], a[132:135], a[28:31], v[188:191]\n" \
"s_branch Lh_epi_%=\n" \
"Lhsp1_%=:\n" \
"s_cmp_ge_u32 s58, 12\n" \
"s_cbranch_scc1 Lhs21_%=\n" \
"s_add_u32 s24, s24, s75\n" \
"s_addc_u32 s25, s25, 0\n" \
"s_add_u32 s55, s55, 24576\n" \
"s_add_u32 s63, s63, 1\n" \
"s_cmp_eq_u32 s63, 4\n" \
"s_cmov_b32 s55, s62\n" \
"s_cmov_b32 s63, 0\n" \
"s_add_u32 m0, s55, 0\n" \
"s_nop 0\n" \
"buffer_load_dwordx4 v234, s[24:27], s48 offen lds\n" \
"s_add_u32 m0, s55, 4096\n" \
"s_nop 0\n" \
"buffer_load_dwordx4 v234, s[24:27], s49 offen lds\n" \
"s_add_u32 m0, s55, 8192\n" \
"s_nop 0\n" \
"buffer_load_dwordx4 v234, s[24:27], s50 offen lds\n" \
"s_add_u32 m0, s55, 12288\n" \
"s_nop 0\n" \
"buffer_load_dwordx4 v234, s[24:27], s51 offen lds\n" \
"s_add_u32 m0, s55, 16384\n" \
"s_nop 0\n" \
"buffer_load_dwordx4 v234, s[24:27], s52 offen lds\n" \
"s_add_u32 m0, s55, 20480\n" \
"s_nop 0\n" \
"buffer_load_dwordx4 v234, s[24:27], s53 offen lds\n" \
"s_branch Lhm1_%=\n" \
"Lhs11_%=:\n" \
"Lhs21_%=:\n" \
"v_accvgpr_read_b32 v247, a253\n" \
"s_cmp_eq_u32 s58, 14\n" \
"s_cbranch_scc1 Lhs41_%=\n" \
"v_accvgpr_read_b32 v234, a233\n" \
"v_readlane_b32 s60, v247, 0\n" \
"s_mov_b32 s75, 196608\n" \
"s_add_u32 s48, s81, 0\n" \
"s_add_u32 s49, s80, 0\n" \
"s_add_u32 s50, s81, 65536\n" \
"s_add_u32 s51, s80, 65536\n" \
"s_add_u32 s52, s81, 131072\n" \
"s_add_u32 s53, s80, 131072\n" \
"s_add_u32 s24, s68, s60\n" \
"s_addc_u32 s25, s69, 0\n" \
"s_sub_u32 s24, s24, s75\n" \
"s_subb_u32 s25, s25, 0\n" \
"s_getpc_b64 s[28:29]\n" \
"Lct2_%=:\n" \
"s_add_u32 s28, s28, Lh_epi_%=-Lct2_%=\n" \
"s_addc_u32 s29, s29, 0\n" \
"s_mov_b32 s30, Lq_entry_%=-Lh_epi_%=\n" \
"s_mov_b32 s31, 0x20000\n" \
"v_accvgpr_read_b32 v249, a243\n" \
"buffer_load_dword a208, v249, s[28:31], 0 offen\n" \
"s_branch Lhm1_%=\n" \
"Lhs41_%=:\n" \
"v_accvgpr_read_b32 v232, a233\n" \
"v_readlane_b32 s60, v247, 13\n" \
"v_readlane_b32 s82, v247, 9\n" \
"v_readlane_b32 s61, v247, 6\n" \
"s_add_u32 s40, s81, 0\n" \
"s_add_u32 s41, s81, 16384\n" \
"s_add_u32 s42, s81, 32768\n" \
"s_add_u32 s43, s81, 49152\n" \
"s_add_u32 s44, s80, 0\n" \
"s_add_u32 s45, s80, 16384\n" \
"s_add_u32 s46, s80, 32768\n" \
"s_add_u32 s47, s80, 49152\n" \
"s_add_u32 s20, s68, s60\n" \
"s_addc_u32 s21, s69, 0\n" \
"s_sub_u32 s82, s82, s60\n" \
"s_ashr_i32 s83, s82, 31\n" \
"s_add_u32 s24, s68, s61\n" \
"s_addc_u32 s25, s69, 0\n" \
"s_sub_u32 s24, s24, s75\n" \
"s_subb_u32 s25, s25, 0\n" \
"v_subrev_u32 v232, 16384, v232\n" \
"v_mov_b32 v233, v232\n" \
"s_branch Lhm1_%=\n" \
"Lhe2_%=:\n" \
"s_add_u32 m0, s55, 0\n" \
"s_nop 0\n" \
"buffer_load_dwordx4 v234, s[24:27], s48 offen lds\n" \
"s_add_u32 m0, s55, 4096\n" \
"s_nop 0\n" \
"buffer_load_dwordx4 v234, s[24:27], s49 offen lds\n" \
"s_add_u32 m0, s55, 8192\n" \
"s_nop 0\n" \
"buffer_load_dwordx4 v234, s[24:27], s50 offen lds\n" \
"s_add_u32 m0, s55, 12288\n" \
"s_nop 0\n" \
"buffer_load_dwordx4 v234, s[24:27], s51 offen lds\n" \
"s_add_u32 m0, s55, 16384\n" \
"s_nop 0\n" \
"buffer_load_dwordx4 v234, s[24:27], s52 offen lds\n" \
"s_add_u32 m0, s55, 20480\n" \
"s_nop 0\n" \
"buffer_load_dwordx4 v234, s[24:27], s53 offen lds\n" \
"s_add_u32 s24, s24, s75\n" \
"s_addc_u32 s25, s25, 0\n" \
"s_add_u32 s55, s55, 24576\n" \
"s_branch Lhin2_%=\n" \
".p2align 6\n" \
"Lhm2_%=:\n" \
"v_mfma_f32_16x16x32_bf16 v[156:159], a[124:127], a[28:31], v[156:159]\n" \
"s_add_u32 s24, s24, s75\n" \
"s_addc_u32 s25, s25, 0\n" \
"v_mfma_f32_16x16x32_bf16 v[184:187], a[128:131], a[28:31], v[184:187]\n" \
"s_add_u32 s55, s55, 24576\n" \
"v_mfma_f32_16x16x32_bf16 v[188:191], a[132:135], a[28:31], v[188:191]\n" \
"ds_read_b128 a[28:31], v238 offset:6144\n" \
"Lhin2_%=:\n" \
"s_waitcnt lgkmcnt(1)\n" \
"v_mfma_f32_16x16x32_bf16 v[0:3], a[32:35], a[0:3], v[0:3]\n" \
"s_add_u32 s63, s63, 1\n" \
"ds_read_b64_tr_b16 a[88:89], v239 offset:12288\n" \
"v_mfma_f32_16x16x32_bf16 v[8:11], a[32:35], a[4:7], v[8:11]\n" \
"ds_read_b64_tr_b16 a[90:91], v239 offset:12800\n" \
"v_mfma_f32_16x16x32_bf16 v[16:19], a[32:35], a[8:11], v[16:19]\n" \
"s_cmp_eq_u32 s63, 4\n" \
"s_cmov_b32 s55, s62\n" \
"s_cmov_b32 s63, 0\n" \
"v_mfma_f32_16x16x32_bf16 v[24:27], a[32:35], a[12:15], v[24:27]\n" \
"s_add_u32 m0, s54, 0\n" \
"ds_read_b64_tr_b16 a[92:93], v240 offset:12288\n" \
"v_mfma_f32_16x16x32_bf16 v[28:31], a[36:39], a[12:15], v[28:31]\n" \
"buffer_load_dwordx4 v232, s[20:23], s40 offen lds\n" \
"v_mfma_f32_16x16x32_bf16 v[20:23], a[36:39], a[8:11], v[20:23]\n" \
"s_add_u32 s74, s74, 1048576\n" \
"ds_read_b64_tr_b16 a[94:95], v240 offset:12800\n" \
"v_mfma_f32_16x16x32_bf16 v[12:15], a[36:39], a[4:7], v[12:15]\n" \
"s_cmp_eq_u32 s58, 15\n" \
"s_cselect_b32 s26, 0, s26\n" \
"s_cselect_b32 s55, 0x30000, s55\n" \
"v_mfma_f32_16x16x32_bf16 v[4:7], a[36:39], a[0:3], v[4:7]\n" \
"ds_read_b64_tr_b16 a[96:97], v241 offset:12288\n" \
"v_mfma_f32_16x16x32_bf16 v[32:35], a[40:43], a[0:3], v[32:35]\n" \
"s_add_u32 m0, s54, 1024\n" \
"s_add_u32 s57, s57, 1\n" \
"v_mfma_f32_16x16x32_bf16 v[40:43], a[40:43], a[4:7], v[40:43]\n" \
"buffer_load_dwordx4 v233, s[20:23], s41 offen lds\n" \
"v_mfma_f32_16x16x32_bf16 v[48:51], a[40:43], a[8:11], v[48:51]\n" \
"s_cmp_eq_u32 s58, 3\n" \
"s_cmov_b64 s[76:77], s[0:1]\n" \
"s_cmov_b32 s78, 4128768\n" \
"s_cmov_b32 s74, 0\n" \
"v_mfma_f32_16x16x32_bf16 v[56:59], a[40:43], a[12:15], v[56:59]\n" \
"s_cmp_eq_u32 s57, 3\n" \
"s_cselect_b32 s56, s95, s85\n" \
"s_cselect_b32 s57, -1, s57\n" \
"v_mfma_f32_16x16x32_bf16 v[60:63], a[44:47], a[12:15], v[60:63]\n" \
"ds_read_b64_tr_b16 a[98:99], v241 offset:12800\n" \
"v_mfma_f32_16x16x32_bf16 v[52:55], a[44:47], a[8:11], v[52:55]\n" \
"s_add_u32 m0, s54, 2048\n" \
"ds_read_b64_tr_b16 a[100:101], v242 offset:12288\n" \
"v_mfma_f32_16x16x32_bf16 v[44:47], a[44:47], a[4:7], v[44:47]\n" \
"buffer_load_dwordx4 v232, s[20:23], s42 offen lds\n" \
"v_mfma_f32_16x16x32_bf16 v[36:39], a[44:47], a[0:3], v[36:39]\n" \
"s_cmp_eq_u32 s58, 7\n" \
"s_cmov_b64 s[76:77], s[34:35]\n" \
"s_cmov_b32 s78, 4194304\n" \
"s_cmov_b32 s74, 0\n" \
"v_mfma_f32_16x16x32_bf16 v[64:67], a[48:51], a[0:3], v[64:67]\n" \
"ds_read_b64_tr_b16 a[102:103], v242 offset:12800\n" \
"v_mfma_f32_16x16x32_bf16 v[72:75], a[48:51], a[4:7], v[72:75]\n" \
"ds_read_b64_tr_b16 a[104:105], v239 offset:16384\n" \
"v_mfma_f32_16x16x32_bf16 v[80:83], a[48:51], a[8:11], v[80:83]\n" \
"ds_read_b64_tr_b16 a[106:107], v239 offset:16896\n" \
"v_mfma_f32_16x16x32_bf16 v[88:91], a[48:51], a[12:15], v[88:91]\n" \
"s_add_u32 m0, s54, 3072\n" \
"ds_read_b64_tr_b16 a[108:109], v240 offset:16384\n" \
"v_mfma_f32_16x16x32_bf16 v[92:95], a[52:55], a[12:15], v[92:95]\n" \
"buffer_load_dwordx4 v233, s[20:23], s43 offen lds\n" \
"v_mfma_f32_16x16x32_bf16 v[84:87], a[52:55], a[8:11], v[84:87]\n" \
"ds_read_b64_tr_b16 a[110:111], v240 offset:16896\n" \
"v_mfma_f32_16x16x32_bf16 v[76:79], a[52:55], a[4:7], v[76:79]\n" \
"v_mfma_f32_16x16x32_bf16 v[68:71], a[52:55], a[0:3], v[68:71]\n" \
"ds_read_b64_tr_b16 a[112:113], v241 offset:16384\n" \
"v_mfma_f32_16x16x32_bf16 v[96:99], a[56:59], a[0:3], v[96:99]\n" \
"s_add_u32 m0, s54, 4096\n" \
"ds_read_b64_tr_b16 a[114:115], v241 offset:16896\n" \
"v_mfma_f32_16x16x32_bf16 v[104:107], a[56:59], a[4:7], v[104:107]\n" \
"buffer_load_dwordx4 v232, s[20:23], s44 offen lds\n" \
"v_mfma_f32_16x16x32_bf16 v[112:115], a[56:59], a[8:11], v[112:115]\n" \
"ds_read_b64_tr_b16 a[116:117], v242 offset:16384\n" \
"v_mfma_f32_16x16x32_bf16 v[120:123], a[56:59], a[12:15], v[120:123]\n" \
"ds_read_b64_tr_b16 a[118:119], v242 offset:16896\n" \
"v_mfma_f32_16x16x32_bf16 v[124:127], a[60:63], a[12:15], v[124:127]\n" \
"ds_read_b64_tr_b16 a[120:121], v239 offset:20480\n" \
"v_mfma_f32_16x16x32_bf16 v[116:119], a[60:63], a[8:11], v[116:119]\n" \
"s_add_u32 m0, s54, 5120\n" \
"ds_read_b64_tr_b16 a[122:123], v239 offset:20992\n" \
"v_mfma_f32_16x16x32_bf16 v[108:111], a[60:63], a[4:7], v[108:111]\n" \
"buffer_load_dwordx4 v233, s[20:23], s45 offen lds\n" \
"v_mfma_f32_16x16x32_bf16 v[100:103], a[60:63], a[0:3], v[100:103]\n" \
"ds_read_b64_tr_b16 a[124:125], v240 offset:20480\n" \
"v_mfma_f32_16x16x32_bf16 v[128:131], a[64:67], a[0:3], v[128:131]\n" \
"ds_read_b64_tr_b16 a[126:127], v240 offset:20992\n" \
"v_mfma_f32_16x16x32_bf16 v[136:139], a[64:67], a[4:7], v[136:139]\n" \
"ds_read_b64_tr_b16 a[128:129], v241 offset:20480\n" \
"v_mfma_f32_16x16x32_bf16 v[144:147], a[64:67], a[8:11], v[144:147]\n" \
"ds_read_b64_tr_b16 a[130:131], v241 offset:20992\n" \
"v_mfma_f32_16x16x32_bf16 v[152:155], a[64:67], a[12:15], v[152:155]\n" \
"s_add_u32 m0, s54, 6144\n" \
"ds_read_b64_tr_b16 a[132:133], v242 offset:20480\n" \
"v_mfma_f32_16x16x32_bf16 v[156:159], a[68:71], a[12:15], v[156:159]\n" \
"s_waitcnt lgkmcnt(15)\n" \
"buffer_load_dwordx4 v232, s[20:23], s46 offen lds\n" \
"v_mfma_f32_16x16x32_bf16 v[148:151], a[68:71], a[8:11], v[148:151]\n" \
"ds_read_b64_tr_b16 a[134:135], v242 offset:20992\n" \
"v_mfma_f32_16x16x32_bf16 v[140:143], a[68:71], a[4:7], v[140:143]\n" \
"v_mfma_f32_16x16x32_bf16 v[132:135], a[68:71], a[0:3], v[132:135]\n" \
"v_mfma_f32_16x16x32_bf16 v[160:163], a[72:75], a[0:3], v[160:163]\n" \
"s_add_u32 m0, s54, 7168\n" \
"v_mfma_f32_16x16x32_bf16 v[168:171], a[72:75], a[4:7], v[168:171]\n" \
"buffer_load_dwordx4 v233, s[20:23], s47 offen lds\n" \
"v_mfma_f32_16x16x32_bf16 v[176:179], a[72:75], a[8:11], v[176:179]\n" \
"s_add_u32 s20, s20, s82\n" \
"s_addc_u32 s21, s21, s83\n" \
"v_mfma_f32_16x16x32_bf16 v[184:187], a[72:75], a[12:15], v[184:187]\n" \
"s_xor_b32 s54, s54, 32768\n" \
"v_mfma_f32_16x16x32_bf16 v[188:191], a[76:79], a[12:15], v[188:191]\n" \
"v_mfma_f32_16x16x32_bf16 v[180:183], a[76:79], a[8:11], v[180:183]\n" \
"v_mfma_f32_16x16x32_bf16 v[172:175], a[76:79], a[4:7], v[172:175]\n" \
"v_mfma_f32_16x16x32_bf16 v[164:167], a[76:79], a[0:3], v[164:167]\n" \
"s_waitcnt vmcnt(15) lgkmcnt(0)\n" \
"s_barrier\n" \
"v_mfma_f32_16x16x32_bf16 v[0:3], a[88:91], a[16:19], v[0:3]\n" \
"v_xor_b32 v237, 0x8000, v237\n" \
"v_xor_b32 v238, 0x8000, v238\n" \
"v_mfma_f32_16x16x32_bf16 v[4:7], a[92:95], a[16:19], v[4:7]\n" \
"v_add_u32 v239, s56, v239\n" \
"v_add_u32 v240, s56, v240\n" \
"v_mfma_f32_16x16x32_bf16 v[32:35], a[96:99], a[16:19], v[32:35]\n" \
"v_add_u32 v241, s56, v241\n" \
"v_add_u32 v242, s56, v242\n" \
"v_mfma_f32_16x16x32_bf16 v[36:39], a[100:103], a[16:19], v[36:39]\n" \
"ds_read_b128 a[0:3], v237 offset:0\n" \
"v_mfma_f32_16x16x32_bf16 v[64:67], a[104:107], a[16:19], v[64:67]\n" \
"ds_read_b128 a[4:7], v237 offset:2048\n" \
"v_mfma_f32_16x16x32_bf16 v[68:71], a[108:111], a[16:19], v[68:71]\n" \
"s_add_u32 m0, s55, 0\n" \
"ds_read_b128 a[8:11], v237 offset:4096\n" \
"v_mfma_f32_16x16x32_bf16 v[96:99], a[112:115], a[16:19], v[96:99]\n" \
"buffer_load_dwordx4 v234, s[24:27], s48 offen lds\n" \
"v_mfma_f32_16x16x32_bf16 v[100:103], a[116:119], a[16:19], v[100:103]\n" \
"ds_read_b128 a[12:15], v237 offset:6144\n" \
"v_mfma_f32_16x16x32_bf16 v[128:131], a[120:123], a[16:19], v[128:131]\n" \
"ds_read_b64_tr_b16 a[32:33], v239 offset:0\n" \
"v_mfma_f32_16x16x32_bf16 v[132:135], a[124:127], a[16:19], v[132:135]\n" \
"ds_read_b64_tr_b16 a[34:35], v239 offset:512\n" \
"v_mfma_f32_16x16x32_bf16 v[160:163], a[128:131], a[16:19], v[160:163]\n" \
"s_add_u32 m0, s55, 4096\n" \
"ds_read_b64_tr_b16 a[36:37], v240 offset:0\n" \
"v_mfma_f32_16x16x32_bf16 v[164:167], a[132:135], a[16:19], v[164:167]\n" \
"buffer_load_dwordx4 v234, s[24:27], s49 offen lds\n" \
"v_mfma_f32_16x16x32_bf16 v[8:11], a[88:91], a[20:23], v[8:11]\n" \
"ds_read_b64_tr_b16 a[38:39], v240 offset:512\n" \
"v_mfma_f32_16x16x32_bf16 v[12:15], a[92:95], a[20:23], v[12:15]\n" \
"ds_read_b128 a[16:19], v238 offset:0\n" \
"ds_read_b64_tr_b16 a[40:41], v241 offset:0\n" \
"v_mfma_f32_16x16x32_bf16 v[40:43], a[96:99], a[20:23], v[40:43]\n" \
"ds_read_b64_tr_b16 a[42:43], v241 offset:512\n" \
"v_mfma_f32_16x16x32_bf16 v[44:47], a[100:103], a[20:23], v[44:47]\n" \
"s_add_u32 m0, s55, 8192\n" \
"ds_read_b64_tr_b16 a[44:45], v242 offset:0\n" \
"v_mfma_f32_16x16x32_bf16 v[72:75], a[104:107], a[20:23], v[72:75]\n" \
"buffer_load_dwordx4 v234, s[24:27], s50 offen lds\n" \
"v_mfma_f32_16x16x32_bf16 v[76:79], a[108:111], a[20:23], v[76:79]\n" \
"ds_read_b64_tr_b16 a[46:47], v242 offset:512\n" \
"v_mfma_f32_16x16x32_bf16 v[104:107], a[112:115], a[20:23], v[104:107]\n" \
"ds_read_b64_tr_b16 a[48:49], v239 offset:4096\n" \
"v_mfma_f32_16x16x32_bf16 v[108:111], a[116:119], a[20:23], v[108:111]\n" \
"ds_read_b64_tr_b16 a[50:51], v239 offset:4608\n" \
"v_mfma_f32_16x16x32_bf16 v[136:139], a[120:123], a[20:23], v[136:139]\n" \
"s_add_u32 m0, s55, 12288\n" \
"ds_read_b64_tr_b16 a[52:53], v240 offset:4096\n" \
"v_mfma_f32_16x16x32_bf16 v[140:143], a[124:127], a[20:23], v[140:143]\n" \
"buffer_load_dwordx4 v234, s[24:27], s51 offen lds\n" \
"v_mfma_f32_16x16x32_bf16 v[168:171], a[128:131], a[20:23], v[168:171]\n" \
"ds_read_b64_tr_b16 a[54:55], v240 offset:4608\n" \
"v_mfma_f32_16x16x32_bf16 v[172:175], a[132:135], a[20:23], v[172:175]\n" \
"ds_read_b64_tr_b16 a[56:57], v241 offset:4096\n" \
"v_mfma_f32_16x16x32_bf16 v[16:19], a[88:91], a[24:27], v[16:19]\n" \
"ds_read_b64_tr_b16 a[58:59], v241 offset:4608\n" \
"v_mfma_f32_16x16x32_bf16 v[20:23], a[92:95], a[24:27], v[20:23]\n" \
"ds_read_b128 a[20:23], v238 offset:2048\n" \
"ds_read_b64_tr_b16 a[60:61], v242 offset:4096\n" \
"v_mfma_f32_16x16x32_bf16 v[48:51], a[96:99], a[24:27], v[48:51]\n" \
"s_add_u32 m0, s55, 16384\n" \
"ds_read_b64_tr_b16 a[62:63], v242 offset:4608\n" \
"v_mfma_f32_16x16x32_bf16 v[52:55], a[100:103], a[24:27], v[52:55]\n" \
"buffer_load_dwordx4 v234, s[24:27], s52 offen lds\n" \
"v_mfma_f32_16x16x32_bf16 v[80:83], a[104:107], a[24:27], v[80:83]\n" \
"ds_read_b64_tr_b16 a[64:65], v239 offset:8192\n" \
"v_mfma_f32_16x16x32_bf16 v[84:87], a[108:111], a[24:27], v[84:87]\n" \
"ds_read_b64_tr_b16 a[66:67], v239 offset:8704\n" \
"v_mfma_f32_16x16x32_bf16 v[112:115], a[112:115], a[24:27], v[112:115]\n" \
"ds_read_b64_tr_b16 a[68:69], v240 offset:8192\n" \
"v_mfma_f32_16x16x32_bf16 v[116:119], a[116:119], a[24:27], v[116:119]\n" \
"s_add_u32 m0, s55, 20480\n" \
"ds_read_b64_tr_b16 a[70:71], v240 offset:8704\n" \
"v_mfma_f32_16x16x32_bf16 v[144:147], a[120:123], a[24:27], v[144:147]\n" \
"buffer_load_dwordx4 v234, s[24:27], s53 offen lds\n" \
"v_mfma_f32_16x16x32_bf16 v[148:151], a[124:127], a[24:27], v[148:151]\n" \
"ds_read_b64_tr_b16 a[72:73], v241 offset:8192\n" \
"v_mfma_f32_16x16x32_bf16 v[176:179], a[128:131], a[24:27], v[176:179]\n" \
"ds_read_b64_tr_b16 a[74:75], v241 offset:8704\n" \
"v_mfma_f32_16x16x32_bf16 v[180:183], a[132:135], a[24:27], v[180:183]\n" \
"ds_read_b64_tr_b16 a[76:77], v242 offset:8192\n" \
"v_mfma_f32_16x16x32_bf16 v[24:27], a[88:91], a[28:31], v[24:27]\n" \
"ds_read_b64_tr_b16 a[78:79], v242 offset:8704\n" \
"v_mfma_f32_16x16x32_bf16 v[28:31], a[92:95], a[28:31], v[28:31]\n" \
"buffer_load_dword a246, v254, s[76:79], s74 offen\n" \
"v_mfma_f32_16x16x32_bf16 v[56:59], a[96:99], a[28:31], v[56:59]\n" \
"ds_read_b128 a[24:27], v238 offset:4096\n" \
"v_mfma_f32_16x16x32_bf16 v[60:63], a[100:103], a[28:31], v[60:63]\n" \
"v_mfma_f32_16x16x32_bf16 v[88:91], a[104:107], a[28:31], v[88:91]\n" \
"v_mfma_f32_16x16x32_bf16 v[92:95], a[108:111], a[28:31], v[92:95]\n" \
"s_add_u32 s58, s58, 1\n" \
"v_mfma_f32_16x16x32_bf16 v[120:123], a[112:115], a[28:31], v[120:123]\n" \
"v_mfma_f32_16x16x32_bf16 v[124:127], a[116:119], a[28:31], v[124:127]\n" \
"v_mfma_f32_16x16x32_bf16 v[152:155], a[120:123], a[28:31], v[152:155]\n" \
"s_bitcmp1_b32 86018, s58\n" \
"s_cbranch_scc0 Lhm2_%=\n" \
"s_cmp_lt_u32 s58, 16\n" \
"s_cbranch_scc1 Lhsp2_%=\n" \
"v_mfma_f32_16x16x32_bf16 v[156:159], a[124:127], a[28:31], v[156:159]\n" \
"v_mfma_f32_16x16x32_bf16 v[184:187], a[128:131], a[28:31], v[184:187]\n" \
"v_mfma_f32_16x16x32_bf16 v[188:191], a[132:135], a[28:31], v[188:191]\n" \
"s_branch Lh_epi_%=\n" \
"Lhsp2_%=:\n" \
"s_cmp_ge_u32 s58, 12\n" \
"s_cbranch_scc1 Lhs22_%=\n" \
"s_add_u32 s24, s24, s75\n" \
"s_addc_u32 s25, s25, 0\n" \
"s_add_u32 s55, s55, 24576\n" \
"s_add_u32 s63, s63, 1\n" \
"s_cmp_eq_u32 s63, 4\n" \
"s_cmov_b32 s55, s62\n" \
"s_cmov_b32 s63, 0\n" \
"s_add_u32 m0, s55, 0\n" \
"s_nop 0\n" \
"buffer_load_dwordx4 v234, s[24:27], s48 offen lds\n" \
"s_add_u32 m0, s55, 4096\n" \
"s_nop 0\n" \
"buffer_load_dwordx4 v234, s[24:27], s49 offen lds\n" \
"s_add_u32 m0, s55, 8192\n" \
"s_nop 0\n" \
"buffer_load_dwordx4 v234, s[24:27], s50 offen lds\n" \
"s_add_u32 m0, s55, 12288\n" \
"s_nop 0\n" \
"buffer_load_dwordx4 v234, s[24:27], s51 offen lds\n" \
"s_add_u32 m0, s55, 16384\n" \
"s_nop 0\n" \
"buffer_load_dwordx4 v234, s[24:27], s52 offen lds\n" \
"s_add_u32 m0, s55, 20480\n" \
"s_nop 0\n" \
"buffer_load_dwordx4 v234, s[24:27], s53 offen lds\n" \
"s_branch Lhm2_%=\n" \
"Lhs12_%=:\n" \
"Lhs22_%=:\n" \
"v_accvgpr_read_b32 v247, a253\n" \
"s_cmp_eq_u32 s58, 14\n" \
"s_cbranch_scc1 Lhs42_%=\n" \
"v_accvgpr_read_b32 v234, a233\n" \
"v_readlane_b32 s60, v247, 0\n" \
"s_mov_b32 s75, 196608\n" \
"s_add_u32 s48, s81, 0\n" \
"s_add_u32 s49, s80, 0\n" \
"s_add_u32 s50, s81, 65536\n" \
"s_add_u32 s51, s80, 65536\n" \
"s_add_u32 s52, s81, 131072\n" \
"s_add_u32 s53, s80, 131072\n" \
"s_add_u32 s24, s68, s60\n" \
"s_addc_u32 s25, s69, 0\n" \
"s_sub_u32 s24, s24, s75\n" \
"s_subb_u32 s25, s25, 0\n" \
"s_getpc_b64 s[28:29]\n" \
"Lct3_%=:\n" \
"s_add_u32 s28, s28, Lh_epi_%=-Lct3_%=\n" \
"s_addc_u32 s29, s29, 0\n" \
"s_mov_b32 s30, Lq_entry_%=-Lh_epi_%=\n" \
"s_mov_b32 s31, 0x20000\n" \
"v_accvgpr_read_b32 v249, a243\n" \
"buffer_load_dword a209, v249, s[28:31], 0 offen\n" \
"s_branch Lhm2_%=\n" \
"Lhs42_%=:\n" \
"v_accvgpr_read_b32 v232, a233\n" \
"v_readlane_b32 s60, v247, 14\n" \
"v_readlane_b32 s82, v247, 10\n" \
"v_readlane_b32 s61, v247, 6\n" \
"s_add_u32 s40, s81, 0\n" \
"s_add_u32 s41, s81, 16384\n" \
"s_add_u32 s42, s81, 32768\n" \
"s_add_u32 s43, s81, 49152\n" \
"s_add_u32 s44, s80, 0\n" \
"s_add_u32 s45, s80, 16384\n" \
"s_add_u32 s46, s80, 32768\n" \
"s_add_u32 s47, s80, 49152\n" \
"s_add_u32 s20, s68, s60\n" \
"s_addc_u32 s21, s69, 0\n" \
"s_sub_u32 s82, s82, s60\n" \
"s_ashr_i32 s83, s82, 31\n" \
"s_add_u32 s24, s68, s61\n" \
"s_addc_u32 s25, s69, 0\n" \
"s_sub_u32 s24, s24, s75\n" \
"s_subb_u32 s25, s25, 0\n" \
"v_subrev_u32 v232, 32768, v232\n" \
"v_mov_b32 v233, v232\n" \
"s_branch Lhm2_%=\n" \
"Lhe3_%=:\n" \
"s_add_u32 m0, s55, 0\n" \
"s_nop 0\n" \
"buffer_load_dwordx4 v234, s[24:27], s48 offen lds\n" \
"s_add_u32 m0, s55, 4096\n" \
"s_nop 0\n" \
"buffer_load_dwordx4 v234, s[24:27], s49 offen lds\n" \
"s_add_u32 m0, s55, 8192\n" \
"s_nop 0\n" \
"buffer_load_dwordx4 v234, s[24:27], s50 offen lds\n" \
"s_add_u32 m0, s55, 12288\n" \
"s_nop 0\n" \
"buffer_load_dwordx4 v234, s[24:27], s51 offen lds\n" \
"s_add_u32 m0, s55, 16384\n" \
"s_nop 0\n" \
"buffer_load_dwordx4 v234, s[24:27], s52 offen lds\n" \
"s_add_u32 m0, s55, 20480\n" \
"s_nop 0\n" \
"buffer_load_dwordx4 v234, s[24:27], s53 offen lds\n" \
"s_add_u32 s24, s24, s75\n" \
"s_addc_u32 s25, s25, 0\n" \
"s_add_u32 s55, s55, 24576\n" \
"s_branch Lhin3_%=\n" \
".p2align 6\n" \
"Lhm3_%=:\n" \
"v_mfma_f32_16x16x32_bf16 v[156:159], a[124:127], a[28:31], v[156:159]\n" \
"s_add_u32 s24, s24, s75\n" \
"s_addc_u32 s25, s25, 0\n" \
"v_mfma_f32_16x16x32_bf16 v[184:187], a[128:131], a[28:31], v[184:187]\n" \
"s_add_u32 s55, s55, 24576\n" \
"v_mfma_f32_16x16x32_bf16 v[188:191], a[132:135], a[28:31], v[188:191]\n" \
"ds_read_b128 a[28:31], v238 offset:6144\n" \
"Lhin3_%=:\n" \
"s_waitcnt lgkmcnt(1)\n" \
"v_mfma_f32_16x16x32_bf16 v[0:3], a[32:35], a[0:3], v[0:3]\n" \
"s_add_u32 s63, s63, 1\n" \
"ds_read_b64_tr_b16 a[88:89], v239 offset:12288\n" \
"v_mfma_f32_16x16x32_bf16 v[8:11], a[32:35], a[4:7], v[8:11]\n" \
"ds_read_b64_tr_b16 a[90:91], v239 offset:12800\n" \
"v_mfma_f32_16x16x32_bf16 v[16:19], a[32:35], a[8:11], v[16:19]\n" \
"s_cmp_eq_u32 s63, 4\n" \
"s_cmov_b32 s55, s62\n" \
"s_cmov_b32 s63, 0\n" \
"v_mfma_f32_16x16x32_bf16 v[24:27], a[32:35], a[12:15], v[24:27]\n" \
"ds_read_b64_tr_b16 a[92:93], v240 offset:12288\n" \
"v_mfma_f32_16x16x32_bf16 v[28:31], a[36:39], a[12:15], v[28:31]\n" \
"s_add_u32 m0, s54, 0\n" \
"ds_read_b64_tr_b16 a[94:95], v240 offset:12800\n" \
"v_mfma_f32_16x16x32_bf16 v[20:23], a[36:39], a[8:11], v[20:23]\n" \
"buffer_load_dwordx4 v232, s[20:23], s40 offen lds\n" \
"v_mfma_f32_16x16x32_bf16 v[12:15], a[36:39], a[4:7], v[12:15]\n" \
"s_add_u32 s74, s74, 1048576\n" \
"ds_read_b64_tr_b16 a[96:97], v241 offset:12288\n" \
"v_mfma_f32_16x16x32_bf16 v[4:7], a[36:39], a[0:3], v[4:7]\n" \
"s_cmp_eq_u32 s58, 15\n" \
"s_cselect_b32 s26, 0, s26\n" \
"s_cselect_b32 s55, 0x30000, s55\n" \
"v_mfma_f32_16x16x32_bf16 v[32:35], a[40:43], a[0:3], v[32:35]\n" \
"ds_read_b64_tr_b16 a[98:99], v241 offset:12800\n" \
"v_mfma_f32_16x16x32_bf16 v[40:43], a[40:43], a[4:7], v[40:43]\n" \
"s_add_u32 m0, s54, 1024\n" \
"s_add_u32 s57, s57, 1\n" \
"v_mfma_f32_16x16x32_bf16 v[48:51], a[40:43], a[8:11], v[48:51]\n" \
"buffer_load_dwordx4 v233, s[20:23], s41 offen lds\n" \
"v_mfma_f32_16x16x32_bf16 v[56:59], a[40:43], a[12:15], v[56:59]\n" \
"s_cmp_eq_u32 s58, 3\n" \
"s_cmov_b64 s[76:77], s[0:1]\n" \
"s_cmov_b32 s78, 4128768\n" \
"s_cmov_b32 s74, 0\n" \
"v_mfma_f32_16x16x32_bf16 v[60:63], a[44:47], a[12:15], v[60:63]\n" \
"s_cmp_eq_u32 s57, 3\n" \
"s_cselect_b32 s56, s95, s85\n" \
"s_cselect_b32 s57, -1, s57\n" \
"v_mfma_f32_16x16x32_bf16 v[52:55], a[44:47], a[8:11], v[52:55]\n" \
"ds_read_b64_tr_b16 a[100:101], v242 offset:12288\n" \
"v_mfma_f32_16x16x32_bf16 v[44:47], a[44:47], a[4:7], v[44:47]\n" \
"s_add_u32 m0, s54, 2048\n" \
"ds_read_b64_tr_b16 a[102:103], v242 offset:12800\n" \
"v_mfma_f32_16x16x32_bf16 v[36:39], a[44:47], a[0:3], v[36:39]\n" \
"buffer_load_dwordx4 v232, s[20:23], s42 offen lds\n" \
"v_mfma_f32_16x16x32_bf16 v[64:67], a[48:51], a[0:3], v[64:67]\n" \
"s_cmp_eq_u32 s58, 7\n" \
"s_cmov_b64 s[76:77], s[34:35]\n" \
"s_cmov_b32 s78, 4194304\n" \
"s_cmov_b32 s74, 0\n" \
"v_mfma_f32_16x16x32_bf16 v[72:75], a[48:51], a[4:7], v[72:75]\n" \
"ds_read_b64_tr_b16 a[104:105], v239 offset:16384\n" \
"v_mfma_f32_16x16x32_bf16 v[80:83], a[48:51], a[8:11], v[80:83]\n" \
"ds_read_b64_tr_b16 a[106:107], v239 offset:16896\n" \
"v_mfma_f32_16x16x32_bf16 v[88:91], a[48:51], a[12:15], v[88:91]\n" \
"ds_read_b64_tr_b16 a[108:109], v240 offset:16384\n" \
"v_mfma_f32_16x16x32_bf16 v[92:95], a[52:55], a[12:15], v[92:95]\n" \
"s_add_u32 m0, s54, 3072\n" \
"ds_read_b64_tr_b16 a[110:111], v240 offset:16896\n" \
"v_mfma_f32_16x16x32_bf16 v[84:87], a[52:55], a[8:11], v[84:87]\n" \
"buffer_load_dwordx4 v233, s[20:23], s43 offen lds\n" \
"v_mfma_f32_16x16x32_bf16 v[76:79], a[52:55], a[4:7], v[76:79]\n" \
"v_mfma_f32_16x16x32_bf16 v[68:71], a[52:55], a[0:3], v[68:71]\n" \
"ds_read_b64_tr_b16 a[112:113], v241 offset:16384\n" \
"v_mfma_f32_16x16x32_bf16 v[96:99], a[56:59], a[0:3], v[96:99]\n" \
"ds_read_b64_tr_b16 a[114:115], v241 offset:16896\n" \
"v_mfma_f32_16x16x32_bf16 v[104:107], a[56:59], a[4:7], v[104:107]\n" \
"s_add_u32 m0, s54, 4096\n" \
"ds_read_b64_tr_b16 a[116:117], v242 offset:16384\n" \
"v_mfma_f32_16x16x32_bf16 v[112:115], a[56:59], a[8:11], v[112:115]\n" \
"buffer_load_dwordx4 v232, s[20:23], s44 offen lds\n" \
"v_mfma_f32_16x16x32_bf16 v[120:123], a[56:59], a[12:15], v[120:123]\n" \
"ds_read_b64_tr_b16 a[118:119], v242 offset:16896\n" \
"v_mfma_f32_16x16x32_bf16 v[124:127], a[60:63], a[12:15], v[124:127]\n" \
"ds_read_b64_tr_b16 a[120:121], v239 offset:20480\n" \
"v_mfma_f32_16x16x32_bf16 v[116:119], a[60:63], a[8:11], v[116:119]\n" \
"ds_read_b64_tr_b16 a[122:123], v239 offset:20992\n" \
"v_mfma_f32_16x16x32_bf16 v[108:111], a[60:63], a[4:7], v[108:111]\n" \
"s_add_u32 m0, s54, 5120\n" \
"ds_read_b64_tr_b16 a[124:125], v240 offset:20480\n" \
"v_mfma_f32_16x16x32_bf16 v[100:103], a[60:63], a[0:3], v[100:103]\n" \
"buffer_load_dwordx4 v233, s[20:23], s45 offen lds\n" \
"v_mfma_f32_16x16x32_bf16 v[128:131], a[64:67], a[0:3], v[128:131]\n" \
"ds_read_b64_tr_b16 a[126:127], v240 offset:20992\n" \
"v_mfma_f32_16x16x32_bf16 v[136:139], a[64:67], a[4:7], v[136:139]\n" \
"ds_read_b64_tr_b16 a[128:129], v241 offset:20480\n" \
"v_mfma_f32_16x16x32_bf16 v[144:147], a[64:67], a[8:11], v[144:147]\n" \
"ds_read_b64_tr_b16 a[130:131], v241 offset:20992\n" \
"v_mfma_f32_16x16x32_bf16 v[152:155], a[64:67], a[12:15], v[152:155]\n" \
"ds_read_b64_tr_b16 a[132:133], v242 offset:20480\n" \
"v_mfma_f32_16x16x32_bf16 v[156:159], a[68:71], a[12:15], v[156:159]\n" \
"s_add_u32 m0, s54, 6144\n" \
"ds_read_b64_tr_b16 a[134:135], v242 offset:20992\n" \
"v_mfma_f32_16x16x32_bf16 v[148:151], a[68:71], a[8:11], v[148:151]\n" \
"s_waitcnt lgkmcnt(15)\n" \
"buffer_load_dwordx4 v232, s[20:23], s46 offen lds\n" \
"v_mfma_f32_16x16x32_bf16 v[140:143], a[68:71], a[4:7], v[140:143]\n" \
"v_mfma_f32_16x16x32_bf16 v[132:135], a[68:71], a[0:3], v[132:135]\n" \
"v_mfma_f32_16x16x32_bf16 v[160:163], a[72:75], a[0:3], v[160:163]\n" \
"v_mfma_f32_16x16x32_bf16 v[168:171], a[72:75], a[4:7], v[168:171]\n" \
"s_add_u32 m0, s54, 7168\n" \
"v_mfma_f32_16x16x32_bf16 v[176:179], a[72:75], a[8:11], v[176:179]\n" \
"buffer_load_dwordx4 v233, s[20:23], s47 offen lds\n" \
"v_mfma_f32_16x16x32_bf16 v[184:187], a[72:75], a[12:15], v[184:187]\n" \
"s_add_u32 s20, s20, s82\n" \
"s_addc_u32 s21, s21, s83\n" \
"v_mfma_f32_16x16x32_bf16 v[188:191], a[76:79], a[12:15], v[188:191]\n" \
"s_xor_b32 s54, s54, 32768\n" \
"v_mfma_f32_16x16x32_bf16 v[180:183], a[76:79], a[8:11], v[180:183]\n" \
"v_mfma_f32_16x16x32_bf16 v[172:175], a[76:79], a[4:7], v[172:175]\n" \
"v_mfma_f32_16x16x32_bf16 v[164:167], a[76:79], a[0:3], v[164:167]\n" \
"s_waitcnt vmcnt(15) lgkmcnt(0)\n" \
"s_barrier\n" \
"v_mfma_f32_16x16x32_bf16 v[0:3], a[88:91], a[16:19], v[0:3]\n" \
"v_xor_b32 v237, 0x8000, v237\n" \
"v_xor_b32 v238, 0x8000, v238\n" \
"v_mfma_f32_16x16x32_bf16 v[4:7], a[92:95], a[16:19], v[4:7]\n" \
"v_add_u32 v239, s56, v239\n" \
"v_add_u32 v240, s56, v240\n" \
"v_mfma_f32_16x16x32_bf16 v[32:35], a[96:99], a[16:19], v[32:35]\n" \
"v_add_u32 v241, s56, v241\n" \
"v_add_u32 v242, s56, v242\n" \
"v_mfma_f32_16x16x32_bf16 v[36:39], a[100:103], a[16:19], v[36:39]\n" \
"ds_read_b128 a[0:3], v237 offset:0\n" \
"v_mfma_f32_16x16x32_bf16 v[64:67], a[104:107], a[16:19], v[64:67]\n" \
"ds_read_b128 a[4:7], v237 offset:2048\n" \
"v_mfma_f32_16x16x32_bf16 v[68:71], a[108:111], a[16:19], v[68:71]\n" \
"ds_read_b128 a[8:11], v237 offset:4096\n" \
"v_mfma_f32_16x16x32_bf16 v[96:99], a[112:115], a[16:19], v[96:99]\n" \
"s_add_u32 m0, s55, 0\n" \
"ds_read_b128 a[12:15], v237 offset:6144\n" \
"v_mfma_f32_16x16x32_bf16 v[100:103], a[116:119], a[16:19], v[100:103]\n" \
"buffer_load_dwordx4 v234, s[24:27], s48 offen lds\n" \
"v_mfma_f32_16x16x32_bf16 v[128:131], a[120:123], a[16:19], v[128:131]\n" \
"ds_read_b64_tr_b16 a[32:33], v239 offset:0\n" \
"v_mfma_f32_16x16x32_bf16 v[132:135], a[124:127], a[16:19], v[132:135]\n" \
"ds_read_b64_tr_b16 a[34:35], v239 offset:512\n" \
"v_mfma_f32_16x16x32_bf16 v[160:163], a[128:131], a[16:19], v[160:163]\n" \
"ds_read_b64_tr_b16 a[36:37], v240 offset:0\n" \
"v_mfma_f32_16x16x32_bf16 v[164:167], a[132:135], a[16:19], v[164:167]\n" \
"s_add_u32 m0, s55, 4096\n" \
"ds_read_b64_tr_b16 a[38:39], v240 offset:512\n" \
"v_mfma_f32_16x16x32_bf16 v[8:11], a[88:91], a[20:23], v[8:11]\n" \
"buffer_load_dwordx4 v234, s[24:27], s49 offen lds\n" \
"v_mfma_f32_16x16x32_bf16 v[12:15], a[92:95], a[20:23], v[12:15]\n" \
"ds_read_b128 a[16:19], v238 offset:0\n" \
"ds_read_b64_tr_b16 a[40:41], v241 offset:0\n" \
"v_mfma_f32_16x16x32_bf16 v[40:43], a[96:99], a[20:23], v[40:43]\n" \
"ds_read_b64_tr_b16 a[42:43], v241 offset:512\n" \
"v_mfma_f32_16x16x32_bf16 v[44:47], a[100:103], a[20:23], v[44:47]\n" \
"ds_read_b64_tr_b16 a[44:45], v242 offset:0\n" \
"v_mfma_f32_16x16x32_bf16 v[72:75], a[104:107], a[20:23], v[72:75]\n" \
"s_add_u32 m0, s55, 8192\n" \
"ds_read_b64_tr_b16 a[46:47], v242 offset:512\n" \
"v_mfma_f32_16x16x32_bf16 v[76:79], a[108:111], a[20:23], v[76:79]\n" \
"buffer_load_dwordx4 v234, s[24:27], s50 offen lds\n" \
"v_mfma_f32_16x16x32_bf16 v[104:107], a[112:115], a[20:23], v[104:107]\n" \
"ds_read_b64_tr_b16 a[48:49], v239 offset:4096\n" \
"v_mfma_f32_16x16x32_bf16 v[108:111], a[116:119], a[20:23], v[108:111]\n" \
"ds_read_b64_tr_b16 a[50:51], v239 offset:4608\n" \
"v_mfma_f32_16x16x32_bf16 v[136:139], a[120:123], a[20:23], v[136:139]\n" \
"ds_read_b64_tr_b16 a[52:53], v240 offset:4096\n" \
"v_mfma_f32_16x16x32_bf16 v[140:143], a[124:127], a[20:23], v[140:143]\n" \
"s_add_u32 m0, s55, 12288\n" \
"ds_read_b64_tr_b16 a[54:55], v240 offset:4608\n" \
"v_mfma_f32_16x16x32_bf16 v[168:171], a[128:131], a[20:23], v[168:171]\n" \
"buffer_load_dwordx4 v234, s[24:27], s51 offen lds\n" \
"v_mfma_f32_16x16x32_bf16 v[172:175], a[132:135], a[20:23], v[172:175]\n" \
"ds_read_b64_tr_b16 a[56:57], v241 offset:4096\n" \
"v_mfma_f32_16x16x32_bf16 v[16:19], a[88:91], a[24:27], v[16:19]\n" \
"ds_read_b64_tr_b16 a[58:59], v241 offset:4608\n" \
"v_mfma_f32_16x16x32_bf16 v[20:23], a[92:95], a[24:27], v[20:23]\n" \
"ds_read_b128 a[20:23], v238 offset:2048\n" \
"ds_read_b64_tr_b16 a[60:61], v242 offset:4096\n" \
"v_mfma_f32_16x16x32_bf16 v[48:51], a[96:99], a[24:27], v[48:51]\n" \
"ds_read_b64_tr_b16 a[62:63], v242 offset:4608\n" \
"v_mfma_f32_16x16x32_bf16 v[52:55], a[100:103], a[24:27], v[52:55]\n" \
"s_add_u32 m0, s55, 16384\n" \
"ds_read_b64_tr_b16 a[64:65], v239 offset:8192\n" \
"v_mfma_f32_16x16x32_bf16 v[80:83], a[104:107], a[24:27], v[80:83]\n" \
"buffer_load_dwordx4 v234, s[24:27], s52 offen lds\n" \
"v_mfma_f32_16x16x32_bf16 v[84:87], a[108:111], a[24:27], v[84:87]\n" \
"ds_read_b64_tr_b16 a[66:67], v239 offset:8704\n" \
"v_mfma_f32_16x16x32_bf16 v[112:115], a[112:115], a[24:27], v[112:115]\n" \
"ds_read_b64_tr_b16 a[68:69], v240 offset:8192\n" \
"v_mfma_f32_16x16x32_bf16 v[116:119], a[116:119], a[24:27], v[116:119]\n" \
"ds_read_b64_tr_b16 a[70:71], v240 offset:8704\n" \
"v_mfma_f32_16x16x32_bf16 v[144:147], a[120:123], a[24:27], v[144:147]\n" \
"s_add_u32 m0, s55, 20480\n" \
"ds_read_b64_tr_b16 a[72:73], v241 offset:8192\n" \
"v_mfma_f32_16x16x32_bf16 v[148:151], a[124:127], a[24:27], v[148:151]\n" \
"buffer_load_dwordx4 v234, s[24:27], s53 offen lds\n" \
"v_mfma_f32_16x16x32_bf16 v[176:179], a[128:131], a[24:27], v[176:179]\n" \
"ds_read_b64_tr_b16 a[74:75], v241 offset:8704\n" \
"v_mfma_f32_16x16x32_bf16 v[180:183], a[132:135], a[24:27], v[180:183]\n" \
"ds_read_b64_tr_b16 a[76:77], v242 offset:8192\n" \
"v_mfma_f32_16x16x32_bf16 v[24:27], a[88:91], a[28:31], v[24:27]\n" \
"ds_read_b64_tr_b16 a[78:79], v242 offset:8704\n" \
"v_mfma_f32_16x16x32_bf16 v[28:31], a[92:95], a[28:31], v[28:31]\n" \
"ds_read_b128 a[24:27], v238 offset:4096\n" \
"v_mfma_f32_16x16x32_bf16 v[56:59], a[96:99], a[28:31], v[56:59]\n" \
"buffer_load_dword a246, v254, s[76:79], s74 offen\n" \
"v_mfma_f32_16x16x32_bf16 v[60:63], a[100:103], a[28:31], v[60:63]\n" \
"v_mfma_f32_16x16x32_bf16 v[88:91], a[104:107], a[28:31], v[88:91]\n" \
"v_mfma_f32_16x16x32_bf16 v[92:95], a[108:111], a[28:31], v[92:95]\n" \
"s_add_u32 s58, s58, 1\n" \
"v_mfma_f32_16x16x32_bf16 v[120:123], a[112:115], a[28:31], v[120:123]\n" \
"v_mfma_f32_16x16x32_bf16 v[124:127], a[116:119], a[28:31], v[124:127]\n" \
"v_mfma_f32_16x16x32_bf16 v[152:155], a[120:123], a[28:31], v[152:155]\n" \
"s_bitcmp1_b32 86018, s58\n" \
"s_cbranch_scc0 Lhm3_%=\n" \
"s_cmp_lt_u32 s58, 16\n" \
"s_cbranch_scc1 Lhsp3_%=\n" \
"v_mfma_f32_16x16x32_bf16 v[156:159], a[124:127], a[28:31], v[156:159]\n" \
"v_mfma_f32_16x16x32_bf16 v[184:187], a[128:131], a[28:31], v[184:187]\n" \
"v_mfma_f32_16x16x32_bf16 v[188:191], a[132:135], a[28:31], v[188:191]\n" \
"s_branch Lh_epi_%=\n" \
"Lhsp3_%=:\n" \
"s_cmp_ge_u32 s58, 12\n" \
"s_cbranch_scc1 Lhs23_%=\n" \
"s_add_u32 s24, s24, s75\n" \
"s_addc_u32 s25, s25, 0\n" \
"s_add_u32 s55, s55, 24576\n" \
"s_add_u32 s63, s63, 1\n" \
"s_cmp_eq_u32 s63, 4\n" \
"s_cmov_b32 s55, s62\n" \
"s_cmov_b32 s63, 0\n" \
"s_add_u32 m0, s55, 0\n" \
"s_nop 0\n" \
"buffer_load_dwordx4 v234, s[24:27], s48 offen lds\n" \
"s_add_u32 m0, s55, 4096\n" \
"s_nop 0\n" \
"buffer_load_dwordx4 v234, s[24:27], s49 offen lds\n" \
"s_add_u32 m0, s55, 8192\n" \
"s_nop 0\n" \
"buffer_load_dwordx4 v234, s[24:27], s50 offen lds\n" \
"s_add_u32 m0, s55, 12288\n" \
"s_nop 0\n" \
"buffer_load_dwordx4 v234, s[24:27], s51 offen lds\n" \
"s_add_u32 m0, s55, 16384\n" \
"s_nop 0\n" \
"buffer_load_dwordx4 v234, s[24:27], s52 offen lds\n" \
"s_add_u32 m0, s55, 20480\n" \
"s_nop 0\n" \
"buffer_load_dwordx4 v234, s[24:27], s53 offen lds\n" \
"s_branch Lhm3_%=\n" \
"Lhs13_%=:\n" \
"Lhs23_%=:\n" \
"v_accvgpr_read_b32 v247, a253\n" \
"s_cmp_eq_u32 s58, 14\n" \
"s_cbranch_scc1 Lhs43_%=\n" \
"v_accvgpr_read_b32 v234, a233\n" \
"v_readlane_b32 s60, v247, 0\n" \
"s_mov_b32 s75, 196608\n" \
"s_add_u32 s48, s81, 0\n" \
"s_add_u32 s49, s80, 0\n" \
"s_add_u32 s50, s81, 65536\n" \
"s_add_u32 s51, s80, 65536\n" \
"s_add_u32 s52, s81, 131072\n" \
"s_add_u32 s53, s80, 131072\n" \
"s_add_u32 s24, s68, s60\n" \
"s_addc_u32 s25, s69, 0\n" \
"s_sub_u32 s24, s24, s75\n" \
"s_subb_u32 s25, s25, 0\n" \
"s_getpc_b64 s[28:29]\n" \
"Lct4_%=:\n" \
"s_add_u32 s28, s28, Lh_epi_%=-Lct4_%=\n" \
"s_addc_u32 s29, s29, 0\n" \
"s_mov_b32 s30, Lq_entry_%=-Lh_epi_%=\n" \
"s_mov_b32 s31, 0x20000\n" \
"v_accvgpr_read_b32 v249, a243\n" \
"buffer_load_dword a206, v249, s[28:31], 0 offen\n" \
"s_branch Lhm3_%=\n" \
"Lhs43_%=:\n" \
"v_accvgpr_read_b32 v232, a233\n" \
"v_readlane_b32 s60, v247, 15\n" \
"v_readlane_b32 s82, v247, 11\n" \
"v_readlane_b32 s61, v247, 6\n" \
"s_add_u32 s40, s81, 0\n" \
"s_add_u32 s41, s81, 16384\n" \
"s_add_u32 s42, s81, 32768\n" \
"s_add_u32 s43, s81, 49152\n" \
"s_add_u32 s44, s80, 0\n" \
"s_add_u32 s45, s80, 16384\n" \
"s_add_u32 s46, s80, 32768\n" \
"s_add_u32 s47, s80, 49152\n" \
"s_add_u32 s20, s68, s60\n" \
"s_addc_u32 s21, s69, 0\n" \
"s_sub_u32 s82, s82, s60\n" \
"s_ashr_i32 s83, s82, 31\n" \
"s_add_u32 s24, s68, s61\n" \
"s_addc_u32 s25, s69, 0\n" \
"s_sub_u32 s24, s24, s75\n" \
"s_subb_u32 s25, s25, 0\n" \
"v_subrev_u32 v232, 49152, v232\n" \
"v_mov_b32 v233, v232\n" \
"s_branch Lhm3_%=\n" \
".p2align 12\n" \
"Lh_epi_%=:\n" \
"v_accvgpr_read_b32 v250, a244\n" \
"v_accvgpr_read_b32 v251, a245\n" \
"s_nop 0\n" \
"v_readfirstlane_b32 s72, v250\n" \
"v_readfirstlane_b32 s73, v251\n" \
"s_nop 0\n" \
"s_add_u32 s72, s72, 1\n" \
"s_addc_u32 s73, s73, 0\n" \
"v_accvgpr_read_b32 v237, a234\n" \
"v_accvgpr_read_b32 v238, a235\n" \
"v_accvgpr_read_b32 v239, a236\n" \
"v_accvgpr_read_b32 v240, a237\n" \
"v_add_u32 v224, 0x10000, v237\n" \
"v_add_u32 v225, 0x10000, v238\n" \
"v_add_u32 v226, 0x10000, v239\n" \
"v_add_u32 v227, 0x10000, v240\n" \
"s_waitcnt lgkmcnt(0)\n" \
"ds_read_b64_tr_b16 a[128:129], v224 offset:0\n" \
"ds_read_b64_tr_b16 a[130:131], v224 offset:2048\n" \
"ds_read_b64_tr_b16 a[132:133], v225 offset:0\n" \
"ds_read_b64_tr_b16 a[134:135], v225 offset:2048\n" \
"ds_read_b64_tr_b16 a[136:137], v226 offset:0\n" \
"ds_read_b64_tr_b16 a[138:139], v226 offset:2048\n" \
"ds_read_b64_tr_b16 a[140:141], v227 offset:0\n" \
"ds_read_b64_tr_b16 a[142:143], v227 offset:2048\n" \
"ds_read_b64_tr_b16 a[144:145], v224 offset:4096\n" \
"ds_read_b64_tr_b16 a[146:147], v224 offset:6144\n" \
"ds_read_b64_tr_b16 a[148:149], v225 offset:4096\n" \
"ds_read_b64_tr_b16 a[150:151], v225 offset:6144\n" \
"ds_read_b64_tr_b16 a[152:153], v226 offset:4096\n" \
"ds_read_b64_tr_b16 a[154:155], v226 offset:6144\n" \
"ds_read_b64_tr_b16 a[156:157], v227 offset:4096\n" \
"ds_read_b64_tr_b16 a[158:159], v227 offset:6144\n" \
"v_accvgpr_read_b32 v232, a232\n" \
"v_accvgpr_read_b32 v235, a233\n" \
"v_accvgpr_read_b32 v241, a252\n" \
"v_accvgpr_read_b32 v254, a253\n" \
"s_and_b32 s60, s91, 1\n" \
"s_lshl_b32 s60, s60, 7\n" \
"v_add_u32 v235, s60, v235\n" \
"v_xor_b32 v236, 128, v235\n" \
"v_add_u32 v233, 0, v232\n" \
"v_add_u32 v234, 2048, v232\n" \
"s_lshl_b32 s80, s59, 10\n" \
"s_mul_i32 s60, s88, 2359296\n" \
"s_mul_i32 s61, s89, 98304\n" \
"s_add_u32 s60, s60, s61\n" \
"s_add_u32 s40, s60, 0\n" \
"s_add_u32 s41, s60, 16384\n" \
"s_add_u32 s42, s60, 32768\n" \
"s_add_u32 s43, s60, 49152\n" \
"s_add_u32 s44, s60, 65536\n" \
"s_add_u32 s45, s60, 81920\n" \
"v_cvt_pk_bf16_f32 v192, v0, v1\n" \
"v_cvt_pk_bf16_f32 v193, v2, v3\n" \
"v_cvt_pk_bf16_f32 v194, v4, v5\n" \
"v_cvt_pk_bf16_f32 v195, v6, v7\n" \
"v_pk_max_i16 v192, v192, 0\n" \
"v_pk_max_i16 v193, v193, 0\n" \
"v_pk_max_i16 v194, v194, 0\n" \
"v_pk_max_i16 v195, v195, 0\n" \
"buffer_store_dwordx4 v[192:195], v232, s[64:67], s40 offen offset:0 sc1\n" \
"v_cvt_pk_bf16_f32 v196, v8, v9\n" \
"v_cvt_pk_bf16_f32 v197, v10, v11\n" \
"v_cvt_pk_bf16_f32 v198, v12, v13\n" \
"v_cvt_pk_bf16_f32 v199, v14, v15\n" \
"v_pk_max_i16 v196, v196, 0\n" \
"v_pk_max_i16 v197, v197, 0\n" \
"v_pk_max_i16 v198, v198, 0\n" \
"v_pk_max_i16 v199, v199, 0\n" \
"buffer_store_dwordx4 v[196:199], v232, s[64:67], s40 offen offset:1024 sc1\n" \
"v_cvt_pk_bf16_f32 v200, v16, v17\n" \
"v_cvt_pk_bf16_f32 v201, v18, v19\n" \
"v_cvt_pk_bf16_f32 v202, v20, v21\n" \
"v_cvt_pk_bf16_f32 v203, v22, v23\n" \
"v_pk_max_i16 v200, v200, 0\n" \
"v_pk_max_i16 v201, v201, 0\n" \
"v_pk_max_i16 v202, v202, 0\n" \
"v_pk_max_i16 v203, v203, 0\n" \
"buffer_store_dwordx4 v[200:203], v232, s[64:67], s40 offen offset:2048 sc1\n" \
"v_cvt_pk_bf16_f32 v204, v24, v25\n" \
"v_cvt_pk_bf16_f32 v205, v26, v27\n" \
"v_cvt_pk_bf16_f32 v206, v28, v29\n" \
"v_cvt_pk_bf16_f32 v207, v30, v31\n" \
"v_pk_max_i16 v204, v204, 0\n" \
"v_pk_max_i16 v205, v205, 0\n" \
"v_pk_max_i16 v206, v206, 0\n" \
"v_pk_max_i16 v207, v207, 0\n" \
"buffer_store_dwordx4 v[204:207], v232, s[64:67], s40 offen offset:3072 sc1\n" \
"v_accvgpr_read_b32 v253, a255\n" \
"v_readlane_b32 s24, v254, 16\n" \
"v_readlane_b32 s22, v241, 6\n" \
"s_waitcnt lgkmcnt(0)\n" \
"v_mfma_f32_16x16x32_bf16 v[0:3], a[128:131], v[192:195], 0\n" \
"v_cvt_pk_bf16_f32 v208, v32, v33\n" \
"v_cvt_pk_bf16_f32 v209, v34, v35\n" \
"v_cvt_pk_bf16_f32 v210, v36, v37\n" \
"v_cvt_pk_bf16_f32 v211, v38, v39\n" \
"v_pk_max_i16 v208, v208, 0\n" \
"v_mfma_f32_16x16x32_bf16 v[8:11], a[128:131], v[196:199], 0\n" \
"v_pk_max_i16 v209, v209, 0\n" \
"v_pk_max_i16 v210, v210, 0\n" \
"v_pk_max_i16 v211, v211, 0\n" \
"buffer_store_dwordx4 v[208:211], v232, s[64:67], s41 offen offset:0 sc1\n" \
"v_mfma_f32_16x16x32_bf16 v[16:19], a[128:131], v[200:203], 0\n" \
"v_cvt_pk_bf16_f32 v212, v40, v41\n" \
"v_cvt_pk_bf16_f32 v213, v42, v43\n" \
"v_cvt_pk_bf16_f32 v214, v44, v45\n" \
"v_cvt_pk_bf16_f32 v215, v46, v47\n" \
"v_pk_max_i16 v212, v212, 0\n" \
"v_mfma_f32_16x16x32_bf16 v[24:27], a[128:131], v[204:207], 0\n" \
"v_pk_max_i16 v213, v213, 0\n" \
"v_pk_max_i16 v214, v214, 0\n" \
"v_pk_max_i16 v215, v215, 0\n" \
"buffer_store_dwordx4 v[212:215], v232, s[64:67], s41 offen offset:1024 sc1\n" \
"v_mfma_f32_16x16x32_bf16 v[28:31], a[132:135], v[204:207], 0\n" \
"v_cvt_pk_bf16_f32 v216, v48, v49\n" \
"v_cvt_pk_bf16_f32 v217, v50, v51\n" \
"v_cvt_pk_bf16_f32 v218, v52, v53\n" \
"v_cvt_pk_bf16_f32 v219, v54, v55\n" \
"v_pk_max_i16 v216, v216, 0\n" \
"v_mfma_f32_16x16x32_bf16 v[20:23], a[132:135], v[200:203], 0\n" \
"v_pk_max_i16 v217, v217, 0\n" \
"v_pk_max_i16 v218, v218, 0\n" \
"v_pk_max_i16 v219, v219, 0\n" \
"buffer_store_dwordx4 v[216:219], v232, s[64:67], s41 offen offset:2048 sc1\n" \
"v_mfma_f32_16x16x32_bf16 v[12:15], a[132:135], v[196:199], 0\n" \
"v_cvt_pk_bf16_f32 v220, v56, v57\n" \
"v_cvt_pk_bf16_f32 v221, v58, v59\n" \
"v_cvt_pk_bf16_f32 v222, v60, v61\n" \
"v_cvt_pk_bf16_f32 v223, v62, v63\n" \
"v_pk_max_i16 v220, v220, 0\n" \
"v_mfma_f32_16x16x32_bf16 v[4:7], a[132:135], v[192:195], 0\n" \
"v_pk_max_i16 v221, v221, 0\n" \
"v_pk_max_i16 v222, v222, 0\n" \
"v_pk_max_i16 v223, v223, 0\n" \
"buffer_store_dwordx4 v[220:223], v232, s[64:67], s41 offen offset:3072 sc1\n" \
"v_mfma_f32_16x16x32_bf16 v[32:35], a[136:139], v[192:195], 0\n" \
"v_cvt_pk_bf16_f32 v64, v64, v65\n" \
"v_cvt_pk_bf16_f32 v65, v66, v67\n" \
"v_cvt_pk_bf16_f32 v66, v68, v69\n" \
"v_cvt_pk_bf16_f32 v67, v70, v71\n" \
"v_pk_max_i16 v64, v64, 0\n" \
"v_pk_max_i16 v65, v65, 0\n" \
"v_pk_max_i16 v66, v66, 0\n" \
"ds_read_b64_tr_b16 a[160:161], v224 offset:8192\n" \
"v_mfma_f32_16x16x32_bf16 v[40:43], a[136:139], v[196:199], 0\n" \
"v_pk_max_i16 v67, v67, 0\n" \
"v_accvgpr_write_b32 a32, v64\n" \
"v_accvgpr_write_b32 a33, v65\n" \
"v_accvgpr_write_b32 a34, v66\n" \
"v_accvgpr_write_b32 a35, v67\n" \
"buffer_store_dwordx4 a[32:35], v232, s[64:67], s42 offen offset:0 sc1\n" \
"ds_read_b64_tr_b16 a[162:163], v224 offset:10240\n" \
"v_mfma_f32_16x16x32_bf16 v[48:51], a[136:139], v[200:203], 0\n" \
"v_cvt_pk_bf16_f32 v72, v72, v73\n" \
"v_cvt_pk_bf16_f32 v73, v74, v75\n" \
"v_cvt_pk_bf16_f32 v74, v76, v77\n" \
"v_cvt_pk_bf16_f32 v75, v78, v79\n" \
"v_pk_max_i16 v72, v72, 0\n" \
"v_pk_max_i16 v73, v73, 0\n" \
"v_pk_max_i16 v74, v74, 0\n" \
"ds_read_b64_tr_b16 a[164:165], v225 offset:8192\n" \
"v_mfma_f32_16x16x32_bf16 v[56:59], a[136:139], v[204:207], 0\n" \
"v_pk_max_i16 v75, v75, 0\n" \
"v_accvgpr_write_b32 a36, v72\n" \
"v_accvgpr_write_b32 a37, v73\n" \
"v_accvgpr_write_b32 a38, v74\n" \
"v_accvgpr_write_b32 a39, v75\n" \
"buffer_store_dwordx4 a[36:39], v232, s[64:67], s42 offen offset:1024 sc1\n" \
"ds_read_b64_tr_b16 a[166:167], v225 offset:10240\n" \
"v_mfma_f32_16x16x32_bf16 v[60:63], a[140:143], v[204:207], 0\n" \
"v_cvt_pk_bf16_f32 v80, v80, v81\n" \
"v_cvt_pk_bf16_f32 v81, v82, v83\n" \
"v_cvt_pk_bf16_f32 v82, v84, v85\n" \
"v_cvt_pk_bf16_f32 v83, v86, v87\n" \
"v_pk_max_i16 v80, v80, 0\n" \
"v_pk_max_i16 v81, v81, 0\n" \
"v_pk_max_i16 v82, v82, 0\n" \
"ds_read_b64_tr_b16 a[168:169], v226 offset:8192\n" \
"v_mfma_f32_16x16x32_bf16 v[52:55], a[140:143], v[200:203], 0\n" \
"v_pk_max_i16 v83, v83, 0\n" \
"v_accvgpr_write_b32 a40, v80\n" \
"v_accvgpr_write_b32 a41, v81\n" \
"v_accvgpr_write_b32 a42, v82\n" \
"v_accvgpr_write_b32 a43, v83\n" \
"buffer_store_dwordx4 a[40:43], v232, s[64:67], s42 offen offset:2048 sc1\n" \
"ds_read_b64_tr_b16 a[170:171], v226 offset:10240\n" \
"v_mfma_f32_16x16x32_bf16 v[44:47], a[140:143], v[196:199], 0\n" \
"v_cvt_pk_bf16_f32 v88, v88, v89\n" \
"v_cvt_pk_bf16_f32 v89, v90, v91\n" \
"v_cvt_pk_bf16_f32 v90, v92, v93\n" \
"v_cvt_pk_bf16_f32 v91, v94, v95\n" \
"v_pk_max_i16 v88, v88, 0\n" \
"v_pk_max_i16 v89, v89, 0\n" \
"v_pk_max_i16 v90, v90, 0\n" \
"v_mfma_f32_16x16x32_bf16 v[36:39], a[140:143], v[192:195], 0\n" \
"v_pk_max_i16 v91, v91, 0\n" \
"v_accvgpr_write_b32 a44, v88\n" \
"v_accvgpr_write_b32 a45, v89\n" \
"v_accvgpr_write_b32 a46, v90\n" \
"v_accvgpr_write_b32 a47, v91\n" \
"buffer_store_dwordx4 a[44:47], v232, s[64:67], s42 offen offset:3072 sc1\n" \
"ds_read_b64_tr_b16 a[172:173], v227 offset:8192\n" \
"v_mfma_f32_16x16x32_bf16 v[64:67], a[144:147], v[192:195], 0\n" \
"v_cvt_pk_bf16_f32 v96, v96, v97\n" \
"v_cvt_pk_bf16_f32 v97, v98, v99\n" \
"v_cvt_pk_bf16_f32 v98, v100, v101\n" \
"v_cvt_pk_bf16_f32 v99, v102, v103\n" \
"v_pk_max_i16 v96, v96, 0\n" \
"v_pk_max_i16 v97, v97, 0\n" \
"ds_read_b64_tr_b16 a[174:175], v227 offset:10240\n" \
"v_mfma_f32_16x16x32_bf16 v[72:75], a[144:147], v[196:199], 0\n" \
"v_pk_max_i16 v98, v98, 0\n" \
"v_pk_max_i16 v99, v99, 0\n" \
"v_accvgpr_write_b32 a48, v96\n" \
"v_accvgpr_write_b32 a49, v97\n" \
"v_accvgpr_write_b32 a50, v98\n" \
"v_accvgpr_write_b32 a51, v99\n" \
"ds_read_b64_tr_b16 a[176:177], v224 offset:12288\n" \
"v_mfma_f32_16x16x32_bf16 v[80:83], a[144:147], v[200:203], 0\n" \
"v_cvt_pk_bf16_f32 v104, v104, v105\n" \
"v_cvt_pk_bf16_f32 v105, v106, v107\n" \
"v_cvt_pk_bf16_f32 v106, v108, v109\n" \
"v_cvt_pk_bf16_f32 v107, v110, v111\n" \
"v_pk_max_i16 v104, v104, 0\n" \
"v_pk_max_i16 v105, v105, 0\n" \
"ds_read_b64_tr_b16 a[178:179], v224 offset:14336\n" \
"v_mfma_f32_16x16x32_bf16 v[88:91], a[144:147], v[204:207], 0\n" \
"v_pk_max_i16 v106, v106, 0\n" \
"v_pk_max_i16 v107, v107, 0\n" \
"v_accvgpr_write_b32 a52, v104\n" \
"v_accvgpr_write_b32 a53, v105\n" \
"v_accvgpr_write_b32 a54, v106\n" \
"v_accvgpr_write_b32 a55, v107\n" \
"ds_read_b64_tr_b16 a[180:181], v225 offset:12288\n" \
"v_mfma_f32_16x16x32_bf16 v[92:95], a[148:151], v[204:207], 0\n" \
"v_cvt_pk_bf16_f32 v112, v112, v113\n" \
"v_cvt_pk_bf16_f32 v113, v114, v115\n" \
"v_cvt_pk_bf16_f32 v114, v116, v117\n" \
"v_cvt_pk_bf16_f32 v115, v118, v119\n" \
"v_pk_max_i16 v112, v112, 0\n" \
"v_pk_max_i16 v113, v113, 0\n" \
"v_mfma_f32_16x16x32_bf16 v[84:87], a[148:151], v[200:203], 0\n" \
"v_pk_max_i16 v114, v114, 0\n" \
"v_pk_max_i16 v115, v115, 0\n" \
"v_accvgpr_write_b32 a56, v112\n" \
"v_accvgpr_write_b32 a57, v113\n" \
"v_accvgpr_write_b32 a58, v114\n" \
"v_accvgpr_write_b32 a59, v115\n" \
"ds_read_b64_tr_b16 a[182:183], v225 offset:14336\n" \
"v_mfma_f32_16x16x32_bf16 v[76:79], a[148:151], v[196:199], 0\n" \
"v_cvt_pk_bf16_f32 v120, v120, v121\n" \
"v_cvt_pk_bf16_f32 v121, v122, v123\n" \
"v_cvt_pk_bf16_f32 v122, v124, v125\n" \
"v_cvt_pk_bf16_f32 v123, v126, v127\n" \
"v_pk_max_i16 v120, v120, 0\n" \
"v_pk_max_i16 v121, v121, 0\n" \
"ds_read_b64_tr_b16 a[184:185], v226 offset:12288\n" \
"v_mfma_f32_16x16x32_bf16 v[68:71], a[148:151], v[192:195], 0\n" \
"v_pk_max_i16 v122, v122, 0\n" \
"v_pk_max_i16 v123, v123, 0\n" \
"v_accvgpr_write_b32 a60, v120\n" \
"v_accvgpr_write_b32 a61, v121\n" \
"v_accvgpr_write_b32 a62, v122\n" \
"v_accvgpr_write_b32 a63, v123\n" \
"ds_read_b64_tr_b16 a[186:187], v226 offset:14336\n" \
"v_mfma_f32_16x16x32_bf16 v[96:99], a[152:155], v[192:195], 0\n" \
"ds_read_b64_tr_b16 a[188:189], v227 offset:12288\n" \
"v_mfma_f32_16x16x32_bf16 v[104:107], a[152:155], v[196:199], 0\n" \
"ds_read_b64_tr_b16 a[190:191], v227 offset:14336\n" \
"v_mfma_f32_16x16x32_bf16 v[112:115], a[152:155], v[200:203], 0\n" \
"v_mfma_f32_16x16x32_bf16 v[120:123], a[152:155], v[204:207], 0\n" \
"v_mfma_f32_16x16x32_bf16 v[124:127], a[156:159], v[204:207], 0\n" \
"v_mfma_f32_16x16x32_bf16 v[116:119], a[156:159], v[200:203], 0\n" \
"v_mfma_f32_16x16x32_bf16 v[108:111], a[156:159], v[196:199], 0\n" \
"v_mfma_f32_16x16x32_bf16 v[100:103], a[156:159], v[192:195], 0\n" \
"s_waitcnt lgkmcnt(0)\n" \
"v_mfma_f32_16x16x32_bf16 v[0:3], a[160:163], v[208:211], v[0:3]\n" \
"ds_read_b64_tr_b16 a[128:129], v224 offset:16384\n" \
"v_accvgpr_read_b32 v248, a194\n" \
"v_accvgpr_read_b32 v249, a195\n" \
"v_mfma_f32_16x16x32_bf16 v[8:11], a[160:163], v[212:215], v[8:11]\n" \
"ds_read_b64_tr_b16 a[130:131], v224 offset:18432\n" \
"s_mov_b64 s[48:49], s[4:5]\n" \
"v_mfma_f32_16x16x32_bf16 v[16:19], a[160:163], v[216:219], v[16:19]\n" \
"s_mov_b32 s50, 4194304\n" \
"v_mfma_f32_16x16x32_bf16 v[24:27], a[160:163], v[220:223], v[24:27]\n" \
"ds_read_b64_tr_b16 a[132:133], v225 offset:16384\n" \
"s_mov_b32 s51, 0x20000\n" \
"s_mov_b64 s[52:53], s[14:15]\n" \
"v_mfma_f32_16x16x32_bf16 v[28:31], a[164:167], v[220:223], v[28:31]\n" \
"s_mov_b32 s54, 2048\n" \
"v_mfma_f32_16x16x32_bf16 v[20:23], a[164:167], v[216:219], v[20:23]\n" \
"ds_read_b64_tr_b16 a[134:135], v225 offset:18432\n" \
"s_mov_b32 s55, 0x20000\n" \
"v_mfma_f32_16x16x32_bf16 v[12:15], a[164:167], v[212:215], v[12:15]\n" \
"ds_read_b64_tr_b16 a[136:137], v226 offset:16384\n" \
"s_mov_b32 s56, 4096\n" \
"v_mfma_f32_16x16x32_bf16 v[4:7], a[164:167], v[208:211], v[4:7]\n" \
"v_cvt_pk_bf16_f32 v128, v128, v129\n" \
"v_cvt_pk_bf16_f32 v129, v130, v131\n" \
"v_mfma_f32_16x16x32_bf16 v[32:35], a[168:171], v[208:211], v[32:35]\n" \
"ds_read_b64_tr_b16 a[138:139], v226 offset:18432\n" \
"v_cvt_pk_bf16_f32 v130, v132, v133\n" \
"v_mfma_f32_16x16x32_bf16 v[40:43], a[168:171], v[212:215], v[40:43]\n" \
"v_cvt_pk_bf16_f32 v131, v134, v135\n" \
"v_mfma_f32_16x16x32_bf16 v[48:51], a[168:171], v[216:219], v[48:51]\n" \
"ds_read_b64_tr_b16 a[140:141], v227 offset:16384\n" \
"v_pk_max_i16 v128, v128, 0\n" \
"v_pk_max_i16 v129, v129, 0\n" \
"v_mfma_f32_16x16x32_bf16 v[56:59], a[168:171], v[220:223], v[56:59]\n" \
"ds_read_b64_tr_b16 a[142:143], v227 offset:18432\n" \
"v_pk_max_i16 v130, v130, 0\n" \
"v_mfma_f32_16x16x32_bf16 v[60:63], a[172:175], v[220:223], v[60:63]\n" \
"v_pk_max_i16 v131, v131, 0\n" \
"v_mfma_f32_16x16x32_bf16 v[52:55], a[172:175], v[216:219], v[52:55]\n" \
"ds_read_b64_tr_b16 a[144:145], v224 offset:20480\n" \
"v_cvt_pk_bf16_f32 v136, v136, v137\n" \
"v_mfma_f32_16x16x32_bf16 v[44:47], a[172:175], v[212:215], v[44:47]\n" \
"v_cvt_pk_bf16_f32 v137, v138, v139\n" \
"v_cvt_pk_bf16_f32 v138, v140, v141\n" \
"v_mfma_f32_16x16x32_bf16 v[36:39], a[172:175], v[208:211], v[36:39]\n" \
"ds_read_b64_tr_b16 a[146:147], v224 offset:22528\n" \
"v_cvt_pk_bf16_f32 v139, v142, v143\n" \
"v_mfma_f32_16x16x32_bf16 v[64:67], a[176:179], v[208:211], v[64:67]\n" \
"ds_read_b64_tr_b16 a[148:149], v225 offset:20480\n" \
"v_pk_max_i16 v136, v136, 0\n" \
"v_mfma_f32_16x16x32_bf16 v[72:75], a[176:179], v[212:215], v[72:75]\n" \
"v_pk_max_i16 v137, v137, 0\n" \
"v_pk_max_i16 v138, v138, 0\n" \
"v_mfma_f32_16x16x32_bf16 v[80:83], a[176:179], v[216:219], v[80:83]\n" \
"ds_read_b64_tr_b16 a[150:151], v225 offset:22528\n" \
"v_pk_max_i16 v139, v139, 0\n" \
"v_mfma_f32_16x16x32_bf16 v[88:91], a[176:179], v[220:223], v[88:91]\n" \
"v_cvt_pk_bf16_f32 v144, v144, v145\n" \
"v_mfma_f32_16x16x32_bf16 v[92:95], a[180:183], v[220:223], v[92:95]\n" \
"ds_read_b64_tr_b16 a[152:153], v226 offset:20480\n" \
"v_cvt_pk_bf16_f32 v145, v146, v147\n" \
"v_mfma_f32_16x16x32_bf16 v[84:87], a[180:183], v[216:219], v[84:87]\n" \
"ds_read_b64_tr_b16 a[154:155], v226 offset:22528\n" \
"v_cvt_pk_bf16_f32 v146, v148, v149\n" \
"v_cvt_pk_bf16_f32 v147, v150, v151\n" \
"v_mfma_f32_16x16x32_bf16 v[76:79], a[180:183], v[212:215], v[76:79]\n" \
"v_pk_max_i16 v144, v144, 0\n" \
"v_mfma_f32_16x16x32_bf16 v[68:71], a[180:183], v[208:211], v[68:71]\n" \
"ds_read_b64_tr_b16 a[156:157], v227 offset:20480\n" \
"v_pk_max_i16 v145, v145, 0\n" \
"v_mfma_f32_16x16x32_bf16 v[96:99], a[184:187], v[208:211], v[96:99]\n" \
"v_pk_max_i16 v146, v146, 0\n" \
"v_pk_max_i16 v147, v147, 0\n" \
"v_mfma_f32_16x16x32_bf16 v[104:107], a[184:187], v[212:215], v[104:107]\n" \
"ds_read_b64_tr_b16 a[158:159], v227 offset:22528\n" \
"v_cvt_pk_bf16_f32 v152, v152, v153\n" \
"v_mfma_f32_16x16x32_bf16 v[112:115], a[184:187], v[216:219], v[112:115]\n" \
"v_cvt_pk_bf16_f32 v153, v154, v155\n" \
"v_mfma_f32_16x16x32_bf16 v[120:123], a[184:187], v[220:223], v[120:123]\n" \
"v_cvt_pk_bf16_f32 v154, v156, v157\n" \
"v_mfma_f32_16x16x32_bf16 v[124:127], a[188:191], v[220:223], v[124:127]\n" \
"s_lshl_b32 s60, s92, 7\n" \
"s_add_u32 s60, s60, s84\n" \
"s_add_u32 s60, s60, 131072\n" \
"v_mov_b32 v246, s72\n" \
"v_cvt_pk_bf16_f32 v155, v158, v159\n" \
"v_pk_max_i16 v152, v152, 0\n" \
"v_mfma_f32_16x16x32_bf16 v[116:119], a[188:191], v[216:219], v[116:119]\n" \
"v_mov_b32 v247, s73\n" \
"v_pk_max_i16 v153, v153, 0\n" \
"v_mfma_f32_16x16x32_bf16 v[108:111], a[188:191], v[212:215], v[108:111]\n" \
"s_waitcnt vmcnt(0)\n" \
"v_pk_max_i16 v154, v154, 0\n" \
"v_mfma_f32_16x16x32_bf16 v[100:103], a[188:191], v[208:211], v[100:103]\n" \
"s_mov_b64 exec, 1\n" \
"buffer_store_dwordx2 v[246:247], off, s[64:67], s60 sc1\n" \
"s_mov_b64 exec, -1\n" \
"v_pk_max_i16 v155, v155, 0\n" \
"s_waitcnt lgkmcnt(0)\n" \
"v_mfma_f32_16x16x32_bf16 v[0:3], a[128:131], a[32:35], v[0:3]\n" \
"ds_read_b64_tr_b16 a[160:161], v224 offset:24576\n" \
"v_cvt_pk_bf16_f32 v160, v160, v161\n" \
"v_mfma_f32_16x16x32_bf16 v[8:11], a[128:131], a[36:39], v[8:11]\n" \
"buffer_load_dwordx4 v[192:195], v249, s[48:51], 0 offen offset:0\n" \
"v_cvt_pk_bf16_f32 v161, v162, v163\n" \
"v_mfma_f32_16x16x32_bf16 v[16:19], a[128:131], a[40:43], v[16:19]\n" \
"ds_read_b64_tr_b16 a[162:163], v224 offset:26624\n" \
"v_cvt_pk_bf16_f32 v162, v164, v165\n" \
"v_mfma_f32_16x16x32_bf16 v[24:27], a[128:131], a[44:47], v[24:27]\n" \
"buffer_load_dwordx4 v[196:199], v249, s[48:51], 0 offen offset:16\n" \
"v_cvt_pk_bf16_f32 v163, v166, v167\n" \
"v_mfma_f32_16x16x32_bf16 v[28:31], a[132:135], a[44:47], v[28:31]\n" \
"ds_read_b64_tr_b16 a[164:165], v225 offset:24576\n" \
"v_pk_max_i16 v160, v160, 0\n" \
"v_mfma_f32_16x16x32_bf16 v[20:23], a[132:135], a[40:43], v[20:23]\n" \
"ds_read_b64_tr_b16 a[166:167], v225 offset:26624\n" \
"v_pk_max_i16 v161, v161, 0\n" \
"v_mfma_f32_16x16x32_bf16 v[12:15], a[132:135], a[36:39], v[12:15]\n" \
"buffer_load_dwordx4 v[200:203], v249, s[48:51], 0 offen offset:2048\n" \
"v_pk_max_i16 v162, v162, 0\n" \
"v_mfma_f32_16x16x32_bf16 v[4:7], a[132:135], a[32:35], v[4:7]\n" \
"ds_read_b64_tr_b16 a[168:169], v226 offset:24576\n" \
"v_pk_max_i16 v163, v163, 0\n" \
"v_mfma_f32_16x16x32_bf16 v[32:35], a[136:139], a[32:35], v[32:35]\n" \
"ds_read_b64_tr_b16 a[170:171], v226 offset:26624\n" \
"v_cvt_pk_bf16_f32 v168, v168, v169\n" \
"v_mfma_f32_16x16x32_bf16 v[40:43], a[136:139], a[36:39], v[40:43]\n" \
"buffer_load_dwordx4 v[204:207], v249, s[48:51], 0 offen offset:2064\n" \
"v_cvt_pk_bf16_f32 v169, v170, v171\n" \
"v_mfma_f32_16x16x32_bf16 v[48:51], a[136:139], a[40:43], v[48:51]\n" \
"ds_read_b64_tr_b16 a[172:173], v227 offset:24576\n" \
"v_cvt_pk_bf16_f32 v170, v172, v173\n" \
"v_mfma_f32_16x16x32_bf16 v[56:59], a[136:139], a[44:47], v[56:59]\n" \
"ds_read_b64_tr_b16 a[174:175], v227 offset:26624\n" \
"v_cvt_pk_bf16_f32 v171, v174, v175\n" \
"v_mfma_f32_16x16x32_bf16 v[60:63], a[140:143], a[44:47], v[60:63]\n" \
"buffer_load_dwordx4 v[208:211], v249, s[48:51], s56 offen offset:0\n" \
"v_pk_max_i16 v168, v168, 0\n" \
"v_mfma_f32_16x16x32_bf16 v[52:55], a[140:143], a[40:43], v[52:55]\n" \
"v_pk_max_i16 v169, v169, 0\n" \
"v_mfma_f32_16x16x32_bf16 v[44:47], a[140:143], a[36:39], v[44:47]\n" \
"ds_read_b64_tr_b16 a[176:177], v224 offset:28672\n" \
"v_pk_max_i16 v170, v170, 0\n" \
"v_mfma_f32_16x16x32_bf16 v[36:39], a[140:143], a[32:35], v[36:39]\n" \
"buffer_load_dwordx4 v[212:215], v249, s[48:51], s56 offen offset:16\n" \
"v_pk_max_i16 v171, v171, 0\n" \
"v_mfma_f32_16x16x32_bf16 v[64:67], a[144:147], a[32:35], v[64:67]\n" \
"ds_read_b64_tr_b16 a[178:179], v224 offset:30720\n" \
"v_cvt_pk_bf16_f32 v176, v176, v177\n" \
"v_mfma_f32_16x16x32_bf16 v[72:75], a[144:147], a[36:39], v[72:75]\n" \
"ds_read_b64_tr_b16 a[180:181], v225 offset:28672\n" \
"v_cvt_pk_bf16_f32 v177, v178, v179\n" \
"v_mfma_f32_16x16x32_bf16 v[80:83], a[144:147], a[40:43], v[80:83]\n" \
"buffer_load_dwordx4 v[216:219], v249, s[48:51], s56 offen offset:2048\n" \
"v_cvt_pk_bf16_f32 v178, v180, v181\n" \
"v_mfma_f32_16x16x32_bf16 v[88:91], a[144:147], a[44:47], v[88:91]\n" \
"ds_read_b64_tr_b16 a[182:183], v225 offset:30720\n" \
"v_cvt_pk_bf16_f32 v179, v182, v183\n" \
"v_mfma_f32_16x16x32_bf16 v[92:95], a[148:151], a[44:47], v[92:95]\n" \
"ds_read_b64_tr_b16 a[184:185], v226 offset:28672\n" \
"v_pk_max_i16 v176, v176, 0\n" \
"v_mfma_f32_16x16x32_bf16 v[84:87], a[148:151], a[40:43], v[84:87]\n" \
"buffer_load_dwordx4 v[220:223], v249, s[48:51], s56 offen offset:2064\n" \
"v_pk_max_i16 v177, v177, 0\n" \
"v_mfma_f32_16x16x32_bf16 v[76:79], a[148:151], a[36:39], v[76:79]\n" \
"ds_read_b64_tr_b16 a[186:187], v226 offset:30720\n" \
"v_pk_max_i16 v178, v178, 0\n" \
"v_mfma_f32_16x16x32_bf16 v[68:71], a[148:151], a[32:35], v[68:71]\n" \
"ds_read_b64_tr_b16 a[188:189], v227 offset:28672\n" \
"v_pk_max_i16 v179, v179, 0\n" \
"v_mfma_f32_16x16x32_bf16 v[96:99], a[152:155], a[32:35], v[96:99]\n" \
"buffer_load_dwordx4 v[132:135], v248, s[52:55], 0 offen offset:0\n" \
"v_cvt_pk_bf16_f32 v184, v184, v185\n" \
"v_mfma_f32_16x16x32_bf16 v[104:107], a[152:155], a[36:39], v[104:107]\n" \
"ds_read_b64_tr_b16 a[190:191], v227 offset:30720\n" \
"v_cvt_pk_bf16_f32 v185, v186, v187\n" \
"v_mfma_f32_16x16x32_bf16 v[112:115], a[152:155], a[40:43], v[112:115]\n" \
"v_cvt_pk_bf16_f32 v186, v188, v189\n" \
"v_mfma_f32_16x16x32_bf16 v[120:123], a[152:155], a[44:47], v[120:123]\n" \
"buffer_load_dwordx4 v[140:143], v248, s[52:55], 0 offen offset:16\n" \
"v_cvt_pk_bf16_f32 v187, v190, v191\n" \
"v_mfma_f32_16x16x32_bf16 v[124:127], a[156:159], a[44:47], v[124:127]\n" \
"v_pk_max_i16 v184, v184, 0\n" \
"v_mfma_f32_16x16x32_bf16 v[116:119], a[156:159], a[40:43], v[116:119]\n" \
"v_pk_max_i16 v185, v185, 0\n" \
"v_mfma_f32_16x16x32_bf16 v[108:111], a[156:159], a[36:39], v[108:111]\n" \
"v_pk_max_i16 v186, v186, 0\n" \
"v_mfma_f32_16x16x32_bf16 v[100:103], a[156:159], a[32:35], v[100:103]\n" \
"v_pk_max_i16 v187, v187, 0\n" \
"s_waitcnt vmcnt(39) lgkmcnt(0)\n" \
"s_barrier\n" \
"v_mfma_f32_16x16x32_bf16 v[0:3], a[160:163], a[48:51], v[0:3]\n" \
"ds_read_b64_tr_b16 a[128:129], v224 offset:32768\n" \
"v_add_u32 v228, 0x20000, v237\n" \
"v_mfma_f32_16x16x32_bf16 v[8:11], a[160:163], a[52:55], v[8:11]\n" \
"ds_read_b64_tr_b16 a[130:131], v224 offset:34816\n" \
"v_mfma_f32_16x16x32_bf16 v[16:19], a[160:163], a[56:59], v[16:19]\n" \
"v_mfma_f32_16x16x32_bf16 v[24:27], a[160:163], a[60:63], v[24:27]\n" \
"ds_read_b64_tr_b16 a[132:133], v225 offset:32768\n" \
"v_mfma_f32_16x16x32_bf16 v[28:31], a[164:167], a[60:63], v[28:31]\n" \
"v_mfma_f32_16x16x32_bf16 v[20:23], a[164:167], a[56:59], v[20:23]\n" \
"ds_read_b64_tr_b16 a[134:135], v225 offset:34816\n" \
"v_mfma_f32_16x16x32_bf16 v[12:15], a[164:167], a[52:55], v[12:15]\n" \
"ds_read_b64_tr_b16 a[136:137], v226 offset:32768\n" \
"v_mfma_f32_16x16x32_bf16 v[4:7], a[164:167], a[48:51], v[4:7]\n" \
"v_mfma_f32_16x16x32_bf16 v[32:35], a[168:171], a[48:51], v[32:35]\n" \
"ds_read_b64_tr_b16 a[138:139], v226 offset:34816\n" \
"v_add_u32 v229, 0x20000, v238\n" \
"v_mfma_f32_16x16x32_bf16 v[40:43], a[168:171], a[52:55], v[40:43]\n" \
"v_mfma_f32_16x16x32_bf16 v[48:51], a[168:171], a[56:59], v[48:51]\n" \
"ds_read_b64_tr_b16 a[140:141], v227 offset:32768\n" \
"v_mfma_f32_16x16x32_bf16 v[56:59], a[168:171], a[60:63], v[56:59]\n" \
"ds_read_b64_tr_b16 a[142:143], v227 offset:34816\n" \
"v_mfma_f32_16x16x32_bf16 v[60:63], a[172:175], a[60:63], v[60:63]\n" \
"v_mfma_f32_16x16x32_bf16 v[52:55], a[172:175], a[56:59], v[52:55]\n" \
"ds_read_b64_tr_b16 a[144:145], v224 offset:36864\n" \
"v_mfma_f32_16x16x32_bf16 v[44:47], a[172:175], a[52:55], v[44:47]\n" \
"v_mfma_f32_16x16x32_bf16 v[36:39], a[172:175], a[48:51], v[36:39]\n" \
"ds_read_b64_tr_b16 a[146:147], v224 offset:38912\n" \
"v_mfma_f32_16x16x32_bf16 v[64:67], a[176:179], a[48:51], v[64:67]\n" \
"ds_read_b64_tr_b16 a[148:149], v225 offset:36864\n" \
"v_add_u32 v230, 0x20000, v239\n" \
"v_mfma_f32_16x16x32_bf16 v[72:75], a[176:179], a[52:55], v[72:75]\n" \
"v_mfma_f32_16x16x32_bf16 v[80:83], a[176:179], a[56:59], v[80:83]\n" \
"ds_read_b64_tr_b16 a[150:151], v225 offset:38912\n" \
"v_mfma_f32_16x16x32_bf16 v[88:91], a[176:179], a[60:63], v[88:91]\n" \
"v_mfma_f32_16x16x32_bf16 v[92:95], a[180:183], a[60:63], v[92:95]\n" \
"ds_read_b64_tr_b16 a[152:153], v226 offset:36864\n" \
"v_mfma_f32_16x16x32_bf16 v[84:87], a[180:183], a[56:59], v[84:87]\n" \
"ds_read_b64_tr_b16 a[154:155], v226 offset:38912\n" \
"v_mfma_f32_16x16x32_bf16 v[76:79], a[180:183], a[52:55], v[76:79]\n" \
"v_mfma_f32_16x16x32_bf16 v[68:71], a[180:183], a[48:51], v[68:71]\n" \
"ds_read_b64_tr_b16 a[156:157], v227 offset:36864\n" \
"v_mfma_f32_16x16x32_bf16 v[96:99], a[184:187], a[48:51], v[96:99]\n" \
"v_add_u32 v231, 0x20000, v240\n" \
"v_mfma_f32_16x16x32_bf16 v[104:107], a[184:187], a[52:55], v[104:107]\n" \
"ds_read_b64_tr_b16 a[158:159], v227 offset:38912\n" \
"v_mfma_f32_16x16x32_bf16 v[112:115], a[184:187], a[56:59], v[112:115]\n" \
"v_mfma_f32_16x16x32_bf16 v[120:123], a[184:187], a[60:63], v[120:123]\n" \
"v_mfma_f32_16x16x32_bf16 v[124:127], a[188:191], a[60:63], v[124:127]\n" \
"v_mfma_f32_16x16x32_bf16 v[116:119], a[188:191], a[56:59], v[116:119]\n" \
"v_mfma_f32_16x16x32_bf16 v[108:111], a[188:191], a[52:55], v[108:111]\n" \
"v_mfma_f32_16x16x32_bf16 v[100:103], a[188:191], a[48:51], v[100:103]\n" \
"s_waitcnt lgkmcnt(0)\n" \
"v_mfma_f32_16x16x32_bf16 v[0:3], a[128:131], v[128:131], v[0:3]\n" \
"ds_read_b64_tr_b16 a[160:161], v224 offset:40960\n" \
"v_mfma_f32_16x16x32_bf16 v[8:11], a[128:131], v[136:139], v[8:11]\n" \
"ds_read_b64_tr_b16 a[162:163], v224 offset:43008\n" \
"v_mfma_f32_16x16x32_bf16 v[16:19], a[128:131], v[144:147], v[16:19]\n" \
"ds_read_b64_tr_b16 a[164:165], v225 offset:40960\n" \
"v_mfma_f32_16x16x32_bf16 v[24:27], a[128:131], v[152:155], v[24:27]\n" \
"ds_read_b64_tr_b16 a[166:167], v225 offset:43008\n" \
"v_mfma_f32_16x16x32_bf16 v[28:31], a[132:135], v[152:155], v[28:31]\n" \
"ds_read_b64_tr_b16 a[168:169], v226 offset:40960\n" \
"v_mfma_f32_16x16x32_bf16 v[20:23], a[132:135], v[144:147], v[20:23]\n" \
"ds_read_b64_tr_b16 a[170:171], v226 offset:43008\n" \
"v_mfma_f32_16x16x32_bf16 v[12:15], a[132:135], v[136:139], v[12:15]\n" \
"ds_read_b64_tr_b16 a[172:173], v227 offset:40960\n" \
"v_mfma_f32_16x16x32_bf16 v[4:7], a[132:135], v[128:131], v[4:7]\n" \
"ds_read_b64_tr_b16 a[174:175], v227 offset:43008\n" \
"v_mfma_f32_16x16x32_bf16 v[32:35], a[136:139], v[128:131], v[32:35]\n" \
"buffer_load_dwordx2 v[250:251], v253, s[64:67], s84 offen sc1\n" \
"v_mfma_f32_16x16x32_bf16 v[40:43], a[136:139], v[136:139], v[40:43]\n" \
"s_add_u32 m0, s80, 131072\n" \
"ds_read_b64_tr_b16 a[176:177], v224 offset:45056\n" \
"v_mfma_f32_16x16x32_bf16 v[48:51], a[136:139], v[144:147], v[48:51]\n" \
"buffer_load_dwordx4 v235, s[68:71], s24 offen lds\n" \
"v_mfma_f32_16x16x32_bf16 v[56:59], a[136:139], v[152:155], v[56:59]\n" \
"v_readlane_b32 s25, v254, 17\n" \
"ds_read_b64_tr_b16 a[178:179], v224 offset:47104\n" \
"v_mfma_f32_16x16x32_bf16 v[60:63], a[140:143], v[152:155], v[60:63]\n" \
"s_add_u32 m0, s80, 135168\n" \
"ds_read_b64_tr_b16 a[180:181], v225 offset:45056\n" \
"v_mfma_f32_16x16x32_bf16 v[52:55], a[140:143], v[144:147], v[52:55]\n" \
"buffer_load_dwordx4 v236, s[68:71], s24 offen lds\n" \
"v_mfma_f32_16x16x32_bf16 v[44:47], a[140:143], v[136:139], v[44:47]\n" \
"s_add_u32 m0, s80, 139264\n" \
"ds_read_b64_tr_b16 a[182:183], v225 offset:47104\n" \
"v_mfma_f32_16x16x32_bf16 v[36:39], a[140:143], v[128:131], v[36:39]\n" \
"buffer_load_dwordx4 v235, s[68:71], s25 offen lds\n" \
"v_mfma_f32_16x16x32_bf16 v[64:67], a[144:147], v[128:131], v[64:67]\n" \
"v_readlane_b32 s26, v254, 18\n" \
"s_add_u32 m0, s80, 143360\n" \
"v_mfma_f32_16x16x32_bf16 v[72:75], a[144:147], v[136:139], v[72:75]\n" \
"buffer_load_dwordx4 v236, s[68:71], s25 offen lds\n" \
"v_mfma_f32_16x16x32_bf16 v[80:83], a[144:147], v[144:147], v[80:83]\n" \
"ds_read_b64_tr_b16 a[184:185], v226 offset:45056\n" \
"v_mfma_f32_16x16x32_bf16 v[88:91], a[144:147], v[152:155], v[88:91]\n" \
"s_add_u32 m0, s80, 147456\n" \
"ds_read_b64_tr_b16 a[186:187], v226 offset:47104\n" \
"v_mfma_f32_16x16x32_bf16 v[92:95], a[148:151], v[152:155], v[92:95]\n" \
"buffer_load_dwordx4 v235, s[68:71], s26 offen lds\n" \
"v_mfma_f32_16x16x32_bf16 v[84:87], a[148:151], v[144:147], v[84:87]\n" \
"v_readlane_b32 s27, v254, 19\n" \
"s_add_u32 m0, s80, 151552\n" \
"v_mfma_f32_16x16x32_bf16 v[76:79], a[148:151], v[136:139], v[76:79]\n" \
"buffer_load_dwordx4 v236, s[68:71], s26 offen lds\n" \
"v_mfma_f32_16x16x32_bf16 v[68:71], a[148:151], v[128:131], v[68:71]\n" \
"ds_read_b64_tr_b16 a[188:189], v227 offset:45056\n" \
"v_mfma_f32_16x16x32_bf16 v[96:99], a[152:155], v[128:131], v[96:99]\n" \
"s_add_u32 m0, s80, 155648\n" \
"ds_read_b64_tr_b16 a[190:191], v227 offset:47104\n" \
"v_mfma_f32_16x16x32_bf16 v[104:107], a[152:155], v[136:139], v[104:107]\n" \
"buffer_load_dwordx4 v235, s[68:71], s27 offen lds\n" \
"v_mfma_f32_16x16x32_bf16 v[112:115], a[152:155], v[144:147], v[112:115]\n" \
"v_readlane_b32 s24, v254, 20\n" \
"s_add_u32 m0, s80, 159744\n" \
"v_mfma_f32_16x16x32_bf16 v[120:123], a[152:155], v[152:155], v[120:123]\n" \
"buffer_load_dwordx4 v236, s[68:71], s27 offen lds\n" \
"v_mfma_f32_16x16x32_bf16 v[124:127], a[156:159], v[152:155], v[124:127]\n" \
"v_mfma_f32_16x16x32_bf16 v[116:119], a[156:159], v[144:147], v[116:119]\n" \
"v_mfma_f32_16x16x32_bf16 v[108:111], a[156:159], v[136:139], v[108:111]\n" \
"v_mfma_f32_16x16x32_bf16 v[100:103], a[156:159], v[128:131], v[100:103]\n" \
"s_mov_b64 vcc, -1\n" \
"s_cmp_eq_u32 s59, 1\n" \
"s_cbranch_scc1 Lpadh1_%=\n" \
"s_cmp_eq_u32 s59, 2\n" \
"s_cbranch_scc1 Lpadh2_%=\n" \
"s_cmp_eq_u32 s59, 3\n" \
"s_cbranch_scc1 Lpadh3_%=\n" \
"s_branch Lpadh0_%=\n" \
"Lprh0_%=:\n" \
"Lprh1_%=:\n" \
"Lprh2_%=:\n" \
"Lprh3_%=:\n" \
"s_mov_b64 vcc, 0\n" \
"Lsch_%=:\n" \
"s_waitcnt vmcnt(9)\n" \
"v_mov_b32 v148, 0\n" \
"v_mov_b32 v149, 0\n" \
"v_mov_b32 v150, 0\n" \
"v_mov_b32 v151, 0\n" \
"v_dot2c_f32_bf16 v148, v192, v132\n" \
"v_dot2c_f32_bf16 v149, v200, v132\n" \
"v_dot2c_f32_bf16 v150, v208, v132\n" \
"v_dot2c_f32_bf16 v151, v216, v132\n" \
"v_dot2c_f32_bf16 v148, v193, v133\n" \
"v_dot2c_f32_bf16 v149, v201, v133\n" \
"v_dot2c_f32_bf16 v150, v209, v133\n" \
"v_dot2c_f32_bf16 v151, v217, v133\n" \
"v_dot2c_f32_bf16 v148, v194, v134\n" \
"v_dot2c_f32_bf16 v149, v202, v134\n" \
"v_dot2c_f32_bf16 v150, v210, v134\n" \
"v_dot2c_f32_bf16 v151, v218, v134\n" \
"v_dot2c_f32_bf16 v148, v195, v135\n" \
"v_dot2c_f32_bf16 v149, v203, v135\n" \
"v_dot2c_f32_bf16 v150, v211, v135\n" \
"v_dot2c_f32_bf16 v151, v219, v135\n" \
"v_dot2c_f32_bf16 v148, v196, v140\n" \
"v_dot2c_f32_bf16 v149, v204, v140\n" \
"v_dot2c_f32_bf16 v150, v212, v140\n" \
"v_dot2c_f32_bf16 v151, v220, v140\n" \
"v_dot2c_f32_bf16 v148, v197, v141\n" \
"v_dot2c_f32_bf16 v149, v205, v141\n" \
"v_dot2c_f32_bf16 v150, v213, v141\n" \
"v_dot2c_f32_bf16 v151, v221, v141\n" \
"v_dot2c_f32_bf16 v148, v198, v142\n" \
"v_dot2c_f32_bf16 v149, v206, v142\n" \
"v_dot2c_f32_bf16 v150, v214, v142\n" \
"v_dot2c_f32_bf16 v151, v222, v142\n" \
"v_dot2c_f32_bf16 v148, v199, v143\n" \
"v_dot2c_f32_bf16 v149, v207, v143\n" \
"v_dot2c_f32_bf16 v150, v215, v143\n" \
"v_dot2c_f32_bf16 v151, v223, v143\n" \
"v_accvgpr_read_b32 v156, a193\n" \
"s_and_b32 s60, s89, 7\n" \
"s_lshl_b32 s60, s60, 5\n" \
"v_permlane32_swap_b32 v148, v149\n" \
"v_permlane32_swap_b32 v150, v151\n" \
"v_add_f32 v148, v148, v149\n" \
"v_add_f32 v150, v150, v151\n" \
"s_and_b32 s61, s89, 8\n" \
"s_add_u32 s60, s60, s61\n" \
"v_permlane16_swap_b32 v148, v150\n" \
"v_add_f32 v148, v148, v150\n" \
"s_add_u32 s57, s60, s94\n" \
"s_nop 0\n" \
"v_add_f32_dpp v148, v148, v148 row_ror:8 row_mask:0xf bank_mask:0xf\n" \
"s_nop 1\n" \
"v_add_f32_dpp v148, v148, v148 row_ror:4 row_mask:0xf bank_mask:0xf\n" \
"s_nop 1\n" \
"v_add_f32_dpp v148, v148, v148 row_ror:2 row_mask:0xf bank_mask:0xf\n" \
"s_nop 1\n" \
"v_add_f32_dpp v148, v148, v148 row_ror:1 row_mask:0xf bank_mask:0xf\n" \
"v_mul_f32 v148, 0xbfb8aa3b, v148\n" \
"v_exp_f32 v148, v148\n" \
"s_nop 0\n" \
"v_add_f32 v148, 1.0, v148\n" \
"v_rcp_f32 v148, v148\n" \
"s_waitcnt vmcnt(8)\n" \
"v_cmp_ne_u64 vcc, s[72:73], v[250:251]\n" \
"s_nop 0\n" \
"s_cbranch_vccz Lpd1_%=\n" \
"s_mov_b32 s61, 0\n" \
"Lp1_%=:\n" \
"buffer_load_dwordx2 v[250:251], v253, s[64:67], s84 offen sc1\n" \
"s_waitcnt vmcnt(0)\n" \
"v_cmp_ne_u64 vcc, s[72:73], v[250:251]\n" \
"s_nop 0\n" \
"s_cbranch_vccz Lpd1_%=\n" \
"s_add_u32 s61, s61, 1\n" \
"s_cmp_lt_u32 s61, 0x40000\n" \
"s_sleep 1\n" \
"s_cbranch_scc1 Lp1_%=\n" \
"s_mov_b64 vcc, 0\n" \
"Lpd1_%=:\n" \
"v_accvgpr_read_b32 v253, a254\n" \
"s_waitcnt lgkmcnt(0)\n" \
"v_mfma_f32_16x16x32_bf16 v[0:3], a[160:163], v[160:163], v[0:3]\n" \
"ds_read_b64_tr_b16 a[128:129], v224 offset:49152\n" \
"v_mfma_f32_16x16x32_bf16 v[8:11], a[160:163], v[168:171], v[8:11]\n" \
"buffer_load_dwordx4 a[96:99], v233, s[64:67], s22 offen offset:0 sc1\n" \
"v_mfma_f32_16x16x32_bf16 v[16:19], a[160:163], v[176:179], v[16:19]\n" \
"v_readlane_b32 s23, v241, 7\n" \
"ds_read_b64_tr_b16 a[130:131], v224 offset:51200\n" \
"v_mfma_f32_16x16x32_bf16 v[24:27], a[160:163], v[184:187], v[24:27]\n" \
"buffer_load_dwordx4 a[100:103], v233, s[64:67], s22 offen offset:1024 sc1\n" \
"v_mfma_f32_16x16x32_bf16 v[28:31], a[164:167], v[184:187], v[28:31]\n" \
"ds_read_b64_tr_b16 a[132:133], v225 offset:49152\n" \
"v_mfma_f32_16x16x32_bf16 v[20:23], a[164:167], v[176:179], v[20:23]\n" \
"buffer_load_dwordx4 a[104:107], v234, s[64:67], s22 offen offset:0 sc1\n" \
"v_mfma_f32_16x16x32_bf16 v[12:15], a[164:167], v[168:171], v[12:15]\n" \
"ds_read_b64_tr_b16 a[134:135], v225 offset:51200\n" \
"v_mfma_f32_16x16x32_bf16 v[4:7], a[164:167], v[160:163], v[4:7]\n" \
"ds_read_b64_tr_b16 a[136:137], v226 offset:49152\n" \
"v_mfma_f32_16x16x32_bf16 v[32:35], a[168:171], v[160:163], v[32:35]\n" \
"buffer_load_dwordx4 a[108:111], v234, s[64:67], s22 offen offset:1024 sc1\n" \
"v_mfma_f32_16x16x32_bf16 v[40:43], a[168:171], v[168:171], v[40:43]\n" \
"ds_read_b64_tr_b16 a[138:139], v226 offset:51200\n" \
"v_mfma_f32_16x16x32_bf16 v[48:51], a[168:171], v[176:179], v[48:51]\n" \
"buffer_load_dwordx4 a[112:115], v233, s[64:67], s23 offen offset:0 sc1\n" \
"v_mfma_f32_16x16x32_bf16 v[56:59], a[168:171], v[184:187], v[56:59]\n" \
"v_readlane_b32 s20, v241, 8\n" \
"ds_read_b64_tr_b16 a[140:141], v227 offset:49152\n" \
"v_mfma_f32_16x16x32_bf16 v[60:63], a[172:175], v[184:187], v[60:63]\n" \
"ds_read_b64_tr_b16 a[142:143], v227 offset:51200\n" \
"v_mfma_f32_16x16x32_bf16 v[52:55], a[172:175], v[176:179], v[52:55]\n" \
"buffer_load_dwordx4 a[116:119], v233, s[64:67], s23 offen offset:1024 sc1\n" \
"v_mfma_f32_16x16x32_bf16 v[44:47], a[172:175], v[168:171], v[44:47]\n" \
"ds_read_b64_tr_b16 a[144:145], v224 offset:53248\n" \
"v_mfma_f32_16x16x32_bf16 v[36:39], a[172:175], v[160:163], v[36:39]\n" \
"buffer_load_dwordx4 a[120:123], v234, s[64:67], s23 offen offset:0 sc1\n" \
"v_mfma_f32_16x16x32_bf16 v[64:67], a[176:179], v[160:163], v[64:67]\n" \
"ds_read_b64_tr_b16 a[146:147], v224 offset:55296\n" \
"v_mfma_f32_16x16x32_bf16 v[72:75], a[176:179], v[168:171], v[72:75]\n" \
"buffer_load_dwordx4 a[124:127], v234, s[64:67], s23 offen offset:1024 sc1\n" \
"v_mfma_f32_16x16x32_bf16 v[80:83], a[176:179], v[176:179], v[80:83]\n" \
"ds_read_b64_tr_b16 a[148:149], v225 offset:53248\n" \
"v_mfma_f32_16x16x32_bf16 v[88:91], a[176:179], v[184:187], v[88:91]\n" \
"ds_read_b64_tr_b16 a[150:151], v225 offset:55296\n" \
"v_mfma_f32_16x16x32_bf16 v[92:95], a[180:183], v[184:187], v[92:95]\n" \
"buffer_store_dwordx4 a[48:51], v232, s[64:67], s43 offen offset:0 sc1\n" \
"v_mfma_f32_16x16x32_bf16 v[84:87], a[180:183], v[176:179], v[84:87]\n" \
"ds_read_b64_tr_b16 a[152:153], v226 offset:53248\n" \
"v_mfma_f32_16x16x32_bf16 v[76:79], a[180:183], v[168:171], v[76:79]\n" \
"buffer_store_dwordx4 a[52:55], v232, s[64:67], s43 offen offset:1024 sc1\n" \
"v_mfma_f32_16x16x32_bf16 v[68:71], a[180:183], v[160:163], v[68:71]\n" \
"ds_read_b64_tr_b16 a[154:155], v226 offset:55296\n" \
"v_mfma_f32_16x16x32_bf16 v[96:99], a[184:187], v[160:163], v[96:99]\n" \
"ds_read_b64_tr_b16 a[156:157], v227 offset:53248\n" \
"v_mfma_f32_16x16x32_bf16 v[104:107], a[184:187], v[168:171], v[104:107]\n" \
"buffer_store_dwordx4 a[56:59], v232, s[64:67], s43 offen offset:2048 sc1\n" \
"v_mfma_f32_16x16x32_bf16 v[112:115], a[184:187], v[176:179], v[112:115]\n" \
"ds_read_b64_tr_b16 a[158:159], v227 offset:55296\n" \
"v_mfma_f32_16x16x32_bf16 v[120:123], a[184:187], v[184:187], v[120:123]\n" \
"buffer_store_dwordx4 a[60:63], v232, s[64:67], s43 offen offset:3072 sc1\n" \
"v_mfma_f32_16x16x32_bf16 v[124:127], a[188:191], v[184:187], v[124:127]\n" \
"v_mfma_f32_16x16x32_bf16 v[116:119], a[188:191], v[176:179], v[116:119]\n" \
"v_mfma_f32_16x16x32_bf16 v[108:111], a[188:191], v[168:171], v[108:111]\n" \
"v_mfma_f32_16x16x32_bf16 v[100:103], a[188:191], v[160:163], v[100:103]\n" \
"s_waitcnt vmcnt(8) lgkmcnt(0)\n" \
"v_mfma_f32_16x16x32_bf16 v[0:3], a[128:131], a[96:99], v[0:3]\n" \
"ds_read_b64_tr_b16 a[160:161], v224 offset:57344\n" \
"v_mfma_f32_16x16x32_bf16 v[8:11], a[128:131], a[100:103], v[8:11]\n" \
"buffer_load_dwordx4 a[0:3], v233, s[64:67], s20 offen offset:0 sc1\n" \
"v_mfma_f32_16x16x32_bf16 v[16:19], a[128:131], a[104:107], v[16:19]\n" \
"v_readlane_b32 s21, v241, 9\n" \
"ds_read_b64_tr_b16 a[162:163], v224 offset:59392\n" \
"v_mfma_f32_16x16x32_bf16 v[24:27], a[128:131], a[108:111], v[24:27]\n" \
"ds_read_b64_tr_b16 a[164:165], v225 offset:57344\n" \
"v_mfma_f32_16x16x32_bf16 v[28:31], a[132:135], a[108:111], v[28:31]\n" \
"buffer_load_dwordx4 a[4:7], v233, s[64:67], s20 offen offset:1024 sc1\n" \
"v_mfma_f32_16x16x32_bf16 v[20:23], a[132:135], a[104:107], v[20:23]\n" \
"ds_read_b64_tr_b16 a[166:167], v225 offset:59392\n" \
"v_mfma_f32_16x16x32_bf16 v[12:15], a[132:135], a[100:103], v[12:15]\n" \
"ds_read_b64_tr_b16 a[168:169], v226 offset:57344\n" \
"v_mfma_f32_16x16x32_bf16 v[4:7], a[132:135], a[96:99], v[4:7]\n" \
"buffer_load_dwordx4 a[8:11], v234, s[64:67], s20 offen offset:0 sc1\n" \
"v_mfma_f32_16x16x32_bf16 v[32:35], a[136:139], a[96:99], v[32:35]\n" \
"ds_read_b64_tr_b16 a[170:171], v226 offset:59392\n" \
"v_mfma_f32_16x16x32_bf16 v[40:43], a[136:139], a[100:103], v[40:43]\n" \
"ds_read_b64_tr_b16 a[172:173], v227 offset:57344\n" \
"v_mfma_f32_16x16x32_bf16 v[48:51], a[136:139], a[104:107], v[48:51]\n" \
"buffer_load_dwordx4 a[12:15], v234, s[64:67], s20 offen offset:1024 sc1\n" \
"v_mfma_f32_16x16x32_bf16 v[56:59], a[136:139], a[108:111], v[56:59]\n" \
"ds_read_b64_tr_b16 a[174:175], v227 offset:59392\n" \
"v_mfma_f32_16x16x32_bf16 v[60:63], a[140:143], a[108:111], v[60:63]\n" \
"v_mfma_f32_16x16x32_bf16 v[52:55], a[140:143], a[104:107], v[52:55]\n" \
"buffer_store_dwordx4 v[128:131], v232, s[64:67], s44 offen offset:0 sc1\n" \
"v_mfma_f32_16x16x32_bf16 v[44:47], a[140:143], a[100:103], v[44:47]\n" \
"ds_read_b64_tr_b16 a[176:177], v224 offset:61440\n" \
"v_mfma_f32_16x16x32_bf16 v[36:39], a[140:143], a[96:99], v[36:39]\n" \
"ds_read_b64_tr_b16 a[178:179], v224 offset:63488\n" \
"v_mfma_f32_16x16x32_bf16 v[64:67], a[144:147], a[96:99], v[64:67]\n" \
"ds_read_b64_tr_b16 a[180:181], v225 offset:61440\n" \
"v_mfma_f32_16x16x32_bf16 v[72:75], a[144:147], a[100:103], v[72:75]\n" \
"buffer_store_dwordx4 v[136:139], v232, s[64:67], s44 offen offset:1024 sc1\n" \
"v_mfma_f32_16x16x32_bf16 v[80:83], a[144:147], a[104:107], v[80:83]\n" \
"ds_read_b64_tr_b16 a[182:183], v225 offset:63488\n" \
"v_mfma_f32_16x16x32_bf16 v[88:91], a[144:147], a[108:111], v[88:91]\n" \
"ds_read_b64_tr_b16 a[184:185], v226 offset:61440\n" \
"v_mfma_f32_16x16x32_bf16 v[92:95], a[148:151], a[108:111], v[92:95]\n" \
"buffer_store_dwordx4 v[144:147], v232, s[64:67], s44 offen offset:2048 sc1\n" \
"v_mfma_f32_16x16x32_bf16 v[84:87], a[148:151], a[104:107], v[84:87]\n" \
"ds_read_b64_tr_b16 a[186:187], v226 offset:63488\n" \
"v_mfma_f32_16x16x32_bf16 v[76:79], a[148:151], a[100:103], v[76:79]\n" \
"ds_read_b64_tr_b16 a[188:189], v227 offset:61440\n" \
"v_mfma_f32_16x16x32_bf16 v[68:71], a[148:151], a[96:99], v[68:71]\n" \
"buffer_store_dwordx4 v[152:155], v232, s[64:67], s44 offen offset:3072 sc1\n" \
"v_mfma_f32_16x16x32_bf16 v[96:99], a[152:155], a[96:99], v[96:99]\n" \
"ds_read_b64_tr_b16 a[190:191], v227 offset:63488\n" \
"v_mfma_f32_16x16x32_bf16 v[104:107], a[152:155], a[100:103], v[104:107]\n" \
"v_mfma_f32_16x16x32_bf16 v[112:115], a[152:155], a[104:107], v[112:115]\n" \
"s_mov_b32 exec_lo, 0x10001\n" \
"s_mov_b32 exec_hi, 0x10001\n" \
"buffer_store_dword v148, v156, s[64:67], s57 offen sc1\n" \
"s_mov_b64 exec, -1\n" \
"v_mfma_f32_16x16x32_bf16 v[120:123], a[152:155], a[108:111], v[120:123]\n" \
"v_mfma_f32_16x16x32_bf16 v[124:127], a[156:159], a[108:111], v[124:127]\n" \
"v_mfma_f32_16x16x32_bf16 v[116:119], a[156:159], a[104:107], v[116:119]\n" \
"v_mfma_f32_16x16x32_bf16 v[108:111], a[156:159], a[100:103], v[108:111]\n" \
"v_mfma_f32_16x16x32_bf16 v[100:103], a[156:159], a[96:99], v[100:103]\n" \
"s_waitcnt vmcnt(13) lgkmcnt(0)\n" \
"s_barrier\n" \
"v_mfma_f32_16x16x32_bf16 v[0:3], a[160:163], a[112:115], v[0:3]\n" \
"ds_read_b64_tr_b16 a[128:129], v237 offset:0\n" \
"v_mfma_f32_16x16x32_bf16 v[8:11], a[160:163], a[116:119], v[8:11]\n" \
"buffer_load_dwordx4 a[16:19], v233, s[64:67], s21 offen offset:0 sc1\n" \
"v_mfma_f32_16x16x32_bf16 v[16:19], a[160:163], a[120:123], v[16:19]\n" \
"v_readlane_b32 s22, v241, 10\n" \
"ds_read_b64_tr_b16 a[130:131], v237 offset:2048\n" \
"v_mfma_f32_16x16x32_bf16 v[24:27], a[160:163], a[124:127], v[24:27]\n" \
"ds_read_b64_tr_b16 a[132:133], v238 offset:0\n" \
"v_mfma_f32_16x16x32_bf16 v[28:31], a[164:167], a[124:127], v[28:31]\n" \
"buffer_load_dwordx4 a[20:23], v233, s[64:67], s21 offen offset:1024 sc1\n" \
"v_mfma_f32_16x16x32_bf16 v[20:23], a[164:167], a[120:123], v[20:23]\n" \
"ds_read_b64_tr_b16 a[134:135], v238 offset:2048\n" \
"v_mfma_f32_16x16x32_bf16 v[12:15], a[164:167], a[116:119], v[12:15]\n" \
"ds_read_b64_tr_b16 a[136:137], v239 offset:0\n" \
"v_mfma_f32_16x16x32_bf16 v[4:7], a[164:167], a[112:115], v[4:7]\n" \
"ds_read_b64_tr_b16 a[138:139], v239 offset:2048\n" \
"v_mfma_f32_16x16x32_bf16 v[32:35], a[168:171], a[112:115], v[32:35]\n" \
"buffer_load_dwordx4 a[24:27], v234, s[64:67], s21 offen offset:0 sc1\n" \
"v_mfma_f32_16x16x32_bf16 v[40:43], a[168:171], a[116:119], v[40:43]\n" \
"v_mfma_f32_16x16x32_bf16 v[48:51], a[168:171], a[120:123], v[48:51]\n" \
"ds_read_b64_tr_b16 a[140:141], v240 offset:0\n" \
"v_mfma_f32_16x16x32_bf16 v[56:59], a[168:171], a[124:127], v[56:59]\n" \
"buffer_load_dwordx4 a[28:31], v234, s[64:67], s21 offen offset:1024 sc1\n" \
"v_mfma_f32_16x16x32_bf16 v[60:63], a[172:175], a[124:127], v[60:63]\n" \
"ds_read_b64_tr_b16 a[142:143], v240 offset:2048\n" \
"v_mfma_f32_16x16x32_bf16 v[52:55], a[172:175], a[120:123], v[52:55]\n" \
"ds_read_b64_tr_b16 a[144:145], v237 offset:4096\n" \
"v_mfma_f32_16x16x32_bf16 v[44:47], a[172:175], a[116:119], v[44:47]\n" \
"ds_read_b64_tr_b16 a[146:147], v237 offset:6144\n" \
"v_mfma_f32_16x16x32_bf16 v[36:39], a[172:175], a[112:115], v[36:39]\n" \
"buffer_store_dwordx4 v[160:163], v232, s[64:67], s45 offen offset:0 sc1\n" \
"v_mfma_f32_16x16x32_bf16 v[64:67], a[176:179], a[112:115], v[64:67]\n" \
"ds_read_b64_tr_b16 a[148:149], v238 offset:4096\n" \
"v_mfma_f32_16x16x32_bf16 v[72:75], a[176:179], a[116:119], v[72:75]\n" \
"v_mfma_f32_16x16x32_bf16 v[80:83], a[176:179], a[120:123], v[80:83]\n" \
"ds_read_b64_tr_b16 a[150:151], v238 offset:6144\n" \
"v_mfma_f32_16x16x32_bf16 v[88:91], a[176:179], a[124:127], v[88:91]\n" \
"buffer_store_dwordx4 v[168:171], v232, s[64:67], s45 offen offset:1024 sc1\n" \
"v_mfma_f32_16x16x32_bf16 v[92:95], a[180:183], a[124:127], v[92:95]\n" \
"ds_read_b64_tr_b16 a[152:153], v239 offset:4096\n" \
"v_mfma_f32_16x16x32_bf16 v[84:87], a[180:183], a[120:123], v[84:87]\n" \
"ds_read_b64_tr_b16 a[154:155], v239 offset:6144\n" \
"v_mfma_f32_16x16x32_bf16 v[76:79], a[180:183], a[116:119], v[76:79]\n" \
"buffer_store_dwordx4 v[176:179], v232, s[64:67], s45 offen offset:2048 sc1\n" \
"v_mfma_f32_16x16x32_bf16 v[68:71], a[180:183], a[112:115], v[68:71]\n" \
"ds_read_b64_tr_b16 a[156:157], v240 offset:4096\n" \
"v_mfma_f32_16x16x32_bf16 v[96:99], a[184:187], a[112:115], v[96:99]\n" \
"ds_read_b64_tr_b16 a[158:159], v240 offset:6144\n" \
"v_mfma_f32_16x16x32_bf16 v[104:107], a[184:187], a[116:119], v[104:107]\n" \
"v_mfma_f32_16x16x32_bf16 v[112:115], a[184:187], a[120:123], v[112:115]\n" \
"buffer_store_dwordx4 v[184:187], v232, s[64:67], s45 offen offset:3072 sc1\n" \
"v_mfma_f32_16x16x32_bf16 v[120:123], a[184:187], a[124:127], v[120:123]\n" \
"v_mfma_f32_16x16x32_bf16 v[124:127], a[188:191], a[124:127], v[124:127]\n" \
"v_mfma_f32_16x16x32_bf16 v[116:119], a[188:191], a[120:123], v[116:119]\n" \
"v_mfma_f32_16x16x32_bf16 v[108:111], a[188:191], a[116:119], v[108:111]\n" \
"v_mfma_f32_16x16x32_bf16 v[100:103], a[188:191], a[112:115], v[100:103]\n" \
"s_waitcnt vmcnt(13) lgkmcnt(0)\n" \
"v_mfma_f32_16x16x32_bf16 v[0:3], a[128:131], a[0:3], v[0:3]\n" \
"ds_read_b64_tr_b16 a[160:161], v237 offset:8192\n" \
"v_mfma_f32_16x16x32_bf16 v[8:11], a[128:131], a[4:7], v[8:11]\n" \
"buffer_load_dwordx2 v[250:251], v253, s[64:67], s84 offen sc1\n" \
"v_mfma_f32_16x16x32_bf16 v[16:19], a[128:131], a[8:11], v[16:19]\n" \
"ds_read_b64_tr_b16 a[162:163], v237 offset:10240\n" \
"v_mfma_f32_16x16x32_bf16 v[24:27], a[128:131], a[12:15], v[24:27]\n" \
"ds_read_b64_tr_b16 a[164:165], v238 offset:8192\n" \
"v_mfma_f32_16x16x32_bf16 v[28:31], a[132:135], a[12:15], v[28:31]\n" \
"v_mfma_f32_16x16x32_bf16 v[20:23], a[132:135], a[8:11], v[20:23]\n" \
"ds_read_b64_tr_b16 a[166:167], v238 offset:10240\n" \
"v_mfma_f32_16x16x32_bf16 v[12:15], a[132:135], a[4:7], v[12:15]\n" \
"buffer_load_dwordx4 a[64:67], v233, s[64:67], s22 offen offset:0 sc1\n" \
"v_mfma_f32_16x16x32_bf16 v[4:7], a[132:135], a[0:3], v[4:7]\n" \
"v_readlane_b32 s23, v241, 11\n" \
"ds_read_b64_tr_b16 a[168:169], v239 offset:8192\n" \
"v_mfma_f32_16x16x32_bf16 v[32:35], a[136:139], a[0:3], v[32:35]\n" \
"ds_read_b64_tr_b16 a[170:171], v239 offset:10240\n" \
"v_mfma_f32_16x16x32_bf16 v[40:43], a[136:139], a[4:7], v[40:43]\n" \
"v_mfma_f32_16x16x32_bf16 v[48:51], a[136:139], a[8:11], v[48:51]\n" \
"ds_read_b64_tr_b16 a[172:173], v240 offset:8192\n" \
"v_mfma_f32_16x16x32_bf16 v[56:59], a[136:139], a[12:15], v[56:59]\n" \
"ds_read_b64_tr_b16 a[174:175], v240 offset:10240\n" \
"v_mfma_f32_16x16x32_bf16 v[60:63], a[140:143], a[12:15], v[60:63]\n" \
"buffer_load_dwordx4 a[68:71], v233, s[64:67], s22 offen offset:1024 sc1\n" \
"v_mfma_f32_16x16x32_bf16 v[52:55], a[140:143], a[8:11], v[52:55]\n" \
"v_mfma_f32_16x16x32_bf16 v[44:47], a[140:143], a[4:7], v[44:47]\n" \
"ds_read_b64_tr_b16 a[176:177], v237 offset:12288\n" \
"v_mfma_f32_16x16x32_bf16 v[36:39], a[140:143], a[0:3], v[36:39]\n" \
"ds_read_b64_tr_b16 a[178:179], v237 offset:14336\n" \
"v_mfma_f32_16x16x32_bf16 v[64:67], a[144:147], a[0:3], v[64:67]\n" \
"ds_read_b64_tr_b16 a[180:181], v238 offset:12288\n" \
"v_mfma_f32_16x16x32_bf16 v[72:75], a[144:147], a[4:7], v[72:75]\n" \
"v_mfma_f32_16x16x32_bf16 v[80:83], a[144:147], a[8:11], v[80:83]\n" \
"buffer_load_dwordx4 a[72:75], v234, s[64:67], s22 offen offset:0 sc1\n" \
"v_mfma_f32_16x16x32_bf16 v[88:91], a[144:147], a[12:15], v[88:91]\n" \
"ds_read_b64_tr_b16 a[182:183], v238 offset:14336\n" \
"v_mfma_f32_16x16x32_bf16 v[92:95], a[148:151], a[12:15], v[92:95]\n" \
"ds_read_b64_tr_b16 a[184:185], v239 offset:12288\n" \
"v_mfma_f32_16x16x32_bf16 v[84:87], a[148:151], a[8:11], v[84:87]\n" \
"ds_read_b64_tr_b16 a[186:187], v239 offset:14336\n" \
"v_mfma_f32_16x16x32_bf16 v[76:79], a[148:151], a[4:7], v[76:79]\n" \
"v_mfma_f32_16x16x32_bf16 v[68:71], a[148:151], a[0:3], v[68:71]\n" \
"ds_read_b64_tr_b16 a[188:189], v240 offset:12288\n" \
"v_mfma_f32_16x16x32_bf16 v[96:99], a[152:155], a[0:3], v[96:99]\n" \
"buffer_load_dwordx4 a[76:79], v234, s[64:67], s22 offen offset:1024 sc1\n" \
"v_mfma_f32_16x16x32_bf16 v[104:107], a[152:155], a[4:7], v[104:107]\n" \
"ds_read_b64_tr_b16 a[190:191], v240 offset:14336\n" \
"v_mfma_f32_16x16x32_bf16 v[112:115], a[152:155], a[8:11], v[112:115]\n" \
"v_mfma_f32_16x16x32_bf16 v[120:123], a[152:155], a[12:15], v[120:123]\n" \
"v_mfma_f32_16x16x32_bf16 v[124:127], a[156:159], a[12:15], v[124:127]\n" \
"v_mfma_f32_16x16x32_bf16 v[116:119], a[156:159], a[8:11], v[116:119]\n" \
"v_mfma_f32_16x16x32_bf16 v[108:111], a[156:159], a[4:7], v[108:111]\n" \
"v_mfma_f32_16x16x32_bf16 v[100:103], a[156:159], a[0:3], v[100:103]\n" \
"s_waitcnt vmcnt(9) lgkmcnt(0)\n" \
"v_mfma_f32_16x16x32_bf16 v[0:3], a[160:163], a[16:19], v[0:3]\n" \
"ds_read_b64_tr_b16 a[128:129], v237 offset:16384\n" \
"v_mfma_f32_16x16x32_bf16 v[8:11], a[160:163], a[20:23], v[8:11]\n" \
"buffer_load_dwordx4 a[80:83], v233, s[64:67], s23 offen offset:0 sc1\n" \
"v_mfma_f32_16x16x32_bf16 v[16:19], a[160:163], a[24:27], v[16:19]\n" \
"v_readlane_b32 s20, v241, 12\n" \
"ds_read_b64_tr_b16 a[130:131], v237 offset:18432\n" \
"v_mfma_f32_16x16x32_bf16 v[24:27], a[160:163], a[28:31], v[24:27]\n" \
"ds_read_b64_tr_b16 a[132:133], v238 offset:16384\n" \
"v_mfma_f32_16x16x32_bf16 v[28:31], a[164:167], a[28:31], v[28:31]\n" \
"ds_read_b64_tr_b16 a[134:135], v238 offset:18432\n" \
"v_mfma_f32_16x16x32_bf16 v[20:23], a[164:167], a[24:27], v[20:23]\n" \
"buffer_load_dwordx4 a[84:87], v233, s[64:67], s23 offen offset:1024 sc1\n" \
"v_mfma_f32_16x16x32_bf16 v[12:15], a[164:167], a[20:23], v[12:15]\n" \
"v_mfma_f32_16x16x32_bf16 v[4:7], a[164:167], a[16:19], v[4:7]\n" \
"ds_read_b64_tr_b16 a[136:137], v239 offset:16384\n" \
"v_mfma_f32_16x16x32_bf16 v[32:35], a[168:171], a[16:19], v[32:35]\n" \
"ds_read_b64_tr_b16 a[138:139], v239 offset:18432\n" \
"v_mfma_f32_16x16x32_bf16 v[40:43], a[168:171], a[20:23], v[40:43]\n" \
"ds_read_b64_tr_b16 a[140:141], v240 offset:16384\n" \
"v_mfma_f32_16x16x32_bf16 v[48:51], a[168:171], a[24:27], v[48:51]\n" \
"buffer_load_dwordx4 a[88:91], v234, s[64:67], s23 offen offset:0 sc1\n" \
"v_mfma_f32_16x16x32_bf16 v[56:59], a[168:171], a[28:31], v[56:59]\n" \
"v_mfma_f32_16x16x32_bf16 v[60:63], a[172:175], a[28:31], v[60:63]\n" \
"ds_read_b64_tr_b16 a[142:143], v240 offset:18432\n" \
"v_mfma_f32_16x16x32_bf16 v[52:55], a[172:175], a[24:27], v[52:55]\n" \
"ds_read_b64_tr_b16 a[144:145], v237 offset:20480\n" \
"v_mfma_f32_16x16x32_bf16 v[44:47], a[172:175], a[20:23], v[44:47]\n" \
"buffer_load_dwordx4 a[92:95], v234, s[64:67], s23 offen offset:1024 sc1\n" \
"v_mfma_f32_16x16x32_bf16 v[36:39], a[172:175], a[16:19], v[36:39]\n" \
"ds_read_b64_tr_b16 a[146:147], v237 offset:22528\n" \
"v_mfma_f32_16x16x32_bf16 v[64:67], a[176:179], a[16:19], v[64:67]\n" \
"v_mfma_f32_16x16x32_bf16 v[72:75], a[176:179], a[20:23], v[72:75]\n" \
"ds_read_b64_tr_b16 a[148:149], v238 offset:20480\n" \
"v_mfma_f32_16x16x32_bf16 v[80:83], a[176:179], a[24:27], v[80:83]\n" \
"s_add_u32 m0, s80, 65536\n" \
"ds_read_b64_tr_b16 a[150:151], v238 offset:22528\n" \
"v_mfma_f32_16x16x32_bf16 v[88:91], a[176:179], a[28:31], v[88:91]\n" \
"buffer_load_dwordx4 v235, s[68:71], s24 offen lds\n" \
"v_mfma_f32_16x16x32_bf16 v[92:95], a[180:183], a[28:31], v[92:95]\n" \
"v_readlane_b32 s25, v254, 21\n" \
"ds_read_b64_tr_b16 a[152:153], v239 offset:20480\n" \
"v_mfma_f32_16x16x32_bf16 v[84:87], a[180:183], a[24:27], v[84:87]\n" \
"v_mfma_f32_16x16x32_bf16 v[76:79], a[180:183], a[20:23], v[76:79]\n" \
"s_add_u32 m0, s80, 69632\n" \
"ds_read_b64_tr_b16 a[154:155], v239 offset:22528\n" \
"v_mfma_f32_16x16x32_bf16 v[68:71], a[180:183], a[16:19], v[68:71]\n" \
"buffer_load_dwordx4 v236, s[68:71], s24 offen lds\n" \
"v_mfma_f32_16x16x32_bf16 v[96:99], a[184:187], a[16:19], v[96:99]\n" \
"ds_read_b64_tr_b16 a[156:157], v240 offset:20480\n" \
"v_mfma_f32_16x16x32_bf16 v[104:107], a[184:187], a[20:23], v[104:107]\n" \
"ds_read_b64_tr_b16 a[158:159], v240 offset:22528\n" \
"v_mfma_f32_16x16x32_bf16 v[112:115], a[184:187], a[24:27], v[112:115]\n" \
"v_mfma_f32_16x16x32_bf16 v[120:123], a[184:187], a[28:31], v[120:123]\n" \
"v_mfma_f32_16x16x32_bf16 v[124:127], a[188:191], a[28:31], v[124:127]\n" \
"s_waitcnt vmcnt(10)\n" \
"v_mfma_f32_16x16x32_bf16 v[116:119], a[188:191], a[24:27], v[116:119]\n" \
"v_cmp_ne_u64 vcc, s[72:73], v[250:251]\n" \
"v_mfma_f32_16x16x32_bf16 v[108:111], a[188:191], a[20:23], v[108:111]\n" \
"s_cbranch_vccnz Lsl2_%=\n" \
"Lrt2_%=:\n" \
"v_mfma_f32_16x16x32_bf16 v[100:103], a[188:191], a[16:19], v[100:103]\n" \
"v_accvgpr_read_b32 v253, a238\n" \
"s_waitcnt vmcnt(6) lgkmcnt(0)\n" \
"v_mfma_f32_16x16x32_bf16 v[0:3], a[128:131], a[64:67], v[0:3]\n" \
"ds_read_b64_tr_b16 a[160:161], v237 offset:24576\n" \
"v_mfma_f32_16x16x32_bf16 v[8:11], a[128:131], a[68:71], v[8:11]\n" \
"buffer_load_dwordx4 a[32:35], v233, s[64:67], s20 offen offset:0 sc1\n" \
"v_mfma_f32_16x16x32_bf16 v[16:19], a[128:131], a[72:75], v[16:19]\n" \
"v_readlane_b32 s21, v241, 13\n" \
"ds_read_b64_tr_b16 a[162:163], v237 offset:26624\n" \
"v_mfma_f32_16x16x32_bf16 v[24:27], a[128:131], a[76:79], v[24:27]\n" \
"ds_read_b64_tr_b16 a[164:165], v238 offset:24576\n" \
"v_mfma_f32_16x16x32_bf16 v[28:31], a[132:135], a[76:79], v[28:31]\n" \
"buffer_load_dwordx4 a[36:39], v233, s[64:67], s20 offen offset:1024 sc1\n" \
"v_mfma_f32_16x16x32_bf16 v[20:23], a[132:135], a[72:75], v[20:23]\n" \
"ds_read_b64_tr_b16 a[166:167], v238 offset:26624\n" \
"v_mfma_f32_16x16x32_bf16 v[12:15], a[132:135], a[68:71], v[12:15]\n" \
"ds_read_b64_tr_b16 a[168:169], v239 offset:24576\n" \
"v_mfma_f32_16x16x32_bf16 v[4:7], a[132:135], a[64:67], v[4:7]\n" \
"buffer_load_dwordx4 a[40:43], v234, s[64:67], s20 offen offset:0 sc1\n" \
"v_mfma_f32_16x16x32_bf16 v[32:35], a[136:139], a[64:67], v[32:35]\n" \
"ds_read_b64_tr_b16 a[170:171], v239 offset:26624\n" \
"v_mfma_f32_16x16x32_bf16 v[40:43], a[136:139], a[68:71], v[40:43]\n" \
"v_mfma_f32_16x16x32_bf16 v[48:51], a[136:139], a[72:75], v[48:51]\n" \
"ds_read_b64_tr_b16 a[172:173], v240 offset:24576\n" \
"v_mfma_f32_16x16x32_bf16 v[56:59], a[136:139], a[76:79], v[56:59]\n" \
"buffer_load_dwordx4 a[44:47], v234, s[64:67], s20 offen offset:1024 sc1\n" \
"v_mfma_f32_16x16x32_bf16 v[60:63], a[140:143], a[76:79], v[60:63]\n" \
"ds_read_b64_tr_b16 a[174:175], v240 offset:26624\n" \
"v_mfma_f32_16x16x32_bf16 v[52:55], a[140:143], a[72:75], v[52:55]\n" \
"ds_read_b64_tr_b16 a[176:177], v237 offset:28672\n" \
"v_mfma_f32_16x16x32_bf16 v[44:47], a[140:143], a[68:71], v[44:47]\n" \
"buffer_load_dwordx4 a[48:51], v233, s[64:67], s21 offen offset:0 sc1\n" \
"v_mfma_f32_16x16x32_bf16 v[36:39], a[140:143], a[64:67], v[36:39]\n" \
"v_readlane_b32 s22, v241, 14\n" \
"ds_read_b64_tr_b16 a[178:179], v237 offset:30720\n" \
"v_mfma_f32_16x16x32_bf16 v[64:67], a[144:147], a[64:67], v[64:67]\n" \
"ds_read_b64_tr_b16 a[180:181], v238 offset:28672\n" \
"v_mfma_f32_16x16x32_bf16 v[72:75], a[144:147], a[68:71], v[72:75]\n" \
"buffer_load_dwordx4 a[52:55], v233, s[64:67], s21 offen offset:1024 sc1\n" \
"v_mfma_f32_16x16x32_bf16 v[80:83], a[144:147], a[72:75], v[80:83]\n" \
"v_mfma_f32_16x16x32_bf16 v[88:91], a[144:147], a[76:79], v[88:91]\n" \
"ds_read_b64_tr_b16 a[182:183], v238 offset:30720\n" \
"v_mfma_f32_16x16x32_bf16 v[92:95], a[148:151], a[76:79], v[92:95]\n" \
"ds_read_b64_tr_b16 a[184:185], v239 offset:28672\n" \
"v_mfma_f32_16x16x32_bf16 v[84:87], a[148:151], a[72:75], v[84:87]\n" \
"buffer_load_dwordx4 a[56:59], v234, s[64:67], s21 offen offset:0 sc1\n" \
"v_mfma_f32_16x16x32_bf16 v[76:79], a[148:151], a[68:71], v[76:79]\n" \
"ds_read_b64_tr_b16 a[186:187], v239 offset:30720\n" \
"v_mfma_f32_16x16x32_bf16 v[68:71], a[148:151], a[64:67], v[68:71]\n" \
"ds_read_b64_tr_b16 a[188:189], v240 offset:28672\n" \
"v_mfma_f32_16x16x32_bf16 v[96:99], a[152:155], a[64:67], v[96:99]\n" \
"buffer_load_dwordx4 a[60:63], v234, s[64:67], s21 offen offset:1024 sc1\n" \
"v_mfma_f32_16x16x32_bf16 v[104:107], a[152:155], a[68:71], v[104:107]\n" \
"ds_read_b64_tr_b16 a[190:191], v240 offset:30720\n" \
"v_mfma_f32_16x16x32_bf16 v[112:115], a[152:155], a[72:75], v[112:115]\n" \
"v_mfma_f32_16x16x32_bf16 v[120:123], a[152:155], a[76:79], v[120:123]\n" \
"v_mfma_f32_16x16x32_bf16 v[124:127], a[156:159], a[76:79], v[124:127]\n" \
"s_lshl_b32 s60, s92, 7\n" \
"s_add_u32 s60, s60, s84\n" \
"v_mov_b32 v246, s72\n" \
"v_mfma_f32_16x16x32_bf16 v[116:119], a[156:159], a[72:75], v[116:119]\n" \
"v_mov_b32 v247, s73\n" \
"v_mfma_f32_16x16x32_bf16 v[108:111], a[156:159], a[68:71], v[108:111]\n" \
"s_waitcnt vmcnt(19)\n" \
"v_mfma_f32_16x16x32_bf16 v[100:103], a[156:159], a[64:67], v[100:103]\n" \
"s_mov_b64 exec, 1\n" \
"buffer_store_dwordx2 v[246:247], off, s[64:67], s60 sc1\n" \
"s_mov_b64 exec, -1\n" \
"s_waitcnt vmcnt(11) lgkmcnt(0)\n" \
"s_barrier\n" \
"v_mfma_f32_16x16x32_bf16 v[0:3], a[160:163], a[80:83], v[0:3]\n" \
"ds_read_b64_tr_b16 a[128:129], v237 offset:32768\n" \
"v_mfma_f32_16x16x32_bf16 v[8:11], a[160:163], a[84:87], v[8:11]\n" \
"buffer_load_dwordx4 v[192:195], v233, s[64:67], s22 offen offset:0 sc1\n" \
"v_mfma_f32_16x16x32_bf16 v[16:19], a[160:163], a[88:91], v[16:19]\n" \
"v_readlane_b32 s23, v241, 15\n" \
"ds_read_b64_tr_b16 a[130:131], v237 offset:34816\n" \
"v_mfma_f32_16x16x32_bf16 v[24:27], a[160:163], a[92:95], v[24:27]\n" \
"ds_read_b64_tr_b16 a[132:133], v238 offset:32768\n" \
"v_mfma_f32_16x16x32_bf16 v[28:31], a[164:167], a[92:95], v[28:31]\n" \
"buffer_load_dwordx4 v[196:199], v233, s[64:67], s22 offen offset:1024 sc1\n" \
"v_mfma_f32_16x16x32_bf16 v[20:23], a[164:167], a[88:91], v[20:23]\n" \
"ds_read_b64_tr_b16 a[134:135], v238 offset:34816\n" \
"v_mfma_f32_16x16x32_bf16 v[12:15], a[164:167], a[84:87], v[12:15]\n" \
"ds_read_b64_tr_b16 a[136:137], v239 offset:32768\n" \
"v_mfma_f32_16x16x32_bf16 v[4:7], a[164:167], a[80:83], v[4:7]\n" \
"ds_read_b64_tr_b16 a[138:139], v239 offset:34816\n" \
"v_mfma_f32_16x16x32_bf16 v[32:35], a[168:171], a[80:83], v[32:35]\n" \
"buffer_load_dwordx4 v[200:203], v234, s[64:67], s22 offen offset:0 sc1\n" \
"v_mfma_f32_16x16x32_bf16 v[40:43], a[168:171], a[84:87], v[40:43]\n" \
"v_mfma_f32_16x16x32_bf16 v[48:51], a[168:171], a[88:91], v[48:51]\n" \
"ds_read_b64_tr_b16 a[140:141], v240 offset:32768\n" \
"v_mfma_f32_16x16x32_bf16 v[56:59], a[168:171], a[92:95], v[56:59]\n" \
"buffer_load_dwordx4 v[204:207], v234, s[64:67], s22 offen offset:1024 sc1\n" \
"v_mfma_f32_16x16x32_bf16 v[60:63], a[172:175], a[92:95], v[60:63]\n" \
"ds_read_b64_tr_b16 a[142:143], v240 offset:34816\n" \
"v_mfma_f32_16x16x32_bf16 v[52:55], a[172:175], a[88:91], v[52:55]\n" \
"ds_read_b64_tr_b16 a[144:145], v237 offset:36864\n" \
"v_mfma_f32_16x16x32_bf16 v[44:47], a[172:175], a[84:87], v[44:47]\n" \
"ds_read_b64_tr_b16 a[146:147], v237 offset:38912\n" \
"v_mfma_f32_16x16x32_bf16 v[36:39], a[172:175], a[80:83], v[36:39]\n" \
"buffer_load_dwordx4 v[208:211], v233, s[64:67], s23 offen offset:0 sc1\n" \
"v_mfma_f32_16x16x32_bf16 v[64:67], a[176:179], a[80:83], v[64:67]\n" \
"v_readlane_b32 s20, v241, 16\n" \
"ds_read_b64_tr_b16 a[148:149], v238 offset:36864\n" \
"v_mfma_f32_16x16x32_bf16 v[72:75], a[176:179], a[84:87], v[72:75]\n" \
"v_mfma_f32_16x16x32_bf16 v[80:83], a[176:179], a[88:91], v[80:83]\n" \
"ds_read_b64_tr_b16 a[150:151], v238 offset:38912\n" \
"v_mfma_f32_16x16x32_bf16 v[88:91], a[176:179], a[92:95], v[88:91]\n" \
"Lpadh0_%=:\n" \
"s_cbranch_vccnz Lpadfh_%=\n" \
"buffer_load_dwordx4 v[212:215], v233, s[64:67], s23 offen offset:1024 sc1\n" \
"v_mfma_f32_16x16x32_bf16 v[92:95], a[180:183], a[92:95], v[92:95]\n" \
"ds_read_b64_tr_b16 a[152:153], v239 offset:36864\n" \
"v_mfma_f32_16x16x32_bf16 v[84:87], a[180:183], a[88:91], v[84:87]\n" \
"ds_read_b64_tr_b16 a[154:155], v239 offset:38912\n" \
"v_mfma_f32_16x16x32_bf16 v[76:79], a[180:183], a[84:87], v[76:79]\n" \
"buffer_load_dwordx4 v[216:219], v234, s[64:67], s23 offen offset:0 sc1\n" \
"v_mfma_f32_16x16x32_bf16 v[68:71], a[180:183], a[80:83], v[68:71]\n" \
"ds_read_b64_tr_b16 a[156:157], v240 offset:36864\n" \
"v_mfma_f32_16x16x32_bf16 v[96:99], a[184:187], a[80:83], v[96:99]\n" \
"ds_read_b64_tr_b16 a[158:159], v240 offset:38912\n" \
"v_mfma_f32_16x16x32_bf16 v[104:107], a[184:187], a[84:87], v[104:107]\n" \
"v_mfma_f32_16x16x32_bf16 v[112:115], a[184:187], a[88:91], v[112:115]\n" \
"buffer_load_dwordx4 v[220:223], v234, s[64:67], s23 offen offset:1024 sc1\n" \
"v_mfma_f32_16x16x32_bf16 v[120:123], a[184:187], a[92:95], v[120:123]\n" \
"v_mfma_f32_16x16x32_bf16 v[124:127], a[188:191], a[92:95], v[124:127]\n" \
"v_mfma_f32_16x16x32_bf16 v[116:119], a[188:191], a[88:91], v[116:119]\n" \
"v_mfma_f32_16x16x32_bf16 v[108:111], a[188:191], a[84:87], v[108:111]\n" \
"v_mfma_f32_16x16x32_bf16 v[100:103], a[188:191], a[80:83], v[100:103]\n" \
"s_waitcnt vmcnt(13) lgkmcnt(0)\n" \
"v_mfma_f32_16x16x32_bf16 v[0:3], a[128:131], a[32:35], v[0:3]\n" \
"ds_read_b64_tr_b16 a[160:161], v237 offset:40960\n" \
"v_mfma_f32_16x16x32_bf16 v[8:11], a[128:131], a[36:39], v[8:11]\n" \
"buffer_load_dwordx4 v[160:163], v233, s[64:67], s20 offen offset:0 sc1\n" \
"v_mfma_f32_16x16x32_bf16 v[16:19], a[128:131], a[40:43], v[16:19]\n" \
"v_readlane_b32 s21, v241, 17\n" \
"ds_read_b64_tr_b16 a[162:163], v237 offset:43008\n" \
"v_mfma_f32_16x16x32_bf16 v[24:27], a[128:131], a[44:47], v[24:27]\n" \
"ds_read_b64_tr_b16 a[164:165], v238 offset:40960\n" \
"v_mfma_f32_16x16x32_bf16 v[28:31], a[132:135], a[44:47], v[28:31]\n" \
"buffer_load_dwordx4 v[164:167], v233, s[64:67], s20 offen offset:1024 sc1\n" \
"v_mfma_f32_16x16x32_bf16 v[20:23], a[132:135], a[40:43], v[20:23]\n" \
"ds_read_b64_tr_b16 a[166:167], v238 offset:43008\n" \
"v_mfma_f32_16x16x32_bf16 v[12:15], a[132:135], a[36:39], v[12:15]\n" \
"ds_read_b64_tr_b16 a[168:169], v239 offset:40960\n" \
"v_mfma_f32_16x16x32_bf16 v[4:7], a[132:135], a[32:35], v[4:7]\n" \
"ds_read_b64_tr_b16 a[170:171], v239 offset:43008\n" \
"v_mfma_f32_16x16x32_bf16 v[32:35], a[136:139], a[32:35], v[32:35]\n" \
"buffer_load_dwordx4 v[168:171], v234, s[64:67], s20 offen offset:0 sc1\n" \
"v_mfma_f32_16x16x32_bf16 v[40:43], a[136:139], a[36:39], v[40:43]\n" \
"v_mfma_f32_16x16x32_bf16 v[48:51], a[136:139], a[40:43], v[48:51]\n" \
"ds_read_b64_tr_b16 a[172:173], v240 offset:40960\n" \
"v_mfma_f32_16x16x32_bf16 v[56:59], a[136:139], a[44:47], v[56:59]\n" \
"buffer_load_dwordx4 v[172:175], v234, s[64:67], s20 offen offset:1024 sc1\n" \
"v_mfma_f32_16x16x32_bf16 v[60:63], a[140:143], a[44:47], v[60:63]\n" \
"ds_read_b64_tr_b16 a[174:175], v240 offset:43008\n" \
"v_mfma_f32_16x16x32_bf16 v[52:55], a[140:143], a[40:43], v[52:55]\n" \
"ds_read_b64_tr_b16 a[176:177], v237 offset:45056\n" \
"v_mfma_f32_16x16x32_bf16 v[44:47], a[140:143], a[36:39], v[44:47]\n" \
"ds_read_b64_tr_b16 a[178:179], v237 offset:47104\n" \
"v_mfma_f32_16x16x32_bf16 v[36:39], a[140:143], a[32:35], v[36:39]\n" \
"buffer_load_dwordx4 v[176:179], v233, s[64:67], s21 offen offset:0 sc1\n" \
"v_mfma_f32_16x16x32_bf16 v[64:67], a[144:147], a[32:35], v[64:67]\n" \
"v_readlane_b32 s22, v241, 18\n" \
"ds_read_b64_tr_b16 a[180:181], v238 offset:45056\n" \
"v_mfma_f32_16x16x32_bf16 v[72:75], a[144:147], a[36:39], v[72:75]\n" \
"v_mfma_f32_16x16x32_bf16 v[80:83], a[144:147], a[40:43], v[80:83]\n" \
"ds_read_b64_tr_b16 a[182:183], v238 offset:47104\n" \
"v_mfma_f32_16x16x32_bf16 v[88:91], a[144:147], a[44:47], v[88:91]\n" \
"buffer_load_dwordx4 v[180:183], v233, s[64:67], s21 offen offset:1024 sc1\n" \
"v_mfma_f32_16x16x32_bf16 v[92:95], a[148:151], a[44:47], v[92:95]\n" \
"ds_read_b64_tr_b16 a[184:185], v239 offset:45056\n" \
"v_mfma_f32_16x16x32_bf16 v[84:87], a[148:151], a[40:43], v[84:87]\n" \
"ds_read_b64_tr_b16 a[186:187], v239 offset:47104\n" \
"v_mfma_f32_16x16x32_bf16 v[76:79], a[148:151], a[36:39], v[76:79]\n" \
"buffer_load_dwordx4 v[184:187], v234, s[64:67], s21 offen offset:0 sc1\n" \
"v_mfma_f32_16x16x32_bf16 v[68:71], a[148:151], a[32:35], v[68:71]\n" \
"ds_read_b64_tr_b16 a[188:189], v240 offset:45056\n" \
"v_mfma_f32_16x16x32_bf16 v[96:99], a[152:155], a[32:35], v[96:99]\n" \
"ds_read_b64_tr_b16 a[190:191], v240 offset:47104\n" \
"v_mfma_f32_16x16x32_bf16 v[104:107], a[152:155], a[36:39], v[104:107]\n" \
"v_mfma_f32_16x16x32_bf16 v[112:115], a[152:155], a[40:43], v[112:115]\n" \
"buffer_load_dwordx4 v[188:191], v234, s[64:67], s21 offen offset:1024 sc1\n" \
"v_mfma_f32_16x16x32_bf16 v[120:123], a[152:155], a[44:47], v[120:123]\n" \
"v_mfma_f32_16x16x32_bf16 v[124:127], a[156:159], a[44:47], v[124:127]\n" \
"v_mfma_f32_16x16x32_bf16 v[116:119], a[156:159], a[40:43], v[116:119]\n" \
"v_mfma_f32_16x16x32_bf16 v[108:111], a[156:159], a[36:39], v[108:111]\n" \
"v_mfma_f32_16x16x32_bf16 v[100:103], a[156:159], a[32:35], v[100:103]\n" \
"s_waitcnt vmcnt(17) lgkmcnt(0)\n" \
"v_mfma_f32_16x16x32_bf16 v[0:3], a[160:163], a[48:51], v[0:3]\n" \
"ds_read_b64_tr_b16 a[128:129], v237 offset:49152\n" \
"v_mfma_f32_16x16x32_bf16 v[8:11], a[160:163], a[52:55], v[8:11]\n" \
"buffer_load_dwordx2 v[250:251], v253, s[64:67], s84 offen sc1\n" \
"v_mfma_f32_16x16x32_bf16 v[16:19], a[160:163], a[56:59], v[16:19]\n" \
"ds_read_b64_tr_b16 a[130:131], v237 offset:51200\n" \
"v_mfma_f32_16x16x32_bf16 v[24:27], a[160:163], a[60:63], v[24:27]\n" \
"s_add_u32 m0, s80, 73728\n" \
"ds_read_b64_tr_b16 a[132:133], v238 offset:49152\n" \
"v_mfma_f32_16x16x32_bf16 v[28:31], a[164:167], a[60:63], v[28:31]\n" \
"buffer_load_dwordx4 v235, s[68:71], s25 offen lds\n" \
"v_mfma_f32_16x16x32_bf16 v[20:23], a[164:167], a[56:59], v[20:23]\n" \
"v_readlane_b32 s26, v254, 22\n" \
"ds_read_b64_tr_b16 a[134:135], v238 offset:51200\n" \
"v_mfma_f32_16x16x32_bf16 v[12:15], a[164:167], a[52:55], v[12:15]\n" \
"ds_read_b64_tr_b16 a[136:137], v239 offset:49152\n" \
"v_mfma_f32_16x16x32_bf16 v[4:7], a[164:167], a[48:51], v[4:7]\n" \
"s_add_u32 m0, s80, 77824\n" \
"ds_read_b64_tr_b16 a[138:139], v239 offset:51200\n" \
"v_mfma_f32_16x16x32_bf16 v[32:35], a[168:171], a[48:51], v[32:35]\n" \
"buffer_load_dwordx4 v236, s[68:71], s25 offen lds\n" \
"v_mfma_f32_16x16x32_bf16 v[40:43], a[168:171], a[52:55], v[40:43]\n" \
"v_mfma_f32_16x16x32_bf16 v[48:51], a[168:171], a[56:59], v[48:51]\n" \
"s_add_u32 m0, s80, 81920\n" \
"ds_read_b64_tr_b16 a[140:141], v240 offset:49152\n" \
"v_mfma_f32_16x16x32_bf16 v[56:59], a[168:171], a[60:63], v[56:59]\n" \
"buffer_load_dwordx4 v235, s[68:71], s26 offen lds\n" \
"v_mfma_f32_16x16x32_bf16 v[60:63], a[172:175], a[60:63], v[60:63]\n" \
"v_readlane_b32 s27, v254, 23\n" \
"ds_read_b64_tr_b16 a[142:143], v240 offset:51200\n" \
"v_mfma_f32_16x16x32_bf16 v[52:55], a[172:175], a[56:59], v[52:55]\n" \
"ds_read_b64_tr_b16 a[144:145], v237 offset:53248\n" \
"v_mfma_f32_16x16x32_bf16 v[44:47], a[172:175], a[52:55], v[44:47]\n" \
"s_add_u32 m0, s80, 86016\n" \
"ds_read_b64_tr_b16 a[146:147], v237 offset:55296\n" \
"v_mfma_f32_16x16x32_bf16 v[36:39], a[172:175], a[48:51], v[36:39]\n" \
"buffer_load_dwordx4 v236, s[68:71], s26 offen lds\n" \
"v_mfma_f32_16x16x32_bf16 v[64:67], a[176:179], a[48:51], v[64:67]\n" \
"ds_read_b64_tr_b16 a[148:149], v238 offset:53248\n" \
"v_mfma_f32_16x16x32_bf16 v[72:75], a[176:179], a[52:55], v[72:75]\n" \
"v_mfma_f32_16x16x32_bf16 v[80:83], a[176:179], a[56:59], v[80:83]\n" \
"s_add_u32 m0, s80, 90112\n" \
"ds_read_b64_tr_b16 a[150:151], v238 offset:55296\n" \
"v_mfma_f32_16x16x32_bf16 v[88:91], a[176:179], a[60:63], v[88:91]\n" \
"buffer_load_dwordx4 v235, s[68:71], s27 offen lds\n" \
"v_mfma_f32_16x16x32_bf16 v[92:95], a[180:183], a[60:63], v[92:95]\n" \
"v_readlane_b32 s24, v254, 24\n" \
"ds_read_b64_tr_b16 a[152:153], v239 offset:53248\n" \
"v_mfma_f32_16x16x32_bf16 v[84:87], a[180:183], a[56:59], v[84:87]\n" \
"s_add_u32 m0, s80, 94208\n" \
"ds_read_b64_tr_b16 a[154:155], v239 offset:55296\n" \
"v_mfma_f32_16x16x32_bf16 v[76:79], a[180:183], a[52:55], v[76:79]\n" \
"buffer_load_dwordx4 v236, s[68:71], s27 offen lds\n" \
"v_mfma_f32_16x16x32_bf16 v[68:71], a[180:183], a[48:51], v[68:71]\n" \
"ds_read_b64_tr_b16 a[156:157], v240 offset:53248\n" \
"v_mfma_f32_16x16x32_bf16 v[96:99], a[184:187], a[48:51], v[96:99]\n" \
"ds_read_b64_tr_b16 a[158:159], v240 offset:55296\n" \
"v_mfma_f32_16x16x32_bf16 v[104:107], a[184:187], a[52:55], v[104:107]\n" \
"s_add_u32 m0, s80, 98304\n" \
"v_mfma_f32_16x16x32_bf16 v[112:115], a[184:187], a[56:59], v[112:115]\n" \
"buffer_load_dwordx4 v235, s[68:71], s24 offen lds\n" \
"v_mfma_f32_16x16x32_bf16 v[120:123], a[184:187], a[60:63], v[120:123]\n" \
"v_readlane_b32 s25, v254, 25\n" \
"v_mfma_f32_16x16x32_bf16 v[124:127], a[188:191], a[60:63], v[124:127]\n" \
"v_mfma_f32_16x16x32_bf16 v[116:119], a[188:191], a[56:59], v[116:119]\n" \
"v_mfma_f32_16x16x32_bf16 v[108:111], a[188:191], a[52:55], v[108:111]\n" \
"v_mfma_f32_16x16x32_bf16 v[100:103], a[188:191], a[48:51], v[100:103]\n" \
"s_waitcnt vmcnt(20) lgkmcnt(0)\n" \
"v_mfma_f32_16x16x32_bf16 v[0:3], a[128:131], v[192:195], v[0:3]\n" \
"s_add_u32 m0, s80, 102400\n" \
"ds_read_b64_tr_b16 a[160:161], v237 offset:57344\n" \
"v_mfma_f32_16x16x32_bf16 v[8:11], a[128:131], v[196:199], v[8:11]\n" \
"buffer_load_dwordx4 v236, s[68:71], s24 offen lds\n" \
"v_mfma_f32_16x16x32_bf16 v[16:19], a[128:131], v[200:203], v[16:19]\n" \
"ds_read_b64_tr_b16 a[162:163], v237 offset:59392\n" \
"v_mfma_f32_16x16x32_bf16 v[24:27], a[128:131], v[204:207], v[24:27]\n" \
"s_add_u32 m0, s80, 106496\n" \
"ds_read_b64_tr_b16 a[164:165], v238 offset:57344\n" \
"v_mfma_f32_16x16x32_bf16 v[28:31], a[132:135], v[204:207], v[28:31]\n" \
"buffer_load_dwordx4 v235, s[68:71], s25 offen lds\n" \
"v_mfma_f32_16x16x32_bf16 v[20:23], a[132:135], v[200:203], v[20:23]\n" \
"v_readlane_b32 s26, v254, 26\n" \
"ds_read_b64_tr_b16 a[166:167], v238 offset:59392\n" \
"v_mfma_f32_16x16x32_bf16 v[12:15], a[132:135], v[196:199], v[12:15]\n" \
"s_add_u32 m0, s80, 110592\n" \
"ds_read_b64_tr_b16 a[168:169], v239 offset:57344\n" \
"v_mfma_f32_16x16x32_bf16 v[4:7], a[132:135], v[192:195], v[4:7]\n" \
"buffer_load_dwordx4 v236, s[68:71], s25 offen lds\n" \
"v_mfma_f32_16x16x32_bf16 v[32:35], a[136:139], v[192:195], v[32:35]\n" \
"ds_read_b64_tr_b16 a[170:171], v239 offset:59392\n" \
"v_mfma_f32_16x16x32_bf16 v[40:43], a[136:139], v[196:199], v[40:43]\n" \
"v_mfma_f32_16x16x32_bf16 v[48:51], a[136:139], v[200:203], v[48:51]\n" \
"s_add_u32 m0, s80, 114688\n" \
"ds_read_b64_tr_b16 a[172:173], v240 offset:57344\n" \
"v_mfma_f32_16x16x32_bf16 v[56:59], a[136:139], v[204:207], v[56:59]\n" \
"buffer_load_dwordx4 v235, s[68:71], s26 offen lds\n" \
"v_mfma_f32_16x16x32_bf16 v[60:63], a[140:143], v[204:207], v[60:63]\n" \
"v_readlane_b32 s27, v254, 27\n" \
"ds_read_b64_tr_b16 a[174:175], v240 offset:59392\n" \
"v_mfma_f32_16x16x32_bf16 v[52:55], a[140:143], v[200:203], v[52:55]\n" \
"s_add_u32 m0, s80, 118784\n" \
"ds_read_b64_tr_b16 a[176:177], v237 offset:61440\n" \
"v_mfma_f32_16x16x32_bf16 v[44:47], a[140:143], v[196:199], v[44:47]\n" \
"buffer_load_dwordx4 v236, s[68:71], s26 offen lds\n" \
"v_mfma_f32_16x16x32_bf16 v[36:39], a[140:143], v[192:195], v[36:39]\n" \
"ds_read_b64_tr_b16 a[178:179], v237 offset:63488\n" \
"v_mfma_f32_16x16x32_bf16 v[64:67], a[144:147], v[192:195], v[64:67]\n" \
"s_add_u32 m0, s80, 122880\n" \
"ds_read_b64_tr_b16 a[180:181], v238 offset:61440\n" \
"v_mfma_f32_16x16x32_bf16 v[72:75], a[144:147], v[196:199], v[72:75]\n" \
"buffer_load_dwordx4 v235, s[68:71], s27 offen lds\n" \
"v_mfma_f32_16x16x32_bf16 v[80:83], a[144:147], v[200:203], v[80:83]\n" \
"v_readlane_b32 s24, v254, 28\n" \
"v_mfma_f32_16x16x32_bf16 v[88:91], a[144:147], v[204:207], v[88:91]\n" \
"ds_read_b64_tr_b16 a[182:183], v238 offset:63488\n" \
"v_mfma_f32_16x16x32_bf16 v[92:95], a[148:151], v[204:207], v[92:95]\n" \
"s_add_u32 m0, s80, 126976\n" \
"ds_read_b64_tr_b16 a[184:185], v239 offset:61440\n" \
"v_mfma_f32_16x16x32_bf16 v[84:87], a[148:151], v[200:203], v[84:87]\n" \
"buffer_load_dwordx4 v236, s[68:71], s27 offen lds\n" \
"v_mfma_f32_16x16x32_bf16 v[76:79], a[148:151], v[196:199], v[76:79]\n" \
"ds_read_b64_tr_b16 a[186:187], v239 offset:63488\n" \
"v_mfma_f32_16x16x32_bf16 v[68:71], a[148:151], v[192:195], v[68:71]\n" \
"s_add_u32 m0, s80, 0\n" \
"ds_read_b64_tr_b16 a[188:189], v240 offset:61440\n" \
"v_mfma_f32_16x16x32_bf16 v[96:99], a[152:155], v[192:195], v[96:99]\n" \
"buffer_load_dwordx4 v235, s[68:71], s24 offen lds\n" \
"v_mfma_f32_16x16x32_bf16 v[104:107], a[152:155], v[196:199], v[104:107]\n" \
"v_readlane_b32 s25, v254, 29\n" \
"ds_read_b64_tr_b16 a[190:191], v240 offset:63488\n" \
"v_mfma_f32_16x16x32_bf16 v[112:115], a[152:155], v[200:203], v[112:115]\n" \
"v_mfma_f32_16x16x32_bf16 v[120:123], a[152:155], v[204:207], v[120:123]\n" \
"v_mfma_f32_16x16x32_bf16 v[124:127], a[156:159], v[204:207], v[124:127]\n" \
"s_waitcnt vmcnt(15)\n" \
"v_mfma_f32_16x16x32_bf16 v[116:119], a[156:159], v[200:203], v[116:119]\n" \
"v_cmp_ne_u64 vcc, s[72:73], v[250:251]\n" \
"v_mfma_f32_16x16x32_bf16 v[108:111], a[156:159], v[196:199], v[108:111]\n" \
"s_cbranch_vccnz Lsl3_%=\n" \
"Lrt3_%=:\n" \
"v_mfma_f32_16x16x32_bf16 v[100:103], a[156:159], v[192:195], v[100:103]\n" \
"v_accvgpr_read_b32 v253, a239\n" \
"s_waitcnt vmcnt(24) lgkmcnt(0)\n" \
"s_barrier\n" \
"v_mfma_f32_16x16x32_bf16 v[0:3], a[160:163], v[208:211], v[0:3]\n" \
"ds_read_b64_tr_b16 a[128:129], v228 offset:0\n" \
"v_mfma_f32_16x16x32_bf16 v[8:11], a[160:163], v[212:215], v[8:11]\n" \
"buffer_load_dwordx4 a[96:99], v233, s[64:67], s22 offen offset:0 sc1\n" \
"v_mfma_f32_16x16x32_bf16 v[16:19], a[160:163], v[216:219], v[16:19]\n" \
"v_readlane_b32 s23, v241, 19\n" \
"ds_read_b64_tr_b16 a[130:131], v228 offset:2048\n" \
"v_mfma_f32_16x16x32_bf16 v[24:27], a[160:163], v[220:223], v[24:27]\n" \
"ds_read_b64_tr_b16 a[132:133], v229 offset:0\n" \
"v_mfma_f32_16x16x32_bf16 v[28:31], a[164:167], v[220:223], v[28:31]\n" \
"buffer_load_dwordx4 a[100:103], v233, s[64:67], s22 offen offset:1024 sc1\n" \
"v_mfma_f32_16x16x32_bf16 v[20:23], a[164:167], v[216:219], v[20:23]\n" \
"ds_read_b64_tr_b16 a[134:135], v229 offset:2048\n" \
"v_mfma_f32_16x16x32_bf16 v[12:15], a[164:167], v[212:215], v[12:15]\n" \
"ds_read_b64_tr_b16 a[136:137], v230 offset:0\n" \
"v_mfma_f32_16x16x32_bf16 v[4:7], a[164:167], v[208:211], v[4:7]\n" \
"ds_read_b64_tr_b16 a[138:139], v230 offset:2048\n" \
"v_mfma_f32_16x16x32_bf16 v[32:35], a[168:171], v[208:211], v[32:35]\n" \
"buffer_load_dwordx4 a[104:107], v234, s[64:67], s22 offen offset:0 sc1\n" \
"v_mfma_f32_16x16x32_bf16 v[40:43], a[168:171], v[212:215], v[40:43]\n" \
"v_mfma_f32_16x16x32_bf16 v[48:51], a[168:171], v[216:219], v[48:51]\n" \
"ds_read_b64_tr_b16 a[140:141], v231 offset:0\n" \
"v_mfma_f32_16x16x32_bf16 v[56:59], a[168:171], v[220:223], v[56:59]\n" \
"buffer_load_dwordx4 a[108:111], v234, s[64:67], s22 offen offset:1024 sc1\n" \
"v_mfma_f32_16x16x32_bf16 v[60:63], a[172:175], v[220:223], v[60:63]\n" \
"ds_read_b64_tr_b16 a[142:143], v231 offset:2048\n" \
"v_mfma_f32_16x16x32_bf16 v[52:55], a[172:175], v[216:219], v[52:55]\n" \
"ds_read_b64_tr_b16 a[144:145], v228 offset:4096\n" \
"v_mfma_f32_16x16x32_bf16 v[44:47], a[172:175], v[212:215], v[44:47]\n" \
"ds_read_b64_tr_b16 a[146:147], v228 offset:6144\n" \
"v_mfma_f32_16x16x32_bf16 v[36:39], a[172:175], v[208:211], v[36:39]\n" \
"buffer_load_dwordx4 a[112:115], v233, s[64:67], s23 offen offset:0 sc1\n" \
"v_mfma_f32_16x16x32_bf16 v[64:67], a[176:179], v[208:211], v[64:67]\n" \
"v_readlane_b32 s20, v241, 20\n" \
"ds_read_b64_tr_b16 a[148:149], v229 offset:4096\n" \
"v_mfma_f32_16x16x32_bf16 v[72:75], a[176:179], v[212:215], v[72:75]\n" \
"v_mfma_f32_16x16x32_bf16 v[80:83], a[176:179], v[216:219], v[80:83]\n" \
"ds_read_b64_tr_b16 a[150:151], v229 offset:6144\n" \
"v_mfma_f32_16x16x32_bf16 v[88:91], a[176:179], v[220:223], v[88:91]\n" \
"buffer_load_dwordx4 a[116:119], v233, s[64:67], s23 offen offset:1024 sc1\n" \
"v_mfma_f32_16x16x32_bf16 v[92:95], a[180:183], v[220:223], v[92:95]\n" \
"ds_read_b64_tr_b16 a[152:153], v230 offset:4096\n" \
"v_mfma_f32_16x16x32_bf16 v[84:87], a[180:183], v[216:219], v[84:87]\n" \
"ds_read_b64_tr_b16 a[154:155], v230 offset:6144\n" \
"v_mfma_f32_16x16x32_bf16 v[76:79], a[180:183], v[212:215], v[76:79]\n" \
"buffer_load_dwordx4 a[120:123], v234, s[64:67], s23 offen offset:0 sc1\n" \
"v_mfma_f32_16x16x32_bf16 v[68:71], a[180:183], v[208:211], v[68:71]\n" \
"ds_read_b64_tr_b16 a[156:157], v231 offset:4096\n" \
"v_mfma_f32_16x16x32_bf16 v[96:99], a[184:187], v[208:211], v[96:99]\n" \
"ds_read_b64_tr_b16 a[158:159], v231 offset:6144\n" \
"v_mfma_f32_16x16x32_bf16 v[104:107], a[184:187], v[212:215], v[104:107]\n" \
"v_mfma_f32_16x16x32_bf16 v[112:115], a[184:187], v[216:219], v[112:115]\n" \
"buffer_load_dwordx4 a[124:127], v234, s[64:67], s23 offen offset:1024 sc1\n" \
"v_mfma_f32_16x16x32_bf16 v[120:123], a[184:187], v[220:223], v[120:123]\n" \
"v_mfma_f32_16x16x32_bf16 v[124:127], a[188:191], v[220:223], v[124:127]\n" \
"v_mfma_f32_16x16x32_bf16 v[116:119], a[188:191], v[216:219], v[116:119]\n" \
"v_mfma_f32_16x16x32_bf16 v[108:111], a[188:191], v[212:215], v[108:111]\n" \
"v_mfma_f32_16x16x32_bf16 v[100:103], a[188:191], v[208:211], v[100:103]\n" \
"s_waitcnt vmcnt(28) lgkmcnt(0)\n" \
"v_mfma_f32_16x16x32_bf16 v[0:3], a[128:131], v[160:163], v[0:3]\n" \
"ds_read_b64_tr_b16 a[160:161], v228 offset:8192\n" \
"v_mfma_f32_16x16x32_bf16 v[8:11], a[128:131], v[164:167], v[8:11]\n" \
"buffer_load_dwordx4 a[0:3], v233, s[64:67], s20 offen offset:0 sc1\n" \
"v_mfma_f32_16x16x32_bf16 v[16:19], a[128:131], v[168:171], v[16:19]\n" \
"v_readlane_b32 s21, v241, 21\n" \
"ds_read_b64_tr_b16 a[162:163], v228 offset:10240\n" \
"v_mfma_f32_16x16x32_bf16 v[24:27], a[128:131], v[172:175], v[24:27]\n" \
"ds_read_b64_tr_b16 a[164:165], v229 offset:8192\n" \
"v_mfma_f32_16x16x32_bf16 v[28:31], a[132:135], v[172:175], v[28:31]\n" \
"buffer_load_dwordx4 a[4:7], v233, s[64:67], s20 offen offset:1024 sc1\n" \
"v_mfma_f32_16x16x32_bf16 v[20:23], a[132:135], v[168:171], v[20:23]\n" \
"ds_read_b64_tr_b16 a[166:167], v229 offset:10240\n" \
"v_mfma_f32_16x16x32_bf16 v[12:15], a[132:135], v[164:167], v[12:15]\n" \
"ds_read_b64_tr_b16 a[168:169], v230 offset:8192\n" \
"v_mfma_f32_16x16x32_bf16 v[4:7], a[132:135], v[160:163], v[4:7]\n" \
"ds_read_b64_tr_b16 a[170:171], v230 offset:10240\n" \
"v_mfma_f32_16x16x32_bf16 v[32:35], a[136:139], v[160:163], v[32:35]\n" \
"buffer_load_dwordx4 a[8:11], v234, s[64:67], s20 offen offset:0 sc1\n" \
"v_mfma_f32_16x16x32_bf16 v[40:43], a[136:139], v[164:167], v[40:43]\n" \
"v_mfma_f32_16x16x32_bf16 v[48:51], a[136:139], v[168:171], v[48:51]\n" \
"ds_read_b64_tr_b16 a[172:173], v231 offset:8192\n" \
"v_mfma_f32_16x16x32_bf16 v[56:59], a[136:139], v[172:175], v[56:59]\n" \
"buffer_load_dwordx4 a[12:15], v234, s[64:67], s20 offen offset:1024 sc1\n" \
"v_mfma_f32_16x16x32_bf16 v[60:63], a[140:143], v[172:175], v[60:63]\n" \
"ds_read_b64_tr_b16 a[174:175], v231 offset:10240\n" \
"v_mfma_f32_16x16x32_bf16 v[52:55], a[140:143], v[168:171], v[52:55]\n" \
"ds_read_b64_tr_b16 a[176:177], v228 offset:12288\n" \
"v_mfma_f32_16x16x32_bf16 v[44:47], a[140:143], v[164:167], v[44:47]\n" \
"ds_read_b64_tr_b16 a[178:179], v228 offset:14336\n" \
"v_mfma_f32_16x16x32_bf16 v[36:39], a[140:143], v[160:163], v[36:39]\n" \
"buffer_load_dwordx4 a[16:19], v233, s[64:67], s21 offen offset:0 sc1\n" \
"v_mfma_f32_16x16x32_bf16 v[64:67], a[144:147], v[160:163], v[64:67]\n" \
"v_readlane_b32 s22, v241, 22\n" \
"ds_read_b64_tr_b16 a[180:181], v229 offset:12288\n" \
"v_mfma_f32_16x16x32_bf16 v[72:75], a[144:147], v[164:167], v[72:75]\n" \
"v_mfma_f32_16x16x32_bf16 v[80:83], a[144:147], v[168:171], v[80:83]\n" \
"ds_read_b64_tr_b16 a[182:183], v229 offset:14336\n" \
"v_mfma_f32_16x16x32_bf16 v[88:91], a[144:147], v[172:175], v[88:91]\n" \
"buffer_load_dwordx4 a[20:23], v233, s[64:67], s21 offen offset:1024 sc1\n" \
"v_mfma_f32_16x16x32_bf16 v[92:95], a[148:151], v[172:175], v[92:95]\n" \
"ds_read_b64_tr_b16 a[184:185], v230 offset:12288\n" \
"v_mfma_f32_16x16x32_bf16 v[84:87], a[148:151], v[168:171], v[84:87]\n" \
"ds_read_b64_tr_b16 a[186:187], v230 offset:14336\n" \
"v_mfma_f32_16x16x32_bf16 v[76:79], a[148:151], v[164:167], v[76:79]\n" \
"buffer_load_dwordx4 a[24:27], v234, s[64:67], s21 offen offset:0 sc1\n" \
"v_mfma_f32_16x16x32_bf16 v[68:71], a[148:151], v[160:163], v[68:71]\n" \
"ds_read_b64_tr_b16 a[188:189], v231 offset:12288\n" \
"v_mfma_f32_16x16x32_bf16 v[96:99], a[152:155], v[160:163], v[96:99]\n" \
"ds_read_b64_tr_b16 a[190:191], v231 offset:14336\n" \
"v_mfma_f32_16x16x32_bf16 v[104:107], a[152:155], v[164:167], v[104:107]\n" \
"v_mfma_f32_16x16x32_bf16 v[112:115], a[152:155], v[168:171], v[112:115]\n" \
"buffer_load_dwordx4 a[28:31], v234, s[64:67], s21 offen offset:1024 sc1\n" \
"v_mfma_f32_16x16x32_bf16 v[120:123], a[152:155], v[172:175], v[120:123]\n" \
"v_mfma_f32_16x16x32_bf16 v[124:127], a[156:159], v[172:175], v[124:127]\n" \
"v_mfma_f32_16x16x32_bf16 v[116:119], a[156:159], v[168:171], v[116:119]\n" \
"v_mfma_f32_16x16x32_bf16 v[108:111], a[156:159], v[164:167], v[108:111]\n" \
"v_mfma_f32_16x16x32_bf16 v[100:103], a[156:159], v[160:163], v[100:103]\n" \
"s_waitcnt vmcnt(32) lgkmcnt(0)\n" \
"v_mfma_f32_16x16x32_bf16 v[0:3], a[160:163], v[176:179], v[0:3]\n" \
"ds_read_b64_tr_b16 a[128:129], v228 offset:16384\n" \
"v_mfma_f32_16x16x32_bf16 v[8:11], a[160:163], v[180:183], v[8:11]\n" \
"buffer_load_dwordx4 a[64:67], v233, s[64:67], s22 offen offset:0 sc1\n" \
"v_mfma_f32_16x16x32_bf16 v[16:19], a[160:163], v[184:187], v[16:19]\n" \
"v_readlane_b32 s23, v241, 23\n" \
"ds_read_b64_tr_b16 a[130:131], v228 offset:18432\n" \
"v_mfma_f32_16x16x32_bf16 v[24:27], a[160:163], v[188:191], v[24:27]\n" \
"ds_read_b64_tr_b16 a[132:133], v229 offset:16384\n" \
"v_mfma_f32_16x16x32_bf16 v[28:31], a[164:167], v[188:191], v[28:31]\n" \
"buffer_load_dwordx4 a[68:71], v233, s[64:67], s22 offen offset:1024 sc1\n" \
"v_mfma_f32_16x16x32_bf16 v[20:23], a[164:167], v[184:187], v[20:23]\n" \
"ds_read_b64_tr_b16 a[134:135], v229 offset:18432\n" \
"v_mfma_f32_16x16x32_bf16 v[12:15], a[164:167], v[180:183], v[12:15]\n" \
"ds_read_b64_tr_b16 a[136:137], v230 offset:16384\n" \
"v_mfma_f32_16x16x32_bf16 v[4:7], a[164:167], v[176:179], v[4:7]\n" \
"ds_read_b64_tr_b16 a[138:139], v230 offset:18432\n" \
"v_mfma_f32_16x16x32_bf16 v[32:35], a[168:171], v[176:179], v[32:35]\n" \
"buffer_load_dwordx4 a[72:75], v234, s[64:67], s22 offen offset:0 sc1\n" \
"v_mfma_f32_16x16x32_bf16 v[40:43], a[168:171], v[180:183], v[40:43]\n" \
"v_mfma_f32_16x16x32_bf16 v[48:51], a[168:171], v[184:187], v[48:51]\n" \
"ds_read_b64_tr_b16 a[140:141], v231 offset:16384\n" \
"v_mfma_f32_16x16x32_bf16 v[56:59], a[168:171], v[188:191], v[56:59]\n" \
"buffer_load_dwordx4 a[76:79], v234, s[64:67], s22 offen offset:1024 sc1\n" \
"v_mfma_f32_16x16x32_bf16 v[60:63], a[172:175], v[188:191], v[60:63]\n" \
"ds_read_b64_tr_b16 a[142:143], v231 offset:18432\n" \
"v_mfma_f32_16x16x32_bf16 v[52:55], a[172:175], v[184:187], v[52:55]\n" \
"ds_read_b64_tr_b16 a[144:145], v228 offset:20480\n" \
"v_mfma_f32_16x16x32_bf16 v[44:47], a[172:175], v[180:183], v[44:47]\n" \
"ds_read_b64_tr_b16 a[146:147], v228 offset:22528\n" \
"v_mfma_f32_16x16x32_bf16 v[36:39], a[172:175], v[176:179], v[36:39]\n" \
"buffer_load_dwordx4 a[80:83], v233, s[64:67], s23 offen offset:0 sc1\n" \
"v_mfma_f32_16x16x32_bf16 v[64:67], a[176:179], v[176:179], v[64:67]\n" \
"v_readlane_b32 s20, v241, 24\n" \
"ds_read_b64_tr_b16 a[148:149], v229 offset:20480\n" \
"v_mfma_f32_16x16x32_bf16 v[72:75], a[176:179], v[180:183], v[72:75]\n" \
"v_mfma_f32_16x16x32_bf16 v[80:83], a[176:179], v[184:187], v[80:83]\n" \
"ds_read_b64_tr_b16 a[150:151], v229 offset:22528\n" \
"v_mfma_f32_16x16x32_bf16 v[88:91], a[176:179], v[188:191], v[88:91]\n" \
"buffer_load_dwordx4 a[84:87], v233, s[64:67], s23 offen offset:1024 sc1\n" \
"v_mfma_f32_16x16x32_bf16 v[92:95], a[180:183], v[188:191], v[92:95]\n" \
"ds_read_b64_tr_b16 a[152:153], v230 offset:20480\n" \
"v_mfma_f32_16x16x32_bf16 v[84:87], a[180:183], v[184:187], v[84:87]\n" \
"ds_read_b64_tr_b16 a[154:155], v230 offset:22528\n" \
"v_mfma_f32_16x16x32_bf16 v[76:79], a[180:183], v[180:183], v[76:79]\n" \
"buffer_load_dwordx4 a[88:91], v234, s[64:67], s23 offen offset:0 sc1\n" \
"v_mfma_f32_16x16x32_bf16 v[68:71], a[180:183], v[176:179], v[68:71]\n" \
"ds_read_b64_tr_b16 a[156:157], v231 offset:20480\n" \
"v_mfma_f32_16x16x32_bf16 v[96:99], a[184:187], v[176:179], v[96:99]\n" \
"ds_read_b64_tr_b16 a[158:159], v231 offset:22528\n" \
"v_mfma_f32_16x16x32_bf16 v[104:107], a[184:187], v[180:183], v[104:107]\n" \
"v_mfma_f32_16x16x32_bf16 v[112:115], a[184:187], v[184:187], v[112:115]\n" \
"buffer_load_dwordx4 a[92:95], v234, s[64:67], s23 offen offset:1024 sc1\n" \
"v_mfma_f32_16x16x32_bf16 v[120:123], a[184:187], v[188:191], v[120:123]\n" \
"v_mfma_f32_16x16x32_bf16 v[124:127], a[188:191], v[188:191], v[124:127]\n" \
"v_mfma_f32_16x16x32_bf16 v[116:119], a[188:191], v[184:187], v[116:119]\n" \
"v_mfma_f32_16x16x32_bf16 v[108:111], a[188:191], v[180:183], v[108:111]\n" \
"v_mfma_f32_16x16x32_bf16 v[100:103], a[188:191], v[176:179], v[100:103]\n" \
"s_waitcnt vmcnt(20) lgkmcnt(0)\n" \
"v_mfma_f32_16x16x32_bf16 v[0:3], a[128:131], a[96:99], v[0:3]\n" \
"ds_read_b64_tr_b16 a[160:161], v228 offset:24576\n" \
"v_mfma_f32_16x16x32_bf16 v[8:11], a[128:131], a[100:103], v[8:11]\n" \
"buffer_load_dwordx2 v[250:251], v253, s[64:67], s84 offen sc1\n" \
"v_mfma_f32_16x16x32_bf16 v[16:19], a[128:131], a[104:107], v[16:19]\n" \
"ds_read_b64_tr_b16 a[162:163], v228 offset:26624\n" \
"v_mfma_f32_16x16x32_bf16 v[24:27], a[128:131], a[108:111], v[24:27]\n" \
"ds_read_b64_tr_b16 a[164:165], v229 offset:24576\n" \
"v_mfma_f32_16x16x32_bf16 v[28:31], a[132:135], a[108:111], v[28:31]\n" \
"buffer_load_dwordx4 a[32:35], v233, s[64:67], s20 offen offset:0 sc1\n" \
"v_mfma_f32_16x16x32_bf16 v[20:23], a[132:135], a[104:107], v[20:23]\n" \
"v_readlane_b32 s21, v241, 25\n" \
"ds_read_b64_tr_b16 a[166:167], v229 offset:26624\n" \
"v_mfma_f32_16x16x32_bf16 v[12:15], a[132:135], a[100:103], v[12:15]\n" \
"ds_read_b64_tr_b16 a[168:169], v230 offset:24576\n" \
"v_mfma_f32_16x16x32_bf16 v[4:7], a[132:135], a[96:99], v[4:7]\n" \
"ds_read_b64_tr_b16 a[170:171], v230 offset:26624\n" \
"v_mfma_f32_16x16x32_bf16 v[32:35], a[136:139], a[96:99], v[32:35]\n" \
"buffer_load_dwordx4 a[36:39], v233, s[64:67], s20 offen offset:1024 sc1\n" \
"v_mfma_f32_16x16x32_bf16 v[40:43], a[136:139], a[100:103], v[40:43]\n" \
"v_mfma_f32_16x16x32_bf16 v[48:51], a[136:139], a[104:107], v[48:51]\n" \
"ds_read_b64_tr_b16 a[172:173], v231 offset:24576\n" \
"v_mfma_f32_16x16x32_bf16 v[56:59], a[136:139], a[108:111], v[56:59]\n" \
"buffer_load_dwordx4 a[40:43], v234, s[64:67], s20 offen offset:0 sc1\n" \
"v_mfma_f32_16x16x32_bf16 v[60:63], a[140:143], a[108:111], v[60:63]\n" \
"ds_read_b64_tr_b16 a[174:175], v231 offset:26624\n" \
"v_mfma_f32_16x16x32_bf16 v[52:55], a[140:143], a[104:107], v[52:55]\n" \
"ds_read_b64_tr_b16 a[176:177], v228 offset:28672\n" \
"v_mfma_f32_16x16x32_bf16 v[44:47], a[140:143], a[100:103], v[44:47]\n" \
"ds_read_b64_tr_b16 a[178:179], v228 offset:30720\n" \
"v_mfma_f32_16x16x32_bf16 v[36:39], a[140:143], a[96:99], v[36:39]\n" \
"buffer_load_dwordx4 a[44:47], v234, s[64:67], s20 offen offset:1024 sc1\n" \
"v_mfma_f32_16x16x32_bf16 v[64:67], a[144:147], a[96:99], v[64:67]\n" \
"ds_read_b64_tr_b16 a[180:181], v229 offset:28672\n" \
"v_mfma_f32_16x16x32_bf16 v[72:75], a[144:147], a[100:103], v[72:75]\n" \
"v_mfma_f32_16x16x32_bf16 v[80:83], a[144:147], a[104:107], v[80:83]\n" \
"ds_read_b64_tr_b16 a[182:183], v229 offset:30720\n" \
"v_mfma_f32_16x16x32_bf16 v[88:91], a[144:147], a[108:111], v[88:91]\n" \
"buffer_load_dwordx4 a[48:51], v233, s[64:67], s21 offen offset:0 sc1\n" \
"v_mfma_f32_16x16x32_bf16 v[92:95], a[148:151], a[108:111], v[92:95]\n" \
"v_readlane_b32 s22, v241, 26\n" \
"ds_read_b64_tr_b16 a[184:185], v230 offset:28672\n" \
"v_mfma_f32_16x16x32_bf16 v[84:87], a[148:151], a[104:107], v[84:87]\n" \
"ds_read_b64_tr_b16 a[186:187], v230 offset:30720\n" \
"v_mfma_f32_16x16x32_bf16 v[76:79], a[148:151], a[100:103], v[76:79]\n" \
"buffer_load_dwordx4 a[52:55], v233, s[64:67], s21 offen offset:1024 sc1\n" \
"v_mfma_f32_16x16x32_bf16 v[68:71], a[148:151], a[96:99], v[68:71]\n" \
"ds_read_b64_tr_b16 a[188:189], v231 offset:28672\n" \
"v_mfma_f32_16x16x32_bf16 v[96:99], a[152:155], a[96:99], v[96:99]\n" \
"ds_read_b64_tr_b16 a[190:191], v231 offset:30720\n" \
"v_mfma_f32_16x16x32_bf16 v[104:107], a[152:155], a[100:103], v[104:107]\n" \
"v_mfma_f32_16x16x32_bf16 v[112:115], a[152:155], a[104:107], v[112:115]\n" \
"buffer_load_dwordx4 a[56:59], v234, s[64:67], s21 offen offset:0 sc1\n" \
"v_mfma_f32_16x16x32_bf16 v[120:123], a[152:155], a[108:111], v[120:123]\n" \
"v_mfma_f32_16x16x32_bf16 v[124:127], a[156:159], a[108:111], v[124:127]\n" \
"v_mfma_f32_16x16x32_bf16 v[116:119], a[156:159], a[104:107], v[116:119]\n" \
"v_mfma_f32_16x16x32_bf16 v[108:111], a[156:159], a[100:103], v[108:111]\n" \
"v_mfma_f32_16x16x32_bf16 v[100:103], a[156:159], a[96:99], v[100:103]\n" \
"s_waitcnt vmcnt(24) lgkmcnt(0)\n" \
"s_barrier\n" \
"v_mfma_f32_16x16x32_bf16 v[0:3], a[160:163], a[112:115], v[0:3]\n" \
"ds_read_b64_tr_b16 a[128:129], v224 offset:0\n" \
"v_mfma_f32_16x16x32_bf16 v[8:11], a[160:163], a[116:119], v[8:11]\n" \
"buffer_load_dwordx4 a[60:63], v234, s[64:67], s21 offen offset:1024 sc1\n" \
"v_mfma_f32_16x16x32_bf16 v[16:19], a[160:163], a[120:123], v[16:19]\n" \
"ds_read_b64_tr_b16 a[130:131], v224 offset:2048\n" \
"v_mfma_f32_16x16x32_bf16 v[24:27], a[160:163], a[124:127], v[24:27]\n" \
"s_add_u32 m0, s80, 4096\n" \
"ds_read_b64_tr_b16 a[132:133], v225 offset:0\n" \
"v_mfma_f32_16x16x32_bf16 v[28:31], a[164:167], a[124:127], v[28:31]\n" \
"buffer_load_dwordx4 v236, s[68:71], s24 offen lds\n" \
"v_mfma_f32_16x16x32_bf16 v[20:23], a[164:167], a[120:123], v[20:23]\n" \
"ds_read_b64_tr_b16 a[134:135], v225 offset:2048\n" \
"v_mfma_f32_16x16x32_bf16 v[12:15], a[164:167], a[116:119], v[12:15]\n" \
"s_add_u32 m0, s80, 8192\n" \
"ds_read_b64_tr_b16 a[136:137], v226 offset:0\n" \
"v_mfma_f32_16x16x32_bf16 v[4:7], a[164:167], a[112:115], v[4:7]\n" \
"buffer_load_dwordx4 v235, s[68:71], s25 offen lds\n" \
"v_mfma_f32_16x16x32_bf16 v[32:35], a[168:171], a[112:115], v[32:35]\n" \
"v_readlane_b32 s26, v254, 30\n" \
"ds_read_b64_tr_b16 a[138:139], v226 offset:2048\n" \
"v_mfma_f32_16x16x32_bf16 v[40:43], a[168:171], a[116:119], v[40:43]\n" \
"s_add_u32 m0, s80, 12288\n" \
"v_mfma_f32_16x16x32_bf16 v[48:51], a[168:171], a[120:123], v[48:51]\n" \
"buffer_load_dwordx4 v236, s[68:71], s25 offen lds\n" \
"v_mfma_f32_16x16x32_bf16 v[56:59], a[168:171], a[124:127], v[56:59]\n" \
"ds_read_b64_tr_b16 a[140:141], v227 offset:0\n" \
"v_mfma_f32_16x16x32_bf16 v[60:63], a[172:175], a[124:127], v[60:63]\n" \
"ds_read_b64_tr_b16 a[142:143], v227 offset:2048\n" \
"v_mfma_f32_16x16x32_bf16 v[52:55], a[172:175], a[120:123], v[52:55]\n" \
"s_add_u32 m0, s80, 16384\n" \
"ds_read_b64_tr_b16 a[144:145], v224 offset:4096\n" \
"v_mfma_f32_16x16x32_bf16 v[44:47], a[172:175], a[116:119], v[44:47]\n" \
"buffer_load_dwordx4 v235, s[68:71], s26 offen lds\n" \
"v_mfma_f32_16x16x32_bf16 v[36:39], a[172:175], a[112:115], v[36:39]\n" \
"v_readlane_b32 s27, v254, 31\n" \
"ds_read_b64_tr_b16 a[146:147], v224 offset:6144\n" \
"v_mfma_f32_16x16x32_bf16 v[64:67], a[176:179], a[112:115], v[64:67]\n" \
"s_add_u32 m0, s80, 20480\n" \
"ds_read_b64_tr_b16 a[148:149], v225 offset:4096\n" \
"v_mfma_f32_16x16x32_bf16 v[72:75], a[176:179], a[116:119], v[72:75]\n" \
"buffer_load_dwordx4 v236, s[68:71], s26 offen lds\n" \
"v_mfma_f32_16x16x32_bf16 v[80:83], a[176:179], a[120:123], v[80:83]\n" \
"v_mfma_f32_16x16x32_bf16 v[88:91], a[176:179], a[124:127], v[88:91]\n" \
"s_add_u32 m0, s80, 24576\n" \
"ds_read_b64_tr_b16 a[150:151], v225 offset:6144\n" \
"v_mfma_f32_16x16x32_bf16 v[92:95], a[180:183], a[124:127], v[92:95]\n" \
"buffer_load_dwordx4 v235, s[68:71], s27 offen lds\n" \
"v_mfma_f32_16x16x32_bf16 v[84:87], a[180:183], a[120:123], v[84:87]\n" \
"v_readlane_b32 s24, v254, 32\n" \
"ds_read_b64_tr_b16 a[152:153], v226 offset:4096\n" \
"v_mfma_f32_16x16x32_bf16 v[76:79], a[180:183], a[116:119], v[76:79]\n" \
"s_add_u32 m0, s80, 28672\n" \
"ds_read_b64_tr_b16 a[154:155], v226 offset:6144\n" \
"v_mfma_f32_16x16x32_bf16 v[68:71], a[180:183], a[112:115], v[68:71]\n" \
"buffer_load_dwordx4 v236, s[68:71], s27 offen lds\n" \
"v_mfma_f32_16x16x32_bf16 v[96:99], a[184:187], a[112:115], v[96:99]\n" \
"ds_read_b64_tr_b16 a[156:157], v227 offset:4096\n" \
"v_mfma_f32_16x16x32_bf16 v[104:107], a[184:187], a[116:119], v[104:107]\n" \
"ds_read_b64_tr_b16 a[158:159], v227 offset:6144\n" \
"Lpadh1_%=:\n" \
"s_cbranch_vccnz Lprh1_%=\n" \
"v_mfma_f32_16x16x32_bf16 v[112:115], a[184:187], a[120:123], v[112:115]\n" \
"v_mfma_f32_16x16x32_bf16 v[120:123], a[184:187], a[124:127], v[120:123]\n" \
"s_waitcnt vmcnt(15)\n" \
"v_mfma_f32_16x16x32_bf16 v[124:127], a[188:191], a[124:127], v[124:127]\n" \
"v_cmp_ne_u64 vcc, s[72:73], v[250:251]\n" \
"v_mfma_f32_16x16x32_bf16 v[116:119], a[188:191], a[120:123], v[116:119]\n" \
"s_cbranch_vccnz Lsl4_%=\n" \
"Lrt4_%=:\n" \
"v_mfma_f32_16x16x32_bf16 v[108:111], a[188:191], a[116:119], v[108:111]\n" \
"v_accvgpr_read_b32 v252, a241\n" \
"s_add_u32 s61, s94, 0\n" \
"v_mfma_f32_16x16x32_bf16 v[100:103], a[188:191], a[112:115], v[100:103]\n" \
"buffer_load_dwordx2 v[242:243], v252, s[64:67], s61 offen sc1\n" \
"s_waitcnt vmcnt(29) lgkmcnt(0)\n" \
"v_mfma_f32_16x16x32_bf16 v[0:3], a[128:131], a[0:3], v[0:3]\n" \
"ds_read_b64_tr_b16 a[160:161], v224 offset:8192\n" \
"v_mfma_f32_16x16x32_bf16 v[8:11], a[128:131], a[4:7], v[8:11]\n" \
"buffer_load_dwordx4 v[192:195], v233, s[64:67], s22 offen offset:0 sc1\n" \
"v_mfma_f32_16x16x32_bf16 v[16:19], a[128:131], a[8:11], v[16:19]\n" \
"v_readlane_b32 s23, v241, 27\n" \
"ds_read_b64_tr_b16 a[162:163], v224 offset:10240\n" \
"v_mfma_f32_16x16x32_bf16 v[24:27], a[128:131], a[12:15], v[24:27]\n" \
"ds_read_b64_tr_b16 a[164:165], v225 offset:8192\n" \
"v_mfma_f32_16x16x32_bf16 v[28:31], a[132:135], a[12:15], v[28:31]\n" \
"buffer_load_dwordx4 v[196:199], v233, s[64:67], s22 offen offset:1024 sc1\n" \
"v_mfma_f32_16x16x32_bf16 v[20:23], a[132:135], a[8:11], v[20:23]\n" \
"ds_read_b64_tr_b16 a[166:167], v225 offset:10240\n" \
"v_mfma_f32_16x16x32_bf16 v[12:15], a[132:135], a[4:7], v[12:15]\n" \
"ds_read_b64_tr_b16 a[168:169], v226 offset:8192\n" \
"v_mfma_f32_16x16x32_bf16 v[4:7], a[132:135], a[0:3], v[4:7]\n" \
"ds_read_b64_tr_b16 a[170:171], v226 offset:10240\n" \
"v_mfma_f32_16x16x32_bf16 v[32:35], a[136:139], a[0:3], v[32:35]\n" \
"buffer_load_dwordx4 v[200:203], v234, s[64:67], s22 offen offset:0 sc1\n" \
"v_mfma_f32_16x16x32_bf16 v[40:43], a[136:139], a[4:7], v[40:43]\n" \
"v_mfma_f32_16x16x32_bf16 v[48:51], a[136:139], a[8:11], v[48:51]\n" \
"ds_read_b64_tr_b16 a[172:173], v227 offset:8192\n" \
"v_mfma_f32_16x16x32_bf16 v[56:59], a[136:139], a[12:15], v[56:59]\n" \
"buffer_load_dwordx4 v[204:207], v234, s[64:67], s22 offen offset:1024 sc1\n" \
"v_mfma_f32_16x16x32_bf16 v[60:63], a[140:143], a[12:15], v[60:63]\n" \
"ds_read_b64_tr_b16 a[174:175], v227 offset:10240\n" \
"v_mfma_f32_16x16x32_bf16 v[52:55], a[140:143], a[8:11], v[52:55]\n" \
"ds_read_b64_tr_b16 a[176:177], v224 offset:12288\n" \
"v_mfma_f32_16x16x32_bf16 v[44:47], a[140:143], a[4:7], v[44:47]\n" \
"ds_read_b64_tr_b16 a[178:179], v224 offset:14336\n" \
"v_mfma_f32_16x16x32_bf16 v[36:39], a[140:143], a[0:3], v[36:39]\n" \
"buffer_load_dwordx4 v[208:211], v233, s[64:67], s23 offen offset:0 sc1\n" \
"v_mfma_f32_16x16x32_bf16 v[64:67], a[144:147], a[0:3], v[64:67]\n" \
"v_readlane_b32 s20, v241, 28\n" \
"ds_read_b64_tr_b16 a[180:181], v225 offset:12288\n" \
"v_mfma_f32_16x16x32_bf16 v[72:75], a[144:147], a[4:7], v[72:75]\n" \
"v_mfma_f32_16x16x32_bf16 v[80:83], a[144:147], a[8:11], v[80:83]\n" \
"ds_read_b64_tr_b16 a[182:183], v225 offset:14336\n" \
"v_mfma_f32_16x16x32_bf16 v[88:91], a[144:147], a[12:15], v[88:91]\n" \
"buffer_load_dwordx4 v[212:215], v233, s[64:67], s23 offen offset:1024 sc1\n" \
"v_mfma_f32_16x16x32_bf16 v[92:95], a[148:151], a[12:15], v[92:95]\n" \
"ds_read_b64_tr_b16 a[184:185], v226 offset:12288\n" \
"v_mfma_f32_16x16x32_bf16 v[84:87], a[148:151], a[8:11], v[84:87]\n" \
"ds_read_b64_tr_b16 a[186:187], v226 offset:14336\n" \
"v_mfma_f32_16x16x32_bf16 v[76:79], a[148:151], a[4:7], v[76:79]\n" \
"buffer_load_dwordx4 v[216:219], v234, s[64:67], s23 offen offset:0 sc1\n" \
"v_mfma_f32_16x16x32_bf16 v[68:71], a[148:151], a[0:3], v[68:71]\n" \
"ds_read_b64_tr_b16 a[188:189], v227 offset:12288\n" \
"v_mfma_f32_16x16x32_bf16 v[96:99], a[152:155], a[0:3], v[96:99]\n" \
"ds_read_b64_tr_b16 a[190:191], v227 offset:14336\n" \
"v_mfma_f32_16x16x32_bf16 v[104:107], a[152:155], a[4:7], v[104:107]\n" \
"v_mfma_f32_16x16x32_bf16 v[112:115], a[152:155], a[8:11], v[112:115]\n" \
"buffer_load_dwordx4 v[220:223], v234, s[64:67], s23 offen offset:1024 sc1\n" \
"v_mfma_f32_16x16x32_bf16 v[120:123], a[152:155], a[12:15], v[120:123]\n" \
"v_mfma_f32_16x16x32_bf16 v[124:127], a[156:159], a[12:15], v[124:127]\n" \
"v_mfma_f32_16x16x32_bf16 v[116:119], a[156:159], a[8:11], v[116:119]\n" \
"v_mfma_f32_16x16x32_bf16 v[108:111], a[156:159], a[4:7], v[108:111]\n" \
"v_mfma_f32_16x16x32_bf16 v[100:103], a[156:159], a[0:3], v[100:103]\n" \
"s_waitcnt vmcnt(33) lgkmcnt(0)\n" \
"v_mfma_f32_16x16x32_bf16 v[0:3], a[160:163], a[16:19], v[0:3]\n" \
"ds_read_b64_tr_b16 a[128:129], v224 offset:16384\n" \
"v_mfma_f32_16x16x32_bf16 v[8:11], a[160:163], a[20:23], v[8:11]\n" \
"buffer_load_dwordx4 v[160:163], v233, s[64:67], s20 offen offset:0 sc1\n" \
"v_mfma_f32_16x16x32_bf16 v[16:19], a[160:163], a[24:27], v[16:19]\n" \
"v_readlane_b32 s21, v241, 29\n" \
"ds_read_b64_tr_b16 a[130:131], v224 offset:18432\n" \
"v_mfma_f32_16x16x32_bf16 v[24:27], a[160:163], a[28:31], v[24:27]\n" \
"ds_read_b64_tr_b16 a[132:133], v225 offset:16384\n" \
"v_mfma_f32_16x16x32_bf16 v[28:31], a[164:167], a[28:31], v[28:31]\n" \
"buffer_load_dwordx4 v[164:167], v233, s[64:67], s20 offen offset:1024 sc1\n" \
"v_mfma_f32_16x16x32_bf16 v[20:23], a[164:167], a[24:27], v[20:23]\n" \
"ds_read_b64_tr_b16 a[134:135], v225 offset:18432\n" \
"v_mfma_f32_16x16x32_bf16 v[12:15], a[164:167], a[20:23], v[12:15]\n" \
"ds_read_b64_tr_b16 a[136:137], v226 offset:16384\n" \
"v_mfma_f32_16x16x32_bf16 v[4:7], a[164:167], a[16:19], v[4:7]\n" \
"ds_read_b64_tr_b16 a[138:139], v226 offset:18432\n" \
"v_mfma_f32_16x16x32_bf16 v[32:35], a[168:171], a[16:19], v[32:35]\n" \
"buffer_load_dwordx4 v[168:171], v234, s[64:67], s20 offen offset:0 sc1\n" \
"v_mfma_f32_16x16x32_bf16 v[40:43], a[168:171], a[20:23], v[40:43]\n" \
"v_mfma_f32_16x16x32_bf16 v[48:51], a[168:171], a[24:27], v[48:51]\n" \
"ds_read_b64_tr_b16 a[140:141], v227 offset:16384\n" \
"v_mfma_f32_16x16x32_bf16 v[56:59], a[168:171], a[28:31], v[56:59]\n" \
"buffer_load_dwordx4 v[172:175], v234, s[64:67], s20 offen offset:1024 sc1\n" \
"v_mfma_f32_16x16x32_bf16 v[60:63], a[172:175], a[28:31], v[60:63]\n" \
"ds_read_b64_tr_b16 a[142:143], v227 offset:18432\n" \
"v_mfma_f32_16x16x32_bf16 v[52:55], a[172:175], a[24:27], v[52:55]\n" \
"ds_read_b64_tr_b16 a[144:145], v224 offset:20480\n" \
"v_mfma_f32_16x16x32_bf16 v[44:47], a[172:175], a[20:23], v[44:47]\n" \
"ds_read_b64_tr_b16 a[146:147], v224 offset:22528\n" \
"v_mfma_f32_16x16x32_bf16 v[36:39], a[172:175], a[16:19], v[36:39]\n" \
"buffer_load_dwordx4 v[176:179], v233, s[64:67], s21 offen offset:0 sc1\n" \
"v_mfma_f32_16x16x32_bf16 v[64:67], a[176:179], a[16:19], v[64:67]\n" \
"v_readlane_b32 s22, v241, 30\n" \
"ds_read_b64_tr_b16 a[148:149], v225 offset:20480\n" \
"v_mfma_f32_16x16x32_bf16 v[72:75], a[176:179], a[20:23], v[72:75]\n" \
"v_mfma_f32_16x16x32_bf16 v[80:83], a[176:179], a[24:27], v[80:83]\n" \
"ds_read_b64_tr_b16 a[150:151], v225 offset:22528\n" \
"v_mfma_f32_16x16x32_bf16 v[88:91], a[176:179], a[28:31], v[88:91]\n" \
"buffer_load_dwordx4 v[180:183], v233, s[64:67], s21 offen offset:1024 sc1\n" \
"v_mfma_f32_16x16x32_bf16 v[92:95], a[180:183], a[28:31], v[92:95]\n" \
"ds_read_b64_tr_b16 a[152:153], v226 offset:20480\n" \
"v_mfma_f32_16x16x32_bf16 v[84:87], a[180:183], a[24:27], v[84:87]\n" \
"ds_read_b64_tr_b16 a[154:155], v226 offset:22528\n" \
"v_mfma_f32_16x16x32_bf16 v[76:79], a[180:183], a[20:23], v[76:79]\n" \
"buffer_load_dwordx4 v[184:187], v234, s[64:67], s21 offen offset:0 sc1\n" \
"v_mfma_f32_16x16x32_bf16 v[68:71], a[180:183], a[16:19], v[68:71]\n" \
"ds_read_b64_tr_b16 a[156:157], v227 offset:20480\n" \
"v_mfma_f32_16x16x32_bf16 v[96:99], a[184:187], a[16:19], v[96:99]\n" \
"ds_read_b64_tr_b16 a[158:159], v227 offset:22528\n" \
"v_mfma_f32_16x16x32_bf16 v[104:107], a[184:187], a[20:23], v[104:107]\n" \
"v_mfma_f32_16x16x32_bf16 v[112:115], a[184:187], a[24:27], v[112:115]\n" \
"buffer_load_dwordx4 v[188:191], v234, s[64:67], s21 offen offset:1024 sc1\n" \
"v_mfma_f32_16x16x32_bf16 v[120:123], a[184:187], a[28:31], v[120:123]\n" \
"v_mfma_f32_16x16x32_bf16 v[124:127], a[188:191], a[28:31], v[124:127]\n" \
"v_mfma_f32_16x16x32_bf16 v[116:119], a[188:191], a[24:27], v[116:119]\n" \
"v_mfma_f32_16x16x32_bf16 v[108:111], a[188:191], a[20:23], v[108:111]\n" \
"v_mfma_f32_16x16x32_bf16 v[100:103], a[188:191], a[16:19], v[100:103]\n" \
"s_waitcnt vmcnt(37) lgkmcnt(0)\n" \
"v_mfma_f32_16x16x32_bf16 v[0:3], a[128:131], a[64:67], v[0:3]\n" \
"s_add_u32 m0, s80, 32768\n" \
"ds_read_b64_tr_b16 a[160:161], v224 offset:24576\n" \
"v_mfma_f32_16x16x32_bf16 v[8:11], a[128:131], a[68:71], v[8:11]\n" \
"buffer_load_dwordx4 v235, s[68:71], s24 offen lds\n" \
"v_mfma_f32_16x16x32_bf16 v[16:19], a[128:131], a[72:75], v[16:19]\n" \
"v_readlane_b32 s25, v254, 33\n" \
"ds_read_b64_tr_b16 a[162:163], v224 offset:26624\n" \
"v_mfma_f32_16x16x32_bf16 v[24:27], a[128:131], a[76:79], v[24:27]\n" \
"s_add_u32 m0, s80, 36864\n" \
"ds_read_b64_tr_b16 a[164:165], v225 offset:24576\n" \
"v_mfma_f32_16x16x32_bf16 v[28:31], a[132:135], a[76:79], v[28:31]\n" \
"buffer_load_dwordx4 v236, s[68:71], s24 offen lds\n" \
"v_mfma_f32_16x16x32_bf16 v[20:23], a[132:135], a[72:75], v[20:23]\n" \
"ds_read_b64_tr_b16 a[166:167], v225 offset:26624\n" \
"v_mfma_f32_16x16x32_bf16 v[12:15], a[132:135], a[68:71], v[12:15]\n" \
"ds_read_b64_tr_b16 a[168:169], v226 offset:24576\n" \
"v_mfma_f32_16x16x32_bf16 v[4:7], a[132:135], a[64:67], v[4:7]\n" \
"s_add_u32 m0, s80, 40960\n" \
"ds_read_b64_tr_b16 a[170:171], v226 offset:26624\n" \
"v_mfma_f32_16x16x32_bf16 v[32:35], a[136:139], a[64:67], v[32:35]\n" \
"s_mov_b64 s[68:69], s[96:97]\n" \
"buffer_load_dwordx4 v235, s[68:71], s25 offen lds\n" \
"v_mfma_f32_16x16x32_bf16 v[40:43], a[136:139], a[68:71], v[40:43]\n" \
"v_readlane_b32 s26, v254, 34\n" \
"v_mfma_f32_16x16x32_bf16 v[48:51], a[136:139], a[72:75], v[48:51]\n" \
"s_add_u32 m0, s80, 45056\n" \
"ds_read_b64_tr_b16 a[172:173], v227 offset:24576\n" \
"v_mfma_f32_16x16x32_bf16 v[56:59], a[136:139], a[76:79], v[56:59]\n" \
"v_add_u32 v236, 0x10000, v235\n" \
"buffer_load_dwordx4 v236, s[68:71], s25 offen lds\n" \
"v_mfma_f32_16x16x32_bf16 v[60:63], a[140:143], a[76:79], v[60:63]\n" \
"ds_read_b64_tr_b16 a[174:175], v227 offset:26624\n" \
"v_mfma_f32_16x16x32_bf16 v[52:55], a[140:143], a[72:75], v[52:55]\n" \
"ds_read_b64_tr_b16 a[176:177], v224 offset:28672\n" \
"v_mfma_f32_16x16x32_bf16 v[44:47], a[140:143], a[68:71], v[44:47]\n" \
"s_add_u32 m0, s80, 49152\n" \
"ds_read_b64_tr_b16 a[178:179], v224 offset:30720\n" \
"v_mfma_f32_16x16x32_bf16 v[36:39], a[140:143], a[64:67], v[36:39]\n" \
"buffer_load_dwordx4 v235, s[68:71], s26 offen lds\n" \
"v_mfma_f32_16x16x32_bf16 v[64:67], a[144:147], a[64:67], v[64:67]\n" \
"v_readlane_b32 s27, v254, 35\n" \
"ds_read_b64_tr_b16 a[180:181], v225 offset:28672\n" \
"v_mfma_f32_16x16x32_bf16 v[72:75], a[144:147], a[68:71], v[72:75]\n" \
"v_mfma_f32_16x16x32_bf16 v[80:83], a[144:147], a[72:75], v[80:83]\n" \
"s_add_u32 m0, s80, 53248\n" \
"ds_read_b64_tr_b16 a[182:183], v225 offset:30720\n" \
"v_mfma_f32_16x16x32_bf16 v[88:91], a[144:147], a[76:79], v[88:91]\n" \
"buffer_load_dwordx4 v236, s[68:71], s26 offen lds\n" \
"v_mfma_f32_16x16x32_bf16 v[92:95], a[148:151], a[76:79], v[92:95]\n" \
"ds_read_b64_tr_b16 a[184:185], v226 offset:28672\n" \
"v_mfma_f32_16x16x32_bf16 v[84:87], a[148:151], a[72:75], v[84:87]\n" \
"s_add_u32 m0, s80, 57344\n" \
"ds_read_b64_tr_b16 a[186:187], v226 offset:30720\n" \
"v_mfma_f32_16x16x32_bf16 v[76:79], a[148:151], a[68:71], v[76:79]\n" \
"buffer_load_dwordx4 v235, s[68:71], s27 offen lds\n" \
"v_mfma_f32_16x16x32_bf16 v[68:71], a[148:151], a[64:67], v[68:71]\n" \
"v_readlane_b32 s24, v254, 36\n" \
"ds_read_b64_tr_b16 a[188:189], v227 offset:28672\n" \
"v_mfma_f32_16x16x32_bf16 v[96:99], a[152:155], a[64:67], v[96:99]\n" \
"ds_read_b64_tr_b16 a[190:191], v227 offset:30720\n" \
"v_mfma_f32_16x16x32_bf16 v[104:107], a[152:155], a[68:71], v[104:107]\n" \
"s_add_u32 m0, s80, 61440\n" \
"v_mfma_f32_16x16x32_bf16 v[112:115], a[152:155], a[72:75], v[112:115]\n" \
"buffer_load_dwordx4 v236, s[68:71], s27 offen lds\n" \
"v_mfma_f32_16x16x32_bf16 v[120:123], a[152:155], a[76:79], v[120:123]\n" \
"v_mfma_f32_16x16x32_bf16 v[124:127], a[156:159], a[76:79], v[124:127]\n" \
"v_mfma_f32_16x16x32_bf16 v[116:119], a[156:159], a[72:75], v[116:119]\n" \
"v_mfma_f32_16x16x32_bf16 v[108:111], a[156:159], a[68:71], v[108:111]\n" \
"v_mfma_f32_16x16x32_bf16 v[100:103], a[156:159], a[64:67], v[100:103]\n" \
"s_waitcnt vmcnt(41) lgkmcnt(0)\n" \
"s_barrier\n" \
"v_mfma_f32_16x16x32_bf16 v[0:3], a[160:163], a[80:83], v[0:3]\n" \
"ds_read_b64_tr_b16 a[128:129], v224 offset:32768\n" \
"s_getpc_b64 s[28:29]\n" \
"Lct5_%=:\n" \
"s_add_u32 s28, s28, Lh_late_%=-Lct5_%=\n" \
"s_addc_u32 s29, s29, 0\n" \
"v_mfma_f32_16x16x32_bf16 v[8:11], a[160:163], a[84:87], v[8:11]\n" \
"buffer_load_dwordx4 a[96:99], v233, s[64:67], s22 offen offset:0 sc1\n" \
"v_mfma_f32_16x16x32_bf16 v[16:19], a[160:163], a[88:91], v[16:19]\n" \
"v_readlane_b32 s23, v241, 31\n" \
"ds_read_b64_tr_b16 a[130:131], v224 offset:34816\n" \
"v_mfma_f32_16x16x32_bf16 v[24:27], a[160:163], a[92:95], v[24:27]\n" \
"ds_read_b64_tr_b16 a[132:133], v225 offset:32768\n" \
"v_mfma_f32_16x16x32_bf16 v[28:31], a[164:167], a[92:95], v[28:31]\n" \
"ds_read_b64_tr_b16 a[134:135], v225 offset:34816\n" \
"v_mfma_f32_16x16x32_bf16 v[20:23], a[164:167], a[88:91], v[20:23]\n" \
"buffer_load_dwordx4 a[100:103], v233, s[64:67], s22 offen offset:1024 sc1\n" \
"v_mfma_f32_16x16x32_bf16 v[12:15], a[164:167], a[84:87], v[12:15]\n" \
"v_mfma_f32_16x16x32_bf16 v[4:7], a[164:167], a[80:83], v[4:7]\n" \
"ds_read_b64_tr_b16 a[136:137], v226 offset:32768\n" \
"v_mfma_f32_16x16x32_bf16 v[32:35], a[168:171], a[80:83], v[32:35]\n" \
"ds_read_b64_tr_b16 a[138:139], v226 offset:34816\n" \
"s_mov_b32 s30, Lq_entry_%=-Lh_late_%=\n" \
"s_mov_b32 s31, 0x20000\n" \
"v_mfma_f32_16x16x32_bf16 v[40:43], a[168:171], a[84:87], v[40:43]\n" \
"buffer_load_dwordx4 a[104:107], v234, s[64:67], s22 offen offset:0 sc1\n" \
"v_mfma_f32_16x16x32_bf16 v[48:51], a[168:171], a[88:91], v[48:51]\n" \
"ds_read_b64_tr_b16 a[140:141], v227 offset:32768\n" \
"v_mfma_f32_16x16x32_bf16 v[56:59], a[168:171], a[92:95], v[56:59]\n" \
"ds_read_b64_tr_b16 a[142:143], v227 offset:34816\n" \
"v_mfma_f32_16x16x32_bf16 v[60:63], a[172:175], a[92:95], v[60:63]\n" \
"v_mfma_f32_16x16x32_bf16 v[52:55], a[172:175], a[88:91], v[52:55]\n" \
"buffer_load_dwordx4 a[108:111], v234, s[64:67], s22 offen offset:1024 sc1\n" \
"v_mfma_f32_16x16x32_bf16 v[44:47], a[172:175], a[84:87], v[44:47]\n" \
"ds_read_b64_tr_b16 a[144:145], v224 offset:36864\n" \
"v_mfma_f32_16x16x32_bf16 v[36:39], a[172:175], a[80:83], v[36:39]\n" \
"ds_read_b64_tr_b16 a[146:147], v224 offset:38912\n" \
"v_mfma_f32_16x16x32_bf16 v[64:67], a[176:179], a[80:83], v[64:67]\n" \
"ds_read_b64_tr_b16 a[148:149], v225 offset:36864\n" \
"v_accvgpr_read_b32 v248, a243\n" \
"v_mfma_f32_16x16x32_bf16 v[72:75], a[176:179], a[84:87], v[72:75]\n" \
"buffer_load_dwordx4 a[112:115], v233, s[64:67], s23 offen offset:0 sc1\n" \
"v_mfma_f32_16x16x32_bf16 v[80:83], a[176:179], a[88:91], v[80:83]\n" \
"v_readlane_b32 s20, v241, 32\n" \
"ds_read_b64_tr_b16 a[150:151], v225 offset:38912\n" \
"v_mfma_f32_16x16x32_bf16 v[88:91], a[176:179], a[92:95], v[88:91]\n" \
"v_mfma_f32_16x16x32_bf16 v[92:95], a[180:183], a[92:95], v[92:95]\n" \
"ds_read_b64_tr_b16 a[152:153], v226 offset:36864\n" \
"v_mfma_f32_16x16x32_bf16 v[84:87], a[180:183], a[88:91], v[84:87]\n" \
"buffer_load_dwordx4 a[116:119], v233, s[64:67], s23 offen offset:1024 sc1\n" \
"v_mfma_f32_16x16x32_bf16 v[76:79], a[180:183], a[84:87], v[76:79]\n" \
"ds_read_b64_tr_b16 a[154:155], v226 offset:38912\n" \
"v_mfma_f32_16x16x32_bf16 v[68:71], a[180:183], a[80:83], v[68:71]\n" \
"ds_read_b64_tr_b16 a[156:157], v227 offset:36864\n" \
"v_mfma_f32_16x16x32_bf16 v[96:99], a[184:187], a[80:83], v[96:99]\n" \
"ds_read_b64_tr_b16 a[158:159], v227 offset:38912\n" \
"v_mfma_f32_16x16x32_bf16 v[104:107], a[184:187], a[84:87], v[104:107]\n" \
"buffer_load_dwordx4 a[120:123], v234, s[64:67], s23 offen offset:0 sc1\n" \
"v_mfma_f32_16x16x32_bf16 v[112:115], a[184:187], a[88:91], v[112:115]\n" \
"buffer_load_dword a207, v248, s[28:31], 0 offen\n" \
"v_mfma_f32_16x16x32_bf16 v[120:123], a[184:187], a[92:95], v[120:123]\n" \
"v_mfma_f32_16x16x32_bf16 v[124:127], a[188:191], a[92:95], v[124:127]\n" \
"v_mfma_f32_16x16x32_bf16 v[116:119], a[188:191], a[88:91], v[116:119]\n" \
"v_mfma_f32_16x16x32_bf16 v[108:111], a[188:191], a[84:87], v[108:111]\n" \
"v_mfma_f32_16x16x32_bf16 v[100:103], a[188:191], a[80:83], v[100:103]\n" \
"s_waitcnt vmcnt(44) lgkmcnt(0)\n" \
"v_mfma_f32_16x16x32_bf16 v[0:3], a[128:131], a[32:35], v[0:3]\n" \
"ds_read_b64_tr_b16 a[160:161], v224 offset:40960\n" \
"s_getpc_b64 s[28:29]\n" \
"Lct6_%=:\n" \
"s_add_u32 s28, s28, Lfin_%=-Lct6_%=\n" \
"s_addc_u32 s29, s29, 0\n" \
"v_mfma_f32_16x16x32_bf16 v[8:11], a[128:131], a[36:39], v[8:11]\n" \
"buffer_load_dwordx4 a[124:127], v234, s[64:67], s23 offen offset:1024 sc1\n" \
"v_mfma_f32_16x16x32_bf16 v[16:19], a[128:131], a[40:43], v[16:19]\n" \
"ds_read_b64_tr_b16 a[162:163], v224 offset:43008\n" \
"v_mfma_f32_16x16x32_bf16 v[24:27], a[128:131], a[44:47], v[24:27]\n" \
"ds_read_b64_tr_b16 a[164:165], v225 offset:40960\n" \
"v_mfma_f32_16x16x32_bf16 v[28:31], a[132:135], a[44:47], v[28:31]\n" \
"ds_read_b64_tr_b16 a[166:167], v225 offset:43008\n" \
"v_mfma_f32_16x16x32_bf16 v[20:23], a[132:135], a[40:43], v[20:23]\n" \
"buffer_load_dwordx4 a[0:3], v233, s[64:67], s20 offen offset:0 sc1\n" \
"v_mfma_f32_16x16x32_bf16 v[12:15], a[132:135], a[36:39], v[12:15]\n" \
"v_readlane_b32 s21, v241, 33\n" \
"v_mfma_f32_16x16x32_bf16 v[4:7], a[132:135], a[32:35], v[4:7]\n" \
"ds_read_b64_tr_b16 a[168:169], v226 offset:40960\n" \
"v_mfma_f32_16x16x32_bf16 v[32:35], a[136:139], a[32:35], v[32:35]\n" \
"ds_read_b64_tr_b16 a[170:171], v226 offset:43008\n" \
"s_mov_b32 s30, gmk_code_end-Lfin_%=\n" \
"s_mov_b32 s31, 0x20000\n" \
"v_mfma_f32_16x16x32_bf16 v[40:43], a[136:139], a[36:39], v[40:43]\n" \
"buffer_load_dwordx4 a[4:7], v233, s[64:67], s20 offen offset:1024 sc1\n" \
"v_mfma_f32_16x16x32_bf16 v[48:51], a[136:139], a[40:43], v[48:51]\n" \
"ds_read_b64_tr_b16 a[172:173], v227 offset:40960\n" \
"v_mfma_f32_16x16x32_bf16 v[56:59], a[136:139], a[44:47], v[56:59]\n" \
"ds_read_b64_tr_b16 a[174:175], v227 offset:43008\n" \
"v_mfma_f32_16x16x32_bf16 v[60:63], a[140:143], a[44:47], v[60:63]\n" \
"v_mfma_f32_16x16x32_bf16 v[52:55], a[140:143], a[40:43], v[52:55]\n" \
"buffer_load_dwordx4 a[8:11], v234, s[64:67], s20 offen offset:0 sc1\n" \
"v_mfma_f32_16x16x32_bf16 v[44:47], a[140:143], a[36:39], v[44:47]\n" \
"ds_read_b64_tr_b16 a[176:177], v224 offset:45056\n" \
"v_mfma_f32_16x16x32_bf16 v[36:39], a[140:143], a[32:35], v[36:39]\n" \
"ds_read_b64_tr_b16 a[178:179], v224 offset:47104\n" \
"v_mfma_f32_16x16x32_bf16 v[64:67], a[144:147], a[32:35], v[64:67]\n" \
"ds_read_b64_tr_b16 a[180:181], v225 offset:45056\n" \
"v_accvgpr_read_b32 v248, a243\n" \
"v_mfma_f32_16x16x32_bf16 v[72:75], a[144:147], a[36:39], v[72:75]\n" \
"buffer_load_dwordx4 a[12:15], v234, s[64:67], s20 offen offset:1024 sc1\n" \
"v_mfma_f32_16x16x32_bf16 v[80:83], a[144:147], a[40:43], v[80:83]\n" \
"ds_read_b64_tr_b16 a[182:183], v225 offset:47104\n" \
"v_mfma_f32_16x16x32_bf16 v[88:91], a[144:147], a[44:47], v[88:91]\n" \
"v_mfma_f32_16x16x32_bf16 v[92:95], a[148:151], a[44:47], v[92:95]\n" \
"ds_read_b64_tr_b16 a[184:185], v226 offset:45056\n" \
"v_mfma_f32_16x16x32_bf16 v[84:87], a[148:151], a[40:43], v[84:87]\n" \
"buffer_load_dwordx4 a[16:19], v233, s[64:67], s21 offen offset:0 sc1\n" \
"v_mfma_f32_16x16x32_bf16 v[76:79], a[148:151], a[36:39], v[76:79]\n" \
"v_readlane_b32 s22, v241, 34\n" \
"ds_read_b64_tr_b16 a[186:187], v226 offset:47104\n" \
"v_mfma_f32_16x16x32_bf16 v[68:71], a[148:151], a[32:35], v[68:71]\n" \
"ds_read_b64_tr_b16 a[188:189], v227 offset:45056\n" \
"v_mfma_f32_16x16x32_bf16 v[96:99], a[152:155], a[32:35], v[96:99]\n" \
"ds_read_b64_tr_b16 a[190:191], v227 offset:47104\n" \
"v_mfma_f32_16x16x32_bf16 v[104:107], a[152:155], a[36:39], v[104:107]\n" \
"buffer_load_dwordx4 a[20:23], v233, s[64:67], s21 offen offset:1024 sc1\n" \
"v_mfma_f32_16x16x32_bf16 v[112:115], a[152:155], a[40:43], v[112:115]\n" \
"buffer_load_dword a208, v248, s[28:31], 0 offen\n" \
"v_mfma_f32_16x16x32_bf16 v[120:123], a[152:155], a[44:47], v[120:123]\n" \
"v_mfma_f32_16x16x32_bf16 v[124:127], a[156:159], a[44:47], v[124:127]\n" \
"v_mfma_f32_16x16x32_bf16 v[116:119], a[156:159], a[40:43], v[116:119]\n" \
"v_mfma_f32_16x16x32_bf16 v[108:111], a[156:159], a[36:39], v[108:111]\n" \
"v_mfma_f32_16x16x32_bf16 v[100:103], a[156:159], a[32:35], v[100:103]\n" \
"s_waitcnt vmcnt(48) lgkmcnt(0)\n" \
"v_mfma_f32_16x16x32_bf16 v[0:3], a[160:163], a[48:51], v[0:3]\n" \
"ds_read_b64_tr_b16 a[128:129], v224 offset:49152\n" \
"v_mfma_f32_16x16x32_bf16 v[8:11], a[160:163], a[52:55], v[8:11]\n" \
"v_add_u32 v234, 16384, v233\n" \
"buffer_load_dwordx4 a[24:27], v234, s[64:67], s21 offen offset:0 sc1\n" \
"v_mfma_f32_16x16x32_bf16 v[16:19], a[160:163], a[56:59], v[16:19]\n" \
"ds_read_b64_tr_b16 a[130:131], v224 offset:51200\n" \
"v_mfma_f32_16x16x32_bf16 v[24:27], a[160:163], a[60:63], v[24:27]\n" \
"ds_read_b64_tr_b16 a[132:133], v225 offset:49152\n" \
"v_mfma_f32_16x16x32_bf16 v[28:31], a[164:167], a[60:63], v[28:31]\n" \
"buffer_load_dwordx4 a[28:31], v234, s[64:67], s21 offen offset:1024 sc1\n" \
"v_mfma_f32_16x16x32_bf16 v[20:23], a[164:167], a[56:59], v[20:23]\n" \
"ds_read_b64_tr_b16 a[134:135], v225 offset:51200\n" \
"v_mfma_f32_16x16x32_bf16 v[12:15], a[164:167], a[52:55], v[12:15]\n" \
"ds_read_b64_tr_b16 a[136:137], v226 offset:49152\n" \
"v_mfma_f32_16x16x32_bf16 v[4:7], a[164:167], a[48:51], v[4:7]\n" \
"s_add_u32 m0, s80, 131072\n" \
"ds_read_b64_tr_b16 a[138:139], v226 offset:51200\n" \
"v_mfma_f32_16x16x32_bf16 v[32:35], a[168:171], a[48:51], v[32:35]\n" \
"buffer_load_dwordx4 v235, s[68:71], s24 offen lds\n" \
"v_mfma_f32_16x16x32_bf16 v[40:43], a[168:171], a[52:55], v[40:43]\n" \
"v_readlane_b32 s25, v254, 37\n" \
"v_mfma_f32_16x16x32_bf16 v[48:51], a[168:171], a[56:59], v[48:51]\n" \
"s_add_u32 m0, s80, 135168\n" \
"ds_read_b64_tr_b16 a[140:141], v227 offset:49152\n" \
"v_mfma_f32_16x16x32_bf16 v[56:59], a[168:171], a[60:63], v[56:59]\n" \
"buffer_load_dwordx4 v236, s[68:71], s24 offen lds\n" \
"v_mfma_f32_16x16x32_bf16 v[60:63], a[172:175], a[60:63], v[60:63]\n" \
"ds_read_b64_tr_b16 a[142:143], v227 offset:51200\n" \
"v_mfma_f32_16x16x32_bf16 v[52:55], a[172:175], a[56:59], v[52:55]\n" \
"ds_read_b64_tr_b16 a[144:145], v224 offset:53248\n" \
"v_mfma_f32_16x16x32_bf16 v[44:47], a[172:175], a[52:55], v[44:47]\n" \
"s_add_u32 m0, s80, 139264\n" \
"ds_read_b64_tr_b16 a[146:147], v224 offset:55296\n" \
"v_mfma_f32_16x16x32_bf16 v[36:39], a[172:175], a[48:51], v[36:39]\n" \
"buffer_load_dwordx4 v235, s[68:71], s25 offen lds\n" \
"v_mfma_f32_16x16x32_bf16 v[64:67], a[176:179], a[48:51], v[64:67]\n" \
"v_readlane_b32 s26, v254, 38\n" \
"ds_read_b64_tr_b16 a[148:149], v225 offset:53248\n" \
"v_mfma_f32_16x16x32_bf16 v[72:75], a[176:179], a[52:55], v[72:75]\n" \
"v_mfma_f32_16x16x32_bf16 v[80:83], a[176:179], a[56:59], v[80:83]\n" \
"s_add_u32 m0, s80, 143360\n" \
"ds_read_b64_tr_b16 a[150:151], v225 offset:55296\n" \
"v_mfma_f32_16x16x32_bf16 v[88:91], a[176:179], a[60:63], v[88:91]\n" \
"buffer_load_dwordx4 v236, s[68:71], s25 offen lds\n" \
"v_mfma_f32_16x16x32_bf16 v[92:95], a[180:183], a[60:63], v[92:95]\n" \
"ds_read_b64_tr_b16 a[152:153], v226 offset:53248\n" \
"v_mfma_f32_16x16x32_bf16 v[84:87], a[180:183], a[56:59], v[84:87]\n" \
"s_add_u32 m0, s80, 147456\n" \
"ds_read_b64_tr_b16 a[154:155], v226 offset:55296\n" \
"v_mfma_f32_16x16x32_bf16 v[76:79], a[180:183], a[52:55], v[76:79]\n" \
"buffer_load_dwordx4 v235, s[68:71], s26 offen lds\n" \
"v_mfma_f32_16x16x32_bf16 v[68:71], a[180:183], a[48:51], v[68:71]\n" \
"v_readlane_b32 s27, v254, 39\n" \
"ds_read_b64_tr_b16 a[156:157], v227 offset:53248\n" \
"v_mfma_f32_16x16x32_bf16 v[96:99], a[184:187], a[48:51], v[96:99]\n" \
"ds_read_b64_tr_b16 a[158:159], v227 offset:55296\n" \
"v_mfma_f32_16x16x32_bf16 v[104:107], a[184:187], a[52:55], v[104:107]\n" \
"s_add_u32 m0, s80, 151552\n" \
"v_mfma_f32_16x16x32_bf16 v[112:115], a[184:187], a[56:59], v[112:115]\n" \
"buffer_load_dwordx4 v236, s[68:71], s26 offen lds\n" \
"v_mfma_f32_16x16x32_bf16 v[120:123], a[184:187], a[60:63], v[120:123]\n" \
"v_mfma_f32_16x16x32_bf16 v[124:127], a[188:191], a[60:63], v[124:127]\n" \
"v_mfma_f32_16x16x32_bf16 v[116:119], a[188:191], a[56:59], v[116:119]\n" \
"v_mfma_f32_16x16x32_bf16 v[108:111], a[188:191], a[52:55], v[108:111]\n" \
"v_mfma_f32_16x16x32_bf16 v[100:103], a[188:191], a[48:51], v[100:103]\n" \
"s_waitcnt vmcnt(44) lgkmcnt(0)\n" \
"v_mfma_f32_16x16x32_bf16 v[0:3], a[128:131], v[192:195], v[0:3]\n" \
"s_add_u32 m0, s80, 155648\n" \
"ds_read_b64_tr_b16 a[160:161], v224 offset:57344\n" \
"v_mfma_f32_16x16x32_bf16 v[8:11], a[128:131], v[196:199], v[8:11]\n" \
"buffer_load_dwordx4 v235, s[68:71], s27 offen lds\n" \
"v_mfma_f32_16x16x32_bf16 v[16:19], a[128:131], v[200:203], v[16:19]\n" \
"v_readlane_b32 s24, v254, 40\n" \
"ds_read_b64_tr_b16 a[162:163], v224 offset:59392\n" \
"v_mfma_f32_16x16x32_bf16 v[24:27], a[128:131], v[204:207], v[24:27]\n" \
"s_add_u32 m0, s80, 159744\n" \
"ds_read_b64_tr_b16 a[164:165], v225 offset:57344\n" \
"v_mfma_f32_16x16x32_bf16 v[28:31], a[132:135], v[204:207], v[28:31]\n" \
"buffer_load_dwordx4 v236, s[68:71], s27 offen lds\n" \
"v_mfma_f32_16x16x32_bf16 v[20:23], a[132:135], v[200:203], v[20:23]\n" \
"ds_read_b64_tr_b16 a[166:167], v225 offset:59392\n" \
"v_mfma_f32_16x16x32_bf16 v[12:15], a[132:135], v[196:199], v[12:15]\n" \
"ds_read_b64_tr_b16 a[168:169], v226 offset:57344\n" \
"v_mfma_f32_16x16x32_bf16 v[4:7], a[132:135], v[192:195], v[4:7]\n" \
"ds_read_b64_tr_b16 a[170:171], v226 offset:59392\n" \
"v_mfma_f32_16x16x32_bf16 v[32:35], a[136:139], v[192:195], v[32:35]\n" \
"buffer_load_dwordx4 a[64:67], v233, s[64:67], s22 offen offset:0 sc1\n" \
"v_mfma_f32_16x16x32_bf16 v[40:43], a[136:139], v[196:199], v[40:43]\n" \
"v_readlane_b32 s23, v241, 35\n" \
"v_mfma_f32_16x16x32_bf16 v[48:51], a[136:139], v[200:203], v[48:51]\n" \
"ds_read_b64_tr_b16 a[172:173], v227 offset:57344\n" \
"v_mfma_f32_16x16x32_bf16 v[56:59], a[136:139], v[204:207], v[56:59]\n" \
"buffer_load_dwordx4 a[68:71], v233, s[64:67], s22 offen offset:1024 sc1\n" \
"v_mfma_f32_16x16x32_bf16 v[60:63], a[140:143], v[204:207], v[60:63]\n" \
"ds_read_b64_tr_b16 a[174:175], v227 offset:59392\n" \
"v_mfma_f32_16x16x32_bf16 v[52:55], a[140:143], v[200:203], v[52:55]\n" \
"ds_read_b64_tr_b16 a[176:177], v224 offset:61440\n" \
"v_mfma_f32_16x16x32_bf16 v[44:47], a[140:143], v[196:199], v[44:47]\n" \
"ds_read_b64_tr_b16 a[178:179], v224 offset:63488\n" \
"v_mfma_f32_16x16x32_bf16 v[36:39], a[140:143], v[192:195], v[36:39]\n" \
"buffer_load_dwordx4 a[72:75], v234, s[64:67], s22 offen offset:0 sc1\n" \
"v_mfma_f32_16x16x32_bf16 v[64:67], a[144:147], v[192:195], v[64:67]\n" \
"ds_read_b64_tr_b16 a[180:181], v225 offset:61440\n" \
"v_mfma_f32_16x16x32_bf16 v[72:75], a[144:147], v[196:199], v[72:75]\n" \
"v_mfma_f32_16x16x32_bf16 v[80:83], a[144:147], v[200:203], v[80:83]\n" \
"ds_read_b64_tr_b16 a[182:183], v225 offset:63488\n" \
"v_mfma_f32_16x16x32_bf16 v[88:91], a[144:147], v[204:207], v[88:91]\n" \
"buffer_load_dwordx4 a[76:79], v234, s[64:67], s22 offen offset:1024 sc1\n" \
"v_mfma_f32_16x16x32_bf16 v[92:95], a[148:151], v[204:207], v[92:95]\n" \
"ds_read_b64_tr_b16 a[184:185], v226 offset:61440\n" \
"v_mfma_f32_16x16x32_bf16 v[84:87], a[148:151], v[200:203], v[84:87]\n" \
"ds_read_b64_tr_b16 a[186:187], v226 offset:63488\n" \
"v_mfma_f32_16x16x32_bf16 v[76:79], a[148:151], v[196:199], v[76:79]\n" \
"buffer_load_dwordx4 a[80:83], v233, s[64:67], s23 offen offset:0 sc1\n" \
"v_mfma_f32_16x16x32_bf16 v[68:71], a[148:151], v[192:195], v[68:71]\n" \
"v_readlane_b32 s20, v241, 36\n" \
"ds_read_b64_tr_b16 a[188:189], v227 offset:61440\n" \
"v_mfma_f32_16x16x32_bf16 v[96:99], a[152:155], v[192:195], v[96:99]\n" \
"ds_read_b64_tr_b16 a[190:191], v227 offset:63488\n" \
"v_mfma_f32_16x16x32_bf16 v[104:107], a[152:155], v[196:199], v[104:107]\n" \
"v_mfma_f32_16x16x32_bf16 v[112:115], a[152:155], v[200:203], v[112:115]\n" \
"buffer_load_dwordx4 a[84:87], v233, s[64:67], s23 offen offset:1024 sc1\n" \
"v_mfma_f32_16x16x32_bf16 v[120:123], a[152:155], v[204:207], v[120:123]\n" \
"v_mfma_f32_16x16x32_bf16 v[124:127], a[156:159], v[204:207], v[124:127]\n" \
"v_mfma_f32_16x16x32_bf16 v[116:119], a[156:159], v[200:203], v[116:119]\n" \
"v_mfma_f32_16x16x32_bf16 v[108:111], a[156:159], v[196:199], v[108:111]\n" \
"v_mfma_f32_16x16x32_bf16 v[100:103], a[156:159], v[192:195], v[100:103]\n" \
"s_waitcnt vmcnt(48) lgkmcnt(0)\n" \
"s_barrier\n" \
"v_mfma_f32_16x16x32_bf16 v[0:3], a[160:163], v[208:211], v[0:3]\n" \
"ds_read_b64_tr_b16 a[128:129], v237 offset:0\n" \
"v_mfma_f32_16x16x32_bf16 v[8:11], a[160:163], v[212:215], v[8:11]\n" \
"buffer_load_dwordx4 a[88:91], v234, s[64:67], s23 offen offset:0 sc1\n" \
"v_mfma_f32_16x16x32_bf16 v[16:19], a[160:163], v[216:219], v[16:19]\n" \
"ds_read_b64_tr_b16 a[130:131], v237 offset:2048\n" \
"v_mfma_f32_16x16x32_bf16 v[24:27], a[160:163], v[220:223], v[24:27]\n" \
"ds_read_b64_tr_b16 a[132:133], v238 offset:0\n" \
"v_mfma_f32_16x16x32_bf16 v[28:31], a[164:167], v[220:223], v[28:31]\n" \
"buffer_load_dwordx4 a[92:95], v234, s[64:67], s23 offen offset:1024 sc1\n" \
"v_mfma_f32_16x16x32_bf16 v[20:23], a[164:167], v[216:219], v[20:23]\n" \
"ds_read_b64_tr_b16 a[134:135], v238 offset:2048\n" \
"v_mfma_f32_16x16x32_bf16 v[12:15], a[164:167], v[212:215], v[12:15]\n" \
"ds_read_b64_tr_b16 a[136:137], v239 offset:0\n" \
"v_mfma_f32_16x16x32_bf16 v[4:7], a[164:167], v[208:211], v[4:7]\n" \
"ds_read_b64_tr_b16 a[138:139], v239 offset:2048\n" \
"v_mfma_f32_16x16x32_bf16 v[32:35], a[168:171], v[208:211], v[32:35]\n" \
"buffer_load_dwordx4 a[32:35], v233, s[64:67], s20 offen offset:0 sc1\n" \
"v_mfma_f32_16x16x32_bf16 v[40:43], a[168:171], v[212:215], v[40:43]\n" \
"v_readlane_b32 s21, v241, 37\n" \
"v_mfma_f32_16x16x32_bf16 v[48:51], a[168:171], v[216:219], v[48:51]\n" \
"ds_read_b64_tr_b16 a[140:141], v240 offset:0\n" \
"v_mfma_f32_16x16x32_bf16 v[56:59], a[168:171], v[220:223], v[56:59]\n" \
"buffer_load_dwordx4 a[36:39], v233, s[64:67], s20 offen offset:1024 sc1\n" \
"v_mfma_f32_16x16x32_bf16 v[60:63], a[172:175], v[220:223], v[60:63]\n" \
"ds_read_b64_tr_b16 a[142:143], v240 offset:2048\n" \
"v_mfma_f32_16x16x32_bf16 v[52:55], a[172:175], v[216:219], v[52:55]\n" \
"ds_read_b64_tr_b16 a[144:145], v237 offset:4096\n" \
"v_mfma_f32_16x16x32_bf16 v[44:47], a[172:175], v[212:215], v[44:47]\n" \
"ds_read_b64_tr_b16 a[146:147], v237 offset:6144\n" \
"v_mfma_f32_16x16x32_bf16 v[36:39], a[172:175], v[208:211], v[36:39]\n" \
"buffer_load_dwordx4 a[40:43], v234, s[64:67], s20 offen offset:0 sc1\n" \
"v_mfma_f32_16x16x32_bf16 v[64:67], a[176:179], v[208:211], v[64:67]\n" \
"ds_read_b64_tr_b16 a[148:149], v238 offset:4096\n" \
"v_mfma_f32_16x16x32_bf16 v[72:75], a[176:179], v[212:215], v[72:75]\n" \
"v_mfma_f32_16x16x32_bf16 v[80:83], a[176:179], v[216:219], v[80:83]\n" \
"ds_read_b64_tr_b16 a[150:151], v238 offset:6144\n" \
"v_mfma_f32_16x16x32_bf16 v[88:91], a[176:179], v[220:223], v[88:91]\n" \
"buffer_load_dwordx4 a[44:47], v234, s[64:67], s20 offen offset:1024 sc1\n" \
"v_mfma_f32_16x16x32_bf16 v[92:95], a[180:183], v[220:223], v[92:95]\n" \
"ds_read_b64_tr_b16 a[152:153], v239 offset:4096\n" \
"v_mfma_f32_16x16x32_bf16 v[84:87], a[180:183], v[216:219], v[84:87]\n" \
"ds_read_b64_tr_b16 a[154:155], v239 offset:6144\n" \
"v_mfma_f32_16x16x32_bf16 v[76:79], a[180:183], v[212:215], v[76:79]\n" \
"buffer_load_dwordx4 a[48:51], v233, s[64:67], s21 offen offset:0 sc1\n" \
"v_mfma_f32_16x16x32_bf16 v[68:71], a[180:183], v[208:211], v[68:71]\n" \
"v_readlane_b32 s22, v241, 38\n" \
"ds_read_b64_tr_b16 a[156:157], v240 offset:4096\n" \
"v_mfma_f32_16x16x32_bf16 v[96:99], a[184:187], v[208:211], v[96:99]\n" \
"ds_read_b64_tr_b16 a[158:159], v240 offset:6144\n" \
"v_mfma_f32_16x16x32_bf16 v[104:107], a[184:187], v[212:215], v[104:107]\n" \
"v_mfma_f32_16x16x32_bf16 v[112:115], a[184:187], v[216:219], v[112:115]\n" \
"buffer_load_dwordx4 a[52:55], v233, s[64:67], s21 offen offset:1024 sc1\n" \
"v_mfma_f32_16x16x32_bf16 v[120:123], a[184:187], v[220:223], v[120:123]\n" \
"v_mfma_f32_16x16x32_bf16 v[124:127], a[188:191], v[220:223], v[124:127]\n" \
"v_mfma_f32_16x16x32_bf16 v[116:119], a[188:191], v[216:219], v[116:119]\n" \
"v_mfma_f32_16x16x32_bf16 v[108:111], a[188:191], v[212:215], v[108:111]\n" \
"v_mfma_f32_16x16x32_bf16 v[100:103], a[188:191], v[208:211], v[100:103]\n" \
"Lpadh2_%=:\n" \
"s_cbranch_vccnz Lprh2_%=\n" \
"s_waitcnt vmcnt(52) lgkmcnt(0)\n" \
"v_mfma_f32_16x16x32_bf16 v[0:3], a[128:131], v[160:163], v[0:3]\n" \
"ds_read_b64_tr_b16 a[160:161], v237 offset:8192\n" \
"v_mfma_f32_16x16x32_bf16 v[8:11], a[128:131], v[164:167], v[8:11]\n" \
"buffer_load_dwordx4 a[56:59], v234, s[64:67], s21 offen offset:0 sc1\n" \
"v_mfma_f32_16x16x32_bf16 v[16:19], a[128:131], v[168:171], v[16:19]\n" \
"ds_read_b64_tr_b16 a[162:163], v237 offset:10240\n" \
"v_mfma_f32_16x16x32_bf16 v[24:27], a[128:131], v[172:175], v[24:27]\n" \
"ds_read_b64_tr_b16 a[164:165], v238 offset:8192\n" \
"v_mfma_f32_16x16x32_bf16 v[28:31], a[132:135], v[172:175], v[28:31]\n" \
"buffer_load_dwordx4 a[60:63], v234, s[64:67], s21 offen offset:1024 sc1\n" \
"v_mfma_f32_16x16x32_bf16 v[20:23], a[132:135], v[168:171], v[20:23]\n" \
"ds_read_b64_tr_b16 a[166:167], v238 offset:10240\n" \
"v_mfma_f32_16x16x32_bf16 v[12:15], a[132:135], v[164:167], v[12:15]\n" \
"ds_read_b64_tr_b16 a[168:169], v239 offset:8192\n" \
"v_mfma_f32_16x16x32_bf16 v[4:7], a[132:135], v[160:163], v[4:7]\n" \
"s_add_u32 m0, s80, 65536\n" \
"ds_read_b64_tr_b16 a[170:171], v239 offset:10240\n" \
"v_mfma_f32_16x16x32_bf16 v[32:35], a[136:139], v[160:163], v[32:35]\n" \
"buffer_load_dwordx4 v235, s[68:71], s24 offen lds\n" \
"v_mfma_f32_16x16x32_bf16 v[40:43], a[136:139], v[164:167], v[40:43]\n" \
"v_readlane_b32 s25, v254, 41\n" \
"v_mfma_f32_16x16x32_bf16 v[48:51], a[136:139], v[168:171], v[48:51]\n" \
"s_add_u32 m0, s80, 69632\n" \
"ds_read_b64_tr_b16 a[172:173], v240 offset:8192\n" \
"v_mfma_f32_16x16x32_bf16 v[56:59], a[136:139], v[172:175], v[56:59]\n" \
"buffer_load_dwordx4 v236, s[68:71], s24 offen lds\n" \
"v_mfma_f32_16x16x32_bf16 v[60:63], a[140:143], v[172:175], v[60:63]\n" \
"ds_read_b64_tr_b16 a[174:175], v240 offset:10240\n" \
"v_mfma_f32_16x16x32_bf16 v[52:55], a[140:143], v[168:171], v[52:55]\n" \
"ds_read_b64_tr_b16 a[176:177], v237 offset:12288\n" \
"v_mfma_f32_16x16x32_bf16 v[44:47], a[140:143], v[164:167], v[44:47]\n" \
"s_add_u32 m0, s80, 73728\n" \
"ds_read_b64_tr_b16 a[178:179], v237 offset:14336\n" \
"v_mfma_f32_16x16x32_bf16 v[36:39], a[140:143], v[160:163], v[36:39]\n" \
"buffer_load_dwordx4 v235, s[68:71], s25 offen lds\n" \
"v_mfma_f32_16x16x32_bf16 v[64:67], a[144:147], v[160:163], v[64:67]\n" \
"v_readlane_b32 s26, v254, 42\n" \
"ds_read_b64_tr_b16 a[180:181], v238 offset:12288\n" \
"v_mfma_f32_16x16x32_bf16 v[72:75], a[144:147], v[164:167], v[72:75]\n" \
"v_mfma_f32_16x16x32_bf16 v[80:83], a[144:147], v[168:171], v[80:83]\n" \
"s_add_u32 m0, s80, 77824\n" \
"ds_read_b64_tr_b16 a[182:183], v238 offset:14336\n" \
"v_mfma_f32_16x16x32_bf16 v[88:91], a[144:147], v[172:175], v[88:91]\n" \
"buffer_load_dwordx4 v236, s[68:71], s25 offen lds\n" \
"v_mfma_f32_16x16x32_bf16 v[92:95], a[148:151], v[172:175], v[92:95]\n" \
"ds_read_b64_tr_b16 a[184:185], v239 offset:12288\n" \
"v_mfma_f32_16x16x32_bf16 v[84:87], a[148:151], v[168:171], v[84:87]\n" \
"s_add_u32 m0, s80, 81920\n" \
"ds_read_b64_tr_b16 a[186:187], v239 offset:14336\n" \
"v_mfma_f32_16x16x32_bf16 v[76:79], a[148:151], v[164:167], v[76:79]\n" \
"buffer_load_dwordx4 v235, s[68:71], s26 offen lds\n" \
"v_mfma_f32_16x16x32_bf16 v[68:71], a[148:151], v[160:163], v[68:71]\n" \
"v_readlane_b32 s27, v254, 43\n" \
"ds_read_b64_tr_b16 a[188:189], v240 offset:12288\n" \
"v_mfma_f32_16x16x32_bf16 v[96:99], a[152:155], v[160:163], v[96:99]\n" \
"ds_read_b64_tr_b16 a[190:191], v240 offset:14336\n" \
"v_mfma_f32_16x16x32_bf16 v[104:107], a[152:155], v[164:167], v[104:107]\n" \
"s_add_u32 m0, s80, 86016\n" \
"v_mfma_f32_16x16x32_bf16 v[112:115], a[152:155], v[168:171], v[112:115]\n" \
"buffer_load_dwordx4 v236, s[68:71], s26 offen lds\n" \
"v_mfma_f32_16x16x32_bf16 v[120:123], a[152:155], v[172:175], v[120:123]\n" \
"v_mfma_f32_16x16x32_bf16 v[124:127], a[156:159], v[172:175], v[124:127]\n" \
"v_mfma_f32_16x16x32_bf16 v[116:119], a[156:159], v[168:171], v[116:119]\n" \
"v_mfma_f32_16x16x32_bf16 v[108:111], a[156:159], v[164:167], v[108:111]\n" \
"v_mfma_f32_16x16x32_bf16 v[100:103], a[156:159], v[160:163], v[100:103]\n" \
"Lh_late_%=:\n" \
"s_waitcnt vmcnt(56) lgkmcnt(0)\n" \
"v_mfma_f32_16x16x32_bf16 v[0:3], a[160:163], v[176:179], v[0:3]\n" \
"s_add_u32 m0, s80, 90112\n" \
"ds_read_b64_tr_b16 a[128:129], v237 offset:16384\n" \
"v_mfma_f32_16x16x32_bf16 v[8:11], a[160:163], v[180:183], v[8:11]\n" \
"buffer_load_dwordx4 v235, s[68:71], s27 offen lds\n" \
"v_mfma_f32_16x16x32_bf16 v[16:19], a[160:163], v[184:187], v[16:19]\n" \
"v_readlane_b32 s24, v254, 44\n" \
"ds_read_b64_tr_b16 a[130:131], v237 offset:18432\n" \
"v_mfma_f32_16x16x32_bf16 v[24:27], a[160:163], v[188:191], v[24:27]\n" \
"s_add_u32 m0, s80, 94208\n" \
"ds_read_b64_tr_b16 a[132:133], v238 offset:16384\n" \
"v_mfma_f32_16x16x32_bf16 v[28:31], a[164:167], v[188:191], v[28:31]\n" \
"buffer_load_dwordx4 v236, s[68:71], s27 offen lds\n" \
"v_mfma_f32_16x16x32_bf16 v[20:23], a[164:167], v[184:187], v[20:23]\n" \
"ds_read_b64_tr_b16 a[134:135], v238 offset:18432\n" \
"v_mfma_f32_16x16x32_bf16 v[12:15], a[164:167], v[180:183], v[12:15]\n" \
"ds_read_b64_tr_b16 a[136:137], v239 offset:16384\n" \
"v_mfma_f32_16x16x32_bf16 v[4:7], a[164:167], v[176:179], v[4:7]\n" \
"ds_read_b64_tr_b16 a[138:139], v239 offset:18432\n" \
"v_mfma_f32_16x16x32_bf16 v[32:35], a[168:171], v[176:179], v[32:35]\n" \
"buffer_load_dwordx4 v[192:195], v233, s[64:67], s22 offen offset:0 sc1\n" \
"v_mfma_f32_16x16x32_bf16 v[40:43], a[168:171], v[180:183], v[40:43]\n" \
"v_readlane_b32 s23, v241, 39\n" \
"v_mfma_f32_16x16x32_bf16 v[48:51], a[168:171], v[184:187], v[48:51]\n" \
"ds_read_b64_tr_b16 a[140:141], v240 offset:16384\n" \
"v_mfma_f32_16x16x32_bf16 v[56:59], a[168:171], v[188:191], v[56:59]\n" \
"buffer_load_dwordx4 v[196:199], v233, s[64:67], s22 offen offset:1024 sc1\n" \
"v_mfma_f32_16x16x32_bf16 v[60:63], a[172:175], v[188:191], v[60:63]\n" \
"ds_read_b64_tr_b16 a[142:143], v240 offset:18432\n" \
"v_mfma_f32_16x16x32_bf16 v[52:55], a[172:175], v[184:187], v[52:55]\n" \
"ds_read_b64_tr_b16 a[144:145], v237 offset:20480\n" \
"v_mfma_f32_16x16x32_bf16 v[44:47], a[172:175], v[180:183], v[44:47]\n" \
"ds_read_b64_tr_b16 a[146:147], v237 offset:22528\n" \
"v_mfma_f32_16x16x32_bf16 v[36:39], a[172:175], v[176:179], v[36:39]\n" \
"buffer_load_dwordx4 v[200:203], v234, s[64:67], s22 offen offset:0 sc1\n" \
"v_mfma_f32_16x16x32_bf16 v[64:67], a[176:179], v[176:179], v[64:67]\n" \
"ds_read_b64_tr_b16 a[148:149], v238 offset:20480\n" \
"v_mfma_f32_16x16x32_bf16 v[72:75], a[176:179], v[180:183], v[72:75]\n" \
"v_mfma_f32_16x16x32_bf16 v[80:83], a[176:179], v[184:187], v[80:83]\n" \
"ds_read_b64_tr_b16 a[150:151], v238 offset:22528\n" \
"v_mfma_f32_16x16x32_bf16 v[88:91], a[176:179], v[188:191], v[88:91]\n" \
"buffer_load_dwordx4 v[204:207], v234, s[64:67], s22 offen offset:1024 sc1\n" \
"v_mfma_f32_16x16x32_bf16 v[92:95], a[180:183], v[188:191], v[92:95]\n" \
"ds_read_b64_tr_b16 a[152:153], v239 offset:20480\n" \
"v_mfma_f32_16x16x32_bf16 v[84:87], a[180:183], v[184:187], v[84:87]\n" \
"ds_read_b64_tr_b16 a[154:155], v239 offset:22528\n" \
"v_mfma_f32_16x16x32_bf16 v[76:79], a[180:183], v[180:183], v[76:79]\n" \
"buffer_load_dwordx4 v[208:211], v233, s[64:67], s23 offen offset:0 sc1\n" \
"v_mfma_f32_16x16x32_bf16 v[68:71], a[180:183], v[176:179], v[68:71]\n" \
"v_readlane_b32 s20, v241, 40\n" \
"ds_read_b64_tr_b16 a[156:157], v240 offset:20480\n" \
"v_mfma_f32_16x16x32_bf16 v[96:99], a[184:187], v[176:179], v[96:99]\n" \
"ds_read_b64_tr_b16 a[158:159], v240 offset:22528\n" \
"v_mfma_f32_16x16x32_bf16 v[104:107], a[184:187], v[180:183], v[104:107]\n" \
"v_mfma_f32_16x16x32_bf16 v[112:115], a[184:187], v[184:187], v[112:115]\n" \
"buffer_load_dwordx4 v[212:215], v233, s[64:67], s23 offen offset:1024 sc1\n" \
"v_mfma_f32_16x16x32_bf16 v[120:123], a[184:187], v[188:191], v[120:123]\n" \
"v_mfma_f32_16x16x32_bf16 v[124:127], a[188:191], v[188:191], v[124:127]\n" \
"v_mfma_f32_16x16x32_bf16 v[116:119], a[188:191], v[184:187], v[116:119]\n" \
"v_mfma_f32_16x16x32_bf16 v[108:111], a[188:191], v[180:183], v[108:111]\n" \
"v_mfma_f32_16x16x32_bf16 v[100:103], a[188:191], v[176:179], v[100:103]\n" \
"s_waitcnt vmcnt(52) lgkmcnt(0)\n" \
"v_mfma_f32_16x16x32_bf16 v[0:3], a[128:131], a[96:99], v[0:3]\n" \
"ds_read_b64_tr_b16 a[160:161], v237 offset:24576\n" \
"v_mfma_f32_16x16x32_bf16 v[8:11], a[128:131], a[100:103], v[8:11]\n" \
"buffer_load_dwordx4 v[216:219], v234, s[64:67], s23 offen offset:0 sc1\n" \
"v_mfma_f32_16x16x32_bf16 v[16:19], a[128:131], a[104:107], v[16:19]\n" \
"ds_read_b64_tr_b16 a[162:163], v237 offset:26624\n" \
"v_mfma_f32_16x16x32_bf16 v[24:27], a[128:131], a[108:111], v[24:27]\n" \
"ds_read_b64_tr_b16 a[164:165], v238 offset:24576\n" \
"v_mfma_f32_16x16x32_bf16 v[28:31], a[132:135], a[108:111], v[28:31]\n" \
"buffer_load_dwordx4 v[220:223], v234, s[64:67], s23 offen offset:1024 sc1\n" \
"v_mfma_f32_16x16x32_bf16 v[20:23], a[132:135], a[104:107], v[20:23]\n" \
"ds_read_b64_tr_b16 a[166:167], v238 offset:26624\n" \
"v_mfma_f32_16x16x32_bf16 v[12:15], a[132:135], a[100:103], v[12:15]\n" \
"ds_read_b64_tr_b16 a[168:169], v239 offset:24576\n" \
"v_mfma_f32_16x16x32_bf16 v[4:7], a[132:135], a[96:99], v[4:7]\n" \
"ds_read_b64_tr_b16 a[170:171], v239 offset:26624\n" \
"v_mfma_f32_16x16x32_bf16 v[32:35], a[136:139], a[96:99], v[32:35]\n" \
"buffer_load_dwordx4 v[160:163], v233, s[64:67], s20 offen offset:0 sc1\n" \
"v_mfma_f32_16x16x32_bf16 v[40:43], a[136:139], a[100:103], v[40:43]\n" \
"v_readlane_b32 s21, v241, 41\n" \
"v_mfma_f32_16x16x32_bf16 v[48:51], a[136:139], a[104:107], v[48:51]\n" \
"ds_read_b64_tr_b16 a[172:173], v240 offset:24576\n" \
"v_mfma_f32_16x16x32_bf16 v[56:59], a[136:139], a[108:111], v[56:59]\n" \
"buffer_load_dwordx4 v[164:167], v233, s[64:67], s20 offen offset:1024 sc1\n" \
"v_mfma_f32_16x16x32_bf16 v[60:63], a[140:143], a[108:111], v[60:63]\n" \
"ds_read_b64_tr_b16 a[174:175], v240 offset:26624\n" \
"v_mfma_f32_16x16x32_bf16 v[52:55], a[140:143], a[104:107], v[52:55]\n" \
"ds_read_b64_tr_b16 a[176:177], v237 offset:28672\n" \
"v_mfma_f32_16x16x32_bf16 v[44:47], a[140:143], a[100:103], v[44:47]\n" \
"ds_read_b64_tr_b16 a[178:179], v237 offset:30720\n" \
"v_mfma_f32_16x16x32_bf16 v[36:39], a[140:143], a[96:99], v[36:39]\n" \
"buffer_load_dwordx4 v[168:171], v234, s[64:67], s20 offen offset:0 sc1\n" \
"v_mfma_f32_16x16x32_bf16 v[64:67], a[144:147], a[96:99], v[64:67]\n" \
"ds_read_b64_tr_b16 a[180:181], v238 offset:28672\n" \
"v_mfma_f32_16x16x32_bf16 v[72:75], a[144:147], a[100:103], v[72:75]\n" \
"v_mfma_f32_16x16x32_bf16 v[80:83], a[144:147], a[104:107], v[80:83]\n" \
"ds_read_b64_tr_b16 a[182:183], v238 offset:30720\n" \
"v_mfma_f32_16x16x32_bf16 v[88:91], a[144:147], a[108:111], v[88:91]\n" \
"buffer_load_dwordx4 v[172:175], v234, s[64:67], s20 offen offset:1024 sc1\n" \
"v_mfma_f32_16x16x32_bf16 v[92:95], a[148:151], a[108:111], v[92:95]\n" \
"ds_read_b64_tr_b16 a[184:185], v239 offset:28672\n" \
"v_mfma_f32_16x16x32_bf16 v[84:87], a[148:151], a[104:107], v[84:87]\n" \
"ds_read_b64_tr_b16 a[186:187], v239 offset:30720\n" \
"v_mfma_f32_16x16x32_bf16 v[76:79], a[148:151], a[100:103], v[76:79]\n" \
"buffer_load_dwordx4 v[176:179], v233, s[64:67], s21 offen offset:0 sc1\n" \
"v_mfma_f32_16x16x32_bf16 v[68:71], a[148:151], a[96:99], v[68:71]\n" \
"v_readlane_b32 s22, v241, 42\n" \
"ds_read_b64_tr_b16 a[188:189], v240 offset:28672\n" \
"v_mfma_f32_16x16x32_bf16 v[96:99], a[152:155], a[96:99], v[96:99]\n" \
"ds_read_b64_tr_b16 a[190:191], v240 offset:30720\n" \
"v_mfma_f32_16x16x32_bf16 v[104:107], a[152:155], a[100:103], v[104:107]\n" \
"v_mfma_f32_16x16x32_bf16 v[112:115], a[152:155], a[104:107], v[112:115]\n" \
"buffer_load_dwordx4 v[180:183], v233, s[64:67], s21 offen offset:1024 sc1\n" \
"v_mfma_f32_16x16x32_bf16 v[120:123], a[152:155], a[108:111], v[120:123]\n" \
"v_mfma_f32_16x16x32_bf16 v[124:127], a[156:159], a[108:111], v[124:127]\n" \
"v_mfma_f32_16x16x32_bf16 v[116:119], a[156:159], a[104:107], v[116:119]\n" \
"v_mfma_f32_16x16x32_bf16 v[108:111], a[156:159], a[100:103], v[108:111]\n" \
"v_mfma_f32_16x16x32_bf16 v[100:103], a[156:159], a[96:99], v[100:103]\n" \
"s_waitcnt vmcnt(55) lgkmcnt(0)\n" \
"s_barrier\n" \
"v_mfma_f32_16x16x32_bf16 v[0:3], a[160:163], a[112:115], v[0:3]\n" \
"ds_read_b64_tr_b16 a[128:129], v237 offset:32768\n" \
"v_mfma_f32_16x16x32_bf16 v[8:11], a[160:163], a[116:119], v[8:11]\n" \
"buffer_load_dwordx4 v[184:187], v234, s[64:67], s21 offen offset:0 sc1\n" \
"v_mfma_f32_16x16x32_bf16 v[16:19], a[160:163], a[120:123], v[16:19]\n" \
"ds_read_b64_tr_b16 a[130:131], v237 offset:34816\n" \
"v_mfma_f32_16x16x32_bf16 v[24:27], a[160:163], a[124:127], v[24:27]\n" \
"ds_read_b64_tr_b16 a[132:133], v238 offset:32768\n" \
"v_mfma_f32_16x16x32_bf16 v[28:31], a[164:167], a[124:127], v[28:31]\n" \
"buffer_load_dwordx4 v[188:191], v234, s[64:67], s21 offen offset:1024 sc1\n" \
"v_mfma_f32_16x16x32_bf16 v[20:23], a[164:167], a[120:123], v[20:23]\n" \
"ds_read_b64_tr_b16 a[134:135], v238 offset:34816\n" \
"v_mfma_f32_16x16x32_bf16 v[12:15], a[164:167], a[116:119], v[12:15]\n" \
"ds_read_b64_tr_b16 a[136:137], v239 offset:32768\n" \
"v_mfma_f32_16x16x32_bf16 v[4:7], a[164:167], a[112:115], v[4:7]\n" \
"s_add_u32 m0, s80, 98304\n" \
"ds_read_b64_tr_b16 a[138:139], v239 offset:34816\n" \
"v_mfma_f32_16x16x32_bf16 v[32:35], a[168:171], a[112:115], v[32:35]\n" \
"buffer_load_dwordx4 v235, s[68:71], s24 offen lds\n" \
"v_mfma_f32_16x16x32_bf16 v[40:43], a[168:171], a[116:119], v[40:43]\n" \
"v_readlane_b32 s25, v254, 45\n" \
"v_mfma_f32_16x16x32_bf16 v[48:51], a[168:171], a[120:123], v[48:51]\n" \
"s_add_u32 m0, s80, 102400\n" \
"ds_read_b64_tr_b16 a[140:141], v240 offset:32768\n" \
"v_mfma_f32_16x16x32_bf16 v[56:59], a[168:171], a[124:127], v[56:59]\n" \
"buffer_load_dwordx4 v236, s[68:71], s24 offen lds\n" \
"v_mfma_f32_16x16x32_bf16 v[60:63], a[172:175], a[124:127], v[60:63]\n" \
"ds_read_b64_tr_b16 a[142:143], v240 offset:34816\n" \
"v_mfma_f32_16x16x32_bf16 v[52:55], a[172:175], a[120:123], v[52:55]\n" \
"ds_read_b64_tr_b16 a[144:145], v237 offset:36864\n" \
"v_mfma_f32_16x16x32_bf16 v[44:47], a[172:175], a[116:119], v[44:47]\n" \
"s_add_u32 m0, s80, 106496\n" \
"ds_read_b64_tr_b16 a[146:147], v237 offset:38912\n" \
"v_mfma_f32_16x16x32_bf16 v[36:39], a[172:175], a[112:115], v[36:39]\n" \
"buffer_load_dwordx4 v235, s[68:71], s25 offen lds\n" \
"v_mfma_f32_16x16x32_bf16 v[64:67], a[176:179], a[112:115], v[64:67]\n" \
"v_readlane_b32 s26, v254, 46\n" \
"ds_read_b64_tr_b16 a[148:149], v238 offset:36864\n" \
"v_mfma_f32_16x16x32_bf16 v[72:75], a[176:179], a[116:119], v[72:75]\n" \
"v_mfma_f32_16x16x32_bf16 v[80:83], a[176:179], a[120:123], v[80:83]\n" \
"s_add_u32 m0, s80, 110592\n" \
"ds_read_b64_tr_b16 a[150:151], v238 offset:38912\n" \
"v_mfma_f32_16x16x32_bf16 v[88:91], a[176:179], a[124:127], v[88:91]\n" \
"buffer_load_dwordx4 v236, s[68:71], s25 offen lds\n" \
"v_mfma_f32_16x16x32_bf16 v[92:95], a[180:183], a[124:127], v[92:95]\n" \
"ds_read_b64_tr_b16 a[152:153], v239 offset:36864\n" \
"v_mfma_f32_16x16x32_bf16 v[84:87], a[180:183], a[120:123], v[84:87]\n" \
"s_add_u32 m0, s80, 114688\n" \
"ds_read_b64_tr_b16 a[154:155], v239 offset:38912\n" \
"v_mfma_f32_16x16x32_bf16 v[76:79], a[180:183], a[116:119], v[76:79]\n" \
"buffer_load_dwordx4 v235, s[68:71], s26 offen lds\n" \
"v_mfma_f32_16x16x32_bf16 v[68:71], a[180:183], a[112:115], v[68:71]\n" \
"v_readlane_b32 s27, v254, 47\n" \
"ds_read_b64_tr_b16 a[156:157], v240 offset:36864\n" \
"v_mfma_f32_16x16x32_bf16 v[96:99], a[184:187], a[112:115], v[96:99]\n" \
"ds_read_b64_tr_b16 a[158:159], v240 offset:38912\n" \
"v_mfma_f32_16x16x32_bf16 v[104:107], a[184:187], a[116:119], v[104:107]\n" \
"s_add_u32 m0, s80, 118784\n" \
"v_mfma_f32_16x16x32_bf16 v[112:115], a[184:187], a[120:123], v[112:115]\n" \
"buffer_load_dwordx4 v236, s[68:71], s26 offen lds\n" \
"v_mfma_f32_16x16x32_bf16 v[120:123], a[184:187], a[124:127], v[120:123]\n" \
"v_mfma_f32_16x16x32_bf16 v[124:127], a[188:191], a[124:127], v[124:127]\n" \
"v_mfma_f32_16x16x32_bf16 v[116:119], a[188:191], a[120:123], v[116:119]\n" \
"v_mfma_f32_16x16x32_bf16 v[108:111], a[188:191], a[116:119], v[108:111]\n" \
"v_mfma_f32_16x16x32_bf16 v[100:103], a[188:191], a[112:115], v[100:103]\n" \
"s_waitcnt vmcnt(59) lgkmcnt(0)\n" \
"v_mfma_f32_16x16x32_bf16 v[0:3], a[128:131], a[0:3], v[0:3]\n" \
"s_add_u32 m0, s80, 122880\n" \
"ds_read_b64_tr_b16 a[160:161], v237 offset:40960\n" \
"v_mfma_f32_16x16x32_bf16 v[8:11], a[128:131], a[4:7], v[8:11]\n" \
"buffer_load_dwordx4 v235, s[68:71], s27 offen lds\n" \
"v_mfma_f32_16x16x32_bf16 v[16:19], a[128:131], a[8:11], v[16:19]\n" \
"v_readlane_b32 s24, v254, 48\n" \
"ds_read_b64_tr_b16 a[162:163], v237 offset:43008\n" \
"v_mfma_f32_16x16x32_bf16 v[24:27], a[128:131], a[12:15], v[24:27]\n" \
"s_add_u32 m0, s80, 126976\n" \
"ds_read_b64_tr_b16 a[164:165], v238 offset:40960\n" \
"v_mfma_f32_16x16x32_bf16 v[28:31], a[132:135], a[12:15], v[28:31]\n" \
"buffer_load_dwordx4 v236, s[68:71], s27 offen lds\n" \
"v_mfma_f32_16x16x32_bf16 v[20:23], a[132:135], a[8:11], v[20:23]\n" \
"ds_read_b64_tr_b16 a[166:167], v238 offset:43008\n" \
"s_xor_b32 s60, s91, 2\n" \
"s_add_u32 s60, s60, s98\n" \
"v_mfma_f32_16x16x32_bf16 v[12:15], a[132:135], a[4:7], v[12:15]\n" \
"ds_read_b64_tr_b16 a[168:169], v239 offset:40960\n" \
"s_mul_i32 s60, s60, 49152\n" \
"s_add_u32 s41, s60, 20987904\n" \
"v_mfma_f32_16x16x32_bf16 v[4:7], a[132:135], a[0:3], v[4:7]\n" \
"ds_read_b64_tr_b16 a[170:171], v239 offset:43008\n" \
"v_mfma_f32_16x16x32_bf16 v[32:35], a[136:139], a[0:3], v[32:35]\n" \
"buffer_load_dwordx4 a[96:99], v233, s[64:67], s22 offen offset:0 sc1\n" \
"v_mfma_f32_16x16x32_bf16 v[40:43], a[136:139], a[4:7], v[40:43]\n" \
"v_readlane_b32 s23, v241, 43\n" \
"s_xor_b32 s60, s91, 1\n" \
"s_add_u32 s60, s60, s98\n" \
"v_mfma_f32_16x16x32_bf16 v[48:51], a[136:139], a[8:11], v[48:51]\n" \
"ds_read_b64_tr_b16 a[172:173], v240 offset:40960\n" \
"v_mfma_f32_16x16x32_bf16 v[56:59], a[136:139], a[12:15], v[56:59]\n" \
"buffer_load_dwordx4 a[100:103], v233, s[64:67], s22 offen offset:1024 sc1\n" \
"v_mfma_f32_16x16x32_bf16 v[60:63], a[140:143], a[12:15], v[60:63]\n" \
"ds_read_b64_tr_b16 a[174:175], v240 offset:43008\n" \
"s_mul_i32 s60, s60, 49152\n" \
"s_add_u32 s40, s60, 20971520\n" \
"v_mfma_f32_16x16x32_bf16 v[52:55], a[140:143], a[8:11], v[52:55]\n" \
"ds_read_b64_tr_b16 a[176:177], v237 offset:45056\n" \
"v_mfma_f32_16x16x32_bf16 v[44:47], a[140:143], a[4:7], v[44:47]\n" \
"ds_read_b64_tr_b16 a[178:179], v237 offset:47104\n" \
"s_xor_b32 s60, s91, 3\n" \
"s_add_u32 s60, s60, s98\n" \
"v_mfma_f32_16x16x32_bf16 v[36:39], a[140:143], a[0:3], v[36:39]\n" \
"buffer_load_dwordx4 a[104:107], v234, s[64:67], s22 offen offset:0 sc1\n" \
"v_mfma_f32_16x16x32_bf16 v[64:67], a[144:147], a[0:3], v[64:67]\n" \
"ds_read_b64_tr_b16 a[180:181], v238 offset:45056\n" \
"v_mfma_f32_16x16x32_bf16 v[72:75], a[144:147], a[4:7], v[72:75]\n" \
"s_mul_i32 s60, s60, 49152\n" \
"s_add_u32 s42, s60, 21004288\n" \
"v_mfma_f32_16x16x32_bf16 v[80:83], a[144:147], a[8:11], v[80:83]\n" \
"ds_read_b64_tr_b16 a[182:183], v238 offset:47104\n" \
"s_add_u32 s60, s98, s91\n" \
"s_mul_i32 s60, s60, 49152\n" \
"v_mfma_f32_16x16x32_bf16 v[88:91], a[144:147], a[12:15], v[88:91]\n" \
"buffer_load_dwordx4 a[108:111], v234, s[64:67], s22 offen offset:1024 sc1\n" \
"v_mfma_f32_16x16x32_bf16 v[92:95], a[148:151], a[12:15], v[92:95]\n" \
"ds_read_b64_tr_b16 a[184:185], v239 offset:45056\n" \
"v_mfma_f32_16x16x32_bf16 v[84:87], a[148:151], a[8:11], v[84:87]\n" \
"ds_read_b64_tr_b16 a[186:187], v239 offset:47104\n" \
"s_add_u32 s43, s60, 20971520\n" \
"s_add_u32 s44, s43, 16384\n" \
"v_mfma_f32_16x16x32_bf16 v[76:79], a[148:151], a[4:7], v[76:79]\n" \
"buffer_load_dwordx4 a[112:115], v233, s[64:67], s23 offen offset:0 sc1\n" \
"v_mfma_f32_16x16x32_bf16 v[68:71], a[148:151], a[0:3], v[68:71]\n" \
"v_readlane_b32 s20, v241, 44\n" \
"ds_read_b64_tr_b16 a[188:189], v240 offset:45056\n" \
"v_mfma_f32_16x16x32_bf16 v[96:99], a[152:155], a[0:3], v[96:99]\n" \
"ds_read_b64_tr_b16 a[190:191], v240 offset:47104\n" \
"v_mfma_f32_16x16x32_bf16 v[104:107], a[152:155], a[4:7], v[104:107]\n" \
"s_add_u32 s45, s43, 32768\n" \
"s_mov_b32 s47, 0\n" \
"v_mfma_f32_16x16x32_bf16 v[112:115], a[152:155], a[8:11], v[112:115]\n" \
"buffer_load_dwordx4 a[116:119], v233, s[64:67], s23 offen offset:1024 sc1\n" \
"v_mfma_f32_16x16x32_bf16 v[120:123], a[152:155], a[12:15], v[120:123]\n" \
"v_mfma_f32_16x16x32_bf16 v[124:127], a[156:159], a[12:15], v[124:127]\n" \
"s_lshl_b32 s60, s91, 7\n" \
"s_add_u32 s46, s60, s99\n" \
"v_mfma_f32_16x16x32_bf16 v[116:119], a[156:159], a[8:11], v[116:119]\n" \
"v_mfma_f32_16x16x32_bf16 v[108:111], a[156:159], a[4:7], v[108:111]\n" \
"v_accvgpr_read_b32 v253, a240\n" \
"v_mfma_f32_16x16x32_bf16 v[100:103], a[156:159], a[0:3], v[100:103]\n" \
"s_waitcnt vmcnt(62) lgkmcnt(0)\n" \
"v_mfma_f32_16x16x32_bf16 v[128:131], a[160:163], a[16:19], 0\n" \
"ds_read_b64_tr_b16 a[128:129], v237 offset:49152\n" \
"ds_read_b64_tr_b16 a[130:131], v237 offset:51200\n" \
"v_cvt_pk_bf16_f32 v16, v16, v17\n" \
"v_cvt_pk_bf16_f32 v17, v18, v19\n" \
"v_mfma_f32_16x16x32_bf16 v[136:139], a[160:163], a[20:23], 0\n" \
"ds_read_b64_tr_b16 a[132:133], v238 offset:49152\n" \
"v_cvt_pk_bf16_f32 v18, v20, v21\n" \
"v_mfma_f32_16x16x32_bf16 v[140:143], a[164:167], a[20:23], 0\n" \
"ds_read_b64_tr_b16 a[134:135], v238 offset:51200\n" \
"v_cvt_pk_bf16_f32 v19, v22, v23\n" \
"v_mfma_f32_16x16x32_bf16 v[132:135], a[164:167], a[16:19], 0\n" \
"ds_read_b64_tr_b16 a[136:137], v239 offset:49152\n" \
"buffer_store_dwordx4 v[16:19], v232, s[64:67], s41 offen offset:0 sc1\n" \
"v_mfma_f32_16x16x32_bf16 v[144:147], a[168:171], a[16:19], 0\n" \
"ds_read_b64_tr_b16 a[138:139], v239 offset:51200\n" \
"v_cvt_pk_bf16_f32 v24, v24, v25\n" \
"v_cvt_pk_bf16_f32 v25, v26, v27\n" \
"v_mfma_f32_16x16x32_bf16 v[152:155], a[168:171], a[20:23], 0\n" \
"ds_read_b64_tr_b16 a[140:141], v240 offset:49152\n" \
"v_cvt_pk_bf16_f32 v26, v28, v29\n" \
"v_mfma_f32_16x16x32_bf16 v[156:159], a[172:175], a[20:23], 0\n" \
"ds_read_b64_tr_b16 a[142:143], v240 offset:51200\n" \
"v_cvt_pk_bf16_f32 v27, v30, v31\n" \
"v_mfma_f32_16x16x32_bf16 v[148:151], a[172:175], a[16:19], 0\n" \
"ds_read_b64_tr_b16 a[144:145], v237 offset:53248\n" \
"buffer_store_dwordx4 v[24:27], v232, s[64:67], s41 offen offset:2048 sc1\n" \
"v_mfma_f32_16x16x32_bf16 v[128:131], a[176:179], a[24:27], v[128:131]\n" \
"ds_read_b64_tr_b16 a[146:147], v237 offset:55296\n" \
"v_cvt_pk_bf16_f32 v48, v48, v49\n" \
"v_cvt_pk_bf16_f32 v49, v50, v51\n" \
"v_mfma_f32_16x16x32_bf16 v[136:139], a[176:179], a[28:31], v[136:139]\n" \
"ds_read_b64_tr_b16 a[148:149], v238 offset:53248\n" \
"v_cvt_pk_bf16_f32 v50, v52, v53\n" \
"v_mfma_f32_16x16x32_bf16 v[140:143], a[180:183], a[28:31], v[140:143]\n" \
"ds_read_b64_tr_b16 a[150:151], v238 offset:55296\n" \
"v_cvt_pk_bf16_f32 v51, v54, v55\n" \
"v_mfma_f32_16x16x32_bf16 v[132:135], a[180:183], a[24:27], v[132:135]\n" \
"ds_read_b64_tr_b16 a[152:153], v239 offset:53248\n" \
"buffer_store_dwordx4 v[48:51], v232, s[64:67], s41 offen offset:1024 sc1\n" \
"v_mfma_f32_16x16x32_bf16 v[144:147], a[184:187], a[24:27], v[144:147]\n" \
"ds_read_b64_tr_b16 a[154:155], v239 offset:55296\n" \
"v_cvt_pk_bf16_f32 v56, v56, v57\n" \
"v_cvt_pk_bf16_f32 v57, v58, v59\n" \
"v_mfma_f32_16x16x32_bf16 v[152:155], a[184:187], a[28:31], v[152:155]\n" \
"ds_read_b64_tr_b16 a[156:157], v240 offset:53248\n" \
"v_cvt_pk_bf16_f32 v58, v60, v61\n" \
"v_mfma_f32_16x16x32_bf16 v[156:159], a[188:191], a[28:31], v[156:159]\n" \
"ds_read_b64_tr_b16 a[158:159], v240 offset:55296\n" \
"v_cvt_pk_bf16_f32 v59, v62, v63\n" \
"v_mfma_f32_16x16x32_bf16 v[148:151], a[188:191], a[24:27], v[148:151]\n" \
"buffer_store_dwordx4 v[56:59], v232, s[64:67], s41 offen offset:3072 sc1\n" \
"s_waitcnt vmcnt(54) lgkmcnt(8)\n" \
"v_mfma_f32_16x16x32_bf16 v[128:131], a[128:131], a[64:67], v[128:131]\n" \
"ds_read_b64_tr_b16 a[160:161], v237 offset:57344\n" \
"ds_read_b64_tr_b16 a[162:163], v237 offset:59392\n" \
"v_cvt_pk_bf16_f32 v64, v64, v65\n" \
"v_cvt_pk_bf16_f32 v65, v66, v67\n" \
"v_mfma_f32_16x16x32_bf16 v[136:139], a[128:131], a[68:71], v[136:139]\n" \
"ds_read_b64_tr_b16 a[164:165], v238 offset:57344\n" \
"v_cvt_pk_bf16_f32 v66, v68, v69\n" \
"v_mfma_f32_16x16x32_bf16 v[140:143], a[132:135], a[68:71], v[140:143]\n" \
"ds_read_b64_tr_b16 a[166:167], v238 offset:59392\n" \
"v_cvt_pk_bf16_f32 v67, v70, v71\n" \
"v_mfma_f32_16x16x32_bf16 v[132:135], a[132:135], a[64:67], v[132:135]\n" \
"ds_read_b64_tr_b16 a[168:169], v239 offset:57344\n" \
"buffer_store_dwordx4 v[64:67], v232, s[64:67], s40 offen offset:0 sc1\n" \
"v_mfma_f32_16x16x32_bf16 v[144:147], a[136:139], a[64:67], v[144:147]\n" \
"ds_read_b64_tr_b16 a[170:171], v239 offset:59392\n" \
"v_cvt_pk_bf16_f32 v72, v72, v73\n" \
"v_cvt_pk_bf16_f32 v73, v74, v75\n" \
"v_mfma_f32_16x16x32_bf16 v[152:155], a[136:139], a[68:71], v[152:155]\n" \
"ds_read_b64_tr_b16 a[172:173], v240 offset:57344\n" \
"v_cvt_pk_bf16_f32 v74, v76, v77\n" \
"v_mfma_f32_16x16x32_bf16 v[156:159], a[140:143], a[68:71], v[156:159]\n" \
"ds_read_b64_tr_b16 a[174:175], v240 offset:59392\n" \
"v_cvt_pk_bf16_f32 v75, v78, v79\n" \
"v_mfma_f32_16x16x32_bf16 v[148:151], a[140:143], a[64:67], v[148:151]\n" \
"ds_read_b64_tr_b16 a[176:177], v237 offset:61440\n" \
"buffer_store_dwordx4 v[72:75], v232, s[64:67], s40 offen offset:2048 sc1\n" \
"s_waitcnt lgkmcnt(9)\n" \
"v_mfma_f32_16x16x32_bf16 v[128:131], a[144:147], a[72:75], v[128:131]\n" \
"ds_read_b64_tr_b16 a[178:179], v237 offset:63488\n" \
"v_cvt_pk_bf16_f32 v96, v96, v97\n" \
"v_cvt_pk_bf16_f32 v97, v98, v99\n" \
"v_mfma_f32_16x16x32_bf16 v[136:139], a[144:147], a[76:79], v[136:139]\n" \
"ds_read_b64_tr_b16 a[180:181], v238 offset:61440\n" \
"v_cvt_pk_bf16_f32 v98, v100, v101\n" \
"v_mfma_f32_16x16x32_bf16 v[140:143], a[148:151], a[76:79], v[140:143]\n" \
"ds_read_b64_tr_b16 a[182:183], v238 offset:63488\n" \
"v_cvt_pk_bf16_f32 v99, v102, v103\n" \
"v_mfma_f32_16x16x32_bf16 v[132:135], a[148:151], a[72:75], v[132:135]\n" \
"ds_read_b64_tr_b16 a[184:185], v239 offset:61440\n" \
"buffer_store_dwordx4 v[96:99], v232, s[64:67], s40 offen offset:1024 sc1\n" \
"v_mfma_f32_16x16x32_bf16 v[144:147], a[152:155], a[72:75], v[144:147]\n" \
"ds_read_b64_tr_b16 a[186:187], v239 offset:63488\n" \
"v_cvt_pk_bf16_f32 v104, v104, v105\n" \
"v_cvt_pk_bf16_f32 v105, v106, v107\n" \
"v_mfma_f32_16x16x32_bf16 v[152:155], a[152:155], a[76:79], v[152:155]\n" \
"ds_read_b64_tr_b16 a[188:189], v240 offset:61440\n" \
"v_cvt_pk_bf16_f32 v106, v108, v109\n" \
"v_mfma_f32_16x16x32_bf16 v[156:159], a[156:159], a[76:79], v[156:159]\n" \
"ds_read_b64_tr_b16 a[190:191], v240 offset:63488\n" \
"v_cvt_pk_bf16_f32 v107, v110, v111\n" \
"v_mfma_f32_16x16x32_bf16 v[148:151], a[156:159], a[72:75], v[148:151]\n" \
"buffer_store_dwordx4 v[104:107], v232, s[64:67], s40 offen offset:3072 sc1\n" \
"s_waitcnt vmcnt(54) lgkmcnt(0)\n" \
"s_barrier\n" \
"v_mfma_f32_16x16x32_bf16 v[128:131], a[160:163], a[80:83], v[128:131]\n" \
"ds_read_b64_tr_b16 a[128:129], v228 offset:0\n" \
"ds_read_b64_tr_b16 a[130:131], v228 offset:2048\n" \
"v_cvt_pk_bf16_f32 v80, v80, v81\n" \
"v_cvt_pk_bf16_f32 v81, v82, v83\n" \
"v_mfma_f32_16x16x32_bf16 v[136:139], a[160:163], a[84:87], v[136:139]\n" \
"ds_read_b64_tr_b16 a[132:133], v229 offset:0\n" \
"v_cvt_pk_bf16_f32 v82, v84, v85\n" \
"v_mfma_f32_16x16x32_bf16 v[140:143], a[164:167], a[84:87], v[140:143]\n" \
"ds_read_b64_tr_b16 a[134:135], v229 offset:2048\n" \
"v_cvt_pk_bf16_f32 v83, v86, v87\n" \
"v_mfma_f32_16x16x32_bf16 v[132:135], a[164:167], a[80:83], v[132:135]\n" \
"ds_read_b64_tr_b16 a[136:137], v230 offset:0\n" \
"buffer_store_dwordx4 v[80:83], v232, s[64:67], s42 offen offset:0 sc1\n" \
"v_mfma_f32_16x16x32_bf16 v[144:147], a[168:171], a[80:83], v[144:147]\n" \
"ds_read_b64_tr_b16 a[138:139], v230 offset:2048\n" \
"v_cvt_pk_bf16_f32 v88, v88, v89\n" \
"v_cvt_pk_bf16_f32 v89, v90, v91\n" \
"v_mfma_f32_16x16x32_bf16 v[152:155], a[168:171], a[84:87], v[152:155]\n" \
"ds_read_b64_tr_b16 a[140:141], v231 offset:0\n" \
"v_cvt_pk_bf16_f32 v90, v92, v93\n" \
"v_mfma_f32_16x16x32_bf16 v[156:159], a[172:175], a[84:87], v[156:159]\n" \
"ds_read_b64_tr_b16 a[142:143], v231 offset:2048\n" \
"v_cvt_pk_bf16_f32 v91, v94, v95\n" \
"v_mfma_f32_16x16x32_bf16 v[148:151], a[172:175], a[80:83], v[148:151]\n" \
"ds_read_b64_tr_b16 a[144:145], v228 offset:4096\n" \
"buffer_store_dwordx4 v[88:91], v232, s[64:67], s42 offen offset:2048 sc1\n" \
"v_mfma_f32_16x16x32_bf16 v[128:131], a[176:179], a[88:91], v[128:131]\n" \
"ds_read_b64_tr_b16 a[146:147], v228 offset:6144\n" \
"v_cvt_pk_bf16_f32 v112, v112, v113\n" \
"v_cvt_pk_bf16_f32 v113, v114, v115\n" \
"v_mfma_f32_16x16x32_bf16 v[136:139], a[176:179], a[92:95], v[136:139]\n" \
"ds_read_b64_tr_b16 a[148:149], v229 offset:4096\n" \
"v_cvt_pk_bf16_f32 v114, v116, v117\n" \
"v_mfma_f32_16x16x32_bf16 v[140:143], a[180:183], a[92:95], v[140:143]\n" \
"ds_read_b64_tr_b16 a[150:151], v229 offset:6144\n" \
"v_cvt_pk_bf16_f32 v115, v118, v119\n" \
"v_mfma_f32_16x16x32_bf16 v[132:135], a[180:183], a[88:91], v[132:135]\n" \
"ds_read_b64_tr_b16 a[152:153], v230 offset:4096\n" \
"buffer_store_dwordx4 v[112:115], v232, s[64:67], s42 offen offset:1024 sc1\n" \
"v_mfma_f32_16x16x32_bf16 v[144:147], a[184:187], a[88:91], v[144:147]\n" \
"ds_read_b64_tr_b16 a[154:155], v230 offset:6144\n" \
"v_cvt_pk_bf16_f32 v120, v120, v121\n" \
"v_cvt_pk_bf16_f32 v121, v122, v123\n" \
"v_mfma_f32_16x16x32_bf16 v[152:155], a[184:187], a[92:95], v[152:155]\n" \
"ds_read_b64_tr_b16 a[156:157], v231 offset:4096\n" \
"v_cvt_pk_bf16_f32 v122, v124, v125\n" \
"v_mfma_f32_16x16x32_bf16 v[156:159], a[188:191], a[92:95], v[156:159]\n" \
"ds_read_b64_tr_b16 a[158:159], v231 offset:6144\n" \
"v_cvt_pk_bf16_f32 v123, v126, v127\n" \
"v_mfma_f32_16x16x32_bf16 v[148:151], a[188:191], a[88:91], v[148:151]\n" \
"buffer_store_dwordx4 v[120:123], v232, s[64:67], s42 offen offset:3072 sc1\n" \
"s_waitcnt vmcnt(54) lgkmcnt(8)\n" \
"v_mfma_f32_16x16x32_bf16 v[128:131], a[128:131], a[32:35], v[128:131]\n" \
"ds_read_b64_tr_b16 a[160:161], v228 offset:8192\n" \
"ds_read_b64_tr_b16 a[162:163], v228 offset:10240\n" \
"v_mfma_f32_16x16x32_bf16 v[136:139], a[128:131], a[36:39], v[136:139]\n" \
"buffer_load_dwordx4 a[120:123], v234, s[64:67], s23 offen offset:0 sc1\n" \
"v_mfma_f32_16x16x32_bf16 v[140:143], a[132:135], a[36:39], v[140:143]\n" \
"ds_read_b64_tr_b16 a[164:165], v229 offset:8192\n" \
"v_mfma_f32_16x16x32_bf16 v[132:135], a[132:135], a[32:35], v[132:135]\n" \
"ds_read_b64_tr_b16 a[166:167], v229 offset:10240\n" \
"v_mfma_f32_16x16x32_bf16 v[144:147], a[136:139], a[32:35], v[144:147]\n" \
"ds_read_b64_tr_b16 a[168:169], v230 offset:8192\n" \
"v_mfma_f32_16x16x32_bf16 v[152:155], a[136:139], a[36:39], v[152:155]\n" \
"ds_read_b64_tr_b16 a[170:171], v230 offset:10240\n" \
"ds_read_b64_tr_b16 a[172:173], v231 offset:8192\n" \
"v_mfma_f32_16x16x32_bf16 v[156:159], a[140:143], a[36:39], v[156:159]\n" \
"ds_read_b64_tr_b16 a[174:175], v231 offset:10240\n" \
"v_mfma_f32_16x16x32_bf16 v[148:151], a[140:143], a[32:35], v[148:151]\n" \
"ds_read_b64_tr_b16 a[176:177], v228 offset:12288\n" \
"s_waitcnt lgkmcnt(9)\n" \
"v_mfma_f32_16x16x32_bf16 v[128:131], a[144:147], a[40:43], v[128:131]\n" \
"buffer_load_dwordx4 a[124:127], v234, s[64:67], s23 offen offset:1024 sc1\n" \
"v_mfma_f32_16x16x32_bf16 v[136:139], a[144:147], a[44:47], v[136:139]\n" \
"ds_read_b64_tr_b16 a[178:179], v228 offset:14336\n" \
"v_mfma_f32_16x16x32_bf16 v[140:143], a[148:151], a[44:47], v[140:143]\n" \
"Lpadh3_%=:\n" \
"s_cbranch_vccnz Lprh3_%=\n" \
"ds_read_b64_tr_b16 a[180:181], v229 offset:12288\n" \
"ds_read_b64_tr_b16 a[182:183], v229 offset:14336\n" \
"v_mfma_f32_16x16x32_bf16 v[132:135], a[148:151], a[40:43], v[132:135]\n" \
"ds_read_b64_tr_b16 a[184:185], v230 offset:12288\n" \
"v_mfma_f32_16x16x32_bf16 v[144:147], a[152:155], a[40:43], v[144:147]\n" \
"ds_read_b64_tr_b16 a[186:187], v230 offset:14336\n" \
"v_mfma_f32_16x16x32_bf16 v[152:155], a[152:155], a[44:47], v[152:155]\n" \
"ds_read_b64_tr_b16 a[188:189], v231 offset:12288\n" \
"v_mfma_f32_16x16x32_bf16 v[156:159], a[156:159], a[44:47], v[156:159]\n" \
"ds_read_b64_tr_b16 a[190:191], v231 offset:14336\n" \
"v_mfma_f32_16x16x32_bf16 v[148:151], a[156:159], a[40:43], v[148:151]\n" \
"s_waitcnt vmcnt(52) lgkmcnt(8)\n" \
"v_mfma_f32_16x16x32_bf16 v[128:131], a[160:163], a[48:51], v[128:131]\n" \
"ds_read_b64_tr_b16 a[128:129], v228 offset:16384\n" \
"ds_read_b64_tr_b16 a[130:131], v228 offset:18432\n" \
"v_mbcnt_lo_u32_b32 v118, -1, 0\n" \
"v_mbcnt_hi_u32_b32 v118, -1, v118\n" \
"v_mfma_f32_16x16x32_bf16 v[136:139], a[160:163], a[52:55], v[136:139]\n" \
"buffer_load_dwordx4 a[0:3], v233, s[64:67], s20 offen offset:0 sc1\n" \
"v_mov_b64 v[112:113], 0\n" \
"v_mfma_f32_16x16x32_bf16 v[140:143], a[164:167], a[52:55], v[140:143]\n" \
"v_readlane_b32 s21, v241, 45\n" \
"ds_read_b64_tr_b16 a[132:133], v229 offset:16384\n" \
"v_and_b32 v119, 15, v118\n" \
"v_mfma_f32_16x16x32_bf16 v[132:135], a[164:167], a[48:51], v[132:135]\n" \
"ds_read_b64_tr_b16 a[134:135], v229 offset:18432\n" \
"v_lshrrev_b32 v118, 4, v118\n" \
"v_mfma_f32_16x16x32_bf16 v[144:147], a[168:171], a[48:51], v[144:147]\n" \
"buffer_load_dwordx4 a[4:7], v233, s[64:67], s20 offen offset:1024 sc1\n" \
"v_lshlrev_b32 v118, 2, v118\n" \
"v_mfma_f32_16x16x32_bf16 v[152:155], a[168:171], a[52:55], v[152:155]\n" \
"ds_read_b64_tr_b16 a[136:137], v230 offset:16384\n" \
"ds_read_b64_tr_b16 a[138:139], v230 offset:18432\n" \
"v_sub_u32 v118, v119, v118\n" \
"v_mfma_f32_16x16x32_bf16 v[156:159], a[172:175], a[52:55], v[156:159]\n" \
"ds_read_b64_tr_b16 a[140:141], v231 offset:16384\n" \
"v_and_b32 v119, 1, v118\n" \
"v_mfma_f32_16x16x32_bf16 v[148:151], a[172:175], a[48:51], v[148:151]\n" \
"ds_read_b64_tr_b16 a[142:143], v231 offset:18432\n" \
"ds_read_b64_tr_b16 a[144:145], v228 offset:20480\n" \
"v_lshlrev_b32 v119, 4, v119\n" \
"s_waitcnt lgkmcnt(9)\n" \
"v_mfma_f32_16x16x32_bf16 v[128:131], a[176:179], a[56:59], v[128:131]\n" \
"buffer_load_dwordx4 a[8:11], v234, s[64:67], s20 offen offset:0 sc1\n" \
"v_mov_b32 v116, 0x3f80\n" \
"v_lshlrev_b32 v119, v119, v116\n" \
"v_mfma_f32_16x16x32_bf16 v[136:139], a[176:179], a[60:63], v[136:139]\n" \
"ds_read_b64_tr_b16 a[146:147], v228 offset:22528\n" \
"v_lshrrev_b32 v118, 1, v118\n" \
"v_mfma_f32_16x16x32_bf16 v[140:143], a[180:183], a[60:63], v[140:143]\n" \
"ds_read_b64_tr_b16 a[148:149], v229 offset:20480\n" \
"ds_read_b64_tr_b16 a[150:151], v229 offset:22528\n" \
"v_cmp_eq_u32 s[30:31], 0, v118\n" \
"v_mfma_f32_16x16x32_bf16 v[132:135], a[180:183], a[56:59], v[132:135]\n" \
"buffer_load_dwordx4 a[12:15], v234, s[64:67], s20 offen offset:1024 sc1\n" \
"v_cmp_eq_u32 s[28:29], 1, v118\n" \
"v_mfma_f32_16x16x32_bf16 v[144:147], a[184:187], a[56:59], v[144:147]\n" \
"ds_read_b64_tr_b16 a[152:153], v230 offset:20480\n" \
"v_mov_b64 v[116:117], 0\n" \
"v_mfma_f32_16x16x32_bf16 v[152:155], a[184:187], a[60:63], v[152:155]\n" \
"ds_read_b64_tr_b16 a[154:155], v230 offset:22528\n" \
"ds_read_b64_tr_b16 a[156:157], v231 offset:20480\n" \
"s_nop 0\n" \
"v_mfma_f32_16x16x32_bf16 v[156:159], a[188:191], a[60:63], v[156:159]\n" \
"ds_read_b64_tr_b16 a[158:159], v231 offset:22528\n" \
"v_cndmask_b32 v114, v112, v119, s[30:31]\n" \
"v_mfma_f32_16x16x32_bf16 v[148:151], a[188:191], a[56:59], v[148:151]\n" \
"v_cndmask_b32 v115, v112, v119, s[28:29]\n" \
"s_waitcnt vmcnt(44) lgkmcnt(8)\n" \
"v_mfma_f32_16x16x32_bf16 v[128:131], a[128:131], v[192:195], v[128:131]\n" \
"ds_read_b64_tr_b16 a[160:161], v228 offset:24576\n" \
"ds_read_b64_tr_b16 a[162:163], v228 offset:26624\n" \
"v_mfma_f32_16x16x32_bf16 v[136:139], a[128:131], v[196:199], v[136:139]\n" \
"buffer_load_dwordx4 a[16:19], v233, s[64:67], s21 offen offset:0 sc1\n" \
"v_mfma_f32_16x16x32_bf16 v[140:143], a[132:135], v[196:199], v[140:143]\n" \
"v_readlane_b32 s22, v241, 46\n" \
"ds_read_b64_tr_b16 a[164:165], v229 offset:24576\n" \
"v_mfma_f32_16x16x32_bf16 v[132:135], a[132:135], v[192:195], v[132:135]\n" \
"ds_read_b64_tr_b16 a[166:167], v229 offset:26624\n" \
"v_mfma_f32_16x16x32_bf16 v[144:147], a[136:139], v[192:195], v[144:147]\n" \
"ds_read_b64_tr_b16 a[168:169], v230 offset:24576\n" \
"v_mfma_f32_16x16x32_bf16 v[152:155], a[136:139], v[196:199], v[152:155]\n" \
"buffer_load_dwordx4 a[20:23], v233, s[64:67], s21 offen offset:1024 sc1\n" \
"v_mfma_f32_16x16x32_bf16 v[156:159], a[140:143], v[196:199], v[156:159]\n" \
"ds_read_b64_tr_b16 a[170:171], v230 offset:26624\n" \
"ds_read_b64_tr_b16 a[172:173], v231 offset:24576\n" \
"v_mfma_f32_16x16x32_bf16 v[148:151], a[140:143], v[192:195], v[148:151]\n" \
"ds_read_b64_tr_b16 a[174:175], v231 offset:26624\n" \
"s_waitcnt lgkmcnt(8)\n" \
"v_mfma_f32_16x16x32_bf16 v[128:131], a[144:147], v[200:203], v[128:131]\n" \
"ds_read_b64_tr_b16 a[176:177], v228 offset:28672\n" \
"ds_read_b64_tr_b16 a[178:179], v228 offset:30720\n" \
"v_mfma_f32_16x16x32_bf16 v[136:139], a[144:147], v[204:207], v[136:139]\n" \
"ds_read_b64_tr_b16 a[180:181], v229 offset:28672\n" \
"v_mfma_f32_16x16x32_bf16 v[140:143], a[148:151], v[204:207], v[140:143]\n" \
"buffer_load_dwordx4 a[24:27], v234, s[64:67], s21 offen offset:0 sc1\n" \
"v_mfma_f32_16x16x32_bf16 v[132:135], a[148:151], v[200:203], v[132:135]\n" \
"ds_read_b64_tr_b16 a[182:183], v229 offset:30720\n" \
"v_mfma_f32_16x16x32_bf16 v[144:147], a[152:155], v[200:203], v[144:147]\n" \
"ds_read_b64_tr_b16 a[184:185], v230 offset:28672\n" \
"ds_read_b64_tr_b16 a[186:187], v230 offset:30720\n" \
"v_mfma_f32_16x16x32_bf16 v[152:155], a[152:155], v[204:207], v[152:155]\n" \
"ds_read_b64_tr_b16 a[188:189], v231 offset:28672\n" \
"v_mfma_f32_16x16x32_bf16 v[156:159], a[156:159], v[204:207], v[156:159]\n" \
"ds_read_b64_tr_b16 a[190:191], v231 offset:30720\n" \
"v_mfma_f32_16x16x32_bf16 v[148:151], a[156:159], v[200:203], v[148:151]\n" \
"s_waitcnt vmcnt(43) lgkmcnt(0)\n" \
"s_barrier\n" \
"v_mfma_f32_16x16x32_bf16 v[128:131], a[160:163], v[208:211], v[128:131]\n" \
"ds_read_b64_tr_b16 a[128:129], v224 offset:0\n" \
"ds_read_b64_tr_b16 a[130:131], v224 offset:2048\n" \
"v_mfma_f32_16x16x32_bf16 v[136:139], a[160:163], v[212:215], v[136:139]\n" \
"buffer_load_dwordx4 a[28:31], v234, s[64:67], s21 offen offset:1024 sc1\n" \
"ds_read_b64_tr_b16 a[132:133], v225 offset:0\n" \
"v_mfma_f32_16x16x32_bf16 v[140:143], a[164:167], v[212:215], v[140:143]\n" \
"s_add_u32 m0, s80, 0\n" \
"ds_read_b64_tr_b16 a[134:135], v225 offset:2048\n" \
"v_mfma_f32_16x16x32_bf16 v[132:135], a[164:167], v[208:211], v[132:135]\n" \
"buffer_load_dwordx4 v235, s[68:71], s24 offen lds\n" \
"ds_read_b64_tr_b16 a[136:137], v226 offset:0\n" \
"v_mfma_f32_16x16x32_bf16 v[144:147], a[168:171], v[208:211], v[144:147]\n" \
"ds_read_b64_tr_b16 a[138:139], v226 offset:2048\n" \
"v_mfma_f32_16x16x32_bf16 v[152:155], a[168:171], v[212:215], v[152:155]\n" \
"s_add_u32 m0, s80, 4096\n" \
"ds_read_b64_tr_b16 a[140:141], v227 offset:0\n" \
"v_mfma_f32_16x16x32_bf16 v[156:159], a[172:175], v[212:215], v[156:159]\n" \
"buffer_load_dwordx4 v236, s[68:71], s24 offen lds\n" \
"ds_read_b64_tr_b16 a[142:143], v227 offset:2048\n" \
"v_mfma_f32_16x16x32_bf16 v[148:151], a[172:175], v[208:211], v[148:151]\n" \
"ds_read_b64_tr_b16 a[144:145], v224 offset:4096\n" \
"v_mfma_f32_16x16x32_bf16 v[128:131], a[176:179], v[216:219], v[128:131]\n" \
"ds_read_b64_tr_b16 a[146:147], v224 offset:6144\n" \
"v_mfma_f32_16x16x32_bf16 v[136:139], a[176:179], v[220:223], v[136:139]\n" \
"buffer_load_dwordx4 a[64:67], v233, s[64:67], s22 offen offset:0 sc1\n" \
"ds_read_b64_tr_b16 a[148:149], v225 offset:4096\n" \
"v_mfma_f32_16x16x32_bf16 v[140:143], a[180:183], v[220:223], v[140:143]\n" \
"v_readlane_b32 s23, v241, 47\n" \
"ds_read_b64_tr_b16 a[150:151], v225 offset:6144\n" \
"v_mfma_f32_16x16x32_bf16 v[132:135], a[180:183], v[216:219], v[132:135]\n" \
"ds_read_b64_tr_b16 a[152:153], v226 offset:4096\n" \
"v_mfma_f32_16x16x32_bf16 v[144:147], a[184:187], v[216:219], v[144:147]\n" \
"v_mov_b32 v246, s72\n" \
"ds_read_b64_tr_b16 a[154:155], v226 offset:6144\n" \
"v_mfma_f32_16x16x32_bf16 v[152:155], a[184:187], v[220:223], v[152:155]\n" \
"v_mov_b32 v247, s73\n" \
"ds_read_b64_tr_b16 a[156:157], v227 offset:4096\n" \
"v_mfma_f32_16x16x32_bf16 v[156:159], a[188:191], v[220:223], v[156:159]\n" \
"s_waitcnt vmcnt(13)\n" \
"ds_read_b64_tr_b16 a[158:159], v227 offset:6144\n" \
"v_mfma_f32_16x16x32_bf16 v[148:151], a[188:191], v[216:219], v[148:151]\n" \
"s_mov_b64 exec, 1\n" \
"buffer_store_dwordx2 v[246:247], off, s[64:67], s46 sc1\n" \
"s_mov_b64 exec, -1\n" \
"s_waitcnt vmcnt(44) lgkmcnt(8)\n" \
"v_mfma_f32_16x16x32_bf16 v[128:131], a[128:131], v[160:163], v[128:131]\n" \
"ds_read_b64_tr_b16 a[160:161], v224 offset:8192\n" \
"ds_read_b64_tr_b16 a[162:163], v224 offset:10240\n" \
"v_mfma_f32_16x16x32_bf16 v[136:139], a[128:131], v[164:167], v[136:139]\n" \
"buffer_load_dwordx4 a[68:71], v233, s[64:67], s22 offen offset:1024 sc1\n" \
"v_mfma_f32_16x16x32_bf16 v[140:143], a[132:135], v[164:167], v[140:143]\n" \
"ds_read_b64_tr_b16 a[164:165], v225 offset:8192\n" \
"v_mfma_f32_16x16x32_bf16 v[132:135], a[132:135], v[160:163], v[132:135]\n" \
"ds_read_b64_tr_b16 a[166:167], v225 offset:10240\n" \
"v_mfma_f32_16x16x32_bf16 v[144:147], a[136:139], v[160:163], v[144:147]\n" \
"ds_read_b64_tr_b16 a[168:169], v226 offset:8192\n" \
"ds_read_b64_tr_b16 a[170:171], v226 offset:10240\n" \
"v_mfma_f32_16x16x32_bf16 v[152:155], a[136:139], v[164:167], v[152:155]\n" \
"buffer_load_dwordx4 a[72:75], v234, s[64:67], s22 offen offset:0 sc1\n" \
"v_mfma_f32_16x16x32_bf16 v[156:159], a[140:143], v[164:167], v[156:159]\n" \
"ds_read_b64_tr_b16 a[172:173], v227 offset:8192\n" \
"v_mfma_f32_16x16x32_bf16 v[148:151], a[140:143], v[160:163], v[148:151]\n" \
"ds_read_b64_tr_b16 a[174:175], v227 offset:10240\n" \
"s_waitcnt lgkmcnt(8)\n" \
"v_mfma_f32_16x16x32_bf16 v[128:131], a[144:147], v[168:171], v[128:131]\n" \
"ds_read_b64_tr_b16 a[176:177], v224 offset:12288\n" \
"ds_read_b64_tr_b16 a[178:179], v224 offset:14336\n" \
"v_mfma_f32_16x16x32_bf16 v[136:139], a[144:147], v[172:175], v[136:139]\n" \
"ds_read_b64_tr_b16 a[180:181], v225 offset:12288\n" \
"v_mfma_f32_16x16x32_bf16 v[140:143], a[148:151], v[172:175], v[140:143]\n" \
"buffer_load_dwordx4 a[76:79], v234, s[64:67], s22 offen offset:1024 sc1\n" \
"v_mfma_f32_16x16x32_bf16 v[132:135], a[148:151], v[168:171], v[132:135]\n" \
"ds_read_b64_tr_b16 a[182:183], v225 offset:14336\n" \
"v_mfma_f32_16x16x32_bf16 v[144:147], a[152:155], v[168:171], v[144:147]\n" \
"ds_read_b64_tr_b16 a[184:185], v226 offset:12288\n" \
"ds_read_b64_tr_b16 a[186:187], v226 offset:14336\n" \
"v_mfma_f32_16x16x32_bf16 v[152:155], a[152:155], v[172:175], v[152:155]\n" \
"ds_read_b64_tr_b16 a[188:189], v227 offset:12288\n" \
"v_mfma_f32_16x16x32_bf16 v[156:159], a[156:159], v[172:175], v[156:159]\n" \
"ds_read_b64_tr_b16 a[190:191], v227 offset:14336\n" \
"v_mfma_f32_16x16x32_bf16 v[148:151], a[156:159], v[168:171], v[148:151]\n" \
"s_waitcnt vmcnt(43) lgkmcnt(8)\n" \
"v_mfma_f32_16x16x32_bf16 v[128:131], a[160:163], v[176:179], v[128:131]\n" \
"ds_read_b64_tr_b16 a[128:129], v224 offset:16384\n" \
"ds_read_b64_tr_b16 a[130:131], v224 offset:18432\n" \
"v_mfma_f32_16x16x32_bf16 v[136:139], a[160:163], v[180:183], v[136:139]\n" \
"buffer_load_dwordx2 v[250:251], v253, s[64:67], s99 offen sc1\n" \
"v_mfma_f32_16x16x32_bf16 v[140:143], a[164:167], v[180:183], v[140:143]\n" \
"ds_read_b64_tr_b16 a[132:133], v225 offset:16384\n" \
"ds_read_b64_tr_b16 a[134:135], v225 offset:18432\n" \
"v_mfma_f32_16x16x32_bf16 v[132:135], a[164:167], v[176:179], v[132:135]\n" \
"ds_read_b64_tr_b16 a[136:137], v226 offset:16384\n" \
"v_mfma_f32_16x16x32_bf16 v[144:147], a[168:171], v[176:179], v[144:147]\n" \
"buffer_load_dwordx4 a[80:83], v233, s[64:67], s23 offen offset:0 sc1\n" \
"v_mfma_f32_16x16x32_bf16 v[152:155], a[168:171], v[180:183], v[152:155]\n" \
"v_readlane_b32 s20, v241, 48\n" \
"ds_read_b64_tr_b16 a[138:139], v226 offset:18432\n" \
"v_mfma_f32_16x16x32_bf16 v[156:159], a[172:175], v[180:183], v[156:159]\n" \
"ds_read_b64_tr_b16 a[140:141], v227 offset:16384\n" \
"v_mfma_f32_16x16x32_bf16 v[148:151], a[172:175], v[176:179], v[148:151]\n" \
"ds_read_b64_tr_b16 a[142:143], v227 offset:18432\n" \
"ds_read_b64_tr_b16 a[144:145], v224 offset:20480\n" \
"s_waitcnt lgkmcnt(9)\n" \
"v_mfma_f32_16x16x32_bf16 v[128:131], a[176:179], v[184:187], v[128:131]\n" \
"buffer_load_dwordx4 a[84:87], v233, s[64:67], s23 offen offset:1024 sc1\n" \
"v_mfma_f32_16x16x32_bf16 v[136:139], a[176:179], v[188:191], v[136:139]\n" \
"ds_read_b64_tr_b16 a[146:147], v224 offset:22528\n" \
"v_mfma_f32_16x16x32_bf16 v[140:143], a[180:183], v[188:191], v[140:143]\n" \
"ds_read_b64_tr_b16 a[148:149], v225 offset:20480\n" \
"ds_read_b64_tr_b16 a[150:151], v225 offset:22528\n" \
"v_mfma_f32_16x16x32_bf16 v[132:135], a[180:183], v[184:187], v[132:135]\n" \
"buffer_load_dwordx4 a[88:91], v234, s[64:67], s23 offen offset:0 sc1\n" \
"v_mfma_f32_16x16x32_bf16 v[144:147], a[184:187], v[184:187], v[144:147]\n" \
"ds_read_b64_tr_b16 a[152:153], v226 offset:20480\n" \
"v_mfma_f32_16x16x32_bf16 v[152:155], a[184:187], v[188:191], v[152:155]\n" \
"ds_read_b64_tr_b16 a[154:155], v226 offset:22528\n" \
"ds_read_b64_tr_b16 a[156:157], v227 offset:20480\n" \
"v_mfma_f32_16x16x32_bf16 v[156:159], a[188:191], v[188:191], v[156:159]\n" \
"ds_read_b64_tr_b16 a[158:159], v227 offset:22528\n" \
"v_mfma_f32_16x16x32_bf16 v[148:151], a[188:191], v[184:187], v[148:151]\n" \
"s_waitcnt vmcnt(35) lgkmcnt(8)\n" \
"v_mfma_f32_16x16x32_bf16 v[128:131], a[128:131], a[96:99], v[128:131]\n" \
"ds_read_b64_tr_b16 a[160:161], v224 offset:24576\n" \
"ds_read_b64_tr_b16 a[162:163], v224 offset:26624\n" \
"v_mfma_f32_16x16x32_bf16 v[136:139], a[128:131], a[100:103], v[136:139]\n" \
"buffer_load_dwordx4 a[92:95], v234, s[64:67], s23 offen offset:1024 sc1\n" \
"v_mfma_f32_16x16x32_bf16 v[140:143], a[132:135], a[100:103], v[140:143]\n" \
"ds_read_b64_tr_b16 a[164:165], v225 offset:24576\n" \
"ds_read_b64_tr_b16 a[166:167], v225 offset:26624\n" \
"v_mfma_f32_16x16x32_bf16 v[132:135], a[132:135], a[96:99], v[132:135]\n" \
"ds_read_b64_tr_b16 a[168:169], v226 offset:24576\n" \
"ds_read_b64_tr_b16 a[170:171], v226 offset:26624\n" \
"v_mfma_f32_16x16x32_bf16 v[144:147], a[136:139], a[96:99], v[144:147]\n" \
"buffer_load_dwordx4 a[32:35], v233, s[64:67], s20 offen offset:0 sc1\n" \
"v_mfma_f32_16x16x32_bf16 v[152:155], a[136:139], a[100:103], v[152:155]\n" \
"ds_read_b64_tr_b16 a[172:173], v227 offset:24576\n" \
"ds_read_b64_tr_b16 a[174:175], v227 offset:26624\n" \
"v_mfma_f32_16x16x32_bf16 v[156:159], a[140:143], a[100:103], v[156:159]\n" \
"ds_read_b64_tr_b16 a[176:177], v224 offset:28672\n" \
"v_mfma_f32_16x16x32_bf16 v[148:151], a[140:143], a[96:99], v[148:151]\n" \
"ds_read_b64_tr_b16 a[178:179], v224 offset:30720\n" \
"ds_read_b64_tr_b16 a[180:181], v225 offset:28672\n" \
"s_waitcnt lgkmcnt(11)\n" \
"v_mfma_f32_16x16x32_bf16 v[128:131], a[144:147], a[104:107], v[128:131]\n" \
"buffer_load_dwordx4 a[36:39], v233, s[64:67], s20 offen offset:1024 sc1\n" \
"v_mfma_f32_16x16x32_bf16 v[136:139], a[144:147], a[108:111], v[136:139]\n" \
"ds_read_b64_tr_b16 a[182:183], v225 offset:30720\n" \
"ds_read_b64_tr_b16 a[184:185], v226 offset:28672\n" \
"v_mfma_f32_16x16x32_bf16 v[140:143], a[148:151], a[108:111], v[140:143]\n" \
"ds_read_b64_tr_b16 a[186:187], v226 offset:30720\n" \
"ds_read_b64_tr_b16 a[188:189], v227 offset:28672\n" \
"v_mfma_f32_16x16x32_bf16 v[132:135], a[148:151], a[104:107], v[132:135]\n" \
"ds_read_b64_tr_b16 a[190:191], v227 offset:30720\n" \
"v_mfma_f32_16x16x32_bf16 v[144:147], a[152:155], a[104:107], v[144:147]\n" \
"s_waitcnt vmcnt(6)\n" \
"v_mfma_f32_16x16x32_bf16 v[152:155], a[152:155], a[108:111], v[152:155]\n" \
"v_cmp_ne_u64 vcc, s[72:73], v[250:251]\n" \
"v_mfma_f32_16x16x32_bf16 v[156:159], a[156:159], a[108:111], v[156:159]\n" \
"s_nop 0\n" \
"v_mfma_f32_16x16x32_bf16 v[148:151], a[156:159], a[104:107], v[148:151]\n" \
"s_cbranch_vccnz Lhc_nf_%=\n" \
"s_waitcnt vmcnt(22) lgkmcnt(0)\n" \
"s_barrier\n" \
"v_mfma_f32_16x16x32_bf16 v[128:131], a[160:163], a[112:115], v[128:131]\n" \
"ds_read_b64_tr_b16 a[128:129], v224 offset:32768\n" \
"ds_read_b64_tr_b16 a[130:131], v224 offset:34816\n" \
"v_mfma_f32_16x16x32_bf16 v[136:139], a[160:163], a[116:119], v[136:139]\n" \
"buffer_load_dwordx4 v[64:67], v232, s[64:67], s43 offen offset:0 sc1\n" \
"v_mfma_f32_16x16x32_bf16 v[140:143], a[164:167], a[116:119], v[140:143]\n" \
"ds_read_b64_tr_b16 a[132:133], v225 offset:32768\n" \
"ds_read_b64_tr_b16 a[134:135], v225 offset:34816\n" \
"v_mfma_f32_16x16x32_bf16 v[132:135], a[164:167], a[112:115], v[132:135]\n" \
"buffer_load_dwordx4 v[68:71], v232, s[64:67], s43 offen offset:1024 sc1\n" \
"v_mfma_f32_16x16x32_bf16 v[144:147], a[168:171], a[112:115], v[144:147]\n" \
"ds_read_b64_tr_b16 a[136:137], v226 offset:32768\n" \
"ds_read_b64_tr_b16 a[138:139], v226 offset:34816\n" \
"v_mfma_f32_16x16x32_bf16 v[152:155], a[168:171], a[116:119], v[152:155]\n" \
"buffer_load_dwordx4 v[72:75], v232, s[64:67], s43 offen offset:2048 sc1\n" \
"v_mfma_f32_16x16x32_bf16 v[156:159], a[172:175], a[116:119], v[156:159]\n" \
"ds_read_b64_tr_b16 a[140:141], v227 offset:32768\n" \
"ds_read_b64_tr_b16 a[142:143], v227 offset:34816\n" \
"v_mfma_f32_16x16x32_bf16 v[148:151], a[172:175], a[112:115], v[148:151]\n" \
"ds_read_b64_tr_b16 a[144:145], v224 offset:36864\n" \
"v_mfma_f32_16x16x32_bf16 v[128:131], a[176:179], a[120:123], v[128:131]\n" \
"buffer_load_dwordx4 v[76:79], v232, s[64:67], s43 offen offset:3072 sc1\n" \
"v_mfma_f32_16x16x32_bf16 v[136:139], a[176:179], a[124:127], v[136:139]\n" \
"ds_read_b64_tr_b16 a[146:147], v224 offset:38912\n" \
"ds_read_b64_tr_b16 a[148:149], v225 offset:36864\n" \
"v_mfma_f32_16x16x32_bf16 v[140:143], a[180:183], a[124:127], v[140:143]\n" \
"buffer_load_dwordx4 v[80:83], v232, s[64:67], s44 offen offset:0 sc1\n" \
"v_mfma_f32_16x16x32_bf16 v[132:135], a[180:183], a[120:123], v[132:135]\n" \
"ds_read_b64_tr_b16 a[150:151], v225 offset:38912\n" \
"ds_read_b64_tr_b16 a[152:153], v226 offset:36864\n" \
"v_mfma_f32_16x16x32_bf16 v[144:147], a[184:187], a[120:123], v[144:147]\n" \
"buffer_load_dwordx4 v[84:87], v232, s[64:67], s44 offen offset:1024 sc1\n" \
"v_mfma_f32_16x16x32_bf16 v[152:155], a[184:187], a[124:127], v[152:155]\n" \
"ds_read_b64_tr_b16 a[154:155], v226 offset:38912\n" \
"ds_read_b64_tr_b16 a[156:157], v227 offset:36864\n" \
"v_mfma_f32_16x16x32_bf16 v[156:159], a[188:191], a[124:127], v[156:159]\n" \
"ds_read_b64_tr_b16 a[158:159], v227 offset:38912\n" \
"v_mfma_f32_16x16x32_bf16 v[148:151], a[188:191], a[120:123], v[148:151]\n" \
"s_waitcnt vmcnt(24) lgkmcnt(8)\n" \
"v_mfma_f32_16x16x32_bf16 v[128:131], a[128:131], a[0:3], v[128:131]\n" \
"ds_read_b64_tr_b16 a[160:161], v224 offset:40960\n" \
"ds_read_b64_tr_b16 a[162:163], v224 offset:43008\n" \
"v_mfma_f32_16x16x32_bf16 v[136:139], a[128:131], a[4:7], v[136:139]\n" \
"buffer_load_dwordx4 v[88:91], v232, s[64:67], s44 offen offset:2048 sc1\n" \
"ds_read_b64_tr_b16 a[164:165], v225 offset:40960\n" \
"v_mfma_f32_16x16x32_bf16 v[140:143], a[132:135], a[4:7], v[140:143]\n" \
"buffer_load_dwordx4 v[92:95], v232, s[64:67], s44 offen offset:3072 sc1\n" \
"ds_read_b64_tr_b16 a[166:167], v225 offset:43008\n" \
"v_mfma_f32_16x16x32_bf16 v[132:135], a[132:135], a[0:3], v[132:135]\n" \
"ds_read_b64_tr_b16 a[168:169], v226 offset:40960\n" \
"v_mfma_f32_16x16x32_bf16 v[144:147], a[136:139], a[0:3], v[144:147]\n" \
"buffer_load_dwordx4 v[96:99], v232, s[64:67], s45 offen offset:0 sc1\n" \
"ds_read_b64_tr_b16 a[170:171], v226 offset:43008\n" \
"v_mfma_f32_16x16x32_bf16 v[152:155], a[136:139], a[4:7], v[152:155]\n" \
"ds_read_b64_tr_b16 a[172:173], v227 offset:40960\n" \
"v_mfma_f32_16x16x32_bf16 v[156:159], a[140:143], a[4:7], v[156:159]\n" \
"buffer_load_dwordx4 v[100:103], v232, s[64:67], s45 offen offset:1024 sc1\n" \
"ds_read_b64_tr_b16 a[174:175], v227 offset:43008\n" \
"v_mfma_f32_16x16x32_bf16 v[148:151], a[140:143], a[0:3], v[148:151]\n" \
"ds_read_b64_tr_b16 a[176:177], v224 offset:45056\n" \
"s_waitcnt lgkmcnt(9)\n" \
"v_mfma_f32_16x16x32_bf16 v[128:131], a[144:147], a[8:11], v[128:131]\n" \
"buffer_load_dwordx4 v[104:107], v232, s[64:67], s45 offen offset:2048 sc1\n" \
"ds_read_b64_tr_b16 a[178:179], v224 offset:47104\n" \
"v_mfma_f32_16x16x32_bf16 v[136:139], a[144:147], a[12:15], v[136:139]\n" \
"buffer_load_dwordx4 v[108:111], v232, s[64:67], s45 offen offset:3072 sc1\n" \
"ds_read_b64_tr_b16 a[180:181], v225 offset:45056\n" \
"v_mfma_f32_16x16x32_bf16 v[140:143], a[148:151], a[12:15], v[140:143]\n" \
"ds_read_b64_tr_b16 a[182:183], v225 offset:47104\n" \
"v_mfma_f32_16x16x32_bf16 v[132:135], a[148:151], a[8:11], v[132:135]\n" \
"buffer_load_dwordx4 a[40:43], v234, s[64:67], s20 offen offset:0 sc1\n" \
"ds_read_b64_tr_b16 a[184:185], v226 offset:45056\n" \
"v_mfma_f32_16x16x32_bf16 v[144:147], a[152:155], a[8:11], v[144:147]\n" \
"ds_read_b64_tr_b16 a[186:187], v226 offset:47104\n" \
"v_mfma_f32_16x16x32_bf16 v[152:155], a[152:155], a[12:15], v[152:155]\n" \
"buffer_load_dwordx4 a[44:47], v234, s[64:67], s20 offen offset:1024 sc1\n" \
"ds_read_b64_tr_b16 a[188:189], v227 offset:45056\n" \
"v_mfma_f32_16x16x32_bf16 v[156:159], a[156:159], a[12:15], v[156:159]\n" \
"ds_read_b64_tr_b16 a[190:191], v227 offset:47104\n" \
"v_mfma_f32_16x16x32_bf16 v[148:151], a[156:159], a[8:11], v[148:151]\n" \
"s_waitcnt vmcnt(28) lgkmcnt(8)\n" \
"v_mfma_f32_16x16x32_bf16 v[128:131], a[160:163], a[16:19], v[128:131]\n" \
"ds_read_b64_tr_b16 a[128:129], v224 offset:49152\n" \
"ds_read_b64_tr_b16 a[130:131], v224 offset:51200\n" \
"v_mfma_f32_16x16x32_bf16 v[136:139], a[160:163], a[20:23], v[136:139]\n" \
"ds_read_b64_tr_b16 a[132:133], v225 offset:49152\n" \
"v_mfma_f32_16x16x32_bf16 v[140:143], a[164:167], a[20:23], v[140:143]\n" \
"ds_read_b64_tr_b16 a[134:135], v225 offset:51200\n" \
"v_mfma_f32_16x16x32_bf16 v[132:135], a[164:167], a[16:19], v[132:135]\n" \
"ds_read_b64_tr_b16 a[136:137], v226 offset:49152\n" \
"v_mfma_f32_16x16x32_bf16 v[144:147], a[168:171], a[16:19], v[144:147]\n" \
"ds_read_b64_tr_b16 a[138:139], v226 offset:51200\n" \
"v_mfma_f32_16x16x32_bf16 v[152:155], a[168:171], a[20:23], v[152:155]\n" \
"ds_read_b64_tr_b16 a[140:141], v227 offset:49152\n" \
"v_mfma_f32_16x16x32_bf16 v[156:159], a[172:175], a[20:23], v[156:159]\n" \
"ds_read_b64_tr_b16 a[142:143], v227 offset:51200\n" \
"v_mfma_f32_16x16x32_bf16 v[148:151], a[172:175], a[16:19], v[148:151]\n" \
"ds_read_b64_tr_b16 a[144:145], v224 offset:53248\n" \
"s_waitcnt lgkmcnt(9)\n" \
"v_mfma_f32_16x16x32_bf16 v[128:131], a[176:179], a[24:27], v[128:131]\n" \
"ds_read_b64_tr_b16 a[146:147], v224 offset:55296\n" \
"v_mfma_f32_16x16x32_bf16 v[136:139], a[176:179], a[28:31], v[136:139]\n" \
"ds_read_b64_tr_b16 a[148:149], v225 offset:53248\n" \
"v_mfma_f32_16x16x32_bf16 v[140:143], a[180:183], a[28:31], v[140:143]\n" \
"ds_read_b64_tr_b16 a[150:151], v225 offset:55296\n" \
"v_mfma_f32_16x16x32_bf16 v[132:135], a[180:183], a[24:27], v[132:135]\n" \
"ds_read_b64_tr_b16 a[152:153], v226 offset:53248\n" \
"v_mfma_f32_16x16x32_bf16 v[144:147], a[184:187], a[24:27], v[144:147]\n" \
"ds_read_b64_tr_b16 a[154:155], v226 offset:55296\n" \
"v_mfma_f32_16x16x32_bf16 v[152:155], a[184:187], a[28:31], v[152:155]\n" \
"ds_read_b64_tr_b16 a[156:157], v227 offset:53248\n" \
"v_mfma_f32_16x16x32_bf16 v[156:159], a[188:191], a[28:31], v[156:159]\n" \
"ds_read_b64_tr_b16 a[158:159], v227 offset:55296\n" \
"v_mfma_f32_16x16x32_bf16 v[148:151], a[188:191], a[24:27], v[148:151]\n" \
"s_waitcnt vmcnt(10) lgkmcnt(8)\n" \
"v_mfma_f32_16x16x32_bf16 v[128:131], a[128:131], a[64:67], v[128:131]\n" \
"ds_read_b64_tr_b16 a[160:161], v224 offset:57344\n" \
"v_mfma_f32_16x16x32_bf16 v[136:139], a[128:131], a[68:71], v[136:139]\n" \
"ds_read_b64_tr_b16 a[162:163], v224 offset:59392\n" \
"v_mfma_f32_16x16x32_bf16 v[140:143], a[132:135], a[68:71], v[140:143]\n" \
"ds_read_b64_tr_b16 a[164:165], v225 offset:57344\n" \
"v_mfma_f32_16x16x32_bf16 v[132:135], a[132:135], a[64:67], v[132:135]\n" \
"v_mfma_f32_16x16x32_bf16 v[144:147], a[136:139], a[64:67], v[144:147]\n" \
"ds_read_b64_tr_b16 a[166:167], v225 offset:59392\n" \
"v_mfma_f32_16x16x32_bf16 v[152:155], a[136:139], a[68:71], v[152:155]\n" \
"ds_read_b64_tr_b16 a[168:169], v226 offset:57344\n" \
"v_mfma_f32_16x16x32_bf16 v[156:159], a[140:143], a[68:71], v[156:159]\n" \
"v_mfma_f32_16x16x32_bf16 v[148:151], a[140:143], a[64:67], v[148:151]\n" \
"ds_read_b64_tr_b16 a[170:171], v226 offset:59392\n" \
"s_waitcnt lgkmcnt(6)\n" \
"v_mfma_f32_16x16x32_bf16 v[128:131], a[144:147], a[72:75], v[128:131]\n" \
"ds_read_b64_tr_b16 a[172:173], v227 offset:57344\n" \
"v_mfma_f32_16x16x32_bf16 v[136:139], a[144:147], a[76:79], v[136:139]\n" \
"v_mfma_f32_16x16x32_bf16 v[140:143], a[148:151], a[76:79], v[140:143]\n" \
"ds_read_b64_tr_b16 a[174:175], v227 offset:59392\n" \
"v_mfma_f32_16x16x32_bf16 v[132:135], a[148:151], a[72:75], v[132:135]\n" \
"ds_read_b64_tr_b16 a[176:177], v224 offset:61440\n" \
"v_mfma_f32_16x16x32_bf16 v[144:147], a[152:155], a[72:75], v[144:147]\n" \
"ds_read_b64_tr_b16 a[178:179], v224 offset:63488\n" \
"v_mfma_f32_16x16x32_bf16 v[152:155], a[152:155], a[76:79], v[152:155]\n" \
"v_mfma_f32_16x16x32_bf16 v[156:159], a[156:159], a[76:79], v[156:159]\n" \
"ds_read_b64_tr_b16 a[180:181], v225 offset:61440\n" \
"v_mfma_f32_16x16x32_bf16 v[148:151], a[156:159], a[72:75], v[148:151]\n" \
"ds_read_b64_tr_b16 a[182:183], v225 offset:63488\n" \
"v_mfma_f32_16x16x32_bf16 v[0:3], v[114:117], v[64:67], v[0:3]\n" \
"v_mfma_f32_16x16x32_bf16 v[4:7], v[112:115], v[64:67], v[4:7]\n" \
"ds_read_b64_tr_b16 a[184:185], v226 offset:61440\n" \
"v_mfma_f32_16x16x32_bf16 v[32:35], v[114:117], v[68:71], v[32:35]\n" \
"ds_read_b64_tr_b16 a[186:187], v226 offset:63488\n" \
"v_mfma_f32_16x16x32_bf16 v[36:39], v[112:115], v[68:71], v[36:39]\n" \
"v_mfma_f32_16x16x32_bf16 v[8:11], v[114:117], v[72:75], v[8:11]\n" \
"ds_read_b64_tr_b16 a[188:189], v227 offset:61440\n" \
"v_mfma_f32_16x16x32_bf16 v[12:15], v[112:115], v[72:75], v[12:15]\n" \
"ds_read_b64_tr_b16 a[190:191], v227 offset:63488\n" \
"v_mfma_f32_16x16x32_bf16 v[40:43], v[114:117], v[76:79], v[40:43]\n" \
"v_mfma_f32_16x16x32_bf16 v[44:47], v[112:115], v[76:79], v[44:47]\n" \
"s_waitcnt vmcnt(6) lgkmcnt(0)\n" \
"s_barrier\n" \
"v_mfma_f32_16x16x32_bf16 v[128:131], a[160:163], a[80:83], v[128:131]\n" \
"ds_read_b64_tr_b16 a[128:129], v237 offset:0\n" \
"v_mfma_f32_16x16x32_bf16 v[136:139], a[160:163], a[84:87], v[136:139]\n" \
"ds_read_b64_tr_b16 a[130:131], v237 offset:2048\n" \
"v_mfma_f32_16x16x32_bf16 v[140:143], a[164:167], a[84:87], v[140:143]\n" \
"ds_read_b64_tr_b16 a[132:133], v238 offset:0\n" \
"v_mfma_f32_16x16x32_bf16 v[132:135], a[164:167], a[80:83], v[132:135]\n" \
"v_mfma_f32_16x16x32_bf16 v[144:147], a[168:171], a[80:83], v[144:147]\n" \
"ds_read_b64_tr_b16 a[134:135], v238 offset:2048\n" \
"v_mfma_f32_16x16x32_bf16 v[152:155], a[168:171], a[84:87], v[152:155]\n" \
"ds_read_b64_tr_b16 a[136:137], v239 offset:0\n" \
"v_mfma_f32_16x16x32_bf16 v[156:159], a[172:175], a[84:87], v[156:159]\n" \
"v_mfma_f32_16x16x32_bf16 v[148:151], a[172:175], a[80:83], v[148:151]\n" \
"ds_read_b64_tr_b16 a[138:139], v239 offset:2048\n" \
"v_mfma_f32_16x16x32_bf16 v[128:131], a[176:179], a[88:91], v[128:131]\n" \
"ds_read_b64_tr_b16 a[140:141], v240 offset:0\n" \
"v_mfma_f32_16x16x32_bf16 v[136:139], a[176:179], a[92:95], v[136:139]\n" \
"v_mfma_f32_16x16x32_bf16 v[140:143], a[180:183], a[92:95], v[140:143]\n" \
"ds_read_b64_tr_b16 a[142:143], v240 offset:2048\n" \
"v_mfma_f32_16x16x32_bf16 v[132:135], a[180:183], a[88:91], v[132:135]\n" \
"ds_read_b64_tr_b16 a[144:145], v237 offset:4096\n" \
"v_mfma_f32_16x16x32_bf16 v[144:147], a[184:187], a[88:91], v[144:147]\n" \
"ds_read_b64_tr_b16 a[146:147], v237 offset:6144\n" \
"v_mfma_f32_16x16x32_bf16 v[152:155], a[184:187], a[92:95], v[152:155]\n" \
"v_mfma_f32_16x16x32_bf16 v[156:159], a[188:191], a[92:95], v[156:159]\n" \
"ds_read_b64_tr_b16 a[148:149], v238 offset:4096\n" \
"v_mfma_f32_16x16x32_bf16 v[148:151], a[188:191], a[88:91], v[148:151]\n" \
"ds_read_b64_tr_b16 a[150:151], v238 offset:6144\n" \
"v_mfma_f32_16x16x32_bf16 v[0:3], v[114:117], v[80:83], v[0:3]\n" \
"v_mfma_f32_16x16x32_bf16 v[4:7], v[112:115], v[80:83], v[4:7]\n" \
"ds_read_b64_tr_b16 a[152:153], v239 offset:4096\n" \
"v_mfma_f32_16x16x32_bf16 v[32:35], v[114:117], v[84:87], v[32:35]\n" \
"ds_read_b64_tr_b16 a[154:155], v239 offset:6144\n" \
"v_mfma_f32_16x16x32_bf16 v[36:39], v[112:115], v[84:87], v[36:39]\n" \
"v_mfma_f32_16x16x32_bf16 v[8:11], v[114:117], v[88:91], v[8:11]\n" \
"ds_read_b64_tr_b16 a[156:157], v240 offset:4096\n" \
"v_mfma_f32_16x16x32_bf16 v[12:15], v[112:115], v[88:91], v[12:15]\n" \
"ds_read_b64_tr_b16 a[158:159], v240 offset:6144\n" \
"v_mfma_f32_16x16x32_bf16 v[40:43], v[114:117], v[92:95], v[40:43]\n" \
"v_mfma_f32_16x16x32_bf16 v[44:47], v[112:115], v[92:95], v[44:47]\n" \
"s_waitcnt vmcnt(0) lgkmcnt(8)\n" \
"v_mfma_f32_16x16x32_bf16 v[128:131], a[128:131], a[32:35], v[128:131]\n" \
"v_mfma_f32_16x16x32_bf16 v[136:139], a[128:131], a[36:39], v[136:139]\n" \
"v_mfma_f32_16x16x32_bf16 v[140:143], a[132:135], a[36:39], v[140:143]\n" \
"v_mfma_f32_16x16x32_bf16 v[132:135], a[132:135], a[32:35], v[132:135]\n" \
"v_mfma_f32_16x16x32_bf16 v[144:147], a[136:139], a[32:35], v[144:147]\n" \
"v_mfma_f32_16x16x32_bf16 v[152:155], a[136:139], a[36:39], v[152:155]\n" \
"v_mfma_f32_16x16x32_bf16 v[156:159], a[140:143], a[36:39], v[156:159]\n" \
"v_mfma_f32_16x16x32_bf16 v[148:151], a[140:143], a[32:35], v[148:151]\n" \
"s_waitcnt lgkmcnt(0)\n" \
"v_mfma_f32_16x16x32_bf16 v[128:131], a[144:147], a[40:43], v[128:131]\n" \
"v_mfma_f32_16x16x32_bf16 v[136:139], a[144:147], a[44:47], v[136:139]\n" \
"v_mfma_f32_16x16x32_bf16 v[140:143], a[148:151], a[44:47], v[140:143]\n" \
"v_mfma_f32_16x16x32_bf16 v[132:135], a[148:151], a[40:43], v[132:135]\n" \
"v_mfma_f32_16x16x32_bf16 v[144:147], a[152:155], a[40:43], v[144:147]\n" \
"v_mfma_f32_16x16x32_bf16 v[152:155], a[152:155], a[44:47], v[152:155]\n" \
"v_mfma_f32_16x16x32_bf16 v[156:159], a[156:159], a[44:47], v[156:159]\n" \
"v_mfma_f32_16x16x32_bf16 v[148:151], a[156:159], a[40:43], v[148:151]\n" \
"v_mfma_f32_16x16x32_bf16 v[0:3], v[114:117], v[96:99], v[0:3]\n" \
"v_mfma_f32_16x16x32_bf16 v[4:7], v[112:115], v[96:99], v[4:7]\n" \
"v_mfma_f32_16x16x32_bf16 v[32:35], v[114:117], v[100:103], v[32:35]\n" \
"v_mfma_f32_16x16x32_bf16 v[36:39], v[112:115], v[100:103], v[36:39]\n" \
"v_mfma_f32_16x16x32_bf16 v[8:11], v[114:117], v[104:107], v[8:11]\n" \
"v_mfma_f32_16x16x32_bf16 v[12:15], v[112:115], v[104:107], v[12:15]\n" \
"v_mfma_f32_16x16x32_bf16 v[40:43], v[114:117], v[108:111], v[40:43]\n" \
"v_mfma_f32_16x16x32_bf16 v[44:47], v[112:115], v[108:111], v[44:47]\n" \
"s_branch Lfin2_%=\n" \
"Lhc_nf_%=:\n" \
"s_waitcnt vmcnt(22) lgkmcnt(0)\n" \
"s_barrier\n" \
"v_mfma_f32_16x16x32_bf16 v[128:131], a[160:163], a[112:115], v[128:131]\n" \
"ds_read_b64_tr_b16 a[128:129], v224 offset:32768\n" \
"ds_read_b64_tr_b16 a[130:131], v224 offset:34816\n" \
"v_mfma_f32_16x16x32_bf16 v[136:139], a[160:163], a[116:119], v[136:139]\n" \
"buffer_load_dwordx4 a[40:43], v234, s[64:67], s20 offen offset:0 sc1\n" \
"v_mfma_f32_16x16x32_bf16 v[140:143], a[164:167], a[116:119], v[140:143]\n" \
"ds_read_b64_tr_b16 a[132:133], v225 offset:32768\n" \
"v_mfma_f32_16x16x32_bf16 v[132:135], a[164:167], a[112:115], v[132:135]\n" \
"ds_read_b64_tr_b16 a[134:135], v225 offset:34816\n" \
"v_mfma_f32_16x16x32_bf16 v[144:147], a[168:171], a[112:115], v[144:147]\n" \
"ds_read_b64_tr_b16 a[136:137], v226 offset:32768\n" \
"v_mfma_f32_16x16x32_bf16 v[152:155], a[168:171], a[116:119], v[152:155]\n" \
"ds_read_b64_tr_b16 a[138:139], v226 offset:34816\n" \
"ds_read_b64_tr_b16 a[140:141], v227 offset:32768\n" \
"v_mfma_f32_16x16x32_bf16 v[156:159], a[172:175], a[116:119], v[156:159]\n" \
"ds_read_b64_tr_b16 a[142:143], v227 offset:34816\n" \
"v_mfma_f32_16x16x32_bf16 v[148:151], a[172:175], a[112:115], v[148:151]\n" \
"ds_read_b64_tr_b16 a[144:145], v224 offset:36864\n" \
"v_mfma_f32_16x16x32_bf16 v[128:131], a[176:179], a[120:123], v[128:131]\n" \
"buffer_load_dwordx4 a[44:47], v234, s[64:67], s20 offen offset:1024 sc1\n" \
"v_mfma_f32_16x16x32_bf16 v[136:139], a[176:179], a[124:127], v[136:139]\n" \
"ds_read_b64_tr_b16 a[146:147], v224 offset:38912\n" \
"v_mfma_f32_16x16x32_bf16 v[140:143], a[180:183], a[124:127], v[140:143]\n" \
"ds_read_b64_tr_b16 a[148:149], v225 offset:36864\n" \
"ds_read_b64_tr_b16 a[150:151], v225 offset:38912\n" \
"v_mfma_f32_16x16x32_bf16 v[132:135], a[180:183], a[120:123], v[132:135]\n" \
"ds_read_b64_tr_b16 a[152:153], v226 offset:36864\n" \
"v_mfma_f32_16x16x32_bf16 v[144:147], a[184:187], a[120:123], v[144:147]\n" \
"ds_read_b64_tr_b16 a[154:155], v226 offset:38912\n" \
"v_mfma_f32_16x16x32_bf16 v[152:155], a[184:187], a[124:127], v[152:155]\n" \
"ds_read_b64_tr_b16 a[156:157], v227 offset:36864\n" \
"v_mfma_f32_16x16x32_bf16 v[156:159], a[188:191], a[124:127], v[156:159]\n" \
"ds_read_b64_tr_b16 a[158:159], v227 offset:38912\n" \
"v_mfma_f32_16x16x32_bf16 v[148:151], a[188:191], a[120:123], v[148:151]\n" \
"s_waitcnt vmcnt(20) lgkmcnt(8)\n" \
"v_mfma_f32_16x16x32_bf16 v[128:131], a[128:131], a[0:3], v[128:131]\n" \
"ds_read_b64_tr_b16 a[160:161], v224 offset:40960\n" \
"ds_read_b64_tr_b16 a[162:163], v224 offset:43008\n" \
"v_mfma_f32_16x16x32_bf16 v[136:139], a[128:131], a[4:7], v[136:139]\n" \
"ds_read_b64_tr_b16 a[164:165], v225 offset:40960\n" \
"v_mfma_f32_16x16x32_bf16 v[140:143], a[132:135], a[4:7], v[140:143]\n" \
"ds_read_b64_tr_b16 a[166:167], v225 offset:43008\n" \
"v_mfma_f32_16x16x32_bf16 v[132:135], a[132:135], a[0:3], v[132:135]\n" \
"ds_read_b64_tr_b16 a[168:169], v226 offset:40960\n" \
"v_mfma_f32_16x16x32_bf16 v[144:147], a[136:139], a[0:3], v[144:147]\n" \
"ds_read_b64_tr_b16 a[170:171], v226 offset:43008\n" \
"v_mfma_f32_16x16x32_bf16 v[152:155], a[136:139], a[4:7], v[152:155]\n" \
"ds_read_b64_tr_b16 a[172:173], v227 offset:40960\n" \
"v_mfma_f32_16x16x32_bf16 v[156:159], a[140:143], a[4:7], v[156:159]\n" \
"ds_read_b64_tr_b16 a[174:175], v227 offset:43008\n" \
"v_mfma_f32_16x16x32_bf16 v[148:151], a[140:143], a[0:3], v[148:151]\n" \
"ds_read_b64_tr_b16 a[176:177], v224 offset:45056\n" \
"s_waitcnt lgkmcnt(9)\n" \
"v_mfma_f32_16x16x32_bf16 v[128:131], a[144:147], a[8:11], v[128:131]\n" \
"ds_read_b64_tr_b16 a[178:179], v224 offset:47104\n" \
"v_mfma_f32_16x16x32_bf16 v[136:139], a[144:147], a[12:15], v[136:139]\n" \
"ds_read_b64_tr_b16 a[180:181], v225 offset:45056\n" \
"v_mfma_f32_16x16x32_bf16 v[140:143], a[148:151], a[12:15], v[140:143]\n" \
"ds_read_b64_tr_b16 a[182:183], v225 offset:47104\n" \
"v_mfma_f32_16x16x32_bf16 v[132:135], a[148:151], a[8:11], v[132:135]\n" \
"ds_read_b64_tr_b16 a[184:185], v226 offset:45056\n" \
"v_mfma_f32_16x16x32_bf16 v[144:147], a[152:155], a[8:11], v[144:147]\n" \
"ds_read_b64_tr_b16 a[186:187], v226 offset:47104\n" \
"v_mfma_f32_16x16x32_bf16 v[152:155], a[152:155], a[12:15], v[152:155]\n" \
"ds_read_b64_tr_b16 a[188:189], v227 offset:45056\n" \
"v_mfma_f32_16x16x32_bf16 v[156:159], a[156:159], a[12:15], v[156:159]\n" \
"ds_read_b64_tr_b16 a[190:191], v227 offset:47104\n" \
"v_mfma_f32_16x16x32_bf16 v[148:151], a[156:159], a[8:11], v[148:151]\n" \
"s_waitcnt vmcnt(16) lgkmcnt(8)\n" \
"v_mfma_f32_16x16x32_bf16 v[128:131], a[160:163], a[16:19], v[128:131]\n" \
"ds_read_b64_tr_b16 a[128:129], v224 offset:49152\n" \
"ds_read_b64_tr_b16 a[130:131], v224 offset:51200\n" \
"v_mfma_f32_16x16x32_bf16 v[136:139], a[160:163], a[20:23], v[136:139]\n" \
"ds_read_b64_tr_b16 a[132:133], v225 offset:49152\n" \
"v_mfma_f32_16x16x32_bf16 v[140:143], a[164:167], a[20:23], v[140:143]\n" \
"ds_read_b64_tr_b16 a[134:135], v225 offset:51200\n" \
"v_mfma_f32_16x16x32_bf16 v[132:135], a[164:167], a[16:19], v[132:135]\n" \
"ds_read_b64_tr_b16 a[136:137], v226 offset:49152\n" \
"v_mfma_f32_16x16x32_bf16 v[144:147], a[168:171], a[16:19], v[144:147]\n" \
"ds_read_b64_tr_b16 a[138:139], v226 offset:51200\n" \
"v_mfma_f32_16x16x32_bf16 v[152:155], a[168:171], a[20:23], v[152:155]\n" \
"ds_read_b64_tr_b16 a[140:141], v227 offset:49152\n" \
"v_mfma_f32_16x16x32_bf16 v[156:159], a[172:175], a[20:23], v[156:159]\n" \
"ds_read_b64_tr_b16 a[142:143], v227 offset:51200\n" \
"v_mfma_f32_16x16x32_bf16 v[148:151], a[172:175], a[16:19], v[148:151]\n" \
"ds_read_b64_tr_b16 a[144:145], v224 offset:53248\n" \
"s_waitcnt lgkmcnt(9)\n" \
"v_mfma_f32_16x16x32_bf16 v[128:131], a[176:179], a[24:27], v[128:131]\n" \
"ds_read_b64_tr_b16 a[146:147], v224 offset:55296\n" \
"v_mfma_f32_16x16x32_bf16 v[136:139], a[176:179], a[28:31], v[136:139]\n" \
"ds_read_b64_tr_b16 a[148:149], v225 offset:53248\n" \
"v_mfma_f32_16x16x32_bf16 v[140:143], a[180:183], a[28:31], v[140:143]\n" \
"ds_read_b64_tr_b16 a[150:151], v225 offset:55296\n" \
"v_mfma_f32_16x16x32_bf16 v[132:135], a[180:183], a[24:27], v[132:135]\n" \
"ds_read_b64_tr_b16 a[152:153], v226 offset:53248\n" \
"v_mfma_f32_16x16x32_bf16 v[144:147], a[184:187], a[24:27], v[144:147]\n" \
"ds_read_b64_tr_b16 a[154:155], v226 offset:55296\n" \
"v_mfma_f32_16x16x32_bf16 v[152:155], a[184:187], a[28:31], v[152:155]\n" \
"ds_read_b64_tr_b16 a[156:157], v227 offset:53248\n" \
"v_mfma_f32_16x16x32_bf16 v[156:159], a[188:191], a[28:31], v[156:159]\n" \
"ds_read_b64_tr_b16 a[158:159], v227 offset:55296\n" \
"v_mfma_f32_16x16x32_bf16 v[148:151], a[188:191], a[24:27], v[148:151]\n" \
"s_waitcnt vmcnt(9) lgkmcnt(8)\n" \
"v_mfma_f32_16x16x32_bf16 v[128:131], a[128:131], a[64:67], v[128:131]\n" \
"ds_read_b64_tr_b16 a[160:161], v224 offset:57344\n" \
"ds_read_b64_tr_b16 a[162:163], v224 offset:59392\n" \
"v_mfma_f32_16x16x32_bf16 v[136:139], a[128:131], a[68:71], v[136:139]\n" \
"ds_read_b64_tr_b16 a[164:165], v225 offset:57344\n" \
"v_mfma_f32_16x16x32_bf16 v[140:143], a[132:135], a[68:71], v[140:143]\n" \
"ds_read_b64_tr_b16 a[166:167], v225 offset:59392\n" \
"v_mfma_f32_16x16x32_bf16 v[132:135], a[132:135], a[64:67], v[132:135]\n" \
"ds_read_b64_tr_b16 a[168:169], v226 offset:57344\n" \
"v_mfma_f32_16x16x32_bf16 v[144:147], a[136:139], a[64:67], v[144:147]\n" \
"ds_read_b64_tr_b16 a[170:171], v226 offset:59392\n" \
"v_mfma_f32_16x16x32_bf16 v[152:155], a[136:139], a[68:71], v[152:155]\n" \
"ds_read_b64_tr_b16 a[172:173], v227 offset:57344\n" \
"v_mfma_f32_16x16x32_bf16 v[156:159], a[140:143], a[68:71], v[156:159]\n" \
"ds_read_b64_tr_b16 a[174:175], v227 offset:59392\n" \
"v_mfma_f32_16x16x32_bf16 v[148:151], a[140:143], a[64:67], v[148:151]\n" \
"ds_read_b64_tr_b16 a[176:177], v224 offset:61440\n" \
"s_waitcnt lgkmcnt(9)\n" \
"v_mfma_f32_16x16x32_bf16 v[128:131], a[144:147], a[72:75], v[128:131]\n" \
"ds_read_b64_tr_b16 a[178:179], v224 offset:63488\n" \
"v_mfma_f32_16x16x32_bf16 v[136:139], a[144:147], a[76:79], v[136:139]\n" \
"ds_read_b64_tr_b16 a[180:181], v225 offset:61440\n" \
"v_mfma_f32_16x16x32_bf16 v[140:143], a[148:151], a[76:79], v[140:143]\n" \
"ds_read_b64_tr_b16 a[182:183], v225 offset:63488\n" \
"v_mfma_f32_16x16x32_bf16 v[132:135], a[148:151], a[72:75], v[132:135]\n" \
"ds_read_b64_tr_b16 a[184:185], v226 offset:61440\n" \
"v_mfma_f32_16x16x32_bf16 v[144:147], a[152:155], a[72:75], v[144:147]\n" \
"ds_read_b64_tr_b16 a[186:187], v226 offset:63488\n" \
"v_mfma_f32_16x16x32_bf16 v[152:155], a[152:155], a[76:79], v[152:155]\n" \
"ds_read_b64_tr_b16 a[188:189], v227 offset:61440\n" \
"v_mfma_f32_16x16x32_bf16 v[156:159], a[156:159], a[76:79], v[156:159]\n" \
"ds_read_b64_tr_b16 a[190:191], v227 offset:63488\n" \
"v_mfma_f32_16x16x32_bf16 v[148:151], a[156:159], a[72:75], v[148:151]\n" \
"s_waitcnt vmcnt(4) lgkmcnt(0)\n" \
"s_barrier\n" \
"v_mfma_f32_16x16x32_bf16 v[128:131], a[160:163], a[80:83], v[128:131]\n" \
"ds_read_b64_tr_b16 a[128:129], v237 offset:0\n" \
"ds_read_b64_tr_b16 a[130:131], v237 offset:2048\n" \
"v_mfma_f32_16x16x32_bf16 v[136:139], a[160:163], a[84:87], v[136:139]\n" \
"ds_read_b64_tr_b16 a[132:133], v238 offset:0\n" \
"v_mfma_f32_16x16x32_bf16 v[140:143], a[164:167], a[84:87], v[140:143]\n" \
"ds_read_b64_tr_b16 a[134:135], v238 offset:2048\n" \
"v_mfma_f32_16x16x32_bf16 v[132:135], a[164:167], a[80:83], v[132:135]\n" \
"ds_read_b64_tr_b16 a[136:137], v239 offset:0\n" \
"v_mfma_f32_16x16x32_bf16 v[144:147], a[168:171], a[80:83], v[144:147]\n" \
"ds_read_b64_tr_b16 a[138:139], v239 offset:2048\n" \
"v_mfma_f32_16x16x32_bf16 v[152:155], a[168:171], a[84:87], v[152:155]\n" \
"ds_read_b64_tr_b16 a[140:141], v240 offset:0\n" \
"v_mfma_f32_16x16x32_bf16 v[156:159], a[172:175], a[84:87], v[156:159]\n" \
"ds_read_b64_tr_b16 a[142:143], v240 offset:2048\n" \
"v_mfma_f32_16x16x32_bf16 v[148:151], a[172:175], a[80:83], v[148:151]\n" \
"ds_read_b64_tr_b16 a[144:145], v237 offset:4096\n" \
"v_mfma_f32_16x16x32_bf16 v[128:131], a[176:179], a[88:91], v[128:131]\n" \
"ds_read_b64_tr_b16 a[146:147], v237 offset:6144\n" \
"v_mfma_f32_16x16x32_bf16 v[136:139], a[176:179], a[92:95], v[136:139]\n" \
"ds_read_b64_tr_b16 a[148:149], v238 offset:4096\n" \
"v_mfma_f32_16x16x32_bf16 v[140:143], a[180:183], a[92:95], v[140:143]\n" \
"ds_read_b64_tr_b16 a[150:151], v238 offset:6144\n" \
"v_mfma_f32_16x16x32_bf16 v[132:135], a[180:183], a[88:91], v[132:135]\n" \
"ds_read_b64_tr_b16 a[152:153], v239 offset:4096\n" \
"v_mfma_f32_16x16x32_bf16 v[144:147], a[184:187], a[88:91], v[144:147]\n" \
"ds_read_b64_tr_b16 a[154:155], v239 offset:6144\n" \
"v_mfma_f32_16x16x32_bf16 v[152:155], a[184:187], a[92:95], v[152:155]\n" \
"ds_read_b64_tr_b16 a[156:157], v240 offset:4096\n" \
"v_mfma_f32_16x16x32_bf16 v[156:159], a[188:191], a[92:95], v[156:159]\n" \
"ds_read_b64_tr_b16 a[158:159], v240 offset:6144\n" \
"v_mfma_f32_16x16x32_bf16 v[148:151], a[188:191], a[88:91], v[148:151]\n" \
"s_waitcnt vmcnt(0) lgkmcnt(8)\n" \
"v_mfma_f32_16x16x32_bf16 v[128:131], a[128:131], a[32:35], v[128:131]\n" \
"v_mfma_f32_16x16x32_bf16 v[136:139], a[128:131], a[36:39], v[136:139]\n" \
"v_mfma_f32_16x16x32_bf16 v[140:143], a[132:135], a[36:39], v[140:143]\n" \
"v_mfma_f32_16x16x32_bf16 v[132:135], a[132:135], a[32:35], v[132:135]\n" \
"v_mfma_f32_16x16x32_bf16 v[144:147], a[136:139], a[32:35], v[144:147]\n" \
"v_mfma_f32_16x16x32_bf16 v[152:155], a[136:139], a[36:39], v[152:155]\n" \
"v_mfma_f32_16x16x32_bf16 v[156:159], a[140:143], a[36:39], v[156:159]\n" \
"v_mfma_f32_16x16x32_bf16 v[148:151], a[140:143], a[32:35], v[148:151]\n" \
"s_waitcnt lgkmcnt(0)\n" \
"v_mfma_f32_16x16x32_bf16 v[128:131], a[144:147], a[40:43], v[128:131]\n" \
"v_mfma_f32_16x16x32_bf16 v[136:139], a[144:147], a[44:47], v[136:139]\n" \
"v_mfma_f32_16x16x32_bf16 v[140:143], a[148:151], a[44:47], v[140:143]\n" \
"v_mfma_f32_16x16x32_bf16 v[132:135], a[148:151], a[40:43], v[132:135]\n" \
"v_mfma_f32_16x16x32_bf16 v[144:147], a[152:155], a[40:43], v[144:147]\n" \
"v_mfma_f32_16x16x32_bf16 v[152:155], a[152:155], a[44:47], v[152:155]\n" \
"v_mfma_f32_16x16x32_bf16 v[156:159], a[156:159], a[44:47], v[156:159]\n" \
"v_mfma_f32_16x16x32_bf16 v[148:151], a[156:159], a[40:43], v[148:151]\n" \
"s_mov_b32 s61, 0\n" \
"Lp5_%=:\n" \
"buffer_load_dwordx2 v[250:251], v253, s[64:67], s99 offen sc1\n" \
"s_waitcnt vmcnt(0)\n" \
"v_cmp_ne_u64 vcc, s[72:73], v[250:251]\n" \
"s_nop 0\n" \
"s_cbranch_vccz Lpd5_%=\n" \
"s_add_u32 s61, s61, 1\n" \
"s_cmp_lt_u32 s61, 0x40000\n" \
"s_sleep 1\n" \
"s_cbranch_scc1 Lp5_%=\n" \
"s_mov_b64 vcc, 0\n" \
"Lpd5_%=:\n" \
"buffer_load_dwordx4 v[64:67], v232, s[64:67], s43 offen offset:0 sc1\n" \
"buffer_load_dwordx4 v[68:71], v232, s[64:67], s43 offen offset:1024 sc1\n" \
"buffer_load_dwordx4 v[72:75], v232, s[64:67], s43 offen offset:2048 sc1\n" \
"buffer_load_dwordx4 v[76:79], v232, s[64:67], s43 offen offset:3072 sc1\n" \
"buffer_load_dwordx4 v[80:83], v232, s[64:67], s44 offen offset:0 sc1\n" \
"buffer_load_dwordx4 v[84:87], v232, s[64:67], s44 offen offset:1024 sc1\n" \
"buffer_load_dwordx4 v[88:91], v232, s[64:67], s44 offen offset:2048 sc1\n" \
"buffer_load_dwordx4 v[92:95], v232, s[64:67], s44 offen offset:3072 sc1\n" \
"buffer_load_dwordx4 v[96:99], v232, s[64:67], s45 offen offset:0 sc1\n" \
"buffer_load_dwordx4 v[100:103], v232, s[64:67], s45 offen offset:1024 sc1\n" \
"buffer_load_dwordx4 v[104:107], v232, s[64:67], s45 offen offset:2048 sc1\n" \
"buffer_load_dwordx4 v[108:111], v232, s[64:67], s45 offen offset:3072 sc1\n" \
"s_branch Lfin_%=\n" \
"Lsl2_%=:\n" \
"s_mov_b32 s61, 0\n" \
"Lp6_%=:\n" \
"buffer_load_dwordx2 v[250:251], v253, s[64:67], s84 offen sc1\n" \
"s_waitcnt vmcnt(0)\n" \
"v_cmp_ne_u64 vcc, s[72:73], v[250:251]\n" \
"s_nop 0\n" \
"s_cbranch_vccz Lpd6_%=\n" \
"s_add_u32 s61, s61, 1\n" \
"s_cmp_lt_u32 s61, 0x40000\n" \
"s_sleep 1\n" \
"s_cbranch_scc1 Lp6_%=\n" \
"s_mov_b64 vcc, 0\n" \
"Lpd6_%=:\n" \
"s_branch Lrt2_%=\n" \
"Lsl3_%=:\n" \
"s_mov_b32 s61, 0\n" \
"Lp7_%=:\n" \
"buffer_load_dwordx2 v[250:251], v253, s[64:67], s84 offen sc1\n" \
"s_waitcnt vmcnt(0)\n" \
"v_cmp_ne_u64 vcc, s[72:73], v[250:251]\n" \
"s_nop 0\n" \
"s_cbranch_vccz Lpd7_%=\n" \
"s_add_u32 s61, s61, 1\n" \
"s_cmp_lt_u32 s61, 0x40000\n" \
"s_sleep 1\n" \
"s_cbranch_scc1 Lp7_%=\n" \
"s_mov_b64 vcc, 0\n" \
"Lpd7_%=:\n" \
"s_branch Lrt3_%=\n" \
"Lsl4_%=:\n" \
"s_mov_b32 s61, 0\n" \
"Lp8_%=:\n" \
"buffer_load_dwordx2 v[250:251], v253, s[64:67], s84 offen sc1\n" \
"s_waitcnt vmcnt(0)\n" \
"v_cmp_ne_u64 vcc, s[72:73], v[250:251]\n" \
"s_nop 0\n" \
"s_cbranch_vccz Lpd8_%=\n" \
"s_add_u32 s61, s61, 1\n" \
"s_cmp_lt_u32 s61, 0x40000\n" \
"s_sleep 1\n" \
"s_cbranch_scc1 Lp8_%=\n" \
"s_mov_b64 vcc, 0\n" \
"Lpd8_%=:\n" \
"s_branch Lrt4_%=\n" \
"Lq_entry_%=:\n" \
"s_mov_b64 s[24:25], s[6:7]\n" \
"s_mov_b32 s26, 786432\n" \
"s_mov_b32 s27, 0x20000\n" \
"s_mul_i32 s61, s74, 1536\n" \
"v_min_u32 v228, 11, v248\n" \
"v_lshlrev_b32 v228, 7, v228\n" \
"v_add_u32 v228, s61, v228\n" \
"buffer_load_dword a201, v228, s[24:27], 0 offen\n" \
"s_add_u32 m0, s54, 0\n" \
"s_nop 0\n" \
"buffer_load_dwordx4 v232, s[20:23], s40 offen lds\n" \
"s_add_u32 m0, s54, 1024\n" \
"s_nop 0\n" \
"buffer_load_dwordx4 v233, s[20:23], s41 offen lds\n" \
"s_add_u32 m0, s54, 2048\n" \
"s_nop 0\n" \
"buffer_load_dwordx4 v232, s[20:23], s42 offen lds\n" \
"s_add_u32 m0, s54, 3072\n" \
"s_nop 0\n" \
"buffer_load_dwordx4 v233, s[20:23], s43 offen lds\n" \
"s_add_u32 m0, s54, 4096\n" \
"s_nop 0\n" \
"buffer_load_dwordx4 v232, s[20:23], s44 offen lds\n" \
"s_add_u32 m0, s54, 5120\n" \
"s_nop 0\n" \
"buffer_load_dwordx4 v233, s[20:23], s45 offen lds\n" \
"s_add_u32 m0, s54, 6144\n" \
"s_nop 0\n" \
"buffer_load_dwordx4 v232, s[20:23], s46 offen lds\n" \
"s_add_u32 m0, s54, 7168\n" \
"s_nop 0\n" \
"buffer_load_dwordx4 v233, s[20:23], s47 offen lds\n" \
"s_xor_b32 s54, s54, 32768\n" \
"s_add_u32 s20, s20, 128\n" \
"s_addc_u32 s21, s21, 0\n" \
"s_bfe_u32 s90, %[info], 0x3000c\n" \
"s_bfe_u32 s91, %[info], 0x20010\n" \
"s_bfe_u32 s92, %[info], 0x50004\n" \
"s_lshl_b32 s60, s89, 7\n" \
"s_add_u32 s60, s60, 0\n" \
"s_mov_b64 s[24:25], s[6:7]\n" \
"s_add_u32 s24, s24, s60\n" \
"s_addc_u32 s25, s25, 0\n" \
"s_mov_b32 s26, 0x7fffffff\n" \
"s_mov_b32 s27, 0x20000\n" \
"s_lshl_b32 s60, s89, 6\n" \
"s_add_u32 s60, s60, 6144\n" \
"s_mov_b64 s[28:29], s[10:11]\n" \
"s_add_u32 s28, s28, s60\n" \
"s_addc_u32 s29, s29, 0\n" \
"s_mov_b32 s30, 0x7fffffff\n" \
"s_mov_b32 s31, 0x20000\n" \
"s_lshl_b32 s60, s88, 19\n" \
"s_max_u32 s60, s60, 32768\n" \
"s_sub_u32 s60, s60, 32768\n" \
"s_mov_b64 s[36:37], s[4:5]\n" \
"s_add_u32 s36, s36, s60\n" \
"s_addc_u32 s37, s37, 0\n" \
"s_mov_b32 s38, 0x7fffffff\n" \
"s_mov_b32 s39, 0x20000\n" \
"v_and_b32 v231, 1, v247\n" \
"v_lshlrev_b32 v229, 6, v231\n" \
"v_xor_b32 v230, v230, v229\n" \
"v_lshl_add_u32 v236, v231, 11, v230\n" \
"s_mov_b32 s48, 0\n" \
"s_mov_b32 s49, 2048\n" \
"s_mov_b32 s50, 4096\n" \
"s_mov_b32 s51, 196608\n" \
"s_mov_b32 s52, 198656\n" \
"s_mov_b32 s53, 200704\n" \
"s_lshl_b32 s62, s59, 10\n" \
"s_add_u32 s62, s62, 65536\n" \
"s_add_u32 s55, s62, 0\n" \
"s_mov_b32 s63, 0\n" \
"v_lshrrev_b32 v230, 3, v248\n" \
"v_lshl_add_u32 v231, v247, 3, v230\n" \
"v_mul_u32_u24 v231, 6144, v231\n" \
"v_and_b32 v230, 2, v230\n" \
"v_and_b32 v229, 1, v247\n" \
"v_or_b32 v230, v230, v229\n" \
"v_lshlrev_b32 v230, 1, v230\n" \
"v_and_b32 v229, 7, v248\n" \
"v_xor_b32 v230, v230, v229\n" \
"v_lshl_add_u32 v234, v230, 4, v231\n" \
"v_lshrrev_b32 v230, 2, v248\n" \
"v_lshl_add_u32 v231, v247, 4, v230\n" \
"v_mul_u32_u24 v231, 7168, v231\n" \
"v_lshrrev_b32 v230, 5, v248\n" \
"v_lshlrev_b32 v230, 1, v230\n" \
"v_and_b32 v229, 3, v248\n" \
"v_xor_b32 v230, v230, v229\n" \
"v_lshl_add_u32 v235, v230, 4, v231\n" \
"s_mov_b64 s[64:65], s[18:19]\n" \
"s_mov_b32 s66, 0x2200000\n" \
"s_mov_b32 s67, 0x20000\n" \
"s_cmp_ge_u32 s59, 2\n" \
"s_cbranch_scc1 Lhp0_%=\n" \
"s_add_u32 m0, s55, 28672\n" \
"s_nop 0\n" \
"buffer_load_dwordx4 v236, s[36:39], 0 offen lds\n" \
"Lhp0_%=:\n" \
"s_add_u32 m0, s55, 0\n" \
"s_nop 0\n" \
"buffer_load_dwordx4 v234, s[24:27], s48 offen lds\n" \
"v_mov_b64 v[224:225], 0\n" \
"v_mov_b64 v[226:227], 0\n" \
"s_lshl_b32 s60, s88, 2\n" \
"s_add_u32 s60, s60, s59\n" \
"s_lshl_b32 s61, s60, 7\n" \
"s_add_u32 s93, s61, 33751040\n" \
"s_lshl_b32 s61, s60, 8\n" \
"s_add_u32 s94, s61, 33554432\n" \
"s_lshl_b32 s61, s60, 12\n" \
"s_add_u32 s84, s61, 34078720\n" \
"v_and_b32 v250, 15, v248\n" \
"v_lshrrev_b32 v251, 4, v248\n" \
"v_lshrrev_b32 v252, 1, v250\n" \
"v_xor_b32 v252, v252, v251\n" \
"v_lshlrev_b32 v252, 4, v252\n" \
"v_lshl_add_u32 v252, v250, 7, v252\n" \
"s_add_u32 m0, s55, 4096\n" \
"s_nop 0\n" \
"buffer_load_dwordx4 v234, s[24:27], s49 offen lds\n" \
"v_lshl_add_u32 v237, v247, 13, v252\n" \
"v_add_u32 v237, 0x8000, v237\n" \
"v_xor_b32 v238, 64, v237\n" \
"v_add_u32 v245, 94208, v252\n" \
"v_xor_b32 v246, 64, v245\n" \
"v_lshrrev_b32 v252, 2, v250\n" \
"v_and_b32 v253, 3, v248\n" \
"v_mfma_f32_32x32x16_bf16 v[48:63], v[224:227], v[224:227], 0\n" \
"v_lshlrev_b32 v254, 10, v251\n" \
"v_lshl_add_u32 v254, v252, 7, v254\n" \
"s_add_u32 m0, s55, 8192\n" \
"s_nop 0\n" \
"buffer_load_dwordx4 v234, s[24:27], s50 offen lds\n" \
"v_lshl_add_u32 v254, v253, 3, v254\n" \
"v_add_u32 v254, 65536, v254\n" \
"v_and_b32 v26, 2, v252\n" \
"v_and_b32 v27, 1, v251\n" \
"v_or_b32 v26, v26, v27\n" \
"v_lshlrev_b32 v26, 5, v26\n" \
"v_xor_b32 v28, 0, v26\n" \
"v_add_u32 v239, v254, v28\n" \
"v_xor_b32 v28, 32, v26\n" \
"v_add_u32 v240, v254, v28\n" \
"s_add_u32 m0, s55, 12288\n" \
"s_nop 0\n" \
"buffer_load_dwordx4 v234, s[24:27], s51 offen lds\n" \
"v_xor_b32 v28, 64, v26\n" \
"v_add_u32 v241, v254, v28\n" \
"v_xor_b32 v28, 96, v26\n" \
"v_mfma_f32_32x32x16_bf16 v[64:79], v[224:227], v[224:227], 0\n" \
"v_add_u32 v242, v254, v28\n" \
"v_lshrrev_b32 v254, 1, v251\n" \
"v_lshlrev_b32 v254, 10, v254\n" \
"v_lshl_add_u32 v254, v27, 9, v254\n" \
"v_lshl_add_u32 v254, v252, 6, v254\n" \
"v_lshl_add_u32 v254, v253, 3, v254\n" \
"s_add_u32 m0, s55, 16384\n" \
"s_nop 0\n" \
"buffer_load_dwordx4 v234, s[24:27], s52 offen lds\n" \
"v_add_u32 v254, 90112, v254\n" \
"v_lshlrev_b32 v29, 5, v27\n" \
"v_xor_b32 v28, 0, v29\n" \
"v_add_u32 v243, v254, v28\n" \
"v_xor_b32 v28, 32, v29\n" \
"v_add_u32 v244, v254, v28\n" \
"v_lshl_add_u32 v29, v27, 2, v252\n" \
"v_lshrrev_b32 v30, 1, v251\n" \
"v_lshlrev_b32 v30, 10, v30\n" \
"v_mfma_f32_32x32x16_bf16 v[80:95], v[224:227], v[224:227], 0\n" \
"s_add_u32 m0, s55, 20480\n" \
"s_nop 0\n" \
"buffer_load_dwordx4 v234, s[24:27], s53 offen lds\n" \
"v_lshl_add_u32 v30, v29, 7, v30\n" \
"v_lshl_add_u32 v30, v253, 3, v30\n" \
"v_lshrrev_b32 v29, 1, v29\n" \
"v_lshlrev_b32 v29, 5, v29\n" \
"v_xor_b32 v28, 0, v29\n" \
"v_add_u32 v28, v30, v28\n" \
"v_accvgpr_write_b32 a234, v28\n" \
"v_xor_b32 v28, 32, v29\n" \
"v_add_u32 v28, v30, v28\n" \
"v_accvgpr_write_b32 a235, v28\n" \
"s_add_u32 m0, s55, 24576\n" \
"s_nop 0\n" \
"buffer_load_dwordx4 v235, s[28:31], 0 offen lds\n" \
"v_xor_b32 v28, 64, v29\n" \
"v_add_u32 v28, v30, v28\n" \
"v_accvgpr_write_b32 a236, v28\n" \
"v_xor_b32 v28, 96, v29\n" \
"v_add_u32 v28, v30, v28\n" \
"v_mfma_f32_32x32x16_bf16 v[96:111], v[224:227], v[224:227], 0\n" \
"v_accvgpr_write_b32 a237, v28\n" \
"v_and_b32 v28, 7, v250\n" \
"v_lshlrev_b32 v28, 12, v28\n" \
"v_lshrrev_b32 v29, 3, v250\n" \
"s_add_u32 s24, s24, 393216\n" \
"s_addc_u32 s25, s25, 0\n" \
"s_add_u32 s28, s28, 458752\n" \
"s_addc_u32 s29, s29, 0\n" \
"s_add_u32 s36, s36, 128\n" \
"s_addc_u32 s37, s37, 0\n" \
"s_add_u32 s55, s55, 30720\n" \
"s_add_u32 s63, s63, 1\n" \
"s_cmp_eq_u32 s63, 3\n" \
"s_cmov_b32 s55, s62\n" \
"s_cmov_b32 s63, 0\n" \
"s_add_u32 m0, s54, 0\n" \
"s_nop 0\n" \
"buffer_load_dwordx4 v232, s[20:23], s40 offen lds\n" \
"v_lshl_add_u32 v28, v29, 6, v28\n" \
"v_lshl_add_u32 v28, v27, 5, v28\n" \
"v_lshrrev_b32 v29, 1, v251\n" \
"v_lshl_add_u32 v28, v29, 4, v28\n" \
"s_lshl_b32 s60, s88, 2\n" \
"s_add_u32 s60, s60, s59\n" \
"s_lshl_b32 s60, s60, 17\n" \
"s_and_b32 s61, s91, 2\n" \
"s_lshl_b32 s61, s61, 15\n" \
"s_add_u32 s60, s60, s61\n" \
"s_lshl_b32 s61, s90, 8\n" \
"s_add_u32 s60, s60, s61\n" \
"s_and_b32 s61, s91, 1\n" \
"s_lshl_b32 s61, s61, 7\n" \
"s_add_u32 s60, s60, s61\n" \
"v_add_u32 v28, s60, v28\n" \
"v_accvgpr_write_b32 a242, v28\n" \
"v_lshlrev_b32 v28, 4, v250\n" \
"v_accvgpr_write_b32 a241, v28\n" \
"s_add_u32 m0, s54, 1024\n" \
"s_nop 0\n" \
"buffer_load_dwordx4 v233, s[20:23], s41 offen lds\n" \
"v_lshrrev_b32 v26, 3, v248\n" \
"v_mfma_f32_32x32x16_bf16 v[112:127], v[224:227], v[224:227], 0\n" \
"v_lshl_add_u32 v29, v247, 3, v26\n" \
"v_lshlrev_b32 v29, 11, v29\n" \
"v_and_b32 v26, 6, v26\n" \
"v_and_b32 v28, 7, v248\n" \
"v_xor_b32 v26, v26, v28\n" \
"v_lshl_add_u32 v29, v26, 4, v29\n" \
"v_accvgpr_write_b32 a233, v29\n" \
"v_lshlrev_b32 v28, 12, v247\n" \
"s_add_u32 m0, s54, 2048\n" \
"s_nop 0\n" \
"buffer_load_dwordx4 v232, s[20:23], s42 offen lds\n" \
"v_lshl_add_u32 v28, v248, 4, v28\n" \
"v_accvgpr_write_b32 a232, v28\n" \
"s_bfe_u32 s60, %[info], 0x5001a\n" \
"s_sub_u32 s60, s60, 1\n" \
"s_bfe_u32 s61, %[info], 0x50014\n" \
"v_min_u32 v28, s60, v248\n" \
"v_add_u32 v28, s61, v28\n" \
"v_lshlrev_b32 v28, 7, v28\n" \
"v_accvgpr_write_b32 a238, v28\n" \
"v_mfma_f32_32x32x16_bf16 v[128:143], v[224:227], v[224:227], 0\n" \
"v_min_u32 v28, 31, v248\n" \
"v_lshlrev_b32 v28, 7, v28\n" \
"s_add_u32 m0, s54, 3072\n" \
"s_nop 0\n" \
"buffer_load_dwordx4 v233, s[20:23], s43 offen lds\n" \
"v_accvgpr_write_b32 a239, v28\n" \
"v_min_u32 v28, 2, v248\n" \
"v_add_u32 v28, 1, v28\n" \
"v_xor_b32 v28, s91, v28\n" \
"v_lshlrev_b32 v28, 7, v28\n" \
"v_accvgpr_write_b32 a240, v28\n" \
"s_lshl_b32 s60, s88, 3\n" \
"s_add_u32 s60, s60, s90\n" \
"s_lshl_b32 s98, s60, 2\n" \
"s_add_u32 s60, s98, s59\n" \
"s_lshl_b32 s60, s60, 9\n" \
"s_add_u32 s99, s60, 34340864\n" \
"s_mov_b64 s[96:97], s[8:9]\n" \
"s_and_b32 s60, s88, 1\n" \
"s_lshl_b32 s60, s60, 3\n" \
"s_add_u32 s60, s60, s90\n" \
"s_lshl_b32 s60, s60, 2\n" \
"s_add_u32 s60, s60, s59\n" \
"s_lshl_b32 s60, s60, 9\n" \
"v_lshlrev_b32 v28, 7, v248\n" \
"s_add_u32 m0, s54, 4096\n" \
"s_nop 0\n" \
"buffer_load_dwordx4 v232, s[20:23], s44 offen lds\n" \
"v_add_u32 v28, s60, v28\n" \
"v_mov_b32 v29, 0x80000000\n" \
"v_cmp_gt_u32 vcc, 4, v248\n" \
"s_nop 1\n" \
"v_cndmask_b32 v28, v29, v28, vcc\n" \
"v_mfma_f32_32x32x16_bf16 v[144:159], v[224:227], v[224:227], 0\n" \
"v_accvgpr_write_b32 a243, v28\n" \
"v_lshlrev_b32 v28, 2, v251\n" \
"v_sub_u32 v28, v250, v28\n" \
"v_mov_b32 v30, 0xf149f2ca\n" \
"v_mov_b32 v31, 0\n" \
"s_or_b32 s61, s88, s59\n" \
"s_cmp_eq_u32 s61, 0\n" \
"s_cselect_b64 s[82:83], -1, 0\n" \
"s_add_u32 m0, s54, 5120\n" \
"s_nop 0\n" \
"buffer_load_dwordx4 v233, s[20:23], s45 offen lds\n" \
"v_subrev_u32 v29, 0, v28\n" \
"v_cmp_gt_u32 vcc, 8, v29\n" \
"s_nop 1\n" \
"v_cndmask_b32 v32, v30, v31, vcc\n" \
"v_accvgpr_write_b32 a220, v32\n" \
"v_cmp_gt_i32 vcc, -8, v29\n" \
"s_nop 1\n" \
"v_cndmask_b32 v32, v30, v31, vcc\n" \
"v_accvgpr_write_b32 a224, v32\n" \
"v_cndmask_b32 v32, v32, v30, s[82:83]\n" \
"v_accvgpr_write_b32 a228, v32\n" \
"v_subrev_u32 v29, 1, v28\n" \
"v_cmp_gt_u32 vcc, 8, v29\n" \
"s_nop 1\n" \
"v_cndmask_b32 v32, v30, v31, vcc\n" \
"v_mfma_f32_32x32x16_bf16 v[160:175], v[224:227], v[224:227], 0\n" \
"s_add_u32 m0, s54, 6144\n" \
"s_nop 0\n" \
"buffer_load_dwordx4 v232, s[20:23], s46 offen lds\n" \
"v_accvgpr_write_b32 a221, v32\n" \
"v_cmp_gt_i32 vcc, -8, v29\n" \
"s_nop 1\n" \
"v_cndmask_b32 v32, v30, v31, vcc\n" \
"v_accvgpr_write_b32 a225, v32\n" \
"v_cndmask_b32 v32, v32, v30, s[82:83]\n" \
"v_accvgpr_write_b32 a229, v32\n" \
"v_subrev_u32 v29, 2, v28\n" \
"v_cmp_gt_u32 vcc, 8, v29\n" \
"s_nop 1\n" \
"v_cndmask_b32 v32, v30, v31, vcc\n" \
"v_accvgpr_write_b32 a222, v32\n" \
"v_cmp_gt_i32 vcc, -8, v29\n" \
"s_nop 1\n" \
"v_cndmask_b32 v32, v30, v31, vcc\n" \
"v_accvgpr_write_b32 a226, v32\n" \
"s_add_u32 m0, s54, 7168\n" \
"s_nop 0\n" \
"buffer_load_dwordx4 v233, s[20:23], s47 offen lds\n" \
"v_cndmask_b32 v32, v32, v30, s[82:83]\n" \
"v_accvgpr_write_b32 a230, v32\n" \
"v_subrev_u32 v29, 3, v28\n" \
"v_cmp_gt_u32 vcc, 8, v29\n" \
"s_nop 1\n" \
"v_cndmask_b32 v32, v30, v31, vcc\n" \
"v_accvgpr_write_b32 a223, v32\n" \
"v_mfma_f32_32x32x16_bf16 v[176:191], v[224:227], v[224:227], 0\n" \
"v_cmp_gt_i32 vcc, -8, v29\n" \
"s_nop 1\n" \
"v_cndmask_b32 v32, v30, v31, vcc\n" \
"v_accvgpr_write_b32 a227, v32\n" \
"v_cndmask_b32 v32, v32, v30, s[82:83]\n" \
"v_accvgpr_write_b32 a231, v32\n" \
"s_xor_b32 s54, s54, 32768\n" \
"s_add_u32 s20, s20, 128\n" \
"s_addc_u32 s21, s21, 0\n" \
"s_waitcnt vmcnt(8)\n" \
"s_barrier\n" \
"s_cmp_ge_u32 s59, 2\n" \
"s_cbranch_scc1 Lhp1_%=\n" \
"s_add_u32 m0, s55, 28672\n" \
"s_nop 0\n" \
"buffer_load_dwordx4 v236, s[36:39], 0 offen lds\n" \
"Lhp1_%=:\n" \
"ds_read_b128 a[0:3], v237 offset:0\n" \
"ds_read_b128 a[4:7], v237 offset:2048\n" \
"ds_read_b128 a[8:11], v237 offset:4096\n" \
"ds_read_b128 a[12:15], v237 offset:6144\n" \
"ds_read_b64_tr_b16 a[32:33], v239 offset:0\n" \
"ds_read_b64_tr_b16 a[34:35], v239 offset:512\n" \
"s_add_u32 m0, s55, 0\n" \
"s_nop 0\n" \
"buffer_load_dwordx4 v234, s[24:27], s48 offen lds\n" \
"v_subrev_u32 v29, 0, v28\n" \
"v_cmp_gt_i32 vcc, -8, v29\n" \
"v_mov_b32 v33, 0xffff\n" \
"v_mov_b32 v34, 0xffff0000\n" \
"v_cndmask_b32 v32, v31, v33, vcc\n" \
"v_subrev_u32 v29, 1, v28\n" \
"v_cmp_gt_i32 vcc, -8, v29\n" \
"s_nop 1\n" \
"v_cndmask_b32 v33, v31, v34, vcc\n" \
"v_or_b32 v32, v32, v33\n" \
"v_accvgpr_write_b32 a188, v32\n" \
"v_subrev_u32 v29, 0, v28\n" \
"v_cmp_gt_u32 vcc, 8, v29\n" \
"v_mov_b32 v33, 0xffff\n" \
"v_mov_b32 v34, 0xffff0000\n" \
"v_cndmask_b32 v32, v31, v33, vcc\n" \
"v_subrev_u32 v29, 1, v28\n" \
"v_cmp_gt_u32 vcc, 8, v29\n" \
"s_nop 1\n" \
"v_cndmask_b32 v33, v31, v34, vcc\n" \
"ds_read_b64_tr_b16 a[36:37], v240 offset:0\n" \
"ds_read_b64_tr_b16 a[38:39], v240 offset:512\n" \
"ds_read_b64_tr_b16 a[40:41], v241 offset:0\n" \
"ds_read_b64_tr_b16 a[42:43], v241 offset:512\n" \
"ds_read_b64_tr_b16 a[44:45], v242 offset:0\n" \
"ds_read_b64_tr_b16 a[46:47], v242 offset:512\n" \
"s_add_u32 m0, s55, 4096\n" \
"s_nop 0\n" \
"buffer_load_dwordx4 v234, s[24:27], s49 offen lds\n" \
"v_or_b32 v32, v32, v33\n" \
"v_mfma_f32_32x32x16_bf16 v[192:207], v[224:227], v[224:227], 0\n" \
"v_accvgpr_write_b32 a190, v32\n" \
"v_subrev_u32 v29, 2, v28\n" \
"v_cmp_gt_i32 vcc, -8, v29\n" \
"v_mov_b32 v33, 0xffff\n" \
"v_mov_b32 v34, 0xffff0000\n" \
"v_cndmask_b32 v32, v31, v33, vcc\n" \
"v_subrev_u32 v29, 3, v28\n" \
"v_cmp_gt_i32 vcc, -8, v29\n" \
"s_nop 1\n" \
"v_cndmask_b32 v33, v31, v34, vcc\n" \
"v_or_b32 v32, v32, v33\n" \
"v_accvgpr_write_b32 a189, v32\n" \
"v_subrev_u32 v29, 2, v28\n" \
"ds_read_b64_tr_b16 a[48:49], v239 offset:4096\n" \
"ds_read_b64_tr_b16 a[50:51], v239 offset:4608\n" \
"ds_read_b64_tr_b16 a[52:53], v240 offset:4096\n" \
"ds_read_b64_tr_b16 a[54:55], v240 offset:4608\n" \
"ds_read_b64_tr_b16 a[56:57], v241 offset:4096\n" \
"ds_read_b64_tr_b16 a[58:59], v241 offset:4608\n" \
"s_add_u32 m0, s55, 8192\n" \
"s_nop 0\n" \
"buffer_load_dwordx4 v234, s[24:27], s50 offen lds\n" \
"v_cmp_gt_u32 vcc, 8, v29\n" \
"v_mov_b32 v33, 0xffff\n" \
"v_mov_b32 v34, 0xffff0000\n" \
"v_cndmask_b32 v32, v31, v33, vcc\n" \
"v_subrev_u32 v29, 3, v28\n" \
"v_cmp_gt_u32 vcc, 8, v29\n" \
"s_nop 1\n" \
"v_cndmask_b32 v33, v31, v34, vcc\n" \
"v_or_b32 v32, v32, v33\n" \
"v_accvgpr_write_b32 a191, v32\n" \
"v_lshlrev_b32 v28, 4, v248\n" \
"v_lshlrev_b32 v29, 3, v248\n" \
"v_mfma_f32_32x32x16_bf16 v[208:223], v[224:227], v[224:227], 0\n" \
"s_lshl_b32 s60, s59, 11\n" \
"s_add_u32 s60, s60, 132096\n" \
"s_cmp_lt_u32 s59, 2\n" \
"s_cselect_b32 s61, 8, 512\n" \
"s_cselect_b64 s[82:83], -1, 0\n" \
"v_cndmask_b32 v30, v29, v28, s[82:83]\n" \
"ds_read_b64_tr_b16 a[60:61], v242 offset:4096\n" \
"ds_read_b64_tr_b16 a[62:63], v242 offset:4608\n" \
"ds_read_b64_tr_b16 a[64:65], v239 offset:8192\n" \
"ds_read_b64_tr_b16 a[66:67], v239 offset:8704\n" \
"ds_read_b64_tr_b16 a[68:69], v240 offset:8192\n" \
"ds_read_b64_tr_b16 a[70:71], v240 offset:8704\n" \
"s_add_u32 m0, s55, 12288\n" \
"s_nop 0\n" \
"buffer_load_dwordx4 v234, s[24:27], s51 offen lds\n" \
"v_add_u32 v30, s60, v30\n" \
"v_accvgpr_write_b32 a212, v30\n" \
"v_add_u32 v30, s61, v30\n" \
"v_accvgpr_write_b32 a213, v30\n" \
"s_lshl_b32 s60, s59, 13\n" \
"s_add_u32 s60, s60, 131072\n" \
"s_cmp_eq_u32 s59, 0\n" \
"s_cselect_b64 s[82:83], -1, 0\n" \
"v_add_u32 v28, s60, v28\n" \
"v_add_u32 v29, s60, v29\n" \
"v_accvgpr_write_b32 a214, v28\n" \
"v_accvgpr_write_b32 a215, v29\n" \
"v_and_b32 v30, 8, v248\n" \
"ds_read_b64_tr_b16 a[72:73], v241 offset:8192\n" \
"ds_read_b64_tr_b16 a[74:75], v241 offset:8704\n" \
"ds_read_b64_tr_b16 a[76:77], v242 offset:8192\n" \
"ds_read_b64_tr_b16 a[78:79], v242 offset:8704\n" \
"ds_read_b64_tr_b16 a[80:81], v243 offset:0\n" \
"ds_read_b64_tr_b16 a[82:83], v243 offset:256\n" \
"s_add_u32 m0, s55, 16384\n" \
"s_nop 0\n" \
"buffer_load_dwordx4 v234, s[24:27], s52 offen lds\n" \
"v_lshlrev_b32 v30, 4, v30\n" \
"v_sub_u32 v30, 1024, v30\n" \
"v_cndmask_b32 v30, 0, v30, s[82:83]\n" \
"v_add_u32 v28, v28, v30\n" \
"v_accvgpr_write_b32 a216, v28\n" \
"v_and_b32 v30, 32, v248\n" \
"v_lshlrev_b32 v30, 3, v30\n" \
"v_sub_u32 v30, 1024, v30\n" \
"v_cndmask_b32 v30, 0, v30, s[82:83]\n" \
"v_add_u32 v29, v29, v30\n" \
"ds_read_b64_tr_b16 a[84:85], v244 offset:0\n" \
"ds_read_b64_tr_b16 a[86:87], v244 offset:256\n" \
"ds_read_b128 a[144:147], v245\n" \
"ds_read_b128 a[16:19], v238 offset:0\n" \
"ds_read_b128 a[20:23], v238 offset:2048\n" \
"ds_read_b128 a[24:27], v238 offset:4096\n" \
"s_add_u32 m0, s55, 20480\n" \
"s_nop 0\n" \
"buffer_load_dwordx4 v234, s[24:27], s53 offen lds\n" \
"v_accvgpr_write_b32 a217, v29\n" \
"s_and_b32 s81, s91, 1\n" \
"s_lshl_b32 s81, s81, 7\n" \
"s_xor_b32 s80, s81, 128\n" \
"v_mfma_f32_32x32x16_bf16 v[0:15], v[224:227], v[224:227], 0\n" \
"v_mfma_f32_32x32x16_bf16 v[16:31], v[224:227], v[224:227], 0\n" \
"v_mfma_f32_32x32x16_bf16 v[32:47], v[224:227], v[224:227], 0\n" \
"v_mov_b64 v[228:229], 0\n" \
"v_mov_b64 v[230:231], 0\n" \
"ds_read_b128 a[28:31], v238 offset:6144\n" \
"s_add_u32 m0, s55, 24576\n" \
"s_nop 0\n" \
"buffer_load_dwordx4 v235, s[28:31], 0 offen lds\n" \
"s_add_u32 s24, s24, 393216\n" \
"s_addc_u32 s25, s25, 0\n" \
"s_add_u32 s28, s28, 458752\n" \
"s_addc_u32 s29, s29, 0\n" \
"s_add_u32 s36, s36, 128\n" \
"s_addc_u32 s37, s37, 0\n" \
"s_add_u32 s55, s55, 30720\n" \
"s_add_u32 s63, s63, 1\n" \
"s_cmp_eq_u32 s63, 3\n" \
"s_cmov_b32 s55, s62\n" \
"s_cmov_b32 s63, 0\n" \
"v_mov_b32 v252, 0\n" \
"buffer_load_dwordx2 a[244:245], v252, s[64:67], s93 offen sc1\n" \
"s_lshl_b32 s60, s91, 7\n" \
"s_add_u32 s60, s60, s99\n" \
"v_mov_b32 v252, s60\n" \
"v_writelane_b32 v252, s94, 2\n" \
"s_lshl_b32 s60, s92, 7\n" \
"s_add_u32 s60, s60, s84\n" \
"v_writelane_b32 v252, s60, 1\n" \
"s_add_u32 s60, s60, 131072\n" \
"v_writelane_b32 v252, s60, 3\n" \
"s_add_u32 s60, s94, 128\n" \
"v_writelane_b32 v252, s60, 4\n" \
"buffer_load_dword a210, v252, s[64:67], 0 offen sc1\n" \
"s_mov_b32 s60, s74\n" \
"s_mov_b64 s[76:77], s[4:5]\n" \
"s_add_u32 s76, s76, 2097152\n" \
"s_addc_u32 s77, s77, 0\n" \
"s_mov_b32 s78, 2097152\n" \
"s_mov_b32 s79, 0x20000\n" \
"s_add_u32 s0, s6, 2752512\n" \
"s_addc_u32 s1, s7, 0\n" \
"s_add_u32 s34, s12, 4194304\n" \
"s_addc_u32 s35, s13, 0\n" \
"s_mov_b32 s74, 4293918720\n" \
"s_mul_i32 s61, s60, 2048\n" \
"v_lshlrev_b32 v253, 7, v248\n" \
"v_add_u32 v252, s61, v253\n" \
"v_cmp_gt_u32 vcc, 16, v248\n" \
"v_mov_b32 v254, 0x80000000\n" \
"s_nop 0\n" \
"v_cndmask_b32 v254, v254, v252, vcc\n" \
"s_mov_b32 s82, 128\n" \
"s_mov_b32 s83, 0\n" \
"s_mov_b64 s[68:69], s[12:13]\n" \
"s_mov_b32 s70, 7340032\n" \
"s_mov_b32 s71, 0x20000\n" \
"s_mov_b32 s57, -1\n" \
"s_mov_b32 s58, 0\n" \
"s_mov_b32 s95, 4294905856\n" \
"s_mov_b32 s85, 30720\n" \
"s_cmp_eq_u32 s59, 1\n" \
"s_cbranch_scc1 Lqe1_%=\n" \
"s_cmp_eq_u32 s59, 2\n" \
"s_cbranch_scc1 Lqe2_%=\n" \
"s_cmp_eq_u32 s59, 3\n" \
"s_cbranch_scc1 Lqe3_%=\n" \
"Lqe0_%=:\n" \
"s_add_u32 m0, s55, 0\n" \
"s_nop 0\n" \
"buffer_load_dwordx4 v234, s[24:27], s48 offen lds\n" \
"s_add_u32 m0, s55, 4096\n" \
"s_nop 0\n" \
"buffer_load_dwordx4 v234, s[24:27], s49 offen lds\n" \
"s_add_u32 m0, s55, 8192\n" \
"s_nop 0\n" \
"buffer_load_dwordx4 v234, s[24:27], s50 offen lds\n" \
"s_add_u32 m0, s55, 12288\n" \
"s_nop 0\n" \
"buffer_load_dwordx4 v234, s[24:27], s51 offen lds\n" \
"s_add_u32 m0, s55, 16384\n" \
"s_nop 0\n" \
"buffer_load_dwordx4 v234, s[24:27], s52 offen lds\n" \
"s_add_u32 m0, s55, 20480\n" \
"s_nop 0\n" \
"buffer_load_dwordx4 v234, s[24:27], s53 offen lds\n" \
"s_add_u32 m0, s55, 24576\n" \
"s_nop 0\n" \
"buffer_load_dwordx4 v235, s[28:31], 0 offen lds\n" \
"s_add_u32 m0, s55, 28672\n" \
"s_nop 0\n" \
"buffer_load_dwordx4 v236, s[36:39], 0 offen lds\n" \
"s_add_u32 s24, s24, 393216\n" \
"s_addc_u32 s25, s25, 0\n" \
"s_add_u32 s28, s28, 458752\n" \
"s_addc_u32 s29, s29, 0\n" \
"s_branch Lqin0_%=\n" \
".p2align 6\n" \
"Lqm0_%=:\n" \
"v_mfma_f32_16x16x32_bf16 v[188:191], a[28:31], a[132:135], v[188:191]\n" \
"s_add_u32 s24, s24, 393216\n" \
"s_addc_u32 s25, s25, 0\n" \
"v_mfma_f32_16x16x32_bf16 v[216:219], a[136:139], a[28:31], v[216:219]\n" \
"s_add_u32 s28, s28, 458752\n" \
"s_addc_u32 s29, s29, 0\n" \
"v_mfma_f32_16x16x32_bf16 v[220:223], a[140:143], a[28:31], v[220:223]\n" \
"ds_read_b128 a[28:31], v238 offset:6144\n" \
"Lqin0_%=:\n" \
"s_waitcnt lgkmcnt(1)\n" \
"v_mfma_f32_16x16x32_bf16 v[0:3], a[32:35], a[0:3], v[0:3]\n" \
"s_add_u32 s36, s36, 128\n" \
"s_addc_u32 s37, s37, 0\n" \
"v_mfma_f32_16x16x32_bf16 v[8:11], a[32:35], a[4:7], v[8:11]\n" \
"s_add_u32 m0, s54, 0\n" \
"ds_read_b64_tr_b16 a[88:89], v239 offset:12288\n" \
"v_mfma_f32_16x16x32_bf16 v[16:19], a[32:35], a[8:11], v[16:19]\n" \
"buffer_load_dwordx4 v232, s[20:23], s40 offen lds\n" \
"v_mfma_f32_16x16x32_bf16 v[24:27], a[32:35], a[12:15], v[24:27]\n" \
"s_add_u32 s55, s55, 30720\n" \
"ds_read_b64_tr_b16 a[90:91], v239 offset:12800\n" \
"v_mfma_f32_16x16x32_bf16 v[28:31], a[36:39], a[12:15], v[28:31]\n" \
"ds_read_b64_tr_b16 a[92:93], v240 offset:12288\n" \
"v_mfma_f32_16x16x32_bf16 v[20:23], a[36:39], a[8:11], v[20:23]\n" \
"s_add_u32 s74, s74, 1048576\n" \
"s_add_u32 s63, s63, 1\n" \
"v_mfma_f32_16x16x32_bf16 v[12:15], a[36:39], a[4:7], v[12:15]\n" \
"ds_read_b64_tr_b16 a[94:95], v240 offset:12800\n" \
"v_mfma_f32_16x16x32_bf16 v[4:7], a[36:39], a[0:3], v[4:7]\n" \
"s_add_u32 m0, s54, 1024\n" \
"ds_read_b64_tr_b16 a[96:97], v241 offset:12288\n" \
"v_mfma_f32_16x16x32_bf16 v[32:35], a[40:43], a[0:3], v[32:35]\n" \
"buffer_load_dwordx4 v233, s[20:23], s41 offen lds\n" \
"v_mfma_f32_16x16x32_bf16 v[40:43], a[40:43], a[4:7], v[40:43]\n" \
"s_cmp_eq_u32 s58, 3\n" \
"s_cmov_b64 s[76:77], s[0:1]\n" \
"s_cmov_b32 s78, 3538944\n" \
"s_cmov_b32 s74, 0\n" \
"v_mfma_f32_16x16x32_bf16 v[48:51], a[40:43], a[8:11], v[48:51]\n" \
"s_cmp_eq_u32 s63, 3\n" \
"s_cmov_b32 s55, s62\n" \
"s_cmov_b32 s63, 0\n" \
"v_mfma_f32_16x16x32_bf16 v[56:59], a[40:43], a[12:15], v[56:59]\n" \
"ds_read_b64_tr_b16 a[98:99], v241 offset:12800\n" \
"v_mfma_f32_16x16x32_bf16 v[60:63], a[44:47], a[12:15], v[60:63]\n" \
"s_cmp_ge_u32 s58, 13\n" \
"s_cselect_b32 s26, 0, s26\n" \
"s_cselect_b32 s55, 0x30000, s55\n" \
"v_mfma_f32_16x16x32_bf16 v[52:55], a[44:47], a[8:11], v[52:55]\n" \
"s_cmp_eq_u32 s58, 7\n" \
"s_cmov_b64 s[76:77], s[34:35]\n" \
"s_cmov_b32 s78, 3145728\n" \
"s_cmov_b32 s74, 0\n" \
"v_mfma_f32_16x16x32_bf16 v[44:47], a[44:47], a[4:7], v[44:47]\n" \
"s_add_u32 m0, s54, 2048\n" \
"ds_read_b64_tr_b16 a[100:101], v242 offset:12288\n" \
"v_mfma_f32_16x16x32_bf16 v[36:39], a[44:47], a[0:3], v[36:39]\n" \
"buffer_load_dwordx4 v232, s[20:23], s42 offen lds\n" \
"v_mfma_f32_16x16x32_bf16 v[64:67], a[48:51], a[0:3], v[64:67]\n" \
"s_cmp_ge_u32 s58, 13\n" \
"s_cselect_b32 s30, 0, s30\n" \
"s_cselect_b32 s38, 0, s38\n" \
"v_mfma_f32_16x16x32_bf16 v[72:75], a[48:51], a[4:7], v[72:75]\n" \
"s_cmp_eq_u32 s58, 10\n" \
"s_cmov_b64 s[76:77], s[96:97]\n" \
"s_cmov_b32 s78, 2097152\n" \
"s_cmov_b32 s74, 0\n" \
"v_mfma_f32_16x16x32_bf16 v[80:83], a[48:51], a[8:11], v[80:83]\n" \
"s_add_u32 s57, s57, 1\n" \
"ds_read_b64_tr_b16 a[102:103], v242 offset:12800\n" \
"v_mfma_f32_16x16x32_bf16 v[88:91], a[48:51], a[12:15], v[88:91]\n" \
"ds_read_b64_tr_b16 a[104:105], v239 offset:16384\n" \
"v_mfma_f32_16x16x32_bf16 v[92:95], a[52:55], a[12:15], v[92:95]\n" \
"s_add_u32 m0, s54, 3072\n" \
"ds_read_b64_tr_b16 a[106:107], v239 offset:16896\n" \
"v_mfma_f32_16x16x32_bf16 v[84:87], a[52:55], a[8:11], v[84:87]\n" \
"buffer_load_dwordx4 v233, s[20:23], s43 offen lds\n" \
"v_mfma_f32_16x16x32_bf16 v[76:79], a[52:55], a[4:7], v[76:79]\n" \
"s_cmp_eq_u32 s57, 2\n" \
"s_cselect_b32 s56, s95, s85\n" \
"s_cselect_b32 s57, -1, s57\n" \
"v_mfma_f32_16x16x32_bf16 v[68:71], a[52:55], a[0:3], v[68:71]\n" \
"v_mfma_f32_16x16x32_bf16 v[96:99], a[56:59], a[0:3], v[96:99]\n" \
"ds_read_b64_tr_b16 a[108:109], v240 offset:16384\n" \
"v_mfma_f32_16x16x32_bf16 v[104:107], a[56:59], a[4:7], v[104:107]\n" \
"ds_read_b64_tr_b16 a[110:111], v240 offset:16896\n" \
"v_mfma_f32_16x16x32_bf16 v[112:115], a[56:59], a[8:11], v[112:115]\n" \
"ds_read_b64_tr_b16 a[112:113], v241 offset:16384\n" \
"v_mfma_f32_16x16x32_bf16 v[120:123], a[56:59], a[12:15], v[120:123]\n" \
"s_add_u32 m0, s54, 4096\n" \
"ds_read_b64_tr_b16 a[114:115], v241 offset:16896\n" \
"v_mfma_f32_16x16x32_bf16 v[124:127], a[60:63], a[12:15], v[124:127]\n" \
"buffer_load_dwordx4 v232, s[20:23], s44 offen lds\n" \
"v_mfma_f32_16x16x32_bf16 v[116:119], a[60:63], a[8:11], v[116:119]\n" \
"ds_read_b64_tr_b16 a[116:117], v242 offset:16384\n" \
"v_mfma_f32_16x16x32_bf16 v[108:111], a[60:63], a[4:7], v[108:111]\n" \
"ds_read_b64_tr_b16 a[118:119], v242 offset:16896\n" \
"v_mfma_f32_16x16x32_bf16 v[100:103], a[60:63], a[0:3], v[100:103]\n" \
"ds_read_b64_tr_b16 a[120:121], v239 offset:20480\n" \
"v_mfma_f32_16x16x32_bf16 v[128:131], a[0:3], a[64:67], v[128:131]\n" \
"ds_read_b64_tr_b16 a[122:123], v239 offset:20992\n" \
"v_mfma_f32_16x16x32_bf16 v[136:139], a[4:7], a[64:67], v[136:139]\n" \
"ds_read_b64_tr_b16 a[124:125], v240 offset:20480\n" \
"v_mfma_f32_16x16x32_bf16 v[144:147], a[8:11], a[64:67], v[144:147]\n" \
"s_add_u32 m0, s54, 5120\n" \
"ds_read_b64_tr_b16 a[126:127], v240 offset:20992\n" \
"v_mfma_f32_16x16x32_bf16 v[152:155], a[12:15], a[64:67], v[152:155]\n" \
"buffer_load_dwordx4 v233, s[20:23], s45 offen lds\n" \
"v_mfma_f32_16x16x32_bf16 v[156:159], a[12:15], a[68:71], v[156:159]\n" \
"v_mfma_f32_16x16x32_bf16 v[148:151], a[8:11], a[68:71], v[148:151]\n" \
"ds_read_b64_tr_b16 a[128:129], v241 offset:20480\n" \
"v_mfma_f32_16x16x32_bf16 v[140:143], a[4:7], a[68:71], v[140:143]\n" \
"ds_read_b64_tr_b16 a[130:131], v241 offset:20992\n" \
"v_mfma_f32_16x16x32_bf16 v[132:135], a[0:3], a[68:71], v[132:135]\n" \
"ds_read_b64_tr_b16 a[132:133], v242 offset:20480\n" \
"v_mfma_f32_16x16x32_bf16 v[160:163], a[0:3], a[72:75], v[160:163]\n" \
"s_add_u32 m0, s54, 6144\n" \
"ds_read_b64_tr_b16 a[134:135], v242 offset:20992\n" \
"v_mfma_f32_16x16x32_bf16 v[168:171], a[4:7], a[72:75], v[168:171]\n" \
"s_waitcnt lgkmcnt(15)\n" \
"buffer_load_dwordx4 v232, s[20:23], s46 offen lds\n" \
"v_mfma_f32_16x16x32_bf16 v[176:179], a[8:11], a[72:75], v[176:179]\n" \
"ds_read_b64_tr_b16 a[136:137], v243 offset:2048\n" \
"v_mfma_f32_16x16x32_bf16 v[184:187], a[12:15], a[72:75], v[184:187]\n" \
"ds_read_b64_tr_b16 a[138:139], v243 offset:2304\n" \
"v_mfma_f32_16x16x32_bf16 v[188:191], a[12:15], a[76:79], v[188:191]\n" \
"ds_read_b64_tr_b16 a[140:141], v244 offset:2048\n" \
"v_mfma_f32_16x16x32_bf16 v[180:183], a[8:11], a[76:79], v[180:183]\n" \
"ds_read_b64_tr_b16 a[142:143], v244 offset:2304\n" \
"v_mfma_f32_16x16x32_bf16 v[172:175], a[4:7], a[76:79], v[172:175]\n" \
"ds_read_b128 a[148:151], v246\n" \
"v_mfma_f32_16x16x32_bf16 v[164:167], a[0:3], a[76:79], v[164:167]\n" \
"s_add_u32 m0, s54, 7168\n" \
"v_mfma_f32_16x16x32_bf16 v[192:195], a[80:83], a[0:3], v[192:195]\n" \
"buffer_load_dwordx4 v233, s[20:23], s47 offen lds\n" \
"v_mfma_f32_16x16x32_bf16 v[200:203], a[80:83], a[4:7], v[200:203]\n" \
"s_add_u32 s20, s20, s82\n" \
"s_addc_u32 s21, s21, s83\n" \
"v_mfma_f32_16x16x32_bf16 v[208:211], a[80:83], a[8:11], v[208:211]\n" \
"s_xor_b32 s54, s54, 32768\n" \
"v_mfma_f32_16x16x32_bf16 v[216:219], a[80:83], a[12:15], v[216:219]\n" \
"v_mfma_f32_16x16x32_bf16 v[220:223], a[84:87], a[12:15], v[220:223]\n" \
"v_mfma_f32_16x16x32_bf16 v[212:215], a[84:87], a[8:11], v[212:215]\n" \
"v_mfma_f32_16x16x32_bf16 v[204:207], a[84:87], a[4:7], v[204:207]\n" \
"v_mfma_f32_16x16x32_bf16 v[196:199], a[84:87], a[0:3], v[196:199]\n" \
"v_mfma_f32_16x16x32_bf16 v[224:227], a[48:51], a[144:147], v[224:227]\n" \
"v_mfma_f32_16x16x32_bf16 v[228:231], a[52:55], a[144:147], v[228:231]\n" \
"s_waitcnt vmcnt(17) lgkmcnt(0)\n" \
"s_barrier\n" \
"v_mfma_f32_16x16x32_bf16 v[0:3], a[88:91], a[16:19], v[0:3]\n" \
"v_xor_b32 v237, 0x8000, v237\n" \
"v_xor_b32 v238, 0x8000, v238\n" \
"v_mfma_f32_16x16x32_bf16 v[4:7], a[92:95], a[16:19], v[4:7]\n" \
"v_add_u32 v239, s56, v239\n" \
"v_add_u32 v240, s56, v240\n" \
"v_mfma_f32_16x16x32_bf16 v[32:35], a[96:99], a[16:19], v[32:35]\n" \
"v_add_u32 v241, s56, v241\n" \
"v_add_u32 v242, s56, v242\n" \
"v_mfma_f32_16x16x32_bf16 v[36:39], a[100:103], a[16:19], v[36:39]\n" \
"v_add_u32 v243, s56, v243\n" \
"v_add_u32 v244, s56, v244\n" \
"v_mfma_f32_16x16x32_bf16 v[64:67], a[104:107], a[16:19], v[64:67]\n" \
"v_add_u32 v245, s56, v245\n" \
"v_add_u32 v246, s56, v246\n" \
"v_mfma_f32_16x16x32_bf16 v[68:71], a[108:111], a[16:19], v[68:71]\n" \
"s_add_u32 m0, s55, 0\n" \
"ds_read_b128 a[0:3], v237 offset:0\n" \
"v_mfma_f32_16x16x32_bf16 v[96:99], a[112:115], a[16:19], v[96:99]\n" \
"buffer_load_dwordx4 v234, s[24:27], s48 offen lds\n" \
"v_mfma_f32_16x16x32_bf16 v[100:103], a[116:119], a[16:19], v[100:103]\n" \
"ds_read_b128 a[4:7], v237 offset:2048\n" \
"v_mfma_f32_16x16x32_bf16 v[128:131], a[16:19], a[120:123], v[128:131]\n" \
"ds_read_b128 a[8:11], v237 offset:4096\n" \
"v_mfma_f32_16x16x32_bf16 v[132:135], a[16:19], a[124:127], v[132:135]\n" \
"ds_read_b128 a[12:15], v237 offset:6144\n" \
"v_mfma_f32_16x16x32_bf16 v[160:163], a[16:19], a[128:131], v[160:163]\n" \
"s_add_u32 m0, s55, 4096\n" \
"ds_read_b64_tr_b16 a[32:33], v239 offset:0\n" \
"v_mfma_f32_16x16x32_bf16 v[164:167], a[16:19], a[132:135], v[164:167]\n" \
"buffer_load_dwordx4 v234, s[24:27], s49 offen lds\n" \
"v_mfma_f32_16x16x32_bf16 v[192:195], a[136:139], a[16:19], v[192:195]\n" \
"ds_read_b64_tr_b16 a[34:35], v239 offset:512\n" \
"v_mfma_f32_16x16x32_bf16 v[196:199], a[140:143], a[16:19], v[196:199]\n" \
"ds_read_b64_tr_b16 a[36:37], v240 offset:0\n" \
"v_mfma_f32_16x16x32_bf16 v[8:11], a[88:91], a[20:23], v[8:11]\n" \
"ds_read_b64_tr_b16 a[38:39], v240 offset:512\n" \
"v_mfma_f32_16x16x32_bf16 v[12:15], a[92:95], a[20:23], v[12:15]\n" \
"s_add_u32 m0, s55, 8192\n" \
"ds_read_b128 a[16:19], v238 offset:0\n" \
"v_mfma_f32_16x16x32_bf16 v[40:43], a[96:99], a[20:23], v[40:43]\n" \
"buffer_load_dwordx4 v234, s[24:27], s50 offen lds\n" \
"v_mfma_f32_16x16x32_bf16 v[44:47], a[100:103], a[20:23], v[44:47]\n" \
"ds_read_b64_tr_b16 a[40:41], v241 offset:0\n" \
"v_mfma_f32_16x16x32_bf16 v[72:75], a[104:107], a[20:23], v[72:75]\n" \
"ds_read_b64_tr_b16 a[42:43], v241 offset:512\n" \
"v_mfma_f32_16x16x32_bf16 v[76:79], a[108:111], a[20:23], v[76:79]\n" \
"ds_read_b64_tr_b16 a[44:45], v242 offset:0\n" \
"v_mfma_f32_16x16x32_bf16 v[104:107], a[112:115], a[20:23], v[104:107]\n" \
"s_add_u32 m0, s55, 12288\n" \
"ds_read_b64_tr_b16 a[46:47], v242 offset:512\n" \
"v_mfma_f32_16x16x32_bf16 v[108:111], a[116:119], a[20:23], v[108:111]\n" \
"buffer_load_dwordx4 v234, s[24:27], s51 offen lds\n" \
"v_mfma_f32_16x16x32_bf16 v[136:139], a[20:23], a[120:123], v[136:139]\n" \
"ds_read_b64_tr_b16 a[48:49], v239 offset:4096\n" \
"v_mfma_f32_16x16x32_bf16 v[140:143], a[20:23], a[124:127], v[140:143]\n" \
"ds_read_b64_tr_b16 a[50:51], v239 offset:4608\n" \
"v_mfma_f32_16x16x32_bf16 v[168:171], a[20:23], a[128:131], v[168:171]\n" \
"ds_read_b64_tr_b16 a[52:53], v240 offset:4096\n" \
"v_mfma_f32_16x16x32_bf16 v[172:175], a[20:23], a[132:135], v[172:175]\n" \
"s_add_u32 m0, s55, 16384\n" \
"ds_read_b64_tr_b16 a[54:55], v240 offset:4608\n" \
"v_mfma_f32_16x16x32_bf16 v[200:203], a[136:139], a[20:23], v[200:203]\n" \
"buffer_load_dwordx4 v234, s[24:27], s52 offen lds\n" \
"v_mfma_f32_16x16x32_bf16 v[204:207], a[140:143], a[20:23], v[204:207]\n" \
"ds_read_b64_tr_b16 a[56:57], v241 offset:4096\n" \
"v_mfma_f32_16x16x32_bf16 v[16:19], a[88:91], a[24:27], v[16:19]\n" \
"ds_read_b64_tr_b16 a[58:59], v241 offset:4608\n" \
"v_mfma_f32_16x16x32_bf16 v[20:23], a[92:95], a[24:27], v[20:23]\n" \
"ds_read_b128 a[20:23], v238 offset:2048\n" \
"ds_read_b64_tr_b16 a[60:61], v242 offset:4096\n" \
"v_mfma_f32_16x16x32_bf16 v[48:51], a[96:99], a[24:27], v[48:51]\n" \
"s_add_u32 m0, s55, 20480\n" \
"ds_read_b64_tr_b16 a[62:63], v242 offset:4608\n" \
"v_mfma_f32_16x16x32_bf16 v[52:55], a[100:103], a[24:27], v[52:55]\n" \
"buffer_load_dwordx4 v234, s[24:27], s53 offen lds\n" \
"v_mfma_f32_16x16x32_bf16 v[80:83], a[104:107], a[24:27], v[80:83]\n" \
"ds_read_b64_tr_b16 a[64:65], v239 offset:8192\n" \
"v_mfma_f32_16x16x32_bf16 v[84:87], a[108:111], a[24:27], v[84:87]\n" \
"ds_read_b64_tr_b16 a[66:67], v239 offset:8704\n" \
"v_mfma_f32_16x16x32_bf16 v[112:115], a[112:115], a[24:27], v[112:115]\n" \
"ds_read_b64_tr_b16 a[68:69], v240 offset:8192\n" \
"v_mfma_f32_16x16x32_bf16 v[116:119], a[116:119], a[24:27], v[116:119]\n" \
"s_add_u32 m0, s55, 24576\n" \
"ds_read_b64_tr_b16 a[70:71], v240 offset:8704\n" \
"v_mfma_f32_16x16x32_bf16 v[144:147], a[24:27], a[120:123], v[144:147]\n" \
"buffer_load_dwordx4 v235, s[28:31], 0 offen lds\n" \
"v_mfma_f32_16x16x32_bf16 v[148:151], a[24:27], a[124:127], v[148:151]\n" \
"ds_read_b64_tr_b16 a[72:73], v241 offset:8192\n" \
"v_mfma_f32_16x16x32_bf16 v[176:179], a[24:27], a[128:131], v[176:179]\n" \
"ds_read_b64_tr_b16 a[74:75], v241 offset:8704\n" \
"v_mfma_f32_16x16x32_bf16 v[180:183], a[24:27], a[132:135], v[180:183]\n" \
"ds_read_b64_tr_b16 a[76:77], v242 offset:8192\n" \
"v_mfma_f32_16x16x32_bf16 v[208:211], a[136:139], a[24:27], v[208:211]\n" \
"s_add_u32 m0, s55, 28672\n" \
"ds_read_b64_tr_b16 a[78:79], v242 offset:8704\n" \
"v_mfma_f32_16x16x32_bf16 v[212:215], a[140:143], a[24:27], v[212:215]\n" \
"buffer_load_dwordx4 v236, s[36:39], 0 offen lds\n" \
"v_mfma_f32_16x16x32_bf16 v[224:227], a[104:107], a[148:151], v[224:227]\n" \
"ds_read_b64_tr_b16 a[80:81], v243 offset:0\n" \
"v_mfma_f32_16x16x32_bf16 v[228:231], a[108:111], a[148:151], v[228:231]\n" \
"ds_read_b128 a[24:27], v238 offset:4096\n" \
"ds_read_b64_tr_b16 a[82:83], v243 offset:256\n" \
"v_mfma_f32_16x16x32_bf16 v[24:27], a[88:91], a[28:31], v[24:27]\n" \
"ds_read_b64_tr_b16 a[84:85], v244 offset:0\n" \
"v_mfma_f32_16x16x32_bf16 v[28:31], a[92:95], a[28:31], v[28:31]\n" \
"ds_read_b64_tr_b16 a[86:87], v244 offset:256\n" \
"v_mfma_f32_16x16x32_bf16 v[56:59], a[96:99], a[28:31], v[56:59]\n" \
"buffer_load_dword a246, v254, s[76:79], s74 offen\n" \
"v_mfma_f32_16x16x32_bf16 v[60:63], a[100:103], a[28:31], v[60:63]\n" \
"ds_read_b128 a[144:147], v245\n" \
"v_mfma_f32_16x16x32_bf16 v[88:91], a[104:107], a[28:31], v[88:91]\n" \
"v_mfma_f32_16x16x32_bf16 v[92:95], a[108:111], a[28:31], v[92:95]\n" \
"v_mfma_f32_16x16x32_bf16 v[120:123], a[112:115], a[28:31], v[120:123]\n" \
"v_mfma_f32_16x16x32_bf16 v[124:127], a[116:119], a[28:31], v[124:127]\n" \
"s_add_u32 s58, s58, 1\n" \
"v_mfma_f32_16x16x32_bf16 v[152:155], a[28:31], a[120:123], v[152:155]\n" \
"v_mfma_f32_16x16x32_bf16 v[156:159], a[28:31], a[124:127], v[156:159]\n" \
"v_mfma_f32_16x16x32_bf16 v[184:187], a[28:31], a[128:131], v[184:187]\n" \
"s_bitcmp1_b32 81920, s58\n" \
"s_cbranch_scc0 Lqm0_%=\n" \
"s_cmp_lt_u32 s58, 16\n" \
"s_cbranch_scc1 Lqsp0_%=\n" \
"v_mfma_f32_16x16x32_bf16 v[188:191], a[28:31], a[132:135], v[188:191]\n" \
"v_mfma_f32_16x16x32_bf16 v[216:219], a[136:139], a[28:31], v[216:219]\n" \
"v_mfma_f32_16x16x32_bf16 v[220:223], a[140:143], a[28:31], v[220:223]\n" \
"s_branch Lq_epi_%=\n" \
"Lqsp0_%=:\n" \
"s_cmp_ge_u32 s58, 12\n" \
"s_cbranch_scc1 Lqs20_%=\n" \
"Lqs20_%=:\n" \
"v_accvgpr_read_b32 v247, a253\n" \
"v_accvgpr_read_b32 v232, a233\n" \
"v_readlane_b32 s60, v247, 0\n" \
"v_readlane_b32 s82, v247, 12\n" \
"s_add_u32 s40, s81, 0\n" \
"s_add_u32 s41, s81, 16384\n" \
"s_add_u32 s42, s81, 32768\n" \
"s_add_u32 s43, s81, 49152\n" \
"s_add_u32 s44, s80, 0\n" \
"s_add_u32 s45, s80, 16384\n" \
"s_add_u32 s46, s80, 32768\n" \
"s_add_u32 s47, s80, 49152\n" \
"s_add_u32 s20, s68, s60\n" \
"s_addc_u32 s21, s69, 0\n" \
"s_sub_u32 s82, s82, s60\n" \
"s_ashr_i32 s83, s82, 31\n" \
"v_subrev_u32 v232, 0, v232\n" \
"v_mov_b32 v233, v232\n" \
"s_getpc_b64 s[76:77]\n" \
"Lct7_%=:\n" \
"s_add_u32 s76, s76, Lq_epi_%=-Lct7_%=\n" \
"s_addc_u32 s77, s77, 0\n" \
"s_mov_b32 s78, gmk_code_end-Lq_epi_%=\n" \
"s_mov_b32 s79, 0x20000\n" \
"v_accvgpr_read_b32 v249, a243\n" \
"buffer_load_dword a209, v249, s[76:79], 0 offen\n" \
"s_mov_b32 s78, 0\n" \
"s_branch Lqm0_%=\n" \
"Lqe1_%=:\n" \
"s_add_u32 m0, s55, 0\n" \
"s_nop 0\n" \
"buffer_load_dwordx4 v234, s[24:27], s48 offen lds\n" \
"s_add_u32 m0, s55, 4096\n" \
"s_nop 0\n" \
"buffer_load_dwordx4 v234, s[24:27], s49 offen lds\n" \
"s_add_u32 m0, s55, 8192\n" \
"s_nop 0\n" \
"buffer_load_dwordx4 v234, s[24:27], s50 offen lds\n" \
"s_add_u32 m0, s55, 12288\n" \
"s_nop 0\n" \
"buffer_load_dwordx4 v234, s[24:27], s51 offen lds\n" \
"s_add_u32 m0, s55, 16384\n" \
"s_nop 0\n" \
"buffer_load_dwordx4 v234, s[24:27], s52 offen lds\n" \
"s_add_u32 m0, s55, 20480\n" \
"s_nop 0\n" \
"buffer_load_dwordx4 v234, s[24:27], s53 offen lds\n" \
"s_add_u32 m0, s55, 24576\n" \
"s_nop 0\n" \
"buffer_load_dwordx4 v235, s[28:31], 0 offen lds\n" \
"s_add_u32 m0, s55, 28672\n" \
"s_nop 0\n" \
"buffer_load_dwordx4 v236, s[36:39], 0 offen lds\n" \
"s_add_u32 s24, s24, 393216\n" \
"s_addc_u32 s25, s25, 0\n" \
"s_add_u32 s28, s28, 458752\n" \
"s_addc_u32 s29, s29, 0\n" \
"s_branch Lqin1_%=\n" \
".p2align 6\n" \
"Lqm1_%=:\n" \
"v_mfma_f32_16x16x32_bf16 v[188:191], a[28:31], a[132:135], v[188:191]\n" \
"s_add_u32 s24, s24, 393216\n" \
"s_addc_u32 s25, s25, 0\n" \
"v_mfma_f32_16x16x32_bf16 v[216:219], a[136:139], a[28:31], v[216:219]\n" \
"s_add_u32 s28, s28, 458752\n" \
"s_addc_u32 s29, s29, 0\n" \
"v_mfma_f32_16x16x32_bf16 v[220:223], a[140:143], a[28:31], v[220:223]\n" \
"ds_read_b128 a[28:31], v238 offset:6144\n" \
"Lqin1_%=:\n" \
"s_waitcnt lgkmcnt(1)\n" \
"v_mfma_f32_16x16x32_bf16 v[0:3], a[32:35], a[0:3], v[0:3]\n" \
"s_add_u32 s36, s36, 128\n" \
"s_addc_u32 s37, s37, 0\n" \
"v_mfma_f32_16x16x32_bf16 v[8:11], a[32:35], a[4:7], v[8:11]\n" \
"ds_read_b64_tr_b16 a[88:89], v239 offset:12288\n" \
"v_mfma_f32_16x16x32_bf16 v[16:19], a[32:35], a[8:11], v[16:19]\n" \
"s_add_u32 m0, s54, 0\n" \
"s_add_u32 s55, s55, 30720\n" \
"v_mfma_f32_16x16x32_bf16 v[24:27], a[32:35], a[12:15], v[24:27]\n" \
"buffer_load_dwordx4 v232, s[20:23], s40 offen lds\n" \
"v_mfma_f32_16x16x32_bf16 v[28:31], a[36:39], a[12:15], v[28:31]\n" \
"s_add_u32 s63, s63, 1\n" \
"ds_read_b64_tr_b16 a[90:91], v239 offset:12800\n" \
"v_mfma_f32_16x16x32_bf16 v[20:23], a[36:39], a[8:11], v[20:23]\n" \
"s_add_u32 s74, s74, 1048576\n" \
"ds_read_b64_tr_b16 a[92:93], v240 offset:12288\n" \
"v_mfma_f32_16x16x32_bf16 v[12:15], a[36:39], a[4:7], v[12:15]\n" \
"s_cmp_eq_u32 s63, 3\n" \
"s_cmov_b32 s55, s62\n" \
"s_cmov_b32 s63, 0\n" \
"v_mfma_f32_16x16x32_bf16 v[4:7], a[36:39], a[0:3], v[4:7]\n" \
"ds_read_b64_tr_b16 a[94:95], v240 offset:12800\n" \
"v_mfma_f32_16x16x32_bf16 v[32:35], a[40:43], a[0:3], v[32:35]\n" \
"s_add_u32 m0, s54, 1024\n" \
"ds_read_b64_tr_b16 a[96:97], v241 offset:12288\n" \
"v_mfma_f32_16x16x32_bf16 v[40:43], a[40:43], a[4:7], v[40:43]\n" \
"buffer_load_dwordx4 v233, s[20:23], s41 offen lds\n" \
"v_mfma_f32_16x16x32_bf16 v[48:51], a[40:43], a[8:11], v[48:51]\n" \
"s_cmp_eq_u32 s58, 3\n" \
"s_cmov_b64 s[76:77], s[0:1]\n" \
"s_cmov_b32 s78, 3538944\n" \
"s_cmov_b32 s74, 0\n" \
"v_mfma_f32_16x16x32_bf16 v[56:59], a[40:43], a[12:15], v[56:59]\n" \
"s_cmp_ge_u32 s58, 13\n" \
"s_cselect_b32 s26, 0, s26\n" \
"s_cselect_b32 s55, 0x30000, s55\n" \
"v_mfma_f32_16x16x32_bf16 v[60:63], a[44:47], a[12:15], v[60:63]\n" \
"ds_read_b64_tr_b16 a[98:99], v241 offset:12800\n" \
"v_mfma_f32_16x16x32_bf16 v[52:55], a[44:47], a[8:11], v[52:55]\n" \
"s_cmp_ge_u32 s58, 13\n" \
"s_cselect_b32 s30, 0, s30\n" \
"s_cselect_b32 s38, 0, s38\n" \
"v_mfma_f32_16x16x32_bf16 v[44:47], a[44:47], a[4:7], v[44:47]\n" \
"s_cmp_eq_u32 s58, 7\n" \
"s_cmov_b64 s[76:77], s[34:35]\n" \
"s_cmov_b32 s78, 3145728\n" \
"s_cmov_b32 s74, 0\n" \
"v_mfma_f32_16x16x32_bf16 v[36:39], a[44:47], a[0:3], v[36:39]\n" \
"s_add_u32 m0, s54, 2048\n" \
"s_add_u32 s57, s57, 1\n" \
"v_mfma_f32_16x16x32_bf16 v[64:67], a[48:51], a[0:3], v[64:67]\n" \
"buffer_load_dwordx4 v232, s[20:23], s42 offen lds\n" \
"v_mfma_f32_16x16x32_bf16 v[72:75], a[48:51], a[4:7], v[72:75]\n" \
"s_cmp_eq_u32 s57, 2\n" \
"s_cselect_b32 s56, s95, s85\n" \
"s_cselect_b32 s57, -1, s57\n" \
"v_mfma_f32_16x16x32_bf16 v[80:83], a[48:51], a[8:11], v[80:83]\n" \
"s_cmp_eq_u32 s58, 10\n" \
"s_cmov_b64 s[76:77], s[96:97]\n" \
"s_cmov_b32 s78, 2097152\n" \
"s_cmov_b32 s74, 0\n" \
"v_mfma_f32_16x16x32_bf16 v[88:91], a[48:51], a[12:15], v[88:91]\n" \
"ds_read_b64_tr_b16 a[100:101], v242 offset:12288\n" \
"v_mfma_f32_16x16x32_bf16 v[92:95], a[52:55], a[12:15], v[92:95]\n" \
"ds_read_b64_tr_b16 a[102:103], v242 offset:12800\n" \
"v_mfma_f32_16x16x32_bf16 v[84:87], a[52:55], a[8:11], v[84:87]\n" \
"s_add_u32 m0, s54, 3072\n" \
"ds_read_b64_tr_b16 a[104:105], v239 offset:16384\n" \
"v_mfma_f32_16x16x32_bf16 v[76:79], a[52:55], a[4:7], v[76:79]\n" \
"buffer_load_dwordx4 v233, s[20:23], s43 offen lds\n" \
"v_mfma_f32_16x16x32_bf16 v[68:71], a[52:55], a[0:3], v[68:71]\n" \
"ds_read_b64_tr_b16 a[106:107], v239 offset:16896\n" \
"v_mfma_f32_16x16x32_bf16 v[96:99], a[56:59], a[0:3], v[96:99]\n" \
"v_mfma_f32_16x16x32_bf16 v[104:107], a[56:59], a[4:7], v[104:107]\n" \
"ds_read_b64_tr_b16 a[108:109], v240 offset:16384\n" \
"v_mfma_f32_16x16x32_bf16 v[112:115], a[56:59], a[8:11], v[112:115]\n" \
"ds_read_b64_tr_b16 a[110:111], v240 offset:16896\n" \
"v_mfma_f32_16x16x32_bf16 v[120:123], a[56:59], a[12:15], v[120:123]\n" \
"ds_read_b64_tr_b16 a[112:113], v241 offset:16384\n" \
"v_mfma_f32_16x16x32_bf16 v[124:127], a[60:63], a[12:15], v[124:127]\n" \
"s_add_u32 m0, s54, 4096\n" \
"ds_read_b64_tr_b16 a[114:115], v241 offset:16896\n" \
"v_mfma_f32_16x16x32_bf16 v[116:119], a[60:63], a[8:11], v[116:119]\n" \
"buffer_load_dwordx4 v232, s[20:23], s44 offen lds\n" \
"v_mfma_f32_16x16x32_bf16 v[108:111], a[60:63], a[4:7], v[108:111]\n" \
"ds_read_b64_tr_b16 a[116:117], v242 offset:16384\n" \
"v_mfma_f32_16x16x32_bf16 v[100:103], a[60:63], a[0:3], v[100:103]\n" \
"ds_read_b64_tr_b16 a[118:119], v242 offset:16896\n" \
"v_mfma_f32_16x16x32_bf16 v[128:131], a[0:3], a[64:67], v[128:131]\n" \
"ds_read_b64_tr_b16 a[120:121], v239 offset:20480\n" \
"v_mfma_f32_16x16x32_bf16 v[136:139], a[4:7], a[64:67], v[136:139]\n" \
"ds_read_b64_tr_b16 a[122:123], v239 offset:20992\n" \
"v_mfma_f32_16x16x32_bf16 v[144:147], a[8:11], a[64:67], v[144:147]\n" \
"ds_read_b64_tr_b16 a[124:125], v240 offset:20480\n" \
"v_mfma_f32_16x16x32_bf16 v[152:155], a[12:15], a[64:67], v[152:155]\n" \
"s_add_u32 m0, s54, 5120\n" \
"ds_read_b64_tr_b16 a[126:127], v240 offset:20992\n" \
"v_mfma_f32_16x16x32_bf16 v[156:159], a[12:15], a[68:71], v[156:159]\n" \
"buffer_load_dwordx4 v233, s[20:23], s45 offen lds\n" \
"v_mfma_f32_16x16x32_bf16 v[148:151], a[8:11], a[68:71], v[148:151]\n" \
"v_mfma_f32_16x16x32_bf16 v[140:143], a[4:7], a[68:71], v[140:143]\n" \
"ds_read_b64_tr_b16 a[128:129], v241 offset:20480\n" \
"v_mfma_f32_16x16x32_bf16 v[132:135], a[0:3], a[68:71], v[132:135]\n" \
"ds_read_b64_tr_b16 a[130:131], v241 offset:20992\n" \
"v_mfma_f32_16x16x32_bf16 v[160:163], a[0:3], a[72:75], v[160:163]\n" \
"ds_read_b64_tr_b16 a[132:133], v242 offset:20480\n" \
"v_mfma_f32_16x16x32_bf16 v[168:171], a[4:7], a[72:75], v[168:171]\n" \
"s_add_u32 m0, s54, 6144\n" \
"ds_read_b64_tr_b16 a[134:135], v242 offset:20992\n" \
"v_mfma_f32_16x16x32_bf16 v[176:179], a[8:11], a[72:75], v[176:179]\n" \
"s_waitcnt lgkmcnt(15)\n" \
"buffer_load_dwordx4 v232, s[20:23], s46 offen lds\n" \
"v_mfma_f32_16x16x32_bf16 v[184:187], a[12:15], a[72:75], v[184:187]\n" \
"ds_read_b64_tr_b16 a[136:137], v243 offset:2048\n" \
"v_mfma_f32_16x16x32_bf16 v[188:191], a[12:15], a[76:79], v[188:191]\n" \
"ds_read_b64_tr_b16 a[138:139], v243 offset:2304\n" \
"v_mfma_f32_16x16x32_bf16 v[180:183], a[8:11], a[76:79], v[180:183]\n" \
"ds_read_b64_tr_b16 a[140:141], v244 offset:2048\n" \
"v_mfma_f32_16x16x32_bf16 v[172:175], a[4:7], a[76:79], v[172:175]\n" \
"ds_read_b64_tr_b16 a[142:143], v244 offset:2304\n" \
"v_mfma_f32_16x16x32_bf16 v[164:167], a[0:3], a[76:79], v[164:167]\n" \
"ds_read_b128 a[148:151], v246\n" \
"v_mfma_f32_16x16x32_bf16 v[192:195], a[80:83], a[0:3], v[192:195]\n" \
"s_add_u32 m0, s54, 7168\n" \
"v_mfma_f32_16x16x32_bf16 v[200:203], a[80:83], a[4:7], v[200:203]\n" \
"buffer_load_dwordx4 v233, s[20:23], s47 offen lds\n" \
"v_mfma_f32_16x16x32_bf16 v[208:211], a[80:83], a[8:11], v[208:211]\n" \
"s_add_u32 s20, s20, s82\n" \
"s_addc_u32 s21, s21, s83\n" \
"v_mfma_f32_16x16x32_bf16 v[216:219], a[80:83], a[12:15], v[216:219]\n" \
"s_xor_b32 s54, s54, 32768\n" \
"v_mfma_f32_16x16x32_bf16 v[220:223], a[84:87], a[12:15], v[220:223]\n" \
"v_mfma_f32_16x16x32_bf16 v[212:215], a[84:87], a[8:11], v[212:215]\n" \
"v_mfma_f32_16x16x32_bf16 v[204:207], a[84:87], a[4:7], v[204:207]\n" \
"v_mfma_f32_16x16x32_bf16 v[196:199], a[84:87], a[0:3], v[196:199]\n" \
"v_mfma_f32_16x16x32_bf16 v[224:227], a[56:59], a[144:147], v[224:227]\n" \
"v_mfma_f32_16x16x32_bf16 v[228:231], a[60:63], a[144:147], v[228:231]\n" \
"s_waitcnt vmcnt(17) lgkmcnt(0)\n" \
"s_barrier\n" \
"v_mfma_f32_16x16x32_bf16 v[0:3], a[88:91], a[16:19], v[0:3]\n" \
"v_xor_b32 v237, 0x8000, v237\n" \
"v_xor_b32 v238, 0x8000, v238\n" \
"v_mfma_f32_16x16x32_bf16 v[4:7], a[92:95], a[16:19], v[4:7]\n" \
"v_add_u32 v239, s56, v239\n" \
"v_add_u32 v240, s56, v240\n" \
"v_mfma_f32_16x16x32_bf16 v[32:35], a[96:99], a[16:19], v[32:35]\n" \
"v_add_u32 v241, s56, v241\n" \
"v_add_u32 v242, s56, v242\n" \
"v_mfma_f32_16x16x32_bf16 v[36:39], a[100:103], a[16:19], v[36:39]\n" \
"v_add_u32 v243, s56, v243\n" \
"v_add_u32 v244, s56, v244\n" \
"v_mfma_f32_16x16x32_bf16 v[64:67], a[104:107], a[16:19], v[64:67]\n" \
"v_add_u32 v245, s56, v245\n" \
"v_add_u32 v246, s56, v246\n" \
"v_mfma_f32_16x16x32_bf16 v[68:71], a[108:111], a[16:19], v[68:71]\n" \
"ds_read_b128 a[0:3], v237 offset:0\n" \
"v_mfma_f32_16x16x32_bf16 v[96:99], a[112:115], a[16:19], v[96:99]\n" \
"s_add_u32 m0, s55, 0\n" \
"ds_read_b128 a[4:7], v237 offset:2048\n" \
"v_mfma_f32_16x16x32_bf16 v[100:103], a[116:119], a[16:19], v[100:103]\n" \
"buffer_load_dwordx4 v234, s[24:27], s48 offen lds\n" \
"v_mfma_f32_16x16x32_bf16 v[128:131], a[16:19], a[120:123], v[128:131]\n" \
"ds_read_b128 a[8:11], v237 offset:4096\n" \
"v_mfma_f32_16x16x32_bf16 v[132:135], a[16:19], a[124:127], v[132:135]\n" \
"ds_read_b128 a[12:15], v237 offset:6144\n" \
"v_mfma_f32_16x16x32_bf16 v[160:163], a[16:19], a[128:131], v[160:163]\n" \
"ds_read_b64_tr_b16 a[32:33], v239 offset:0\n" \
"v_mfma_f32_16x16x32_bf16 v[164:167], a[16:19], a[132:135], v[164:167]\n" \
"s_add_u32 m0, s55, 4096\n" \
"ds_read_b64_tr_b16 a[34:35], v239 offset:512\n" \
"v_mfma_f32_16x16x32_bf16 v[192:195], a[136:139], a[16:19], v[192:195]\n" \
"buffer_load_dwordx4 v234, s[24:27], s49 offen lds\n" \
"v_mfma_f32_16x16x32_bf16 v[196:199], a[140:143], a[16:19], v[196:199]\n" \
"ds_read_b64_tr_b16 a[36:37], v240 offset:0\n" \
"v_mfma_f32_16x16x32_bf16 v[8:11], a[88:91], a[20:23], v[8:11]\n" \
"ds_read_b64_tr_b16 a[38:39], v240 offset:512\n" \
"v_mfma_f32_16x16x32_bf16 v[12:15], a[92:95], a[20:23], v[12:15]\n" \
"ds_read_b128 a[16:19], v238 offset:0\n" \
"ds_read_b64_tr_b16 a[40:41], v241 offset:0\n" \
"v_mfma_f32_16x16x32_bf16 v[40:43], a[96:99], a[20:23], v[40:43]\n" \
"s_add_u32 m0, s55, 8192\n" \
"ds_read_b64_tr_b16 a[42:43], v241 offset:512\n" \
"v_mfma_f32_16x16x32_bf16 v[44:47], a[100:103], a[20:23], v[44:47]\n" \
"buffer_load_dwordx4 v234, s[24:27], s50 offen lds\n" \
"v_mfma_f32_16x16x32_bf16 v[72:75], a[104:107], a[20:23], v[72:75]\n" \
"ds_read_b64_tr_b16 a[44:45], v242 offset:0\n" \
"v_mfma_f32_16x16x32_bf16 v[76:79], a[108:111], a[20:23], v[76:79]\n" \
"ds_read_b64_tr_b16 a[46:47], v242 offset:512\n" \
"v_mfma_f32_16x16x32_bf16 v[104:107], a[112:115], a[20:23], v[104:107]\n" \
"ds_read_b64_tr_b16 a[48:49], v239 offset:4096\n" \
"v_mfma_f32_16x16x32_bf16 v[108:111], a[116:119], a[20:23], v[108:111]\n" \
"s_add_u32 m0, s55, 12288\n" \
"ds_read_b64_tr_b16 a[50:51], v239 offset:4608\n" \
"v_mfma_f32_16x16x32_bf16 v[136:139], a[20:23], a[120:123], v[136:139]\n" \
"buffer_load_dwordx4 v234, s[24:27], s51 offen lds\n" \
"v_mfma_f32_16x16x32_bf16 v[140:143], a[20:23], a[124:127], v[140:143]\n" \
"ds_read_b64_tr_b16 a[52:53], v240 offset:4096\n" \
"v_mfma_f32_16x16x32_bf16 v[168:171], a[20:23], a[128:131], v[168:171]\n" \
"ds_read_b64_tr_b16 a[54:55], v240 offset:4608\n" \
"v_mfma_f32_16x16x32_bf16 v[172:175], a[20:23], a[132:135], v[172:175]\n" \
"ds_read_b64_tr_b16 a[56:57], v241 offset:4096\n" \
"v_mfma_f32_16x16x32_bf16 v[200:203], a[136:139], a[20:23], v[200:203]\n" \
"s_add_u32 m0, s55, 16384\n" \
"v_mfma_f32_16x16x32_bf16 v[204:207], a[140:143], a[20:23], v[204:207]\n" \
"buffer_load_dwordx4 v234, s[24:27], s52 offen lds\n" \
"v_mfma_f32_16x16x32_bf16 v[16:19], a[88:91], a[24:27], v[16:19]\n" \
"ds_read_b64_tr_b16 a[58:59], v241 offset:4608\n" \
"v_mfma_f32_16x16x32_bf16 v[20:23], a[92:95], a[24:27], v[20:23]\n" \
"ds_read_b128 a[20:23], v238 offset:2048\n" \
"ds_read_b64_tr_b16 a[60:61], v242 offset:4096\n" \
"v_mfma_f32_16x16x32_bf16 v[48:51], a[96:99], a[24:27], v[48:51]\n" \
"ds_read_b64_tr_b16 a[62:63], v242 offset:4608\n" \
"v_mfma_f32_16x16x32_bf16 v[52:55], a[100:103], a[24:27], v[52:55]\n" \
"s_add_u32 m0, s55, 20480\n" \
"ds_read_b64_tr_b16 a[64:65], v239 offset:8192\n" \
"v_mfma_f32_16x16x32_bf16 v[80:83], a[104:107], a[24:27], v[80:83]\n" \
"buffer_load_dwordx4 v234, s[24:27], s53 offen lds\n" \
"v_mfma_f32_16x16x32_bf16 v[84:87], a[108:111], a[24:27], v[84:87]\n" \
"ds_read_b64_tr_b16 a[66:67], v239 offset:8704\n" \
"v_mfma_f32_16x16x32_bf16 v[112:115], a[112:115], a[24:27], v[112:115]\n" \
"ds_read_b64_tr_b16 a[68:69], v240 offset:8192\n" \
"v_mfma_f32_16x16x32_bf16 v[116:119], a[116:119], a[24:27], v[116:119]\n" \
"ds_read_b64_tr_b16 a[70:71], v240 offset:8704\n" \
"v_mfma_f32_16x16x32_bf16 v[144:147], a[24:27], a[120:123], v[144:147]\n" \
"s_add_u32 m0, s55, 24576\n" \
"ds_read_b64_tr_b16 a[72:73], v241 offset:8192\n" \
"v_mfma_f32_16x16x32_bf16 v[148:151], a[24:27], a[124:127], v[148:151]\n" \
"buffer_load_dwordx4 v235, s[28:31], 0 offen lds\n" \
"v_mfma_f32_16x16x32_bf16 v[176:179], a[24:27], a[128:131], v[176:179]\n" \
"ds_read_b64_tr_b16 a[74:75], v241 offset:8704\n" \
"v_mfma_f32_16x16x32_bf16 v[180:183], a[24:27], a[132:135], v[180:183]\n" \
"ds_read_b64_tr_b16 a[76:77], v242 offset:8192\n" \
"v_mfma_f32_16x16x32_bf16 v[208:211], a[136:139], a[24:27], v[208:211]\n" \
"ds_read_b64_tr_b16 a[78:79], v242 offset:8704\n" \
"v_mfma_f32_16x16x32_bf16 v[212:215], a[140:143], a[24:27], v[212:215]\n" \
"s_add_u32 m0, s55, 28672\n" \
"ds_read_b64_tr_b16 a[80:81], v243 offset:0\n" \
"v_mfma_f32_16x16x32_bf16 v[224:227], a[112:115], a[148:151], v[224:227]\n" \
"buffer_load_dwordx4 v236, s[36:39], 0 offen lds\n" \
"v_mfma_f32_16x16x32_bf16 v[228:231], a[116:119], a[148:151], v[228:231]\n" \
"ds_read_b128 a[24:27], v238 offset:4096\n" \
"ds_read_b64_tr_b16 a[82:83], v243 offset:256\n" \
"v_mfma_f32_16x16x32_bf16 v[24:27], a[88:91], a[28:31], v[24:27]\n" \
"ds_read_b64_tr_b16 a[84:85], v244 offset:0\n" \
"v_mfma_f32_16x16x32_bf16 v[28:31], a[92:95], a[28:31], v[28:31]\n" \
"ds_read_b64_tr_b16 a[86:87], v244 offset:256\n" \
"v_mfma_f32_16x16x32_bf16 v[56:59], a[96:99], a[28:31], v[56:59]\n" \
"ds_read_b128 a[144:147], v245\n" \
"v_mfma_f32_16x16x32_bf16 v[60:63], a[100:103], a[28:31], v[60:63]\n" \
"buffer_load_dword a246, v254, s[76:79], s74 offen\n" \
"v_mfma_f32_16x16x32_bf16 v[88:91], a[104:107], a[28:31], v[88:91]\n" \
"v_mfma_f32_16x16x32_bf16 v[92:95], a[108:111], a[28:31], v[92:95]\n" \
"v_mfma_f32_16x16x32_bf16 v[120:123], a[112:115], a[28:31], v[120:123]\n" \
"v_mfma_f32_16x16x32_bf16 v[124:127], a[116:119], a[28:31], v[124:127]\n" \
"s_add_u32 s58, s58, 1\n" \
"v_mfma_f32_16x16x32_bf16 v[152:155], a[28:31], a[120:123], v[152:155]\n" \
"v_mfma_f32_16x16x32_bf16 v[156:159], a[28:31], a[124:127], v[156:159]\n" \
"v_mfma_f32_16x16x32_bf16 v[184:187], a[28:31], a[128:131], v[184:187]\n" \
"s_bitcmp1_b32 81920, s58\n" \
"s_cbranch_scc0 Lqm1_%=\n" \
"s_cmp_lt_u32 s58, 16\n" \
"s_cbranch_scc1 Lqsp1_%=\n" \
"v_mfma_f32_16x16x32_bf16 v[188:191], a[28:31], a[132:135], v[188:191]\n" \
"v_mfma_f32_16x16x32_bf16 v[216:219], a[136:139], a[28:31], v[216:219]\n" \
"v_mfma_f32_16x16x32_bf16 v[220:223], a[140:143], a[28:31], v[220:223]\n" \
"s_branch Lq_epi_%=\n" \
"Lqsp1_%=:\n" \
"s_cmp_ge_u32 s58, 12\n" \
"s_cbranch_scc1 Lqs21_%=\n" \
"Lqs21_%=:\n" \
"v_accvgpr_read_b32 v247, a253\n" \
"v_accvgpr_read_b32 v232, a233\n" \
"v_readlane_b32 s60, v247, 1\n" \
"v_readlane_b32 s82, v247, 13\n" \
"s_add_u32 s40, s81, 0\n" \
"s_add_u32 s41, s81, 16384\n" \
"s_add_u32 s42, s81, 32768\n" \
"s_add_u32 s43, s81, 49152\n" \
"s_add_u32 s44, s80, 0\n" \
"s_add_u32 s45, s80, 16384\n" \
"s_add_u32 s46, s80, 32768\n" \
"s_add_u32 s47, s80, 49152\n" \
"s_add_u32 s20, s68, s60\n" \
"s_addc_u32 s21, s69, 0\n" \
"s_sub_u32 s82, s82, s60\n" \
"s_ashr_i32 s83, s82, 31\n" \
"v_subrev_u32 v232, 16384, v232\n" \
"v_mov_b32 v233, v232\n" \
"s_getpc_b64 s[76:77]\n" \
"Lct8_%=:\n" \
"s_add_u32 s76, s76, Lq_epi_%=-Lct8_%=\n" \
"s_addc_u32 s77, s77, 0\n" \
"s_mov_b32 s78, gmk_code_end-Lq_epi_%=\n" \
"s_mov_b32 s79, 0x20000\n" \
"v_accvgpr_read_b32 v249, a243\n" \
"buffer_load_dword a206, v249, s[76:79], 0 offen\n" \
"s_mov_b32 s78, 0\n" \
"s_branch Lqm1_%=\n" \
"Lqe2_%=:\n" \
"s_add_u32 m0, s55, 0\n" \
"s_nop 0\n" \
"buffer_load_dwordx4 v234, s[24:27], s48 offen lds\n" \
"s_add_u32 m0, s55, 4096\n" \
"s_nop 0\n" \
"buffer_load_dwordx4 v234, s[24:27], s49 offen lds\n" \
"s_add_u32 m0, s55, 8192\n" \
"s_nop 0\n" \
"buffer_load_dwordx4 v234, s[24:27], s50 offen lds\n" \
"s_add_u32 m0, s55, 12288\n" \
"s_nop 0\n" \
"buffer_load_dwordx4 v234, s[24:27], s51 offen lds\n" \
"s_add_u32 m0, s55, 16384\n" \
"s_nop 0\n" \
"buffer_load_dwordx4 v234, s[24:27], s52 offen lds\n" \
"s_add_u32 m0, s55, 20480\n" \
"s_nop 0\n" \
"buffer_load_dwordx4 v234, s[24:27], s53 offen lds\n" \
"s_add_u32 m0, s55, 24576\n" \
"s_nop 0\n" \
"buffer_load_dwordx4 v235, s[28:31], 0 offen lds\n" \
"s_add_u32 s24, s24, 393216\n" \
"s_addc_u32 s25, s25, 0\n" \
"s_add_u32 s28, s28, 458752\n" \
"s_addc_u32 s29, s29, 0\n" \
"s_branch Lqin2_%=\n" \
".p2align 6\n" \
"Lqm2_%=:\n" \
"v_mfma_f32_16x16x32_bf16 v[188:191], a[28:31], a[132:135], v[188:191]\n" \
"s_add_u32 s24, s24, 393216\n" \
"s_addc_u32 s25, s25, 0\n" \
"v_mfma_f32_16x16x32_bf16 v[216:219], a[136:139], a[28:31], v[216:219]\n" \
"s_add_u32 s28, s28, 458752\n" \
"s_addc_u32 s29, s29, 0\n" \
"v_mfma_f32_16x16x32_bf16 v[220:223], a[140:143], a[28:31], v[220:223]\n" \
"ds_read_b128 a[28:31], v238 offset:6144\n" \
"Lqin2_%=:\n" \
"s_waitcnt lgkmcnt(1)\n" \
"v_mfma_f32_16x16x32_bf16 v[0:3], a[32:35], a[0:3], v[0:3]\n" \
"s_add_u32 s36, s36, 128\n" \
"s_addc_u32 s37, s37, 0\n" \
"v_mfma_f32_16x16x32_bf16 v[8:11], a[32:35], a[4:7], v[8:11]\n" \
"ds_read_b64_tr_b16 a[88:89], v239 offset:12288\n" \
"v_mfma_f32_16x16x32_bf16 v[16:19], a[32:35], a[8:11], v[16:19]\n" \
"s_add_u32 s55, s55, 30720\n" \
"ds_read_b64_tr_b16 a[90:91], v239 offset:12800\n" \
"v_mfma_f32_16x16x32_bf16 v[24:27], a[32:35], a[12:15], v[24:27]\n" \
"s_add_u32 m0, s54, 0\n" \
"ds_read_b64_tr_b16 a[92:93], v240 offset:12288\n" \
"v_mfma_f32_16x16x32_bf16 v[28:31], a[36:39], a[12:15], v[28:31]\n" \
"buffer_load_dwordx4 v232, s[20:23], s40 offen lds\n" \
"v_mfma_f32_16x16x32_bf16 v[20:23], a[36:39], a[8:11], v[20:23]\n" \
"s_add_u32 s74, s74, 1048576\n" \
"s_add_u32 s63, s63, 1\n" \
"v_mfma_f32_16x16x32_bf16 v[12:15], a[36:39], a[4:7], v[12:15]\n" \
"ds_read_b64_tr_b16 a[94:95], v240 offset:12800\n" \
"v_mfma_f32_16x16x32_bf16 v[4:7], a[36:39], a[0:3], v[4:7]\n" \
"s_cmp_eq_u32 s63, 3\n" \
"s_cmov_b32 s55, s62\n" \
"s_cmov_b32 s63, 0\n" \
"v_mfma_f32_16x16x32_bf16 v[32:35], a[40:43], a[0:3], v[32:35]\n" \
"ds_read_b64_tr_b16 a[96:97], v241 offset:12288\n" \
"v_mfma_f32_16x16x32_bf16 v[40:43], a[40:43], a[4:7], v[40:43]\n" \
"s_add_u32 m0, s54, 1024\n" \
"ds_read_b64_tr_b16 a[98:99], v241 offset:12800\n" \
"v_mfma_f32_16x16x32_bf16 v[48:51], a[40:43], a[8:11], v[48:51]\n" \
"buffer_load_dwordx4 v233, s[20:23], s41 offen lds\n" \
"v_mfma_f32_16x16x32_bf16 v[56:59], a[40:43], a[12:15], v[56:59]\n" \
"s_cmp_eq_u32 s58, 3\n" \
"s_cmov_b64 s[76:77], s[0:1]\n" \
"s_cmov_b32 s78, 3538944\n" \
"s_cmov_b32 s74, 0\n" \
"v_mfma_f32_16x16x32_bf16 v[60:63], a[44:47], a[12:15], v[60:63]\n" \
"s_cmp_ge_u32 s58, 13\n" \
"s_cselect_b32 s26, 0, s26\n" \
"s_cselect_b32 s55, 0x30000, s55\n" \
"v_mfma_f32_16x16x32_bf16 v[52:55], a[44:47], a[8:11], v[52:55]\n" \
"ds_read_b64_tr_b16 a[100:101], v242 offset:12288\n" \
"v_mfma_f32_16x16x32_bf16 v[44:47], a[44:47], a[4:7], v[44:47]\n" \
"s_cmp_ge_u32 s58, 13\n" \
"s_cselect_b32 s30, 0, s30\n" \
"s_cselect_b32 s38, 0, s38\n" \
"v_mfma_f32_16x16x32_bf16 v[36:39], a[44:47], a[0:3], v[36:39]\n" \
"s_cmp_eq_u32 s58, 7\n" \
"s_cmov_b64 s[76:77], s[34:35]\n" \
"s_cmov_b32 s78, 3145728\n" \
"s_cmov_b32 s74, 0\n" \
"v_mfma_f32_16x16x32_bf16 v[64:67], a[48:51], a[0:3], v[64:67]\n" \
"s_add_u32 m0, s54, 2048\n" \
"s_add_u32 s57, s57, 1\n" \
"v_mfma_f32_16x16x32_bf16 v[72:75], a[48:51], a[4:7], v[72:75]\n" \
"buffer_load_dwordx4 v232, s[20:23], s42 offen lds\n" \
"v_mfma_f32_16x16x32_bf16 v[80:83], a[48:51], a[8:11], v[80:83]\n" \
"s_cmp_eq_u32 s57, 2\n" \
"s_cselect_b32 s56, s95, s85\n" \
"s_cselect_b32 s57, -1, s57\n" \
"v_mfma_f32_16x16x32_bf16 v[88:91], a[48:51], a[12:15], v[88:91]\n" \
"s_cmp_eq_u32 s58, 10\n" \
"s_cmov_b64 s[76:77], s[96:97]\n" \
"s_cmov_b32 s78, 2097152\n" \
"s_cmov_b32 s74, 0\n" \
"v_mfma_f32_16x16x32_bf16 v[92:95], a[52:55], a[12:15], v[92:95]\n" \
"ds_read_b64_tr_b16 a[102:103], v242 offset:12800\n" \
"v_mfma_f32_16x16x32_bf16 v[84:87], a[52:55], a[8:11], v[84:87]\n" \
"ds_read_b64_tr_b16 a[104:105], v239 offset:16384\n" \
"v_mfma_f32_16x16x32_bf16 v[76:79], a[52:55], a[4:7], v[76:79]\n" \
"s_add_u32 m0, s54, 3072\n" \
"ds_read_b64_tr_b16 a[106:107], v239 offset:16896\n" \
"v_mfma_f32_16x16x32_bf16 v[68:71], a[52:55], a[0:3], v[68:71]\n" \
"buffer_load_dwordx4 v233, s[20:23], s43 offen lds\n" \
"v_mfma_f32_16x16x32_bf16 v[96:99], a[56:59], a[0:3], v[96:99]\n" \
"v_mfma_f32_16x16x32_bf16 v[104:107], a[56:59], a[4:7], v[104:107]\n" \
"ds_read_b64_tr_b16 a[108:109], v240 offset:16384\n" \
"v_mfma_f32_16x16x32_bf16 v[112:115], a[56:59], a[8:11], v[112:115]\n" \
"ds_read_b64_tr_b16 a[110:111], v240 offset:16896\n" \
"v_mfma_f32_16x16x32_bf16 v[120:123], a[56:59], a[12:15], v[120:123]\n" \
"ds_read_b64_tr_b16 a[112:113], v241 offset:16384\n" \
"v_mfma_f32_16x16x32_bf16 v[124:127], a[60:63], a[12:15], v[124:127]\n" \
"ds_read_b64_tr_b16 a[114:115], v241 offset:16896\n" \
"v_mfma_f32_16x16x32_bf16 v[116:119], a[60:63], a[8:11], v[116:119]\n" \
"s_add_u32 m0, s54, 4096\n" \
"ds_read_b64_tr_b16 a[116:117], v242 offset:16384\n" \
"v_mfma_f32_16x16x32_bf16 v[108:111], a[60:63], a[4:7], v[108:111]\n" \
"buffer_load_dwordx4 v232, s[20:23], s44 offen lds\n" \
"v_mfma_f32_16x16x32_bf16 v[100:103], a[60:63], a[0:3], v[100:103]\n" \
"ds_read_b64_tr_b16 a[118:119], v242 offset:16896\n" \
"v_mfma_f32_16x16x32_bf16 v[128:131], a[0:3], a[64:67], v[128:131]\n" \
"ds_read_b64_tr_b16 a[120:121], v239 offset:20480\n" \
"v_mfma_f32_16x16x32_bf16 v[136:139], a[4:7], a[64:67], v[136:139]\n" \
"ds_read_b64_tr_b16 a[122:123], v239 offset:20992\n" \
"v_mfma_f32_16x16x32_bf16 v[144:147], a[8:11], a[64:67], v[144:147]\n" \
"ds_read_b64_tr_b16 a[124:125], v240 offset:20480\n" \
"v_mfma_f32_16x16x32_bf16 v[152:155], a[12:15], a[64:67], v[152:155]\n" \
"ds_read_b64_tr_b16 a[126:127], v240 offset:20992\n" \
"v_mfma_f32_16x16x32_bf16 v[156:159], a[12:15], a[68:71], v[156:159]\n" \
"s_add_u32 m0, s54, 5120\n" \
"v_mfma_f32_16x16x32_bf16 v[148:151], a[8:11], a[68:71], v[148:151]\n" \
"buffer_load_dwordx4 v233, s[20:23], s45 offen lds\n" \
"v_mfma_f32_16x16x32_bf16 v[140:143], a[4:7], a[68:71], v[140:143]\n" \
"ds_read_b64_tr_b16 a[128:129], v241 offset:20480\n" \
"v_mfma_f32_16x16x32_bf16 v[132:135], a[0:3], a[68:71], v[132:135]\n" \
"ds_read_b64_tr_b16 a[130:131], v241 offset:20992\n" \
"v_mfma_f32_16x16x32_bf16 v[160:163], a[0:3], a[72:75], v[160:163]\n" \
"ds_read_b64_tr_b16 a[132:133], v242 offset:20480\n" \
"v_mfma_f32_16x16x32_bf16 v[168:171], a[4:7], a[72:75], v[168:171]\n" \
"ds_read_b64_tr_b16 a[134:135], v242 offset:20992\n" \
"v_mfma_f32_16x16x32_bf16 v[176:179], a[8:11], a[72:75], v[176:179]\n" \
"s_add_u32 m0, s54, 6144\n" \
"ds_read_b64_tr_b16 a[136:137], v243 offset:2048\n" \
"v_mfma_f32_16x16x32_bf16 v[184:187], a[12:15], a[72:75], v[184:187]\n" \
"s_waitcnt lgkmcnt(15)\n" \
"buffer_load_dwordx4 v232, s[20:23], s46 offen lds\n" \
"v_mfma_f32_16x16x32_bf16 v[188:191], a[12:15], a[76:79], v[188:191]\n" \
"ds_read_b64_tr_b16 a[138:139], v243 offset:2304\n" \
"v_mfma_f32_16x16x32_bf16 v[180:183], a[8:11], a[76:79], v[180:183]\n" \
"ds_read_b64_tr_b16 a[140:141], v244 offset:2048\n" \
"v_mfma_f32_16x16x32_bf16 v[172:175], a[4:7], a[76:79], v[172:175]\n" \
"ds_read_b64_tr_b16 a[142:143], v244 offset:2304\n" \
"v_mfma_f32_16x16x32_bf16 v[164:167], a[0:3], a[76:79], v[164:167]\n" \
"ds_read_b128 a[148:151], v246\n" \
"v_mfma_f32_16x16x32_bf16 v[192:195], a[80:83], a[0:3], v[192:195]\n" \
"v_mfma_f32_16x16x32_bf16 v[200:203], a[80:83], a[4:7], v[200:203]\n" \
"s_add_u32 m0, s54, 7168\n" \
"v_mfma_f32_16x16x32_bf16 v[208:211], a[80:83], a[8:11], v[208:211]\n" \
"buffer_load_dwordx4 v233, s[20:23], s47 offen lds\n" \
"v_mfma_f32_16x16x32_bf16 v[216:219], a[80:83], a[12:15], v[216:219]\n" \
"s_add_u32 s20, s20, s82\n" \
"s_addc_u32 s21, s21, s83\n" \
"v_mfma_f32_16x16x32_bf16 v[220:223], a[84:87], a[12:15], v[220:223]\n" \
"s_xor_b32 s54, s54, 32768\n" \
"v_mfma_f32_16x16x32_bf16 v[212:215], a[84:87], a[8:11], v[212:215]\n" \
"v_mfma_f32_16x16x32_bf16 v[204:207], a[84:87], a[4:7], v[204:207]\n" \
"v_mfma_f32_16x16x32_bf16 v[196:199], a[84:87], a[0:3], v[196:199]\n" \
"v_mfma_f32_16x16x32_bf16 v[224:227], a[144:147], a[64:67], v[224:227]\n" \
"v_mfma_f32_16x16x32_bf16 v[228:231], a[144:147], a[68:71], v[228:231]\n" \
"s_waitcnt vmcnt(16) lgkmcnt(0)\n" \
"s_barrier\n" \
"v_mfma_f32_16x16x32_bf16 v[0:3], a[88:91], a[16:19], v[0:3]\n" \
"v_xor_b32 v237, 0x8000, v237\n" \
"v_xor_b32 v238, 0x8000, v238\n" \
"v_mfma_f32_16x16x32_bf16 v[4:7], a[92:95], a[16:19], v[4:7]\n" \
"v_add_u32 v239, s56, v239\n" \
"v_add_u32 v240, s56, v240\n" \
"v_mfma_f32_16x16x32_bf16 v[32:35], a[96:99], a[16:19], v[32:35]\n" \
"v_add_u32 v241, s56, v241\n" \
"v_add_u32 v242, s56, v242\n" \
"v_mfma_f32_16x16x32_bf16 v[36:39], a[100:103], a[16:19], v[36:39]\n" \
"v_add_u32 v243, s56, v243\n" \
"v_add_u32 v244, s56, v244\n" \
"v_mfma_f32_16x16x32_bf16 v[64:67], a[104:107], a[16:19], v[64:67]\n" \
"v_add_u32 v245, s56, v245\n" \
"v_add_u32 v246, s56, v246\n" \
"v_mfma_f32_16x16x32_bf16 v[68:71], a[108:111], a[16:19], v[68:71]\n" \
"ds_read_b128 a[0:3], v237 offset:0\n" \
"v_mfma_f32_16x16x32_bf16 v[96:99], a[112:115], a[16:19], v[96:99]\n" \
"ds_read_b128 a[4:7], v237 offset:2048\n" \
"v_mfma_f32_16x16x32_bf16 v[100:103], a[116:119], a[16:19], v[100:103]\n" \
"s_add_u32 m0, s55, 0\n" \
"ds_read_b128 a[8:11], v237 offset:4096\n" \
"v_mfma_f32_16x16x32_bf16 v[128:131], a[16:19], a[120:123], v[128:131]\n" \
"buffer_load_dwordx4 v234, s[24:27], s48 offen lds\n" \
"v_mfma_f32_16x16x32_bf16 v[132:135], a[16:19], a[124:127], v[132:135]\n" \
"ds_read_b128 a[12:15], v237 offset:6144\n" \
"v_mfma_f32_16x16x32_bf16 v[160:163], a[16:19], a[128:131], v[160:163]\n" \
"ds_read_b64_tr_b16 a[32:33], v239 offset:0\n" \
"v_mfma_f32_16x16x32_bf16 v[164:167], a[16:19], a[132:135], v[164:167]\n" \
"ds_read_b64_tr_b16 a[34:35], v239 offset:512\n" \
"v_mfma_f32_16x16x32_bf16 v[192:195], a[136:139], a[16:19], v[192:195]\n" \
"s_add_u32 m0, s55, 4096\n" \
"ds_read_b64_tr_b16 a[36:37], v240 offset:0\n" \
"v_mfma_f32_16x16x32_bf16 v[196:199], a[140:143], a[16:19], v[196:199]\n" \
"buffer_load_dwordx4 v234, s[24:27], s49 offen lds\n" \
"v_mfma_f32_16x16x32_bf16 v[8:11], a[88:91], a[20:23], v[8:11]\n" \
"ds_read_b64_tr_b16 a[38:39], v240 offset:512\n" \
"v_mfma_f32_16x16x32_bf16 v[12:15], a[92:95], a[20:23], v[12:15]\n" \
"ds_read_b128 a[16:19], v238 offset:0\n" \
"ds_read_b64_tr_b16 a[40:41], v241 offset:0\n" \
"v_mfma_f32_16x16x32_bf16 v[40:43], a[96:99], a[20:23], v[40:43]\n" \
"ds_read_b64_tr_b16 a[42:43], v241 offset:512\n" \
"v_mfma_f32_16x16x32_bf16 v[44:47], a[100:103], a[20:23], v[44:47]\n" \
"ds_read_b64_tr_b16 a[44:45], v242 offset:0\n" \
"v_mfma_f32_16x16x32_bf16 v[72:75], a[104:107], a[20:23], v[72:75]\n" \
"s_add_u32 m0, s55, 8192\n" \
"ds_read_b64_tr_b16 a[46:47], v242 offset:512\n" \
"v_mfma_f32_16x16x32_bf16 v[76:79], a[108:111], a[20:23], v[76:79]\n" \
"buffer_load_dwordx4 v234, s[24:27], s50 offen lds\n" \
"v_mfma_f32_16x16x32_bf16 v[104:107], a[112:115], a[20:23], v[104:107]\n" \
"ds_read_b64_tr_b16 a[48:49], v239 offset:4096\n" \
"v_mfma_f32_16x16x32_bf16 v[108:111], a[116:119], a[20:23], v[108:111]\n" \
"ds_read_b64_tr_b16 a[50:51], v239 offset:4608\n" \
"v_mfma_f32_16x16x32_bf16 v[136:139], a[20:23], a[120:123], v[136:139]\n" \
"ds_read_b64_tr_b16 a[52:53], v240 offset:4096\n" \
"v_mfma_f32_16x16x32_bf16 v[140:143], a[20:23], a[124:127], v[140:143]\n" \
"s_add_u32 m0, s55, 12288\n" \
"ds_read_b64_tr_b16 a[54:55], v240 offset:4608\n" \
"v_mfma_f32_16x16x32_bf16 v[168:171], a[20:23], a[128:131], v[168:171]\n" \
"buffer_load_dwordx4 v234, s[24:27], s51 offen lds\n" \
"v_mfma_f32_16x16x32_bf16 v[172:175], a[20:23], a[132:135], v[172:175]\n" \
"ds_read_b64_tr_b16 a[56:57], v241 offset:4096\n" \
"v_mfma_f32_16x16x32_bf16 v[200:203], a[136:139], a[20:23], v[200:203]\n" \
"v_mfma_f32_16x16x32_bf16 v[204:207], a[140:143], a[20:23], v[204:207]\n" \
"ds_read_b64_tr_b16 a[58:59], v241 offset:4608\n" \
"v_mfma_f32_16x16x32_bf16 v[16:19], a[88:91], a[24:27], v[16:19]\n" \
"ds_read_b64_tr_b16 a[60:61], v242 offset:4096\n" \
"v_mfma_f32_16x16x32_bf16 v[20:23], a[92:95], a[24:27], v[20:23]\n" \
"s_add_u32 m0, s55, 16384\n" \
"ds_read_b128 a[20:23], v238 offset:2048\n" \
"v_mfma_f32_16x16x32_bf16 v[48:51], a[96:99], a[24:27], v[48:51]\n" \
"buffer_load_dwordx4 v234, s[24:27], s52 offen lds\n" \
"v_mfma_f32_16x16x32_bf16 v[52:55], a[100:103], a[24:27], v[52:55]\n" \
"ds_read_b64_tr_b16 a[62:63], v242 offset:4608\n" \
"v_mfma_f32_16x16x32_bf16 v[80:83], a[104:107], a[24:27], v[80:83]\n" \
"ds_read_b64_tr_b16 a[64:65], v239 offset:8192\n" \
"v_mfma_f32_16x16x32_bf16 v[84:87], a[108:111], a[24:27], v[84:87]\n" \
"ds_read_b64_tr_b16 a[66:67], v239 offset:8704\n" \
"v_mfma_f32_16x16x32_bf16 v[112:115], a[112:115], a[24:27], v[112:115]\n" \
"ds_read_b64_tr_b16 a[68:69], v240 offset:8192\n" \
"v_mfma_f32_16x16x32_bf16 v[116:119], a[116:119], a[24:27], v[116:119]\n" \
"s_add_u32 m0, s55, 20480\n" \
"ds_read_b64_tr_b16 a[70:71], v240 offset:8704\n" \
"v_mfma_f32_16x16x32_bf16 v[144:147], a[24:27], a[120:123], v[144:147]\n" \
"buffer_load_dwordx4 v234, s[24:27], s53 offen lds\n" \
"v_mfma_f32_16x16x32_bf16 v[148:151], a[24:27], a[124:127], v[148:151]\n" \
"ds_read_b64_tr_b16 a[72:73], v241 offset:8192\n" \
"v_mfma_f32_16x16x32_bf16 v[176:179], a[24:27], a[128:131], v[176:179]\n" \
"ds_read_b64_tr_b16 a[74:75], v241 offset:8704\n" \
"v_mfma_f32_16x16x32_bf16 v[180:183], a[24:27], a[132:135], v[180:183]\n" \
"ds_read_b64_tr_b16 a[76:77], v242 offset:8192\n" \
"v_mfma_f32_16x16x32_bf16 v[208:211], a[136:139], a[24:27], v[208:211]\n" \
"s_add_u32 m0, s55, 24576\n" \
"ds_read_b64_tr_b16 a[78:79], v242 offset:8704\n" \
"v_mfma_f32_16x16x32_bf16 v[212:215], a[140:143], a[24:27], v[212:215]\n" \
"buffer_load_dwordx4 v235, s[28:31], 0 offen lds\n" \
"v_mfma_f32_16x16x32_bf16 v[224:227], a[148:151], a[120:123], v[224:227]\n" \
"ds_read_b64_tr_b16 a[80:81], v243 offset:0\n" \
"v_mfma_f32_16x16x32_bf16 v[228:231], a[148:151], a[124:127], v[228:231]\n" \
"ds_read_b128 a[24:27], v238 offset:4096\n" \
"ds_read_b64_tr_b16 a[82:83], v243 offset:256\n" \
"v_mfma_f32_16x16x32_bf16 v[24:27], a[88:91], a[28:31], v[24:27]\n" \
"ds_read_b64_tr_b16 a[84:85], v244 offset:0\n" \
"v_mfma_f32_16x16x32_bf16 v[28:31], a[92:95], a[28:31], v[28:31]\n" \
"ds_read_b64_tr_b16 a[86:87], v244 offset:256\n" \
"v_mfma_f32_16x16x32_bf16 v[56:59], a[96:99], a[28:31], v[56:59]\n" \
"ds_read_b128 a[144:147], v245\n" \
"v_mfma_f32_16x16x32_bf16 v[60:63], a[100:103], a[28:31], v[60:63]\n" \
"buffer_load_dword a246, v254, s[76:79], s74 offen\n" \
"v_mfma_f32_16x16x32_bf16 v[88:91], a[104:107], a[28:31], v[88:91]\n" \
"v_mfma_f32_16x16x32_bf16 v[92:95], a[108:111], a[28:31], v[92:95]\n" \
"v_mfma_f32_16x16x32_bf16 v[120:123], a[112:115], a[28:31], v[120:123]\n" \
"v_mfma_f32_16x16x32_bf16 v[124:127], a[116:119], a[28:31], v[124:127]\n" \
"s_add_u32 s58, s58, 1\n" \
"v_mfma_f32_16x16x32_bf16 v[152:155], a[28:31], a[120:123], v[152:155]\n" \
"v_mfma_f32_16x16x32_bf16 v[156:159], a[28:31], a[124:127], v[156:159]\n" \
"v_mfma_f32_16x16x32_bf16 v[184:187], a[28:31], a[128:131], v[184:187]\n" \
"s_bitcmp1_b32 81920, s58\n" \
"s_cbranch_scc0 Lqm2_%=\n" \
"s_cmp_lt_u32 s58, 16\n" \
"s_cbranch_scc1 Lqsp2_%=\n" \
"v_mfma_f32_16x16x32_bf16 v[188:191], a[28:31], a[132:135], v[188:191]\n" \
"v_mfma_f32_16x16x32_bf16 v[216:219], a[136:139], a[28:31], v[216:219]\n" \
"v_mfma_f32_16x16x32_bf16 v[220:223], a[140:143], a[28:31], v[220:223]\n" \
"s_branch Lq_epi_%=\n" \
"Lqsp2_%=:\n" \
"s_cmp_ge_u32 s58, 12\n" \
"s_cbranch_scc1 Lqs22_%=\n" \
"Lqs22_%=:\n" \
"v_accvgpr_read_b32 v247, a253\n" \
"v_accvgpr_read_b32 v232, a233\n" \
"v_readlane_b32 s60, v247, 2\n" \
"v_readlane_b32 s82, v247, 14\n" \
"s_add_u32 s40, s81, 0\n" \
"s_add_u32 s41, s81, 16384\n" \
"s_add_u32 s42, s81, 32768\n" \
"s_add_u32 s43, s81, 49152\n" \
"s_add_u32 s44, s80, 0\n" \
"s_add_u32 s45, s80, 16384\n" \
"s_add_u32 s46, s80, 32768\n" \
"s_add_u32 s47, s80, 49152\n" \
"s_add_u32 s20, s68, s60\n" \
"s_addc_u32 s21, s69, 0\n" \
"s_sub_u32 s82, s82, s60\n" \
"s_ashr_i32 s83, s82, 31\n" \
"v_subrev_u32 v232, 32768, v232\n" \
"v_mov_b32 v233, v232\n" \
"s_getpc_b64 s[76:77]\n" \
"Lct9_%=:\n" \
"s_add_u32 s76, s76, Lq_epi_%=-Lct9_%=\n" \
"s_addc_u32 s77, s77, 0\n" \
"s_mov_b32 s78, gmk_code_end-Lq_epi_%=\n" \
"s_mov_b32 s79, 0x20000\n" \
"v_accvgpr_read_b32 v249, a243\n" \
"buffer_load_dword a207, v249, s[76:79], 0 offen\n" \
"s_mov_b32 s78, 0\n" \
"s_branch Lqm2_%=\n" \
"Lqe3_%=:\n" \
"s_add_u32 m0, s55, 0\n" \
"s_nop 0\n" \
"buffer_load_dwordx4 v234, s[24:27], s48 offen lds\n" \
"s_add_u32 m0, s55, 4096\n" \
"s_nop 0\n" \
"buffer_load_dwordx4 v234, s[24:27], s49 offen lds\n" \
"s_add_u32 m0, s55, 8192\n" \
"s_nop 0\n" \
"buffer_load_dwordx4 v234, s[24:27], s50 offen lds\n" \
"s_add_u32 m0, s55, 12288\n" \
"s_nop 0\n" \
"buffer_load_dwordx4 v234, s[24:27], s51 offen lds\n" \
"s_add_u32 m0, s55, 16384\n" \
"s_nop 0\n" \
"buffer_load_dwordx4 v234, s[24:27], s52 offen lds\n" \
"s_add_u32 m0, s55, 20480\n" \
"s_nop 0\n" \
"buffer_load_dwordx4 v234, s[24:27], s53 offen lds\n" \
"s_add_u32 m0, s55, 24576\n" \
"s_nop 0\n" \
"buffer_load_dwordx4 v235, s[28:31], 0 offen lds\n" \
"s_add_u32 s24, s24, 393216\n" \
"s_addc_u32 s25, s25, 0\n" \
"s_add_u32 s28, s28, 458752\n" \
"s_addc_u32 s29, s29, 0\n" \
"s_branch Lqin3_%=\n" \
".p2align 6\n" \
"Lqm3_%=:\n" \
"v_mfma_f32_16x16x32_bf16 v[188:191], a[28:31], a[132:135], v[188:191]\n" \
"s_add_u32 s24, s24, 393216\n" \
"s_addc_u32 s25, s25, 0\n" \
"v_mfma_f32_16x16x32_bf16 v[216:219], a[136:139], a[28:31], v[216:219]\n" \
"s_add_u32 s28, s28, 458752\n" \
"s_addc_u32 s29, s29, 0\n" \
"v_mfma_f32_16x16x32_bf16 v[220:223], a[140:143], a[28:31], v[220:223]\n" \
"ds_read_b128 a[28:31], v238 offset:6144\n" \
"Lqin3_%=:\n" \
"s_waitcnt lgkmcnt(1)\n" \
"v_mfma_f32_16x16x32_bf16 v[0:3], a[32:35], a[0:3], v[0:3]\n" \
"s_add_u32 s36, s36, 128\n" \
"s_addc_u32 s37, s37, 0\n" \
"v_mfma_f32_16x16x32_bf16 v[8:11], a[32:35], a[4:7], v[8:11]\n" \
"ds_read_b64_tr_b16 a[88:89], v239 offset:12288\n" \
"v_mfma_f32_16x16x32_bf16 v[16:19], a[32:35], a[8:11], v[16:19]\n" \
"s_add_u32 s55, s55, 30720\n" \
"ds_read_b64_tr_b16 a[90:91], v239 offset:12800\n" \
"v_mfma_f32_16x16x32_bf16 v[24:27], a[32:35], a[12:15], v[24:27]\n" \
"ds_read_b64_tr_b16 a[92:93], v240 offset:12288\n" \
"v_mfma_f32_16x16x32_bf16 v[28:31], a[36:39], a[12:15], v[28:31]\n" \
"s_add_u32 m0, s54, 0\n" \
"s_add_u32 s63, s63, 1\n" \
"v_mfma_f32_16x16x32_bf16 v[20:23], a[36:39], a[8:11], v[20:23]\n" \
"buffer_load_dwordx4 v232, s[20:23], s40 offen lds\n" \
"v_mfma_f32_16x16x32_bf16 v[12:15], a[36:39], a[4:7], v[12:15]\n" \
"s_add_u32 s74, s74, 1048576\n" \
"ds_read_b64_tr_b16 a[94:95], v240 offset:12800\n" \
"v_mfma_f32_16x16x32_bf16 v[4:7], a[36:39], a[0:3], v[4:7]\n" \
"s_cmp_eq_u32 s63, 3\n" \
"s_cmov_b32 s55, s62\n" \
"s_cmov_b32 s63, 0\n" \
"v_mfma_f32_16x16x32_bf16 v[32:35], a[40:43], a[0:3], v[32:35]\n" \
"ds_read_b64_tr_b16 a[96:97], v241 offset:12288\n" \
"v_mfma_f32_16x16x32_bf16 v[40:43], a[40:43], a[4:7], v[40:43]\n" \
"s_cmp_ge_u32 s58, 13\n" \
"s_cselect_b32 s26, 0, s26\n" \
"s_cselect_b32 s55, 0x30000, s55\n" \
"v_mfma_f32_16x16x32_bf16 v[48:51], a[40:43], a[8:11], v[48:51]\n" \
"s_add_u32 m0, s54, 1024\n" \
"ds_read_b64_tr_b16 a[98:99], v241 offset:12800\n" \
"v_mfma_f32_16x16x32_bf16 v[56:59], a[40:43], a[12:15], v[56:59]\n" \
"buffer_load_dwordx4 v233, s[20:23], s41 offen lds\n" \
"v_mfma_f32_16x16x32_bf16 v[60:63], a[44:47], a[12:15], v[60:63]\n" \
"s_cmp_eq_u32 s58, 3\n" \
"s_cmov_b64 s[76:77], s[0:1]\n" \
"s_cmov_b32 s78, 3538944\n" \
"s_cmov_b32 s74, 0\n" \
"v_mfma_f32_16x16x32_bf16 v[52:55], a[44:47], a[8:11], v[52:55]\n" \
"s_cmp_ge_u32 s58, 13\n" \
"s_cselect_b32 s30, 0, s30\n" \
"s_cselect_b32 s38, 0, s38\n" \
"v_mfma_f32_16x16x32_bf16 v[44:47], a[44:47], a[4:7], v[44:47]\n" \
"ds_read_b64_tr_b16 a[100:101], v242 offset:12288\n" \
"v_mfma_f32_16x16x32_bf16 v[36:39], a[44:47], a[0:3], v[36:39]\n" \
"s_add_u32 s57, s57, 1\n" \
"ds_read_b64_tr_b16 a[102:103], v242 offset:12800\n" \
"v_mfma_f32_16x16x32_bf16 v[64:67], a[48:51], a[0:3], v[64:67]\n" \
"s_cmp_eq_u32 s58, 7\n" \
"s_cmov_b64 s[76:77], s[34:35]\n" \
"s_cmov_b32 s78, 3145728\n" \
"s_cmov_b32 s74, 0\n" \
"v_mfma_f32_16x16x32_bf16 v[72:75], a[48:51], a[4:7], v[72:75]\n" \
"s_add_u32 m0, s54, 2048\n" \
"v_mfma_f32_16x16x32_bf16 v[80:83], a[48:51], a[8:11], v[80:83]\n" \
"buffer_load_dwordx4 v232, s[20:23], s42 offen lds\n" \
"v_mfma_f32_16x16x32_bf16 v[88:91], a[48:51], a[12:15], v[88:91]\n" \
"s_cmp_eq_u32 s57, 2\n" \
"s_cselect_b32 s56, s95, s85\n" \
"s_cselect_b32 s57, -1, s57\n" \
"v_mfma_f32_16x16x32_bf16 v[92:95], a[52:55], a[12:15], v[92:95]\n" \
"s_cmp_eq_u32 s58, 10\n" \
"s_cmov_b64 s[76:77], s[96:97]\n" \
"s_cmov_b32 s78, 2097152\n" \
"s_cmov_b32 s74, 0\n" \
"v_mfma_f32_16x16x32_bf16 v[84:87], a[52:55], a[8:11], v[84:87]\n" \
"ds_read_b64_tr_b16 a[104:105], v239 offset:16384\n" \
"v_mfma_f32_16x16x32_bf16 v[76:79], a[52:55], a[4:7], v[76:79]\n" \
"ds_read_b64_tr_b16 a[106:107], v239 offset:16896\n" \
"v_mfma_f32_16x16x32_bf16 v[68:71], a[52:55], a[0:3], v[68:71]\n" \
"s_add_u32 m0, s54, 3072\n" \
"ds_read_b64_tr_b16 a[108:109], v240 offset:16384\n" \
"v_mfma_f32_16x16x32_bf16 v[96:99], a[56:59], a[0:3], v[96:99]\n" \
"buffer_load_dwordx4 v233, s[20:23], s43 offen lds\n" \
"v_mfma_f32_16x16x32_bf16 v[104:107], a[56:59], a[4:7], v[104:107]\n" \
"ds_read_b64_tr_b16 a[110:111], v240 offset:16896\n" \
"v_mfma_f32_16x16x32_bf16 v[112:115], a[56:59], a[8:11], v[112:115]\n" \
"ds_read_b64_tr_b16 a[112:113], v241 offset:16384\n" \
"v_mfma_f32_16x16x32_bf16 v[120:123], a[56:59], a[12:15], v[120:123]\n" \
"ds_read_b64_tr_b16 a[114:115], v241 offset:16896\n" \
"v_mfma_f32_16x16x32_bf16 v[124:127], a[60:63], a[12:15], v[124:127]\n" \
"ds_read_b64_tr_b16 a[116:117], v242 offset:16384\n" \
"v_mfma_f32_16x16x32_bf16 v[116:119], a[60:63], a[8:11], v[116:119]\n" \
"v_mfma_f32_16x16x32_bf16 v[108:111], a[60:63], a[4:7], v[108:111]\n" \
"s_add_u32 m0, s54, 4096\n" \
"ds_read_b64_tr_b16 a[118:119], v242 offset:16896\n" \
"v_mfma_f32_16x16x32_bf16 v[100:103], a[60:63], a[0:3], v[100:103]\n" \
"buffer_load_dwordx4 v232, s[20:23], s44 offen lds\n" \
"v_mfma_f32_16x16x32_bf16 v[128:131], a[0:3], a[64:67], v[128:131]\n" \
"ds_read_b64_tr_b16 a[120:121], v239 offset:20480\n" \
"v_mfma_f32_16x16x32_bf16 v[136:139], a[4:7], a[64:67], v[136:139]\n" \
"ds_read_b64_tr_b16 a[122:123], v239 offset:20992\n" \
"v_mfma_f32_16x16x32_bf16 v[144:147], a[8:11], a[64:67], v[144:147]\n" \
"ds_read_b64_tr_b16 a[124:125], v240 offset:20480\n" \
"v_mfma_f32_16x16x32_bf16 v[152:155], a[12:15], a[64:67], v[152:155]\n" \
"ds_read_b64_tr_b16 a[126:127], v240 offset:20992\n" \
"v_mfma_f32_16x16x32_bf16 v[156:159], a[12:15], a[68:71], v[156:159]\n" \
"ds_read_b64_tr_b16 a[128:129], v241 offset:20480\n" \
"v_mfma_f32_16x16x32_bf16 v[148:151], a[8:11], a[68:71], v[148:151]\n" \
"s_add_u32 m0, s54, 5120\n" \
"ds_read_b64_tr_b16 a[130:131], v241 offset:20992\n" \
"v_mfma_f32_16x16x32_bf16 v[140:143], a[4:7], a[68:71], v[140:143]\n" \
"buffer_load_dwordx4 v233, s[20:23], s45 offen lds\n" \
"v_mfma_f32_16x16x32_bf16 v[132:135], a[0:3], a[68:71], v[132:135]\n" \
"v_mfma_f32_16x16x32_bf16 v[160:163], a[0:3], a[72:75], v[160:163]\n" \
"ds_read_b64_tr_b16 a[132:133], v242 offset:20480\n" \
"v_mfma_f32_16x16x32_bf16 v[168:171], a[4:7], a[72:75], v[168:171]\n" \
"ds_read_b64_tr_b16 a[134:135], v242 offset:20992\n" \
"v_mfma_f32_16x16x32_bf16 v[176:179], a[8:11], a[72:75], v[176:179]\n" \
"ds_read_b64_tr_b16 a[136:137], v243 offset:2048\n" \
"v_mfma_f32_16x16x32_bf16 v[184:187], a[12:15], a[72:75], v[184:187]\n" \
"s_add_u32 m0, s54, 6144\n" \
"ds_read_b64_tr_b16 a[138:139], v243 offset:2304\n" \
"v_mfma_f32_16x16x32_bf16 v[188:191], a[12:15], a[76:79], v[188:191]\n" \
"s_waitcnt lgkmcnt(15)\n" \
"buffer_load_dwordx4 v232, s[20:23], s46 offen lds\n" \
"v_mfma_f32_16x16x32_bf16 v[180:183], a[8:11], a[76:79], v[180:183]\n" \
"ds_read_b64_tr_b16 a[140:141], v244 offset:2048\n" \
"v_mfma_f32_16x16x32_bf16 v[172:175], a[4:7], a[76:79], v[172:175]\n" \
"ds_read_b64_tr_b16 a[142:143], v244 offset:2304\n" \
"v_mfma_f32_16x16x32_bf16 v[164:167], a[0:3], a[76:79], v[164:167]\n" \
"ds_read_b128 a[148:151], v246\n" \
"v_mfma_f32_16x16x32_bf16 v[192:195], a[80:83], a[0:3], v[192:195]\n" \
"v_mfma_f32_16x16x32_bf16 v[200:203], a[80:83], a[4:7], v[200:203]\n" \
"v_mfma_f32_16x16x32_bf16 v[208:211], a[80:83], a[8:11], v[208:211]\n" \
"s_add_u32 m0, s54, 7168\n" \
"v_mfma_f32_16x16x32_bf16 v[216:219], a[80:83], a[12:15], v[216:219]\n" \
"buffer_load_dwordx4 v233, s[20:23], s47 offen lds\n" \
"v_mfma_f32_16x16x32_bf16 v[220:223], a[84:87], a[12:15], v[220:223]\n" \
"s_add_u32 s20, s20, s82\n" \
"s_addc_u32 s21, s21, s83\n" \
"v_mfma_f32_16x16x32_bf16 v[212:215], a[84:87], a[8:11], v[212:215]\n" \
"s_xor_b32 s54, s54, 32768\n" \
"v_mfma_f32_16x16x32_bf16 v[204:207], a[84:87], a[4:7], v[204:207]\n" \
"v_mfma_f32_16x16x32_bf16 v[196:199], a[84:87], a[0:3], v[196:199]\n" \
"v_mfma_f32_16x16x32_bf16 v[224:227], a[144:147], a[72:75], v[224:227]\n" \
"v_mfma_f32_16x16x32_bf16 v[228:231], a[144:147], a[76:79], v[228:231]\n" \
"s_waitcnt vmcnt(16) lgkmcnt(0)\n" \
"s_barrier\n" \
"v_mfma_f32_16x16x32_bf16 v[0:3], a[88:91], a[16:19], v[0:3]\n" \
"v_xor_b32 v237, 0x8000, v237\n" \
"v_xor_b32 v238, 0x8000, v238\n" \
"v_mfma_f32_16x16x32_bf16 v[4:7], a[92:95], a[16:19], v[4:7]\n" \
"v_add_u32 v239, s56, v239\n" \
"v_add_u32 v240, s56, v240\n" \
"v_mfma_f32_16x16x32_bf16 v[32:35], a[96:99], a[16:19], v[32:35]\n" \
"v_add_u32 v241, s56, v241\n" \
"v_add_u32 v242, s56, v242\n" \
"v_mfma_f32_16x16x32_bf16 v[36:39], a[100:103], a[16:19], v[36:39]\n" \
"v_add_u32 v243, s56, v243\n" \
"v_add_u32 v244, s56, v244\n" \
"v_mfma_f32_16x16x32_bf16 v[64:67], a[104:107], a[16:19], v[64:67]\n" \
"v_add_u32 v245, s56, v245\n" \
"v_add_u32 v246, s56, v246\n" \
"v_mfma_f32_16x16x32_bf16 v[68:71], a[108:111], a[16:19], v[68:71]\n" \
"ds_read_b128 a[0:3], v237 offset:0\n" \
"v_mfma_f32_16x16x32_bf16 v[96:99], a[112:115], a[16:19], v[96:99]\n" \
"ds_read_b128 a[4:7], v237 offset:2048\n" \
"v_mfma_f32_16x16x32_bf16 v[100:103], a[116:119], a[16:19], v[100:103]\n" \
"ds_read_b128 a[8:11], v237 offset:4096\n" \
"v_mfma_f32_16x16x32_bf16 v[128:131], a[16:19], a[120:123], v[128:131]\n" \
"s_add_u32 m0, s55, 0\n" \
"ds_read_b128 a[12:15], v237 offset:6144\n" \
"v_mfma_f32_16x16x32_bf16 v[132:135], a[16:19], a[124:127], v[132:135]\n" \
"buffer_load_dwordx4 v234, s[24:27], s48 offen lds\n" \
"v_mfma_f32_16x16x32_bf16 v[160:163], a[16:19], a[128:131], v[160:163]\n" \
"ds_read_b64_tr_b16 a[32:33], v239 offset:0\n" \
"v_mfma_f32_16x16x32_bf16 v[164:167], a[16:19], a[132:135], v[164:167]\n" \
"ds_read_b64_tr_b16 a[34:35], v239 offset:512\n" \
"v_mfma_f32_16x16x32_bf16 v[192:195], a[136:139], a[16:19], v[192:195]\n" \
"ds_read_b64_tr_b16 a[36:37], v240 offset:0\n" \
"v_mfma_f32_16x16x32_bf16 v[196:199], a[140:143], a[16:19], v[196:199]\n" \
"s_add_u32 m0, s55, 4096\n" \
"ds_read_b64_tr_b16 a[38:39], v240 offset:512\n" \
"v_mfma_f32_16x16x32_bf16 v[8:11], a[88:91], a[20:23], v[8:11]\n" \
"buffer_load_dwordx4 v234, s[24:27], s49 offen lds\n" \
"v_mfma_f32_16x16x32_bf16 v[12:15], a[92:95], a[20:23], v[12:15]\n" \
"ds_read_b128 a[16:19], v238 offset:0\n" \
"ds_read_b64_tr_b16 a[40:41], v241 offset:0\n" \
"v_mfma_f32_16x16x32_bf16 v[40:43], a[96:99], a[20:23], v[40:43]\n" \
"ds_read_b64_tr_b16 a[42:43], v241 offset:512\n" \
"v_mfma_f32_16x16x32_bf16 v[44:47], a[100:103], a[20:23], v[44:47]\n" \
"ds_read_b64_tr_b16 a[44:45], v242 offset:0\n" \
"v_mfma_f32_16x16x32_bf16 v[72:75], a[104:107], a[20:23], v[72:75]\n" \
"v_mfma_f32_16x16x32_bf16 v[76:79], a[108:111], a[20:23], v[76:79]\n" \
"s_add_u32 m0, s55, 8192\n" \
"ds_read_b64_tr_b16 a[46:47], v242 offset:512\n" \
"v_mfma_f32_16x16x32_bf16 v[104:107], a[112:115], a[20:23], v[104:107]\n" \
"buffer_load_dwordx4 v234, s[24:27], s50 offen lds\n" \
"v_mfma_f32_16x16x32_bf16 v[108:111], a[116:119], a[20:23], v[108:111]\n" \
"ds_read_b64_tr_b16 a[48:49], v239 offset:4096\n" \
"v_mfma_f32_16x16x32_bf16 v[136:139], a[20:23], a[120:123], v[136:139]\n" \
"ds_read_b64_tr_b16 a[50:51], v239 offset:4608\n" \
"v_mfma_f32_16x16x32_bf16 v[140:143], a[20:23], a[124:127], v[140:143]\n" \
"ds_read_b64_tr_b16 a[52:53], v240 offset:4096\n" \
"v_mfma_f32_16x16x32_bf16 v[168:171], a[20:23], a[128:131], v[168:171]\n" \
"s_add_u32 m0, s55, 12288\n" \
"ds_read_b64_tr_b16 a[54:55], v240 offset:4608\n" \
"v_mfma_f32_16x16x32_bf16 v[172:175], a[20:23], a[132:135], v[172:175]\n" \
"buffer_load_dwordx4 v234, s[24:27], s51 offen lds\n" \
"v_mfma_f32_16x16x32_bf16 v[200:203], a[136:139], a[20:23], v[200:203]\n" \
"ds_read_b64_tr_b16 a[56:57], v241 offset:4096\n" \
"v_mfma_f32_16x16x32_bf16 v[204:207], a[140:143], a[20:23], v[204:207]\n" \
"ds_read_b64_tr_b16 a[58:59], v241 offset:4608\n" \
"v_mfma_f32_16x16x32_bf16 v[16:19], a[88:91], a[24:27], v[16:19]\n" \
"ds_read_b64_tr_b16 a[60:61], v242 offset:4096\n" \
"v_mfma_f32_16x16x32_bf16 v[20:23], a[92:95], a[24:27], v[20:23]\n" \
"ds_read_b128 a[20:23], v238 offset:2048\n" \
"ds_read_b64_tr_b16 a[62:63], v242 offset:4608\n" \
"v_mfma_f32_16x16x32_bf16 v[48:51], a[96:99], a[24:27], v[48:51]\n" \
"s_add_u32 m0, s55, 16384\n" \
"ds_read_b64_tr_b16 a[64:65], v239 offset:8192\n" \
"v_mfma_f32_16x16x32_bf16 v[52:55], a[100:103], a[24:27], v[52:55]\n" \
"buffer_load_dwordx4 v234, s[24:27], s52 offen lds\n" \
"v_mfma_f32_16x16x32_bf16 v[80:83], a[104:107], a[24:27], v[80:83]\n" \
"ds_read_b64_tr_b16 a[66:67], v239 offset:8704\n" \
"v_mfma_f32_16x16x32_bf16 v[84:87], a[108:111], a[24:27], v[84:87]\n" \
"v_mfma_f32_16x16x32_bf16 v[112:115], a[112:115], a[24:27], v[112:115]\n" \
"ds_read_b64_tr_b16 a[68:69], v240 offset:8192\n" \
"v_mfma_f32_16x16x32_bf16 v[116:119], a[116:119], a[24:27], v[116:119]\n" \
"ds_read_b64_tr_b16 a[70:71], v240 offset:8704\n" \
"v_mfma_f32_16x16x32_bf16 v[144:147], a[24:27], a[120:123], v[144:147]\n" \
"s_add_u32 m0, s55, 20480\n" \
"ds_read_b64_tr_b16 a[72:73], v241 offset:8192\n" \
"v_mfma_f32_16x16x32_bf16 v[148:151], a[24:27], a[124:127], v[148:151]\n" \
"buffer_load_dwordx4 v234, s[24:27], s53 offen lds\n" \
"v_mfma_f32_16x16x32_bf16 v[176:179], a[24:27], a[128:131], v[176:179]\n" \
"ds_read_b64_tr_b16 a[74:75], v241 offset:8704\n" \
"v_mfma_f32_16x16x32_bf16 v[180:183], a[24:27], a[132:135], v[180:183]\n" \
"ds_read_b64_tr_b16 a[76:77], v242 offset:8192\n" \
"v_mfma_f32_16x16x32_bf16 v[208:211], a[136:139], a[24:27], v[208:211]\n" \
"ds_read_b64_tr_b16 a[78:79], v242 offset:8704\n" \
"v_mfma_f32_16x16x32_bf16 v[212:215], a[140:143], a[24:27], v[212:215]\n" \
"s_add_u32 m0, s55, 24576\n" \
"ds_read_b64_tr_b16 a[80:81], v243 offset:0\n" \
"v_mfma_f32_16x16x32_bf16 v[224:227], a[148:151], a[128:131], v[224:227]\n" \
"buffer_load_dwordx4 v235, s[28:31], 0 offen lds\n" \
"v_mfma_f32_16x16x32_bf16 v[228:231], a[148:151], a[132:135], v[228:231]\n" \
"ds_read_b128 a[24:27], v238 offset:4096\n" \
"ds_read_b64_tr_b16 a[82:83], v243 offset:256\n" \
"v_mfma_f32_16x16x32_bf16 v[24:27], a[88:91], a[28:31], v[24:27]\n" \
"ds_read_b64_tr_b16 a[84:85], v244 offset:0\n" \
"v_mfma_f32_16x16x32_bf16 v[28:31], a[92:95], a[28:31], v[28:31]\n" \
"ds_read_b64_tr_b16 a[86:87], v244 offset:256\n" \
"v_mfma_f32_16x16x32_bf16 v[56:59], a[96:99], a[28:31], v[56:59]\n" \
"ds_read_b128 a[144:147], v245\n" \
"v_mfma_f32_16x16x32_bf16 v[60:63], a[100:103], a[28:31], v[60:63]\n" \
"v_mfma_f32_16x16x32_bf16 v[88:91], a[104:107], a[28:31], v[88:91]\n" \
"buffer_load_dword a246, v254, s[76:79], s74 offen\n" \
"v_mfma_f32_16x16x32_bf16 v[92:95], a[108:111], a[28:31], v[92:95]\n" \
"v_mfma_f32_16x16x32_bf16 v[120:123], a[112:115], a[28:31], v[120:123]\n" \
"v_mfma_f32_16x16x32_bf16 v[124:127], a[116:119], a[28:31], v[124:127]\n" \
"s_add_u32 s58, s58, 1\n" \
"v_mfma_f32_16x16x32_bf16 v[152:155], a[28:31], a[120:123], v[152:155]\n" \
"v_mfma_f32_16x16x32_bf16 v[156:159], a[28:31], a[124:127], v[156:159]\n" \
"v_mfma_f32_16x16x32_bf16 v[184:187], a[28:31], a[128:131], v[184:187]\n" \
"s_bitcmp1_b32 81920, s58\n" \
"s_cbranch_scc0 Lqm3_%=\n" \
"s_cmp_lt_u32 s58, 16\n" \
"s_cbranch_scc1 Lqsp3_%=\n" \
"v_mfma_f32_16x16x32_bf16 v[188:191], a[28:31], a[132:135], v[188:191]\n" \
"v_mfma_f32_16x16x32_bf16 v[216:219], a[136:139], a[28:31], v[216:219]\n" \
"v_mfma_f32_16x16x32_bf16 v[220:223], a[140:143], a[28:31], v[220:223]\n" \
"s_branch Lq_epi_%=\n" \
"Lqsp3_%=:\n" \
"s_cmp_ge_u32 s58, 12\n" \
"s_cbranch_scc1 Lqs23_%=\n" \
"Lqs23_%=:\n" \
"v_accvgpr_read_b32 v247, a253\n" \
"v_accvgpr_read_b32 v232, a233\n" \
"v_readlane_b32 s60, v247, 3\n" \
"v_readlane_b32 s82, v247, 15\n" \
"s_add_u32 s40, s81, 0\n" \
"s_add_u32 s41, s81, 16384\n" \
"s_add_u32 s42, s81, 32768\n" \
"s_add_u32 s43, s81, 49152\n" \
"s_add_u32 s44, s80, 0\n" \
"s_add_u32 s45, s80, 16384\n" \
"s_add_u32 s46, s80, 32768\n" \
"s_add_u32 s47, s80, 49152\n" \
"s_add_u32 s20, s68, s60\n" \
"s_addc_u32 s21, s69, 0\n" \
"s_sub_u32 s82, s82, s60\n" \
"s_ashr_i32 s83, s82, 31\n" \
"v_subrev_u32 v232, 49152, v232\n" \
"v_mov_b32 v233, v232\n" \
"s_getpc_b64 s[76:77]\n" \
"Lct10_%=:\n" \
"s_add_u32 s76, s76, Lq_epi_%=-Lct10_%=\n" \
"s_addc_u32 s77, s77, 0\n" \
"s_mov_b32 s78, gmk_code_end-Lq_epi_%=\n" \
"s_mov_b32 s79, 0x20000\n" \
"v_accvgpr_read_b32 v249, a243\n" \
"buffer_load_dword a208, v249, s[76:79], 0 offen\n" \
"s_mov_b32 s78, 0\n" \
"s_branch Lqm3_%=\n" \
".p2align 12\n" \
"Lq_epi_%=:\n" \
"v_accvgpr_read_b32 v250, a244\n" \
"v_accvgpr_read_b32 v251, a245\n" \
"s_nop 0\n" \
"v_readfirstlane_b32 s72, v250\n" \
"v_readfirstlane_b32 s73, v251\n" \
"s_nop 0\n" \
"s_add_u32 s72, s72, 1\n" \
"s_addc_u32 s73, s73, 0\n" \
"v_accvgpr_read_b32 v253, a238\n" \
"v_add_u32 v253, 131072, v253\n" \
"buffer_load_dwordx2 v[250:251], v253, s[64:67], s84 offen sc1\n" \
"v_accvgpr_read_b32 v232, a232\n" \
"v_accvgpr_read_b32 v235, a233\n" \
"v_accvgpr_read_b32 v241, a252\n" \
"v_accvgpr_read_b32 v254, a253\n" \
"s_and_b32 s60, s91, 1\n" \
"s_lshl_b32 s60, s60, 7\n" \
"v_add_u32 v235, s60, v235\n" \
"v_xor_b32 v236, 128, v235\n" \
"v_add_u32 v233, 2048, v232\n" \
"v_add_u32 v234, 0, v232\n" \
"s_lshl_b32 s80, s59, 10\n" \
"s_mul_i32 s60, s88, 2359296\n" \
"s_lshl_b32 s61, s89, 14\n" \
"s_add_u32 s40, s60, s61\n" \
"s_add_u32 s41, s40, s61\n" \
"s_add_u32 s40, s40, 1572864\n" \
"s_add_u32 s41, s41, 1835008\n" \
"s_add_u32 s42, s41, 16384\n" \
"v_cvt_pk_bf16_f32 v224, v224, v225\n" \
"v_readlane_b32 s24, v254, 4\n" \
"v_cvt_pk_bf16_f32 v225, v226, v227\n" \
"v_cvt_pk_bf16_f32 v226, v228, v229\n" \
"v_cvt_pk_bf16_f32 v227, v230, v231\n" \
"v_cvt_pk_bf16_f32 v192, v192, v193\n" \
"v_cvt_pk_bf16_f32 v193, v194, v195\n" \
"s_add_u32 m0, s80, 65536\n" \
"v_cvt_pk_bf16_f32 v194, v196, v197\n" \
"buffer_load_dwordx4 v235, s[68:71], s24 offen lds\n" \
"v_cvt_pk_bf16_f32 v195, v198, v199\n" \
"v_pk_max_i16 v192, v192, 0\n" \
"v_pk_max_i16 v193, v193, 0\n" \
"v_pk_max_i16 v194, v194, 0\n" \
"v_pk_max_i16 v195, v195, 0\n" \
"buffer_store_dwordx4 v[192:195], v232, s[64:67], s40 offen offset:0 sc1\n" \
"v_cvt_pk_bf16_f32 v200, v200, v201\n" \
"v_cvt_pk_bf16_f32 v201, v202, v203\n" \
"v_cvt_pk_bf16_f32 v202, v204, v205\n" \
"v_cvt_pk_bf16_f32 v203, v206, v207\n" \
"v_pk_max_i16 v200, v200, 0\n" \
"v_pk_max_i16 v201, v201, 0\n" \
"v_pk_max_i16 v202, v202, 0\n" \
"v_pk_max_i16 v203, v203, 0\n" \
"buffer_store_dwordx4 v[200:203], v232, s[64:67], s40 offen offset:1024 sc1\n" \
"v_cvt_pk_bf16_f32 v208, v208, v209\n" \
"v_cvt_pk_bf16_f32 v209, v210, v211\n" \
"v_cvt_pk_bf16_f32 v210, v212, v213\n" \
"v_cvt_pk_bf16_f32 v211, v214, v215\n" \
"v_pk_max_i16 v208, v208, 0\n" \
"s_add_u32 m0, s80, 69632\n" \
"v_pk_max_i16 v209, v209, 0\n" \
"buffer_load_dwordx4 v236, s[68:71], s24 offen lds\n" \
"v_pk_max_i16 v210, v210, 0\n" \
"v_pk_max_i16 v211, v211, 0\n" \
"buffer_store_dwordx4 v[208:211], v232, s[64:67], s40 offen offset:2048 sc1\n" \
"v_cvt_pk_bf16_f32 v216, v216, v217\n" \
"v_cvt_pk_bf16_f32 v217, v218, v219\n" \
"v_cvt_pk_bf16_f32 v218, v220, v221\n" \
"v_cvt_pk_bf16_f32 v219, v222, v223\n" \
"v_pk_max_i16 v216, v216, 0\n" \
"v_pk_max_i16 v217, v217, 0\n" \
"v_pk_max_i16 v218, v218, 0\n" \
"v_pk_max_i16 v219, v219, 0\n" \
"buffer_store_dwordx4 v[216:219], v232, s[64:67], s40 offen offset:3072 sc1\n" \
"v_cvt_pk_bf16_f32 v80, v80, v81\n" \
"v_cvt_pk_bf16_f32 v81, v82, v83\n" \
"v_cvt_pk_bf16_f32 v82, v84, v85\n" \
"v_cvt_pk_bf16_f32 v83, v86, v87\n" \
"v_cvt_pk_bf16_f32 v88, v88, v89\n" \
"v_cvt_pk_bf16_f32 v89, v90, v91\n" \
"v_cvt_pk_bf16_f32 v90, v92, v93\n" \
"v_cvt_pk_bf16_f32 v91, v94, v95\n" \
"v_cvt_pk_bf16_f32 v112, v112, v113\n" \
"v_cvt_pk_bf16_f32 v113, v114, v115\n" \
"v_cvt_pk_bf16_f32 v114, v116, v117\n" \
"v_cvt_pk_bf16_f32 v115, v118, v119\n" \
"v_cvt_pk_bf16_f32 v120, v120, v121\n" \
"v_cvt_pk_bf16_f32 v121, v122, v123\n" \
"v_readlane_b32 s25, v254, 5\n" \
"v_cvt_pk_bf16_f32 v122, v124, v125\n" \
"v_cvt_pk_bf16_f32 v123, v126, v127\n" \
"v_accvgpr_read_b32 v218, a212\n" \
"v_accvgpr_read_b32 v219, a213\n" \
"v_accvgpr_read_b32 v220, a214\n" \
"s_add_u32 m0, s80, 73728\n" \
"v_accvgpr_read_b32 v221, a215\n" \
"buffer_load_dwordx4 v235, s[68:71], s25 offen lds\n" \
"v_accvgpr_read_b32 v222, a216\n" \
"v_accvgpr_read_b32 v223, a217\n" \
"ds_write_b64 v218, v[224:225]\n" \
"ds_write_b64 v219, v[226:227]\n" \
"v_cvt_pk_bf16_f32 v194, v128, v129\n" \
"v_cvt_pk_bf16_f32 v195, v130, v131\n" \
"v_cvt_pk_bf16_f32 v206, v132, v133\n" \
"v_cvt_pk_bf16_f32 v207, v134, v135\n" \
"v_cvt_pk_bf16_f32 v200, v136, v137\n" \
"v_cvt_pk_bf16_f32 v201, v138, v139\n" \
"v_cvt_pk_bf16_f32 v212, v140, v141\n" \
"v_cvt_pk_bf16_f32 v213, v142, v143\n" \
"v_cvt_pk_bf16_f32 v196, v144, v145\n" \
"v_cvt_pk_bf16_f32 v197, v146, v147\n" \
"v_cvt_pk_bf16_f32 v208, v148, v149\n" \
"v_cvt_pk_bf16_f32 v209, v150, v151\n" \
"v_cvt_pk_bf16_f32 v202, v152, v153\n" \
"v_cvt_pk_bf16_f32 v203, v154, v155\n" \
"v_cvt_pk_bf16_f32 v214, v156, v157\n" \
"v_cvt_pk_bf16_f32 v215, v158, v159\n" \
"v_cvt_pk_bf16_f32 v130, v160, v161\n" \
"s_add_u32 m0, s80, 77824\n" \
"v_cvt_pk_bf16_f32 v131, v162, v163\n" \
"buffer_load_dwordx4 v236, s[68:71], s25 offen lds\n" \
"v_cvt_pk_bf16_f32 v142, v164, v165\n" \
"v_cvt_pk_bf16_f32 v143, v166, v167\n" \
"v_cvt_pk_bf16_f32 v136, v168, v169\n" \
"v_cvt_pk_bf16_f32 v137, v170, v171\n" \
"v_cvt_pk_bf16_f32 v148, v172, v173\n" \
"v_cvt_pk_bf16_f32 v149, v174, v175\n" \
"v_cvt_pk_bf16_f32 v132, v176, v177\n" \
"v_cvt_pk_bf16_f32 v133, v178, v179\n" \
"v_cvt_pk_bf16_f32 v144, v180, v181\n" \
"v_cvt_pk_bf16_f32 v145, v182, v183\n" \
"v_cvt_pk_bf16_f32 v138, v184, v185\n" \
"v_cvt_pk_bf16_f32 v139, v186, v187\n" \
"v_cvt_pk_bf16_f32 v150, v188, v189\n" \
"v_cvt_pk_bf16_f32 v151, v190, v191\n" \
"s_cmp_eq_u32 s59, 3\n" \
"s_cbranch_scc1 Lq_nowr_%=\n" \
"ds_write_b128 v220, v[80:83] offset:8192\n" \
"ds_write_b64 v221, v[196:197] offset:12288\n" \
"ds_write_b64 v221, v[208:209] offset:12800\n" \
"ds_write_b128 v220, v[88:91] offset:9216\n" \
"ds_write_b64 v221, v[202:203] offset:13312\n" \
"ds_write_b64 v221, v[214:215] offset:13824\n" \
"ds_write_b128 v220, v[112:115] offset:10240\n" \
"ds_write_b64 v221, v[132:133] offset:14336\n" \
"ds_write_b64 v221, v[144:145] offset:14848\n" \
"ds_write_b128 v220, v[120:123] offset:11264\n" \
"ds_write_b64 v221, v[138:139] offset:15360\n" \
"ds_write_b64 v221, v[150:151] offset:15872\n" \
"Lq_nowr_%=:\n" \
"v_cvt_pk_bf16_f32 v64, v64, v65\n" \
"v_cvt_pk_bf16_f32 v65, v66, v67\n" \
"v_cvt_pk_bf16_f32 v66, v68, v69\n" \
"v_cvt_pk_bf16_f32 v67, v70, v71\n" \
"v_cvt_pk_bf16_f32 v72, v72, v73\n" \
"v_cvt_pk_bf16_f32 v73, v74, v75\n" \
"v_cvt_pk_bf16_f32 v74, v76, v77\n" \
"v_cvt_pk_bf16_f32 v75, v78, v79\n" \
"v_cvt_pk_bf16_f32 v96, v96, v97\n" \
"v_cvt_pk_bf16_f32 v97, v98, v99\n" \
"v_readlane_b32 s26, v254, 6\n" \
"v_cvt_pk_bf16_f32 v98, v100, v101\n" \
"v_cvt_pk_bf16_f32 v99, v102, v103\n" \
"v_cvt_pk_bf16_f32 v104, v104, v105\n" \
"v_cvt_pk_bf16_f32 v105, v106, v107\n" \
"v_cvt_pk_bf16_f32 v106, v108, v109\n" \
"s_add_u32 m0, s80, 81920\n" \
"v_cvt_pk_bf16_f32 v107, v110, v111\n" \
"buffer_load_dwordx4 v235, s[68:71], s26 offen lds\n" \
"v_accvgpr_read_b32 v237, a234\n" \
"v_accvgpr_read_b32 v238, a235\n" \
"v_accvgpr_read_b32 v239, a236\n" \
"v_accvgpr_read_b32 v240, a237\n" \
"v_cvt_pk_bf16_f32 v0, v0, v1\n" \
"v_cvt_pk_bf16_f32 v1, v2, v3\n" \
"v_cvt_pk_bf16_f32 v2, v4, v5\n" \
"v_cvt_pk_bf16_f32 v3, v6, v7\n" \
"v_cvt_pk_bf16_f32 v8, v8, v9\n" \
"v_cvt_pk_bf16_f32 v9, v10, v11\n" \
"v_cvt_pk_bf16_f32 v10, v12, v13\n" \
"v_cvt_pk_bf16_f32 v11, v14, v15\n" \
"v_cvt_pk_bf16_f32 v16, v16, v17\n" \
"v_cvt_pk_bf16_f32 v17, v18, v19\n" \
"v_cvt_pk_bf16_f32 v18, v20, v21\n" \
"v_cvt_pk_bf16_f32 v19, v22, v23\n" \
"v_cvt_pk_bf16_f32 v24, v24, v25\n" \
"v_cvt_pk_bf16_f32 v25, v26, v27\n" \
"v_cvt_pk_bf16_f32 v26, v28, v29\n" \
"s_add_u32 m0, s80, 86016\n" \
"v_cvt_pk_bf16_f32 v27, v30, v31\n" \
"buffer_load_dwordx4 v236, s[68:71], s26 offen lds\n" \
"v_cvt_pk_bf16_f32 v32, v32, v33\n" \
"v_cvt_pk_bf16_f32 v33, v34, v35\n" \
"v_cvt_pk_bf16_f32 v34, v36, v37\n" \
"v_cvt_pk_bf16_f32 v35, v38, v39\n" \
"v_cvt_pk_bf16_f32 v40, v40, v41\n" \
"v_cvt_pk_bf16_f32 v41, v42, v43\n" \
"v_cvt_pk_bf16_f32 v42, v44, v45\n" \
"v_cvt_pk_bf16_f32 v43, v46, v47\n" \
"v_cvt_pk_bf16_f32 v48, v48, v49\n" \
"v_cvt_pk_bf16_f32 v49, v50, v51\n" \
"v_cvt_pk_bf16_f32 v50, v52, v53\n" \
"v_cvt_pk_bf16_f32 v51, v54, v55\n" \
"v_cvt_pk_bf16_f32 v56, v56, v57\n" \
"v_cvt_pk_bf16_f32 v57, v58, v59\n" \
"v_cvt_pk_bf16_f32 v58, v60, v61\n" \
"v_cvt_pk_bf16_f32 v59, v62, v63\n" \
"s_mov_b64 vcc, -1\n" \
"s_cmp_eq_u32 s59, 1\n" \
"s_cbranch_scc1 Lpadq1_%=\n" \
"s_cmp_eq_u32 s59, 2\n" \
"s_cbranch_scc1 Lpadq2_%=\n" \
"s_cmp_eq_u32 s59, 3\n" \
"s_cbranch_scc1 Lpadq3_%=\n" \
"s_branch Lpadq0_%=\n" \
"Lprq0_%=:\n" \
"Lprq1_%=:\n" \
"Lprq2_%=:\n" \
"Lprq3_%=:\n" \
"s_mov_b64 vcc, 0\n" \
"Lscq_%=:\n" \
"s_waitcnt lgkmcnt(0)\n" \
"s_barrier\n" \
"ds_read_b128 v[68:71], v222 offset:0\n" \
"ds_read_b64 v[192:193], v223 offset:4096\n" \
"ds_read_b64 v[204:205], v223 offset:4608\n" \
"ds_read_b128 v[76:79], v220 offset:1024\n" \
"ds_read_b64 v[198:199], v221 offset:5120\n" \
"ds_read_b64 v[210:211], v221 offset:5632\n" \
"ds_read_b128 v[100:103], v222 offset:2048\n" \
"ds_read_b64 v[128:129], v223 offset:6144\n" \
"ds_read_b64 v[140:141], v223 offset:6656\n" \
"ds_read_b128 v[108:111], v220 offset:3072\n" \
"ds_read_b64 v[134:135], v221 offset:7168\n" \
"ds_read_b64 v[146:147], v221 offset:7680\n" \
"v_readlane_b32 s27, v254, 7\n" \
"s_add_u32 m0, s80, 90112\n" \
"s_nop 3\n" \
"buffer_load_dwordx4 v235, s[68:71], s27 offen lds\n" \
"s_add_u32 m0, s80, 94208\n" \
"s_nop 0\n" \
"buffer_load_dwordx4 v236, s[68:71], s27 offen lds\n" \
"s_waitcnt vmcnt(12)\n" \
"v_cmp_ne_u64 vcc, s[72:73], v[250:251]\n" \
"s_nop 0\n" \
"s_cbranch_vccz Lpd9_%=\n" \
"s_mov_b32 s61, 0\n" \
"Lp9_%=:\n" \
"buffer_load_dwordx2 v[250:251], v253, s[64:67], s84 offen sc1\n" \
"s_waitcnt vmcnt(0)\n" \
"v_cmp_ne_u64 vcc, s[72:73], v[250:251]\n" \
"s_nop 0\n" \
"s_cbranch_vccz Lpd9_%=\n" \
"s_add_u32 s61, s61, 1\n" \
"s_cmp_lt_u32 s61, 0x40000\n" \
"s_sleep 1\n" \
"s_cbranch_scc1 Lp9_%=\n" \
"s_mov_b64 vcc, 0\n" \
"Lpd9_%=:\n" \
"v_accvgpr_read_b32 v253, a238\n" \
"s_mov_b32 s60, 0x3e8293ee\n" \
"v_accvgpr_read_b32 v4, a220\n" \
"v_accvgpr_read_b32 v12, a224\n" \
"v_accvgpr_read_b32 v20, a228\n" \
"v_mov_b32 v28, 0x3f803f80\n" \
"v_accvgpr_read_b32 v84, a188\n" \
"v_accvgpr_read_b32 v5, a221\n" \
"v_accvgpr_read_b32 v13, a225\n" \
"v_accvgpr_read_b32 v21, a229\n" \
"v_mov_b32 v29, 0x3f803f80\n" \
"v_accvgpr_read_b32 v85, a189\n" \
"v_accvgpr_read_b32 v6, a222\n" \
"v_accvgpr_read_b32 v14, a226\n" \
"v_accvgpr_read_b32 v22, a230\n" \
"v_mov_b32 v30, 0x3f803f80\n" \
"v_accvgpr_read_b32 v86, a190\n" \
"v_accvgpr_read_b32 v7, a223\n" \
"v_accvgpr_read_b32 v15, a227\n" \
"v_accvgpr_read_b32 v23, a231\n" \
"v_mov_b32 v31, 0x3f803f80\n" \
"v_accvgpr_read_b32 v87, a191\n" \
"v_add_u32 v224, 0x10000, v237\n" \
"v_add_u32 v225, 0x10000, v238\n" \
"v_add_u32 v226, 0x10000, v239\n" \
"v_add_u32 v227, 0x10000, v240\n" \
"v_add_u32 v228, 0x20000, v237\n" \
"v_add_u32 v229, 0x20000, v238\n" \
"v_add_u32 v230, 0x20000, v239\n" \
"v_add_u32 v231, 0x20000, v240\n" \
"s_waitcnt lgkmcnt(0)\n" \
"v_readlane_b32 s20, v241, 0\n" \
"v_mfma_f32_16x16x32_bf16 v[152:155], v[68:71], v[0:3], v[20:23]\n" \
"ds_read_b64_tr_b16 a[128:129], v237 offset:32768\n" \
"ds_read_b64_tr_b16 a[130:131], v237 offset:34816\n" \
"v_mfma_f32_16x16x32_bf16 v[156:159], v[64:67], v[0:3], v[4:7]\n" \
"s_nop 0\n" \
"buffer_load_dwordx4 a[0:3], v233, s[64:67], s20 offen offset:0 sc1\n" \
"v_mfma_f32_16x16x32_bf16 v[162:165], v[76:79], v[8:11], v[20:23]\n" \
"ds_read_b64_tr_b16 a[132:133], v238 offset:32768\n" \
"ds_read_b64_tr_b16 a[134:135], v238 offset:34816\n" \
"v_mfma_f32_16x16x32_bf16 v[166:169], v[72:75], v[8:11], v[4:7]\n" \
"ds_read_b64_tr_b16 a[136:137], v239 offset:32768\n" \
"s_nop 0\n" \
"v_max_f32 v152, v152, v156\n" \
"v_mfma_f32_16x16x32_bf16 v[172:175], v[64:67], v[16:19], v[12:15]\n" \
"v_max_f32 v153, v153, v157\n" \
"v_max_f32 v154, v154, v158\n" \
"v_mfma_f32_16x16x32_bf16 v[176:179], v[80:83], v[16:19], v[4:7]\n" \
"buffer_load_dwordx4 a[4:7], v233, s[64:67], s20 offen offset:1024 sc1\n" \
"v_mfma_f32_16x16x32_bf16 v[182:185], v[72:75], v[24:27], v[12:15]\n" \
"v_max_f32 v162, v162, v166\n" \
"v_max_f32 v163, v163, v167\n" \
"v_mfma_f32_16x16x32_bf16 v[186:189], v[88:91], v[24:27], v[4:7]\n" \
"v_max_f32 v164, v164, v168\n" \
"v_max_f32 v155, v155, v159\n" \
"v_max3_f32 v160, v152, v153, v154\n" \
"v_max_f32 v172, v172, v176\n" \
"v_max_f32 v173, v173, v177\n" \
"v_max_f32 v174, v174, v178\n" \
"v_max_f32 v165, v165, v169\n" \
"v_max3_f32 v170, v162, v163, v164\n" \
"v_max_f32 v160, v160, v155\n" \
"v_max_f32 v182, v182, v186\n" \
"v_max_f32 v183, v183, v187\n" \
"v_max_f32 v184, v184, v188\n" \
"v_max_f32 v175, v175, v179\n" \
"v_max3_f32 v180, v172, v173, v174\n" \
"buffer_load_dwordx4 a[8:11], v234, s[64:67], s20 offen offset:0 sc1\n" \
"v_max_f32 v170, v170, v165\n" \
"v_mov_b32 v161, v160\n" \
"v_max_f32 v185, v185, v189\n" \
"v_max3_f32 v190, v182, v183, v184\n" \
"v_max_f32 v180, v180, v175\n" \
"v_mov_b32 v171, v170\n" \
"v_permlane32_swap_b32 v160, v161\n" \
"v_max_f32 v190, v190, v185\n" \
"v_mov_b32 v181, v180\n" \
"v_permlane32_swap_b32 v170, v171\n" \
"v_max_f32 v160, v160, v161\n" \
"v_mov_b32 v191, v190\n" \
"v_permlane32_swap_b32 v180, v181\n" \
"v_max_f32 v170, v170, v171\n" \
"v_mov_b32 v161, v160\n" \
"v_permlane32_swap_b32 v190, v191\n" \
"v_max_f32 v180, v180, v181\n" \
"v_mov_b32 v171, v170\n" \
"v_permlane16_swap_b32 v160, v161\n" \
"v_max_f32 v190, v190, v191\n" \
"v_mov_b32 v181, v180\n" \
"buffer_load_dwordx4 a[12:15], v234, s[64:67], s20 offen offset:1024 sc1\n" \
"v_permlane16_swap_b32 v170, v171\n" \
"v_max_f32 v160, v160, v161\n" \
"v_mov_b32 v191, v190\n" \
"v_permlane16_swap_b32 v180, v181\n" \
"v_max_f32 v170, v170, v171\n" \
"v_mul_f32 v160, s60, v160\n" \
"v_permlane16_swap_b32 v190, v191\n" \
"v_max_f32 v180, v180, v181\n" \
"v_mul_f32 v170, s60, v170\n" \
"v_fma_f32 v152, v152, s60, -v160\n" \
"v_fma_f32 v153, v153, s60, -v160\n" \
"v_fma_f32 v154, v154, s60, -v160\n" \
"v_fma_f32 v155, v155, s60, -v160\n" \
"v_readlane_b32 s21, v241, 1\n" \
"v_max_f32 v190, v190, v191\n" \
"v_mul_f32 v180, s60, v180\n" \
"v_fma_f32 v162, v162, s60, -v170\n" \
"v_fma_f32 v163, v163, s60, -v170\n" \
"v_fma_f32 v164, v164, s60, -v170\n" \
"v_fma_f32 v165, v165, s60, -v170\n" \
"buffer_load_dwordx4 a[16:19], v233, s[64:67], s21 offen offset:0 sc1\n" \
"v_exp_f32 v152, v152\n" \
"v_mul_f32 v190, s60, v190\n" \
"v_fma_f32 v172, v172, s60, -v180\n" \
"v_exp_f32 v153, v153\n" \
"v_fma_f32 v173, v173, s60, -v180\n" \
"v_fma_f32 v174, v174, s60, -v180\n" \
"v_exp_f32 v154, v154\n" \
"v_fma_f32 v175, v175, s60, -v180\n" \
"v_fma_f32 v182, v182, s60, -v190\n" \
"v_exp_f32 v155, v155\n" \
"v_fma_f32 v183, v183, s60, -v190\n" \
"v_fma_f32 v184, v184, s60, -v190\n" \
"v_exp_f32 v162, v162\n" \
"v_fma_f32 v185, v185, s60, -v190\n" \
"v_cvt_pk_bf16_f32 v156, v152, v153\n" \
"v_exp_f32 v163, v163\n" \
"buffer_load_dwordx4 a[20:23], v233, s[64:67], s21 offen offset:1024 sc1\n" \
"v_exp_f32 v164, v164\n" \
"v_cvt_pk_bf16_f32 v157, v154, v155\n" \
"v_cvt_pk_bf16_f32 v166, v162, v163\n" \
"v_exp_f32 v165, v165\n" \
"v_and_b32 v0, v84, v156\n" \
"v_and_b32 v1, v85, v157\n" \
"v_exp_f32 v172, v172\n" \
"v_cvt_pk_bf16_f32 v167, v164, v165\n" \
"v_and_b32 v2, v86, v156\n" \
"v_exp_f32 v173, v173\n" \
"v_and_b32 v3, v87, v157\n" \
"v_mfma_f32_16x16x32_bf16 v[152:155], v[100:103], v[32:35], v[20:23]\n" \
"v_exp_f32 v174, v174\n" \
"buffer_load_dwordx4 a[24:27], v234, s[64:67], s21 offen offset:0 sc1\n" \
"v_exp_f32 v175, v175\n" \
"v_mfma_f32_16x16x32_bf16 v[156:159], v[96:99], v[32:35], v[4:7]\n" \
"v_exp_f32 v182, v182\n" \
"v_cvt_pk_bf16_f32 v176, v172, v173\n" \
"v_cvt_pk_bf16_f32 v177, v174, v175\n" \
"v_exp_f32 v183, v183\n" \
"v_and_b32 v8, v84, v166\n" \
"v_and_b32 v9, v85, v167\n" \
"v_exp_f32 v184, v184\n" \
"v_and_b32 v10, v86, v166\n" \
"v_and_b32 v11, v87, v167\n" \
"v_exp_f32 v185, v185\n" \
"v_mfma_f32_16x16x32_bf16 v[162:165], v[108:111], v[40:43], v[20:23]\n" \
"v_cvt_pk_bf16_f32 v186, v182, v183\n" \
"buffer_load_dwordx4 a[28:31], v234, s[64:67], s21 offen offset:1024 sc1\n" \
"v_mfma_f32_16x16x32_bf16 v[166:169], v[104:107], v[40:43], v[4:7]\n" \
"v_cvt_pk_bf16_f32 v187, v184, v185\n" \
"v_and_b32 v16, v84, v176\n" \
"v_and_b32 v17, v85, v177\n" \
"v_and_b32 v18, v86, v176\n" \
"v_and_b32 v19, v87, v177\n" \
"v_mfma_f32_16x16x32_bf16 v[172:175], v[96:99], v[48:51], v[12:15]\n" \
"v_and_b32 v24, v84, v186\n" \
"v_and_b32 v25, v85, v187\n" \
"v_mfma_f32_16x16x32_bf16 v[176:179], v[112:115], v[48:51], v[4:7]\n" \
"v_readlane_b32 s22, v241, 2\n" \
"v_and_b32 v26, v86, v186\n" \
"v_and_b32 v27, v87, v187\n" \
"v_mfma_f32_16x16x32_bf16 v[182:185], v[104:107], v[56:59], v[12:15]\n" \
"v_max_f32 v152, v152, v156\n" \
"v_max_f32 v153, v153, v157\n" \
"buffer_load_dwordx4 a[32:35], v233, s[64:67], s22 offen offset:0 sc1\n" \
"v_mfma_f32_16x16x32_bf16 v[186:189], v[120:123], v[56:59], v[4:7]\n" \
"v_max_f32 v154, v154, v158\n" \
"v_max_f32 v162, v162, v166\n" \
"v_max_f32 v163, v163, v167\n" \
"v_max_f32 v164, v164, v168\n" \
"v_max_f32 v155, v155, v159\n" \
"v_max3_f32 v160, v152, v153, v154\n" \
"v_max_f32 v172, v172, v176\n" \
"v_max_f32 v173, v173, v177\n" \
"v_max_f32 v174, v174, v178\n" \
"v_max_f32 v165, v165, v169\n" \
"v_max3_f32 v170, v162, v163, v164\n" \
"v_max_f32 v160, v160, v155\n" \
"v_max_f32 v182, v182, v186\n" \
"v_max_f32 v183, v183, v187\n" \
"v_max_f32 v184, v184, v188\n" \
"v_max_f32 v175, v175, v179\n" \
"v_max3_f32 v180, v172, v173, v174\n" \
"v_max_f32 v170, v170, v165\n" \
"v_mov_b32 v161, v160\n" \
"buffer_load_dwordx4 a[36:39], v233, s[64:67], s22 offen offset:1024 sc1\n" \
"v_max_f32 v185, v185, v189\n" \
"v_max3_f32 v190, v182, v183, v184\n" \
"v_max_f32 v180, v180, v175\n" \
"v_mov_b32 v171, v170\n" \
"v_permlane32_swap_b32 v160, v161\n" \
"v_max_f32 v190, v190, v185\n" \
"v_mov_b32 v181, v180\n" \
"v_permlane32_swap_b32 v170, v171\n" \
"v_max_f32 v160, v160, v161\n" \
"v_mov_b32 v191, v190\n" \
"v_permlane32_swap_b32 v180, v181\n" \
"v_max_f32 v170, v170, v171\n" \
"v_mov_b32 v161, v160\n" \
"v_permlane32_swap_b32 v190, v191\n" \
"v_max_f32 v180, v180, v181\n" \
"v_mov_b32 v171, v170\n" \
"v_permlane16_swap_b32 v160, v161\n" \
"v_max_f32 v190, v190, v191\n" \
"v_mov_b32 v181, v180\n" \
"v_permlane16_swap_b32 v170, v171\n" \
"buffer_load_dwordx4 a[40:43], v234, s[64:67], s22 offen offset:0 sc1\n" \
"v_max_f32 v160, v160, v161\n" \
"v_mov_b32 v191, v190\n" \
"v_permlane16_swap_b32 v180, v181\n" \
"v_max_f32 v170, v170, v171\n" \
"v_mul_f32 v160, s60, v160\n" \
"v_permlane16_swap_b32 v190, v191\n" \
"v_max_f32 v180, v180, v181\n" \
"v_mul_f32 v170, s60, v170\n" \
"v_fma_f32 v152, v152, s60, -v160\n" \
"v_fma_f32 v153, v153, s60, -v160\n" \
"v_fma_f32 v154, v154, s60, -v160\n" \
"v_fma_f32 v155, v155, s60, -v160\n" \
"v_max_f32 v190, v190, v191\n" \
"v_mul_f32 v180, s60, v180\n" \
"v_fma_f32 v162, v162, s60, -v170\n" \
"v_fma_f32 v163, v163, s60, -v170\n" \
"v_fma_f32 v164, v164, s60, -v170\n" \
"v_fma_f32 v165, v165, s60, -v170\n" \
"v_exp_f32 v152, v152\n" \
"v_mfma_f32_16x16x32_bf16 v[36:39], v[28:31], v[0:3], 0\n" \
"buffer_load_dwordx4 a[44:47], v234, s[64:67], s22 offen offset:1024 sc1\n" \
"v_exp_f32 v153, v153\n" \
"v_mul_f32 v190, s60, v190\n" \
"v_fma_f32 v172, v172, s60, -v180\n" \
"v_exp_f32 v154, v154\n" \
"v_fma_f32 v173, v173, s60, -v180\n" \
"v_fma_f32 v174, v174, s60, -v180\n" \
"v_exp_f32 v155, v155\n" \
"v_fma_f32 v175, v175, s60, -v180\n" \
"v_mfma_f32_16x16x32_bf16 v[44:47], v[28:31], v[8:11], 0\n" \
"v_readlane_b32 s23, v241, 3\n" \
"v_exp_f32 v162, v162\n" \
"v_fma_f32 v182, v182, s60, -v190\n" \
"v_fma_f32 v183, v183, s60, -v190\n" \
"v_exp_f32 v163, v163\n" \
"s_nop 0\n" \
"buffer_load_dwordx4 a[48:51], v233, s[64:67], s23 offen offset:0 sc1\n" \
"v_exp_f32 v164, v164\n" \
"v_fma_f32 v184, v184, s60, -v190\n" \
"v_fma_f32 v185, v185, s60, -v190\n" \
"v_exp_f32 v165, v165\n" \
"v_cvt_pk_bf16_f32 v156, v152, v153\n" \
"v_cvt_pk_bf16_f32 v157, v154, v155\n" \
"v_exp_f32 v172, v172\n" \
"v_mfma_f32_16x16x32_bf16 v[52:55], v[28:31], v[16:19], 0\n" \
"v_exp_f32 v173, v173\n" \
"v_mfma_f32_16x16x32_bf16 v[64:67], v[192:195], v[0:3], 0\n" \
"v_exp_f32 v174, v174\n" \
"v_mfma_f32_16x16x32_bf16 v[68:71], v[204:207], v[0:3], 0\n" \
"buffer_load_dwordx4 a[52:55], v233, s[64:67], s23 offen offset:1024 sc1\n" \
"v_exp_f32 v175, v175\n" \
"v_cvt_pk_bf16_f32 v166, v162, v163\n" \
"v_cvt_pk_bf16_f32 v167, v164, v165\n" \
"v_exp_f32 v182, v182\n" \
"v_and_b32 v32, v84, v156\n" \
"v_and_b32 v33, v85, v157\n" \
"v_exp_f32 v183, v183\n" \
"v_and_b32 v34, v86, v156\n" \
"v_and_b32 v35, v87, v157\n" \
"v_exp_f32 v184, v184\n" \
"v_mfma_f32_16x16x32_bf16 v[60:63], v[28:31], v[24:27], 0\n" \
"v_exp_f32 v185, v185\n" \
"v_mfma_f32_16x16x32_bf16 v[72:75], v[198:201], v[8:11], 0\n" \
"buffer_load_dwordx4 a[56:59], v234, s[64:67], s23 offen offset:0 sc1\n" \
"v_mfma_f32_16x16x32_bf16 v[76:79], v[210:213], v[8:11], 0\n" \
"v_cvt_pk_bf16_f32 v176, v172, v173\n" \
"v_cvt_pk_bf16_f32 v177, v174, v175\n" \
"v_and_b32 v40, v84, v166\n" \
"v_and_b32 v41, v85, v167\n" \
"v_and_b32 v42, v86, v166\n" \
"v_and_b32 v43, v87, v167\n" \
"v_mfma_f32_16x16x32_bf16 v[116:119], v[28:31], v[32:35], 0\n" \
"v_rcp_f32 v36, v36\n" \
"v_mfma_f32_16x16x32_bf16 v[216:219], v[194:197], v[16:19], 0\n" \
"v_cvt_pk_bf16_f32 v186, v182, v183\n" \
"v_cvt_pk_bf16_f32 v187, v184, v185\n" \
"v_mfma_f32_16x16x32_bf16 v[220:223], v[206:209], v[16:19], 0\n" \
"v_and_b32 v48, v84, v176\n" \
"v_and_b32 v49, v85, v177\n" \
"v_and_b32 v50, v86, v176\n" \
"buffer_load_dwordx4 a[60:63], v234, s[64:67], s23 offen offset:1024 sc1\n" \
"v_and_b32 v51, v87, v177\n" \
"v_mfma_f32_16x16x32_bf16 v[124:127], v[28:31], v[40:43], 0\n" \
"v_rcp_f32 v44, v44\n" \
"v_mfma_f32_16x16x32_bf16 v[88:91], v[200:203], v[24:27], 0\n" \
"v_and_b32 v56, v84, v186\n" \
"v_and_b32 v57, v85, v187\n" \
"v_mfma_f32_16x16x32_bf16 v[92:95], v[212:215], v[24:27], 0\n" \
"v_and_b32 v58, v86, v186\n" \
"v_and_b32 v59, v87, v187\n" \
"v_readlane_b32 s20, v241, 4\n" \
"v_mfma_f32_16x16x32_bf16 v[100:103], v[28:31], v[48:51], 0\n" \
"v_rcp_f32 v52, v52\n" \
"v_mfma_f32_16x16x32_bf16 v[152:155], v[128:131], v[32:35], 0\n" \
"s_nop 1\n" \
"buffer_load_dwordx4 a[64:67], v233, s[64:67], s20 offen offset:0 sc1\n" \
"v_mfma_f32_16x16x32_bf16 v[156:159], v[140:143], v[32:35], 0\n" \
"v_mul_f32 v64, v64, v36\n" \
"v_mul_f32 v65, v65, v36\n" \
"v_mul_f32 v66, v66, v36\n" \
"v_mul_f32 v67, v67, v36\n" \
"v_mul_f32 v68, v68, v36\n" \
"v_mul_f32 v69, v69, v36\n" \
"v_mul_f32 v70, v70, v36\n" \
"v_mul_f32 v71, v71, v36\n" \
"v_mfma_f32_16x16x32_bf16 v[108:111], v[28:31], v[56:59], 0\n" \
"v_rcp_f32 v60, v60\n" \
"v_mfma_f32_16x16x32_bf16 v[162:165], v[134:137], v[40:43], 0\n" \
"v_mul_f32 v72, v72, v44\n" \
"v_mul_f32 v73, v73, v44\n" \
"v_mfma_f32_16x16x32_bf16 v[166:169], v[146:149], v[40:43], 0\n" \
"v_mul_f32 v74, v74, v44\n" \
"buffer_load_dwordx4 a[68:71], v233, s[64:67], s20 offen offset:1024 sc1\n" \
"v_mul_f32 v75, v75, v44\n" \
"v_mul_f32 v76, v76, v44\n" \
"v_mul_f32 v77, v77, v44\n" \
"v_mul_f32 v78, v78, v44\n" \
"v_mul_f32 v79, v79, v44\n" \
"v_cvt_pk_bf16_f32 v0, v64, v65\n" \
"v_cvt_pk_bf16_f32 v1, v66, v67\n" \
"v_cvt_pk_bf16_f32 v2, v68, v69\n" \
"v_cvt_pk_bf16_f32 v3, v70, v71\n" \
"v_mfma_f32_16x16x32_bf16 v[172:175], v[130:133], v[48:51], 0\n" \
"v_rcp_f32 v116, v116\n" \
"v_mfma_f32_16x16x32_bf16 v[176:179], v[142:145], v[48:51], 0\n" \
"v_mul_f32 v216, v216, v52\n" \
"v_mul_f32 v217, v217, v52\n" \
"v_mul_f32 v218, v218, v52\n" \
"v_mul_f32 v219, v219, v52\n" \
"v_mul_f32 v220, v220, v52\n" \
"buffer_load_dwordx4 a[72:75], v234, s[64:67], s20 offen offset:0 sc1\n" \
"v_mul_f32 v221, v221, v52\n" \
"v_mul_f32 v222, v222, v52\n" \
"v_mul_f32 v223, v223, v52\n" \
"v_cvt_pk_bf16_f32 v8, v72, v73\n" \
"v_cvt_pk_bf16_f32 v9, v74, v75\n" \
"v_cvt_pk_bf16_f32 v10, v76, v77\n" \
"v_cvt_pk_bf16_f32 v11, v78, v79\n" \
"buffer_store_dwordx4 v[0:3], v232, s[64:67], s41 offen offset:0 sc1\n" \
"ds_read_b64_tr_b16 a[138:139], v239 offset:34816\n" \
"v_mfma_f32_16x16x32_bf16 v[182:185], v[136:139], v[56:59], 0\n" \
"v_rcp_f32 v124, v124\n" \
"v_mfma_f32_16x16x32_bf16 v[186:189], v[148:151], v[56:59], 0\n" \
"v_mul_f32 v88, v88, v60\n" \
"v_mul_f32 v89, v89, v60\n" \
"v_mul_f32 v90, v90, v60\n" \
"v_mul_f32 v91, v91, v60\n" \
"v_mul_f32 v92, v92, v60\n" \
"buffer_load_dwordx4 a[76:79], v234, s[64:67], s20 offen offset:1024 sc1\n" \
"v_mul_f32 v93, v93, v60\n" \
"v_mul_f32 v94, v94, v60\n" \
"v_mul_f32 v95, v95, v60\n" \
"v_cvt_pk_bf16_f32 v16, v216, v217\n" \
"v_cvt_pk_bf16_f32 v17, v218, v219\n" \
"v_cvt_pk_bf16_f32 v18, v220, v221\n" \
"v_cvt_pk_bf16_f32 v19, v222, v223\n" \
"buffer_store_dwordx4 v[8:11], v232, s[64:67], s41 offen offset:1024 sc1\n" \
"ds_read_b64_tr_b16 a[140:141], v240 offset:32768\n" \
"v_rcp_f32 v100, v100\n" \
"ds_read_b64_tr_b16 a[142:143], v240 offset:34816\n" \
"v_cvt_pk_bf16_f32 v24, v88, v89\n" \
"v_cvt_pk_bf16_f32 v25, v90, v91\n" \
"v_cvt_pk_bf16_f32 v26, v92, v93\n" \
"v_cvt_pk_bf16_f32 v27, v94, v95\n" \
"buffer_store_dwordx4 v[16:19], v232, s[64:67], s41 offen offset:2048 sc1\n" \
"v_mul_f32 v152, v152, v116\n" \
"v_mul_f32 v153, v153, v116\n" \
"v_mul_f32 v154, v154, v116\n" \
"v_mul_f32 v155, v155, v116\n" \
"Lpadq0_%=:\n" \
"s_cbranch_vccnz Lpadq4_%=\n" \
"v_mul_f32 v156, v156, v116\n" \
"v_mul_f32 v157, v157, v116\n" \
"v_mul_f32 v158, v158, v116\n" \
"v_mul_f32 v159, v159, v116\n" \
"ds_read_b64_tr_b16 a[144:145], v237 offset:36864\n" \
"v_rcp_f32 v108, v108\n" \
"buffer_store_dwordx4 v[24:27], v232, s[64:67], s41 offen offset:3072 sc1\n" \
"v_mul_f32 v162, v162, v124\n" \
"v_mul_f32 v163, v163, v124\n" \
"v_mul_f32 v164, v164, v124\n" \
"v_mul_f32 v165, v165, v124\n" \
"v_mul_f32 v166, v166, v124\n" \
"v_mul_f32 v167, v167, v124\n" \
"v_mul_f32 v168, v168, v124\n" \
"v_mul_f32 v169, v169, v124\n" \
"v_cvt_pk_bf16_f32 v32, v152, v153\n" \
"v_cvt_pk_bf16_f32 v33, v154, v155\n" \
"v_cvt_pk_bf16_f32 v34, v156, v157\n" \
"v_cvt_pk_bf16_f32 v35, v158, v159\n" \
"ds_read_b64_tr_b16 a[146:147], v237 offset:38912\n" \
"ds_read_b64_tr_b16 a[148:149], v238 offset:36864\n" \
"v_mul_f32 v172, v172, v100\n" \
"v_mul_f32 v173, v173, v100\n" \
"v_mul_f32 v174, v174, v100\n" \
"v_mul_f32 v175, v175, v100\n" \
"v_mul_f32 v176, v176, v100\n" \
"v_mul_f32 v177, v177, v100\n" \
"v_mul_f32 v178, v178, v100\n" \
"v_mul_f32 v179, v179, v100\n" \
"v_cvt_pk_bf16_f32 v40, v162, v163\n" \
"v_cvt_pk_bf16_f32 v41, v164, v165\n" \
"v_cvt_pk_bf16_f32 v42, v166, v167\n" \
"v_cvt_pk_bf16_f32 v43, v168, v169\n" \
"buffer_store_dwordx4 v[32:35], v232, s[64:67], s42 offen offset:0 sc1\n" \
"ds_read_b64_tr_b16 a[150:151], v238 offset:38912\n" \
"v_mul_f32 v182, v182, v108\n" \
"v_mul_f32 v183, v183, v108\n" \
"v_mul_f32 v184, v184, v108\n" \
"v_mul_f32 v185, v185, v108\n" \
"v_mul_f32 v186, v186, v108\n" \
"v_mul_f32 v187, v187, v108\n" \
"v_mul_f32 v188, v188, v108\n" \
"v_mul_f32 v189, v189, v108\n" \
"v_cvt_pk_bf16_f32 v48, v172, v173\n" \
"v_cvt_pk_bf16_f32 v49, v174, v175\n" \
"v_cvt_pk_bf16_f32 v50, v176, v177\n" \
"v_cvt_pk_bf16_f32 v51, v178, v179\n" \
"buffer_store_dwordx4 v[40:43], v232, s[64:67], s42 offen offset:1024 sc1\n" \
"ds_read_b64_tr_b16 a[152:153], v239 offset:36864\n" \
"ds_read_b64_tr_b16 a[154:155], v239 offset:38912\n" \
"v_cvt_pk_bf16_f32 v56, v182, v183\n" \
"v_cvt_pk_bf16_f32 v57, v184, v185\n" \
"v_cvt_pk_bf16_f32 v58, v186, v187\n" \
"v_cvt_pk_bf16_f32 v59, v188, v189\n" \
"buffer_store_dwordx4 v[48:51], v232, s[64:67], s42 offen offset:2048 sc1\n" \
"ds_read_b64_tr_b16 a[156:157], v240 offset:36864\n" \
"buffer_store_dwordx4 v[56:59], v232, s[64:67], s42 offen offset:3072 sc1\n" \
"ds_read_b64_tr_b16 a[158:159], v240 offset:38912\n" \
"v_readlane_b32 s24, v254, 8\n" \
"v_readlane_b32 s21, v241, 5\n" \
"s_waitcnt vmcnt(24) lgkmcnt(0)\n" \
"v_mfma_f32_16x16x32_bf16 v[0:3], a[128:131], a[0:3], 0\n" \
"ds_read_b64_tr_b16 a[160:161], v237 offset:40960\n" \
"v_mfma_f32_16x16x32_bf16 v[8:11], a[128:131], a[4:7], 0\n" \
"s_nop 0\n" \
"buffer_load_dwordx4 a[80:83], v233, s[64:67], s21 offen offset:0 sc1\n" \
"v_mfma_f32_16x16x32_bf16 v[16:19], a[128:131], a[8:11], 0\n" \
"v_readlane_b32 s22, v241, 6\n" \
"ds_read_b64_tr_b16 a[162:163], v237 offset:43008\n" \
"v_mfma_f32_16x16x32_bf16 v[24:27], a[128:131], a[12:15], 0\n" \
"ds_read_b64_tr_b16 a[164:165], v238 offset:40960\n" \
"v_mfma_f32_16x16x32_bf16 v[28:31], a[132:135], a[12:15], 0\n" \
"buffer_load_dwordx4 a[84:87], v233, s[64:67], s21 offen offset:1024 sc1\n" \
"v_mfma_f32_16x16x32_bf16 v[20:23], a[132:135], a[8:11], 0\n" \
"ds_read_b64_tr_b16 a[166:167], v238 offset:43008\n" \
"v_mfma_f32_16x16x32_bf16 v[12:15], a[132:135], a[4:7], 0\n" \
"ds_read_b64_tr_b16 a[168:169], v239 offset:40960\n" \
"v_mfma_f32_16x16x32_bf16 v[4:7], a[132:135], a[0:3], 0\n" \
"ds_read_b64_tr_b16 a[170:171], v239 offset:43008\n" \
"v_mfma_f32_16x16x32_bf16 v[32:35], a[136:139], a[0:3], 0\n" \
"buffer_load_dwordx4 a[88:91], v234, s[64:67], s21 offen offset:0 sc1\n" \
"v_mfma_f32_16x16x32_bf16 v[40:43], a[136:139], a[4:7], 0\n" \
"v_mfma_f32_16x16x32_bf16 v[48:51], a[136:139], a[8:11], 0\n" \
"ds_read_b64_tr_b16 a[172:173], v240 offset:40960\n" \
"v_mfma_f32_16x16x32_bf16 v[56:59], a[136:139], a[12:15], 0\n" \
"buffer_load_dwordx4 a[92:95], v234, s[64:67], s21 offen offset:1024 sc1\n" \
"v_mfma_f32_16x16x32_bf16 v[60:63], a[140:143], a[12:15], 0\n" \
"ds_read_b64_tr_b16 a[174:175], v240 offset:43008\n" \
"v_mfma_f32_16x16x32_bf16 v[52:55], a[140:143], a[8:11], 0\n" \
"ds_read_b64_tr_b16 a[176:177], v237 offset:45056\n" \
"v_mfma_f32_16x16x32_bf16 v[44:47], a[140:143], a[4:7], 0\n" \
"s_add_u32 m0, s80, 98304\n" \
"ds_read_b64_tr_b16 a[178:179], v237 offset:47104\n" \
"v_mfma_f32_16x16x32_bf16 v[36:39], a[140:143], a[0:3], 0\n" \
"buffer_load_dwordx4 v235, s[68:71], s24 offen lds\n" \
"v_mfma_f32_16x16x32_bf16 v[64:67], a[144:147], a[0:3], 0\n" \
"v_readlane_b32 s25, v254, 9\n" \
"ds_read_b64_tr_b16 a[180:181], v238 offset:45056\n" \
"v_mfma_f32_16x16x32_bf16 v[72:75], a[144:147], a[4:7], 0\n" \
"v_mfma_f32_16x16x32_bf16 v[80:83], a[144:147], a[8:11], 0\n" \
"s_add_u32 m0, s80, 102400\n" \
"ds_read_b64_tr_b16 a[182:183], v238 offset:47104\n" \
"v_mfma_f32_16x16x32_bf16 v[88:91], a[144:147], a[12:15], 0\n" \
"buffer_load_dwordx4 v236, s[68:71], s24 offen lds\n" \
"v_mfma_f32_16x16x32_bf16 v[92:95], a[148:151], a[12:15], 0\n" \
"ds_read_b64_tr_b16 a[184:185], v239 offset:45056\n" \
"v_mfma_f32_16x16x32_bf16 v[84:87], a[148:151], a[8:11], 0\n" \
"s_add_u32 m0, s80, 106496\n" \
"ds_read_b64_tr_b16 a[186:187], v239 offset:47104\n" \
"v_mfma_f32_16x16x32_bf16 v[76:79], a[148:151], a[4:7], 0\n" \
"buffer_load_dwordx4 v235, s[68:71], s25 offen lds\n" \
"v_mfma_f32_16x16x32_bf16 v[68:71], a[148:151], a[0:3], 0\n" \
"v_readlane_b32 s26, v254, 10\n" \
"ds_read_b64_tr_b16 a[188:189], v240 offset:45056\n" \
"v_mfma_f32_16x16x32_bf16 v[96:99], a[152:155], a[0:3], 0\n" \
"ds_read_b64_tr_b16 a[190:191], v240 offset:47104\n" \
"v_mfma_f32_16x16x32_bf16 v[104:107], a[152:155], a[4:7], 0\n" \
"s_add_u32 m0, s80, 110592\n" \
"v_mfma_f32_16x16x32_bf16 v[112:115], a[152:155], a[8:11], 0\n" \
"buffer_load_dwordx4 v236, s[68:71], s25 offen lds\n" \
"v_mfma_f32_16x16x32_bf16 v[120:123], a[152:155], a[12:15], 0\n" \
"v_mfma_f32_16x16x32_bf16 v[124:127], a[156:159], a[12:15], 0\n" \
"v_mfma_f32_16x16x32_bf16 v[116:119], a[156:159], a[8:11], 0\n" \
"v_mfma_f32_16x16x32_bf16 v[108:111], a[156:159], a[4:7], 0\n" \
"v_mfma_f32_16x16x32_bf16 v[100:103], a[156:159], a[0:3], 0\n" \
"s_waitcnt vmcnt(28) lgkmcnt(0)\n" \
"v_mfma_f32_16x16x32_bf16 v[0:3], a[160:163], a[16:19], v[0:3]\n" \
"s_add_u32 m0, s80, 114688\n" \
"ds_read_b64_tr_b16 a[128:129], v237 offset:49152\n" \
"v_mfma_f32_16x16x32_bf16 v[8:11], a[160:163], a[20:23], v[8:11]\n" \
"buffer_load_dwordx4 v235, s[68:71], s26 offen lds\n" \
"v_mfma_f32_16x16x32_bf16 v[16:19], a[160:163], a[24:27], v[16:19]\n" \
"v_readlane_b32 s27, v254, 11\n" \
"ds_read_b64_tr_b16 a[130:131], v237 offset:51200\n" \
"v_mfma_f32_16x16x32_bf16 v[24:27], a[160:163], a[28:31], v[24:27]\n" \
"s_add_u32 m0, s80, 118784\n" \
"ds_read_b64_tr_b16 a[132:133], v238 offset:49152\n" \
"v_mfma_f32_16x16x32_bf16 v[28:31], a[164:167], a[28:31], v[28:31]\n" \
"buffer_load_dwordx4 v236, s[68:71], s26 offen lds\n" \
"v_mfma_f32_16x16x32_bf16 v[20:23], a[164:167], a[24:27], v[20:23]\n" \
"ds_read_b64_tr_b16 a[134:135], v238 offset:51200\n" \
"v_mfma_f32_16x16x32_bf16 v[12:15], a[164:167], a[20:23], v[12:15]\n" \
"ds_read_b64_tr_b16 a[136:137], v239 offset:49152\n" \
"v_mfma_f32_16x16x32_bf16 v[4:7], a[164:167], a[16:19], v[4:7]\n" \
"s_add_u32 m0, s80, 122880\n" \
"ds_read_b64_tr_b16 a[138:139], v239 offset:51200\n" \
"v_mfma_f32_16x16x32_bf16 v[32:35], a[168:171], a[16:19], v[32:35]\n" \
"buffer_load_dwordx4 v235, s[68:71], s27 offen lds\n" \
"v_mfma_f32_16x16x32_bf16 v[40:43], a[168:171], a[20:23], v[40:43]\n" \
"v_readlane_b32 s24, v254, 16\n" \
"v_mfma_f32_16x16x32_bf16 v[48:51], a[168:171], a[24:27], v[48:51]\n" \
"s_add_u32 m0, s80, 126976\n" \
"ds_read_b64_tr_b16 a[140:141], v240 offset:49152\n" \
"v_mfma_f32_16x16x32_bf16 v[56:59], a[168:171], a[28:31], v[56:59]\n" \
"buffer_load_dwordx4 v236, s[68:71], s27 offen lds\n" \
"v_mfma_f32_16x16x32_bf16 v[60:63], a[172:175], a[28:31], v[60:63]\n" \
"ds_read_b64_tr_b16 a[142:143], v240 offset:51200\n" \
"v_mfma_f32_16x16x32_bf16 v[52:55], a[172:175], a[24:27], v[52:55]\n" \
"ds_read_b64_tr_b16 a[144:145], v237 offset:53248\n" \
"v_mfma_f32_16x16x32_bf16 v[44:47], a[172:175], a[20:23], v[44:47]\n" \
"ds_read_b64_tr_b16 a[146:147], v237 offset:55296\n" \
"v_mfma_f32_16x16x32_bf16 v[36:39], a[172:175], a[16:19], v[36:39]\n" \
"buffer_load_dwordx4 a[96:99], v233, s[64:67], s22 offen offset:0 sc1\n" \
"v_mfma_f32_16x16x32_bf16 v[64:67], a[176:179], a[16:19], v[64:67]\n" \
"v_readlane_b32 s23, v241, 7\n" \
"ds_read_b64_tr_b16 a[148:149], v238 offset:53248\n" \
"v_mfma_f32_16x16x32_bf16 v[72:75], a[176:179], a[20:23], v[72:75]\n" \
"v_mfma_f32_16x16x32_bf16 v[80:83], a[176:179], a[24:27], v[80:83]\n" \
"ds_read_b64_tr_b16 a[150:151], v238 offset:55296\n" \
"v_mfma_f32_16x16x32_bf16 v[88:91], a[176:179], a[28:31], v[88:91]\n" \
"buffer_load_dwordx4 a[100:103], v233, s[64:67], s22 offen offset:1024 sc1\n" \
"v_mfma_f32_16x16x32_bf16 v[92:95], a[180:183], a[28:31], v[92:95]\n" \
"ds_read_b64_tr_b16 a[152:153], v239 offset:53248\n" \
"v_mfma_f32_16x16x32_bf16 v[84:87], a[180:183], a[24:27], v[84:87]\n" \
"ds_read_b64_tr_b16 a[154:155], v239 offset:55296\n" \
"v_mfma_f32_16x16x32_bf16 v[76:79], a[180:183], a[20:23], v[76:79]\n" \
"buffer_load_dwordx4 a[104:107], v234, s[64:67], s22 offen offset:0 sc1\n" \
"v_mfma_f32_16x16x32_bf16 v[68:71], a[180:183], a[16:19], v[68:71]\n" \
"ds_read_b64_tr_b16 a[156:157], v240 offset:53248\n" \
"v_mfma_f32_16x16x32_bf16 v[96:99], a[184:187], a[16:19], v[96:99]\n" \
"ds_read_b64_tr_b16 a[158:159], v240 offset:55296\n" \
"v_mfma_f32_16x16x32_bf16 v[104:107], a[184:187], a[20:23], v[104:107]\n" \
"v_mfma_f32_16x16x32_bf16 v[112:115], a[184:187], a[24:27], v[112:115]\n" \
"buffer_load_dwordx4 a[108:111], v234, s[64:67], s22 offen offset:1024 sc1\n" \
"v_mfma_f32_16x16x32_bf16 v[120:123], a[184:187], a[28:31], v[120:123]\n" \
"v_mfma_f32_16x16x32_bf16 v[124:127], a[188:191], a[28:31], v[124:127]\n" \
"v_mfma_f32_16x16x32_bf16 v[116:119], a[188:191], a[24:27], v[116:119]\n" \
"v_mfma_f32_16x16x32_bf16 v[108:111], a[188:191], a[20:23], v[108:111]\n" \
"v_mfma_f32_16x16x32_bf16 v[100:103], a[188:191], a[16:19], v[100:103]\n" \
"s_waitcnt vmcnt(32) lgkmcnt(0)\n" \
"v_mfma_f32_16x16x32_bf16 v[0:3], a[128:131], a[32:35], v[0:3]\n" \
"ds_read_b64_tr_b16 a[160:161], v237 offset:57344\n" \
"v_mfma_f32_16x16x32_bf16 v[8:11], a[128:131], a[36:39], v[8:11]\n" \
"buffer_load_dwordx4 a[112:115], v233, s[64:67], s23 offen offset:0 sc1\n" \
"v_mfma_f32_16x16x32_bf16 v[16:19], a[128:131], a[40:43], v[16:19]\n" \
"v_readlane_b32 s20, v241, 8\n" \
"ds_read_b64_tr_b16 a[162:163], v237 offset:59392\n" \
"v_mfma_f32_16x16x32_bf16 v[24:27], a[128:131], a[44:47], v[24:27]\n" \
"ds_read_b64_tr_b16 a[164:165], v238 offset:57344\n" \
"v_mfma_f32_16x16x32_bf16 v[28:31], a[132:135], a[44:47], v[28:31]\n" \
"buffer_load_dwordx4 a[116:119], v233, s[64:67], s23 offen offset:1024 sc1\n" \
"v_mfma_f32_16x16x32_bf16 v[20:23], a[132:135], a[40:43], v[20:23]\n" \
"ds_read_b64_tr_b16 a[166:167], v238 offset:59392\n" \
"v_mfma_f32_16x16x32_bf16 v[12:15], a[132:135], a[36:39], v[12:15]\n" \
"ds_read_b64_tr_b16 a[168:169], v239 offset:57344\n" \
"v_mfma_f32_16x16x32_bf16 v[4:7], a[132:135], a[32:35], v[4:7]\n" \
"buffer_load_dwordx4 a[120:123], v234, s[64:67], s23 offen offset:0 sc1\n" \
"v_mfma_f32_16x16x32_bf16 v[32:35], a[136:139], a[32:35], v[32:35]\n" \
"ds_read_b64_tr_b16 a[170:171], v239 offset:59392\n" \
"v_mfma_f32_16x16x32_bf16 v[40:43], a[136:139], a[36:39], v[40:43]\n" \
"v_mfma_f32_16x16x32_bf16 v[48:51], a[136:139], a[40:43], v[48:51]\n" \
"ds_read_b64_tr_b16 a[172:173], v240 offset:57344\n" \
"v_mfma_f32_16x16x32_bf16 v[56:59], a[136:139], a[44:47], v[56:59]\n" \
"buffer_load_dwordx4 a[124:127], v234, s[64:67], s23 offen offset:1024 sc1\n" \
"v_mfma_f32_16x16x32_bf16 v[60:63], a[140:143], a[44:47], v[60:63]\n" \
"ds_read_b64_tr_b16 a[174:175], v240 offset:59392\n" \
"v_mfma_f32_16x16x32_bf16 v[52:55], a[140:143], a[40:43], v[52:55]\n" \
"ds_read_b64_tr_b16 a[176:177], v237 offset:61440\n" \
"v_mfma_f32_16x16x32_bf16 v[44:47], a[140:143], a[36:39], v[44:47]\n" \
"buffer_load_dwordx4 v[160:163], v233, s[64:67], s20 offen offset:0 sc1\n" \
"v_mfma_f32_16x16x32_bf16 v[36:39], a[140:143], a[32:35], v[36:39]\n" \
"v_readlane_b32 s21, v241, 9\n" \
"ds_read_b64_tr_b16 a[178:179], v237 offset:63488\n" \
"v_mfma_f32_16x16x32_bf16 v[64:67], a[144:147], a[32:35], v[64:67]\n" \
"ds_read_b64_tr_b16 a[180:181], v238 offset:61440\n" \
"v_mfma_f32_16x16x32_bf16 v[72:75], a[144:147], a[36:39], v[72:75]\n" \
"buffer_load_dwordx4 v[164:167], v233, s[64:67], s20 offen offset:1024 sc1\n" \
"v_mfma_f32_16x16x32_bf16 v[80:83], a[144:147], a[40:43], v[80:83]\n" \
"v_mfma_f32_16x16x32_bf16 v[88:91], a[144:147], a[44:47], v[88:91]\n" \
"ds_read_b64_tr_b16 a[182:183], v238 offset:63488\n" \
"v_mfma_f32_16x16x32_bf16 v[92:95], a[148:151], a[44:47], v[92:95]\n" \
"ds_read_b64_tr_b16 a[184:185], v239 offset:61440\n" \
"v_mfma_f32_16x16x32_bf16 v[84:87], a[148:151], a[40:43], v[84:87]\n" \
"buffer_load_dwordx4 v[168:171], v234, s[64:67], s20 offen offset:0 sc1\n" \
"v_mfma_f32_16x16x32_bf16 v[76:79], a[148:151], a[36:39], v[76:79]\n" \
"ds_read_b64_tr_b16 a[186:187], v239 offset:63488\n" \
"v_mfma_f32_16x16x32_bf16 v[68:71], a[148:151], a[32:35], v[68:71]\n" \
"ds_read_b64_tr_b16 a[188:189], v240 offset:61440\n" \
"v_mfma_f32_16x16x32_bf16 v[96:99], a[152:155], a[32:35], v[96:99]\n" \
"buffer_load_dwordx4 v[172:175], v234, s[64:67], s20 offen offset:1024 sc1\n" \
"v_mfma_f32_16x16x32_bf16 v[104:107], a[152:155], a[36:39], v[104:107]\n" \
"ds_read_b64_tr_b16 a[190:191], v240 offset:63488\n" \
"v_mfma_f32_16x16x32_bf16 v[112:115], a[152:155], a[40:43], v[112:115]\n" \
"v_mfma_f32_16x16x32_bf16 v[120:123], a[152:155], a[44:47], v[120:123]\n" \
"v_mfma_f32_16x16x32_bf16 v[124:127], a[156:159], a[44:47], v[124:127]\n" \
"s_lshl_b32 s60, s92, 7\n" \
"s_add_u32 s60, s60, s84\n" \
"v_mov_b32 v246, s72\n" \
"v_mfma_f32_16x16x32_bf16 v[116:119], a[156:159], a[40:43], v[116:119]\n" \
"v_mov_b32 v247, s73\n" \
"v_mfma_f32_16x16x32_bf16 v[108:111], a[156:159], a[36:39], v[108:111]\n" \
"s_waitcnt vmcnt(24)\n" \
"v_mfma_f32_16x16x32_bf16 v[100:103], a[156:159], a[32:35], v[100:103]\n" \
"s_mov_b64 exec, 1\n" \
"buffer_store_dwordx2 v[246:247], off, s[64:67], s60 sc1\n" \
"s_mov_b64 exec, -1\n" \
"s_waitcnt vmcnt(37) lgkmcnt(0)\n" \
"s_barrier\n" \
"v_mfma_f32_16x16x32_bf16 v[0:3], a[160:163], a[48:51], v[0:3]\n" \
"s_add_u32 m0, s80, 131072\n" \
"ds_read_b64_tr_b16 a[128:129], v224 offset:0\n" \
"v_mfma_f32_16x16x32_bf16 v[8:11], a[160:163], a[52:55], v[8:11]\n" \
"buffer_load_dwordx4 v235, s[68:71], s24 offen lds\n" \
"v_mfma_f32_16x16x32_bf16 v[16:19], a[160:163], a[56:59], v[16:19]\n" \
"v_readlane_b32 s25, v254, 17\n" \
"ds_read_b64_tr_b16 a[130:131], v224 offset:2048\n" \
"v_mfma_f32_16x16x32_bf16 v[24:27], a[160:163], a[60:63], v[24:27]\n" \
"s_add_u32 m0, s80, 135168\n" \
"ds_read_b64_tr_b16 a[132:133], v225 offset:0\n" \
"v_mfma_f32_16x16x32_bf16 v[28:31], a[164:167], a[60:63], v[28:31]\n" \
"buffer_load_dwordx4 v236, s[68:71], s24 offen lds\n" \
"v_mfma_f32_16x16x32_bf16 v[20:23], a[164:167], a[56:59], v[20:23]\n" \
"ds_read_b64_tr_b16 a[134:135], v225 offset:2048\n" \
"v_mfma_f32_16x16x32_bf16 v[12:15], a[164:167], a[52:55], v[12:15]\n" \
"ds_read_b64_tr_b16 a[136:137], v226 offset:0\n" \
"v_mfma_f32_16x16x32_bf16 v[4:7], a[164:167], a[48:51], v[4:7]\n" \
"s_add_u32 m0, s80, 139264\n" \
"ds_read_b64_tr_b16 a[138:139], v226 offset:2048\n" \
"v_mfma_f32_16x16x32_bf16 v[32:35], a[168:171], a[48:51], v[32:35]\n" \
"buffer_load_dwordx4 v235, s[68:71], s25 offen lds\n" \
"v_mfma_f32_16x16x32_bf16 v[40:43], a[168:171], a[52:55], v[40:43]\n" \
"v_readlane_b32 s26, v254, 18\n" \
"v_mfma_f32_16x16x32_bf16 v[48:51], a[168:171], a[56:59], v[48:51]\n" \
"s_add_u32 m0, s80, 143360\n" \
"ds_read_b64_tr_b16 a[140:141], v227 offset:0\n" \
"v_mfma_f32_16x16x32_bf16 v[56:59], a[168:171], a[60:63], v[56:59]\n" \
"buffer_load_dwordx4 v236, s[68:71], s25 offen lds\n" \
"v_mfma_f32_16x16x32_bf16 v[60:63], a[172:175], a[60:63], v[60:63]\n" \
"ds_read_b64_tr_b16 a[142:143], v227 offset:2048\n" \
"v_mfma_f32_16x16x32_bf16 v[52:55], a[172:175], a[56:59], v[52:55]\n" \
"ds_read_b64_tr_b16 a[144:145], v224 offset:4096\n" \
"v_mfma_f32_16x16x32_bf16 v[44:47], a[172:175], a[52:55], v[44:47]\n" \
"s_add_u32 m0, s80, 147456\n" \
"ds_read_b64_tr_b16 a[146:147], v224 offset:6144\n" \
"v_mfma_f32_16x16x32_bf16 v[36:39], a[172:175], a[48:51], v[36:39]\n" \
"buffer_load_dwordx4 v235, s[68:71], s26 offen lds\n" \
"v_mfma_f32_16x16x32_bf16 v[64:67], a[176:179], a[48:51], v[64:67]\n" \
"v_readlane_b32 s27, v254, 19\n" \
"ds_read_b64_tr_b16 a[148:149], v225 offset:4096\n" \
"v_mfma_f32_16x16x32_bf16 v[72:75], a[176:179], a[52:55], v[72:75]\n" \
"v_mfma_f32_16x16x32_bf16 v[80:83], a[176:179], a[56:59], v[80:83]\n" \
"s_add_u32 m0, s80, 151552\n" \
"ds_read_b64_tr_b16 a[150:151], v225 offset:6144\n" \
"v_mfma_f32_16x16x32_bf16 v[88:91], a[176:179], a[60:63], v[88:91]\n" \
"buffer_load_dwordx4 v236, s[68:71], s26 offen lds\n" \
"v_mfma_f32_16x16x32_bf16 v[92:95], a[180:183], a[60:63], v[92:95]\n" \
"ds_read_b64_tr_b16 a[152:153], v226 offset:4096\n" \
"v_mfma_f32_16x16x32_bf16 v[84:87], a[180:183], a[56:59], v[84:87]\n" \
"s_add_u32 m0, s80, 155648\n" \
"ds_read_b64_tr_b16 a[154:155], v226 offset:6144\n" \
"v_mfma_f32_16x16x32_bf16 v[76:79], a[180:183], a[52:55], v[76:79]\n" \
"buffer_load_dwordx4 v235, s[68:71], s27 offen lds\n" \
"v_mfma_f32_16x16x32_bf16 v[68:71], a[180:183], a[48:51], v[68:71]\n" \
"v_readlane_b32 s24, v254, 20\n" \
"ds_read_b64_tr_b16 a[156:157], v227 offset:4096\n" \
"v_mfma_f32_16x16x32_bf16 v[96:99], a[184:187], a[48:51], v[96:99]\n" \
"ds_read_b64_tr_b16 a[158:159], v227 offset:6144\n" \
"v_mfma_f32_16x16x32_bf16 v[104:107], a[184:187], a[52:55], v[104:107]\n" \
"s_add_u32 m0, s80, 159744\n" \
"v_mfma_f32_16x16x32_bf16 v[112:115], a[184:187], a[56:59], v[112:115]\n" \
"buffer_load_dwordx4 v236, s[68:71], s27 offen lds\n" \
"v_mfma_f32_16x16x32_bf16 v[120:123], a[184:187], a[60:63], v[120:123]\n" \
"v_mfma_f32_16x16x32_bf16 v[124:127], a[188:191], a[60:63], v[124:127]\n" \
"v_mfma_f32_16x16x32_bf16 v[116:119], a[188:191], a[56:59], v[116:119]\n" \
"v_mfma_f32_16x16x32_bf16 v[108:111], a[188:191], a[52:55], v[108:111]\n" \
"v_mfma_f32_16x16x32_bf16 v[100:103], a[188:191], a[48:51], v[100:103]\n" \
"s_waitcnt vmcnt(40) lgkmcnt(0)\n" \
"v_mfma_f32_16x16x32_bf16 v[0:3], a[128:131], a[64:67], v[0:3]\n" \
"s_add_u32 m0, s80, 32768\n" \
"ds_read_b64_tr_b16 a[160:161], v224 offset:8192\n" \
"v_mfma_f32_16x16x32_bf16 v[8:11], a[128:131], a[68:71], v[8:11]\n" \
"buffer_load_dwordx4 v235, s[68:71], s24 offen lds\n" \
"v_mfma_f32_16x16x32_bf16 v[16:19], a[128:131], a[72:75], v[16:19]\n" \
"v_readlane_b32 s25, v254, 21\n" \
"ds_read_b64_tr_b16 a[162:163], v224 offset:10240\n" \
"v_mfma_f32_16x16x32_bf16 v[24:27], a[128:131], a[76:79], v[24:27]\n" \
"s_add_u32 m0, s80, 36864\n" \
"ds_read_b64_tr_b16 a[164:165], v225 offset:8192\n" \
"v_mfma_f32_16x16x32_bf16 v[28:31], a[132:135], a[76:79], v[28:31]\n" \
"buffer_load_dwordx4 v236, s[68:71], s24 offen lds\n" \
"v_mfma_f32_16x16x32_bf16 v[20:23], a[132:135], a[72:75], v[20:23]\n" \
"ds_read_b64_tr_b16 a[166:167], v225 offset:10240\n" \
"v_mfma_f32_16x16x32_bf16 v[12:15], a[132:135], a[68:71], v[12:15]\n" \
"ds_read_b64_tr_b16 a[168:169], v226 offset:8192\n" \
"v_mfma_f32_16x16x32_bf16 v[4:7], a[132:135], a[64:67], v[4:7]\n" \
"s_add_u32 m0, s80, 40960\n" \
"ds_read_b64_tr_b16 a[170:171], v226 offset:10240\n" \
"v_mfma_f32_16x16x32_bf16 v[32:35], a[136:139], a[64:67], v[32:35]\n" \
"buffer_load_dwordx4 v235, s[68:71], s25 offen lds\n" \
"v_mfma_f32_16x16x32_bf16 v[40:43], a[136:139], a[68:71], v[40:43]\n" \
"v_readlane_b32 s26, v254, 22\n" \
"v_mfma_f32_16x16x32_bf16 v[48:51], a[136:139], a[72:75], v[48:51]\n" \
"s_add_u32 m0, s80, 45056\n" \
"ds_read_b64_tr_b16 a[172:173], v227 offset:8192\n" \
"v_mfma_f32_16x16x32_bf16 v[56:59], a[136:139], a[76:79], v[56:59]\n" \
"buffer_load_dwordx4 v236, s[68:71], s25 offen lds\n" \
"v_mfma_f32_16x16x32_bf16 v[60:63], a[140:143], a[76:79], v[60:63]\n" \
"ds_read_b64_tr_b16 a[174:175], v227 offset:10240\n" \
"v_mfma_f32_16x16x32_bf16 v[52:55], a[140:143], a[72:75], v[52:55]\n" \
"ds_read_b64_tr_b16 a[176:177], v224 offset:12288\n" \
"v_mfma_f32_16x16x32_bf16 v[44:47], a[140:143], a[68:71], v[44:47]\n" \
"s_add_u32 m0, s80, 49152\n" \
"ds_read_b64_tr_b16 a[178:179], v224 offset:14336\n" \
"v_mfma_f32_16x16x32_bf16 v[36:39], a[140:143], a[64:67], v[36:39]\n" \
"buffer_load_dwordx4 v235, s[68:71], s26 offen lds\n" \
"v_mfma_f32_16x16x32_bf16 v[64:67], a[144:147], a[64:67], v[64:67]\n" \
"v_readlane_b32 s27, v254, 23\n" \
"ds_read_b64_tr_b16 a[180:181], v225 offset:12288\n" \
"v_mfma_f32_16x16x32_bf16 v[72:75], a[144:147], a[68:71], v[72:75]\n" \
"v_mfma_f32_16x16x32_bf16 v[80:83], a[144:147], a[72:75], v[80:83]\n" \
"s_add_u32 m0, s80, 53248\n" \
"ds_read_b64_tr_b16 a[182:183], v225 offset:14336\n" \
"v_mfma_f32_16x16x32_bf16 v[88:91], a[144:147], a[76:79], v[88:91]\n" \
"buffer_load_dwordx4 v236, s[68:71], s26 offen lds\n" \
"v_mfma_f32_16x16x32_bf16 v[92:95], a[148:151], a[76:79], v[92:95]\n" \
"ds_read_b64_tr_b16 a[184:185], v226 offset:12288\n" \
"v_mfma_f32_16x16x32_bf16 v[84:87], a[148:151], a[72:75], v[84:87]\n" \
"s_add_u32 m0, s80, 57344\n" \
"ds_read_b64_tr_b16 a[186:187], v226 offset:14336\n" \
"v_mfma_f32_16x16x32_bf16 v[76:79], a[148:151], a[68:71], v[76:79]\n" \
"s_mov_b64 s[68:69], s[96:97]\n" \
"buffer_load_dwordx4 v235, s[68:71], s27 offen lds\n" \
"v_mfma_f32_16x16x32_bf16 v[68:71], a[148:151], a[64:67], v[68:71]\n" \
"v_readlane_b32 s24, v254, 24\n" \
"ds_read_b64_tr_b16 a[188:189], v227 offset:12288\n" \
"v_mfma_f32_16x16x32_bf16 v[96:99], a[152:155], a[64:67], v[96:99]\n" \
"ds_read_b64_tr_b16 a[190:191], v227 offset:14336\n" \
"v_mfma_f32_16x16x32_bf16 v[104:107], a[152:155], a[68:71], v[104:107]\n" \
"s_add_u32 m0, s80, 61440\n" \
"v_mfma_f32_16x16x32_bf16 v[112:115], a[152:155], a[72:75], v[112:115]\n" \
"v_add_u32 v236, 0x10000, v235\n" \
"buffer_load_dwordx4 v236, s[68:71], s27 offen lds\n" \
"v_mfma_f32_16x16x32_bf16 v[120:123], a[152:155], a[76:79], v[120:123]\n" \
"v_mfma_f32_16x16x32_bf16 v[124:127], a[156:159], a[76:79], v[124:127]\n" \
"v_mfma_f32_16x16x32_bf16 v[116:119], a[156:159], a[72:75], v[116:119]\n" \
"v_mfma_f32_16x16x32_bf16 v[108:111], a[156:159], a[68:71], v[108:111]\n" \
"v_mfma_f32_16x16x32_bf16 v[100:103], a[156:159], a[64:67], v[100:103]\n" \
"s_waitcnt vmcnt(37) lgkmcnt(0)\n" \
"v_mfma_f32_16x16x32_bf16 v[0:3], a[160:163], a[80:83], v[0:3]\n" \
"ds_read_b64_tr_b16 a[128:129], v224 offset:16384\n" \
"v_mfma_f32_16x16x32_bf16 v[8:11], a[160:163], a[84:87], v[8:11]\n" \
"ds_read_b64_tr_b16 a[130:131], v224 offset:18432\n" \
"v_mfma_f32_16x16x32_bf16 v[16:19], a[160:163], a[88:91], v[16:19]\n" \
"v_mfma_f32_16x16x32_bf16 v[24:27], a[160:163], a[92:95], v[24:27]\n" \
"ds_read_b64_tr_b16 a[132:133], v225 offset:16384\n" \
"v_mfma_f32_16x16x32_bf16 v[28:31], a[164:167], a[92:95], v[28:31]\n" \
"v_mfma_f32_16x16x32_bf16 v[20:23], a[164:167], a[88:91], v[20:23]\n" \
"ds_read_b64_tr_b16 a[134:135], v225 offset:18432\n" \
"v_mfma_f32_16x16x32_bf16 v[12:15], a[164:167], a[84:87], v[12:15]\n" \
"ds_read_b64_tr_b16 a[136:137], v226 offset:16384\n" \
"v_mfma_f32_16x16x32_bf16 v[4:7], a[164:167], a[80:83], v[4:7]\n" \
"v_mfma_f32_16x16x32_bf16 v[32:35], a[168:171], a[80:83], v[32:35]\n" \
"ds_read_b64_tr_b16 a[138:139], v226 offset:18432\n" \
"v_mfma_f32_16x16x32_bf16 v[40:43], a[168:171], a[84:87], v[40:43]\n" \
"v_mfma_f32_16x16x32_bf16 v[48:51], a[168:171], a[88:91], v[48:51]\n" \
"ds_read_b64_tr_b16 a[140:141], v227 offset:16384\n" \
"v_mfma_f32_16x16x32_bf16 v[56:59], a[168:171], a[92:95], v[56:59]\n" \
"ds_read_b64_tr_b16 a[142:143], v227 offset:18432\n" \
"v_mfma_f32_16x16x32_bf16 v[60:63], a[172:175], a[92:95], v[60:63]\n" \
"v_mfma_f32_16x16x32_bf16 v[52:55], a[172:175], a[88:91], v[52:55]\n" \
"ds_read_b64_tr_b16 a[144:145], v224 offset:20480\n" \
"v_mfma_f32_16x16x32_bf16 v[44:47], a[172:175], a[84:87], v[44:47]\n" \
"v_mfma_f32_16x16x32_bf16 v[36:39], a[172:175], a[80:83], v[36:39]\n" \
"ds_read_b64_tr_b16 a[146:147], v224 offset:22528\n" \
"v_mfma_f32_16x16x32_bf16 v[64:67], a[176:179], a[80:83], v[64:67]\n" \
"ds_read_b64_tr_b16 a[148:149], v225 offset:20480\n" \
"v_mfma_f32_16x16x32_bf16 v[72:75], a[176:179], a[84:87], v[72:75]\n" \
"v_mfma_f32_16x16x32_bf16 v[80:83], a[176:179], a[88:91], v[80:83]\n" \
"ds_read_b64_tr_b16 a[150:151], v225 offset:22528\n" \
"v_mfma_f32_16x16x32_bf16 v[88:91], a[176:179], a[92:95], v[88:91]\n" \
"v_mfma_f32_16x16x32_bf16 v[92:95], a[180:183], a[92:95], v[92:95]\n" \
"ds_read_b64_tr_b16 a[152:153], v226 offset:20480\n" \
"v_mfma_f32_16x16x32_bf16 v[84:87], a[180:183], a[88:91], v[84:87]\n" \
"ds_read_b64_tr_b16 a[154:155], v226 offset:22528\n" \
"v_mfma_f32_16x16x32_bf16 v[76:79], a[180:183], a[84:87], v[76:79]\n" \
"v_mfma_f32_16x16x32_bf16 v[68:71], a[180:183], a[80:83], v[68:71]\n" \
"ds_read_b64_tr_b16 a[156:157], v227 offset:20480\n" \
"v_mfma_f32_16x16x32_bf16 v[96:99], a[184:187], a[80:83], v[96:99]\n" \
"v_mfma_f32_16x16x32_bf16 v[104:107], a[184:187], a[84:87], v[104:107]\n" \
"ds_read_b64_tr_b16 a[158:159], v227 offset:22528\n" \
"v_mfma_f32_16x16x32_bf16 v[112:115], a[184:187], a[88:91], v[112:115]\n" \
"v_mfma_f32_16x16x32_bf16 v[120:123], a[184:187], a[92:95], v[120:123]\n" \
"v_mfma_f32_16x16x32_bf16 v[124:127], a[188:191], a[92:95], v[124:127]\n" \
"v_mfma_f32_16x16x32_bf16 v[116:119], a[188:191], a[88:91], v[116:119]\n" \
"v_mfma_f32_16x16x32_bf16 v[108:111], a[188:191], a[84:87], v[108:111]\n" \
"v_mfma_f32_16x16x32_bf16 v[100:103], a[188:191], a[80:83], v[100:103]\n" \
"s_waitcnt vmcnt(25) lgkmcnt(0)\n" \
"v_mfma_f32_16x16x32_bf16 v[0:3], a[128:131], a[96:99], v[0:3]\n" \
"ds_read_b64_tr_b16 a[160:161], v224 offset:24576\n" \
"v_mfma_f32_16x16x32_bf16 v[8:11], a[128:131], a[100:103], v[8:11]\n" \
"buffer_load_dwordx2 v[250:251], v253, s[64:67], s84 offen sc1\n" \
"v_mfma_f32_16x16x32_bf16 v[16:19], a[128:131], a[104:107], v[16:19]\n" \
"ds_read_b64_tr_b16 a[162:163], v224 offset:26624\n" \
"v_mfma_f32_16x16x32_bf16 v[24:27], a[128:131], a[108:111], v[24:27]\n" \
"v_mfma_f32_16x16x32_bf16 v[28:31], a[132:135], a[108:111], v[28:31]\n" \
"ds_read_b64_tr_b16 a[164:165], v225 offset:24576\n" \
"v_mfma_f32_16x16x32_bf16 v[20:23], a[132:135], a[104:107], v[20:23]\n" \
"ds_read_b64_tr_b16 a[166:167], v225 offset:26624\n" \
"v_mfma_f32_16x16x32_bf16 v[12:15], a[132:135], a[100:103], v[12:15]\n" \
"v_mfma_f32_16x16x32_bf16 v[4:7], a[132:135], a[96:99], v[4:7]\n" \
"ds_read_b64_tr_b16 a[168:169], v226 offset:24576\n" \
"v_mfma_f32_16x16x32_bf16 v[32:35], a[136:139], a[96:99], v[32:35]\n" \
"v_mfma_f32_16x16x32_bf16 v[40:43], a[136:139], a[100:103], v[40:43]\n" \
"ds_read_b64_tr_b16 a[170:171], v226 offset:26624\n" \
"v_mfma_f32_16x16x32_bf16 v[48:51], a[136:139], a[104:107], v[48:51]\n" \
"ds_read_b64_tr_b16 a[172:173], v227 offset:24576\n" \
"v_mfma_f32_16x16x32_bf16 v[56:59], a[136:139], a[108:111], v[56:59]\n" \
"v_mfma_f32_16x16x32_bf16 v[60:63], a[140:143], a[108:111], v[60:63]\n" \
"ds_read_b64_tr_b16 a[174:175], v227 offset:26624\n" \
"v_mfma_f32_16x16x32_bf16 v[52:55], a[140:143], a[104:107], v[52:55]\n" \
"v_mfma_f32_16x16x32_bf16 v[44:47], a[140:143], a[100:103], v[44:47]\n" \
"ds_read_b64_tr_b16 a[176:177], v224 offset:28672\n" \
"v_mfma_f32_16x16x32_bf16 v[36:39], a[140:143], a[96:99], v[36:39]\n" \
"ds_read_b64_tr_b16 a[178:179], v224 offset:30720\n" \
"v_mfma_f32_16x16x32_bf16 v[64:67], a[144:147], a[96:99], v[64:67]\n" \
"v_mfma_f32_16x16x32_bf16 v[72:75], a[144:147], a[100:103], v[72:75]\n" \
"ds_read_b64_tr_b16 a[180:181], v225 offset:28672\n" \
"v_mfma_f32_16x16x32_bf16 v[80:83], a[144:147], a[104:107], v[80:83]\n" \
"ds_read_b64_tr_b16 a[182:183], v225 offset:30720\n" \
"v_mfma_f32_16x16x32_bf16 v[88:91], a[144:147], a[108:111], v[88:91]\n" \
"v_mfma_f32_16x16x32_bf16 v[92:95], a[148:151], a[108:111], v[92:95]\n" \
"ds_read_b64_tr_b16 a[184:185], v226 offset:28672\n" \
"v_mfma_f32_16x16x32_bf16 v[84:87], a[148:151], a[104:107], v[84:87]\n" \
"v_mfma_f32_16x16x32_bf16 v[76:79], a[148:151], a[100:103], v[76:79]\n" \
"ds_read_b64_tr_b16 a[186:187], v226 offset:30720\n" \
"v_mfma_f32_16x16x32_bf16 v[68:71], a[148:151], a[96:99], v[68:71]\n" \
"ds_read_b64_tr_b16 a[188:189], v227 offset:28672\n" \
"v_mfma_f32_16x16x32_bf16 v[96:99], a[152:155], a[96:99], v[96:99]\n" \
"v_mfma_f32_16x16x32_bf16 v[104:107], a[152:155], a[100:103], v[104:107]\n" \
"ds_read_b64_tr_b16 a[190:191], v227 offset:30720\n" \
"v_mfma_f32_16x16x32_bf16 v[112:115], a[152:155], a[104:107], v[112:115]\n" \
"v_mfma_f32_16x16x32_bf16 v[120:123], a[152:155], a[108:111], v[120:123]\n" \
"v_mfma_f32_16x16x32_bf16 v[124:127], a[156:159], a[108:111], v[124:127]\n" \
"v_mfma_f32_16x16x32_bf16 v[116:119], a[156:159], a[104:107], v[116:119]\n" \
"v_mfma_f32_16x16x32_bf16 v[108:111], a[156:159], a[100:103], v[108:111]\n" \
"v_mfma_f32_16x16x32_bf16 v[100:103], a[156:159], a[96:99], v[100:103]\n" \
"s_waitcnt vmcnt(22) lgkmcnt(0)\n" \
"s_barrier\n" \
"v_mfma_f32_16x16x32_bf16 v[0:3], a[160:163], a[112:115], v[0:3]\n" \
"s_add_u32 m0, s80, 65536\n" \
"ds_read_b64_tr_b16 a[128:129], v224 offset:32768\n" \
"v_mfma_f32_16x16x32_bf16 v[8:11], a[160:163], a[116:119], v[8:11]\n" \
"buffer_load_dwordx4 v235, s[68:71], s24 offen lds\n" \
"v_mfma_f32_16x16x32_bf16 v[16:19], a[160:163], a[120:123], v[16:19]\n" \
"v_readlane_b32 s25, v254, 25\n" \
"ds_read_b64_tr_b16 a[130:131], v224 offset:34816\n" \
"v_mfma_f32_16x16x32_bf16 v[24:27], a[160:163], a[124:127], v[24:27]\n" \
"s_add_u32 m0, s80, 69632\n" \
"ds_read_b64_tr_b16 a[132:133], v225 offset:32768\n" \
"v_mfma_f32_16x16x32_bf16 v[28:31], a[164:167], a[124:127], v[28:31]\n" \
"buffer_load_dwordx4 v236, s[68:71], s24 offen lds\n" \
"v_mfma_f32_16x16x32_bf16 v[20:23], a[164:167], a[120:123], v[20:23]\n" \
"ds_read_b64_tr_b16 a[134:135], v225 offset:34816\n" \
"v_mfma_f32_16x16x32_bf16 v[12:15], a[164:167], a[116:119], v[12:15]\n" \
"s_add_u32 m0, s80, 73728\n" \
"ds_read_b64_tr_b16 a[136:137], v226 offset:32768\n" \
"v_mfma_f32_16x16x32_bf16 v[4:7], a[164:167], a[112:115], v[4:7]\n" \
"buffer_load_dwordx4 v235, s[68:71], s25 offen lds\n" \
"v_mfma_f32_16x16x32_bf16 v[32:35], a[168:171], a[112:115], v[32:35]\n" \
"v_readlane_b32 s26, v254, 26\n" \
"ds_read_b64_tr_b16 a[138:139], v226 offset:34816\n" \
"v_mfma_f32_16x16x32_bf16 v[40:43], a[168:171], a[116:119], v[40:43]\n" \
"v_mfma_f32_16x16x32_bf16 v[48:51], a[168:171], a[120:123], v[48:51]\n" \
"s_add_u32 m0, s80, 77824\n" \
"ds_read_b64_tr_b16 a[140:141], v227 offset:32768\n" \
"v_mfma_f32_16x16x32_bf16 v[56:59], a[168:171], a[124:127], v[56:59]\n" \
"buffer_load_dwordx4 v236, s[68:71], s25 offen lds\n" \
"v_mfma_f32_16x16x32_bf16 v[60:63], a[172:175], a[124:127], v[60:63]\n" \
"ds_read_b64_tr_b16 a[142:143], v227 offset:34816\n" \
"v_mfma_f32_16x16x32_bf16 v[52:55], a[172:175], a[120:123], v[52:55]\n" \
"s_add_u32 m0, s80, 81920\n" \
"ds_read_b64_tr_b16 a[144:145], v224 offset:36864\n" \
"v_mfma_f32_16x16x32_bf16 v[44:47], a[172:175], a[116:119], v[44:47]\n" \
"buffer_load_dwordx4 v235, s[68:71], s26 offen lds\n" \
"v_mfma_f32_16x16x32_bf16 v[36:39], a[172:175], a[112:115], v[36:39]\n" \
"v_readlane_b32 s27, v254, 27\n" \
"Lpadq1_%=:\n" \
"s_cbranch_vccnz Lpadfq_%=\n" \
"ds_read_b64_tr_b16 a[146:147], v224 offset:38912\n" \
"v_mfma_f32_16x16x32_bf16 v[64:67], a[176:179], a[112:115], v[64:67]\n" \
"s_add_u32 m0, s80, 86016\n" \
"ds_read_b64_tr_b16 a[148:149], v225 offset:36864\n" \
"v_mfma_f32_16x16x32_bf16 v[72:75], a[176:179], a[116:119], v[72:75]\n" \
"buffer_load_dwordx4 v236, s[68:71], s26 offen lds\n" \
"v_mfma_f32_16x16x32_bf16 v[80:83], a[176:179], a[120:123], v[80:83]\n" \
"v_mfma_f32_16x16x32_bf16 v[88:91], a[176:179], a[124:127], v[88:91]\n" \
"ds_read_b64_tr_b16 a[150:151], v225 offset:38912\n" \
"v_mfma_f32_16x16x32_bf16 v[92:95], a[180:183], a[124:127], v[92:95]\n" \
"s_add_u32 m0, s80, 90112\n" \
"ds_read_b64_tr_b16 a[152:153], v226 offset:36864\n" \
"v_mfma_f32_16x16x32_bf16 v[84:87], a[180:183], a[120:123], v[84:87]\n" \
"buffer_load_dwordx4 v235, s[68:71], s27 offen lds\n" \
"v_mfma_f32_16x16x32_bf16 v[76:79], a[180:183], a[116:119], v[76:79]\n" \
"v_readlane_b32 s24, v254, 28\n" \
"ds_read_b64_tr_b16 a[154:155], v226 offset:38912\n" \
"v_mfma_f32_16x16x32_bf16 v[68:71], a[180:183], a[112:115], v[68:71]\n" \
"s_add_u32 m0, s80, 94208\n" \
"ds_read_b64_tr_b16 a[156:157], v227 offset:36864\n" \
"v_mfma_f32_16x16x32_bf16 v[96:99], a[184:187], a[112:115], v[96:99]\n" \
"buffer_load_dwordx4 v236, s[68:71], s27 offen lds\n" \
"v_mfma_f32_16x16x32_bf16 v[104:107], a[184:187], a[116:119], v[104:107]\n" \
"ds_read_b64_tr_b16 a[158:159], v227 offset:38912\n" \
"v_mfma_f32_16x16x32_bf16 v[112:115], a[184:187], a[120:123], v[112:115]\n" \
"v_mfma_f32_16x16x32_bf16 v[120:123], a[184:187], a[124:127], v[120:123]\n" \
"v_mfma_f32_16x16x32_bf16 v[124:127], a[188:191], a[124:127], v[124:127]\n" \
"s_waitcnt vmcnt(8)\n" \
"v_mfma_f32_16x16x32_bf16 v[116:119], a[188:191], a[120:123], v[116:119]\n" \
"v_cmp_ne_u64 vcc, s[72:73], v[250:251]\n" \
"v_mfma_f32_16x16x32_bf16 v[108:111], a[188:191], a[116:119], v[108:111]\n" \
"s_cbranch_vccnz Lsl10_%=\n" \
"Lrt10_%=:\n" \
"v_mfma_f32_16x16x32_bf16 v[100:103], a[188:191], a[112:115], v[100:103]\n" \
"v_accvgpr_read_b32 v253, a239\n" \
"s_waitcnt vmcnt(26) lgkmcnt(0)\n" \
"v_mfma_f32_16x16x32_bf16 v[0:3], a[128:131], v[160:163], v[0:3]\n" \
"ds_read_b64_tr_b16 a[160:161], v224 offset:40960\n" \
"v_mfma_f32_16x16x32_bf16 v[8:11], a[128:131], v[164:167], v[8:11]\n" \
"buffer_load_dwordx4 v[176:179], v233, s[64:67], s21 offen offset:0 sc1\n" \
"v_mfma_f32_16x16x32_bf16 v[16:19], a[128:131], v[168:171], v[16:19]\n" \
"v_readlane_b32 s22, v241, 10\n" \
"ds_read_b64_tr_b16 a[162:163], v224 offset:43008\n" \
"v_mfma_f32_16x16x32_bf16 v[24:27], a[128:131], v[172:175], v[24:27]\n" \
"ds_read_b64_tr_b16 a[164:165], v225 offset:40960\n" \
"v_mfma_f32_16x16x32_bf16 v[28:31], a[132:135], v[172:175], v[28:31]\n" \
"buffer_load_dwordx4 v[180:183], v233, s[64:67], s21 offen offset:1024 sc1\n" \
"v_mfma_f32_16x16x32_bf16 v[20:23], a[132:135], v[168:171], v[20:23]\n" \
"ds_read_b64_tr_b16 a[166:167], v225 offset:43008\n" \
"v_mfma_f32_16x16x32_bf16 v[12:15], a[132:135], v[164:167], v[12:15]\n" \
"ds_read_b64_tr_b16 a[168:169], v226 offset:40960\n" \
"v_mfma_f32_16x16x32_bf16 v[4:7], a[132:135], v[160:163], v[4:7]\n" \
"ds_read_b64_tr_b16 a[170:171], v226 offset:43008\n" \
"v_mfma_f32_16x16x32_bf16 v[32:35], a[136:139], v[160:163], v[32:35]\n" \
"buffer_load_dwordx4 v[184:187], v234, s[64:67], s21 offen offset:0 sc1\n" \
"v_mfma_f32_16x16x32_bf16 v[40:43], a[136:139], v[164:167], v[40:43]\n" \
"v_mfma_f32_16x16x32_bf16 v[48:51], a[136:139], v[168:171], v[48:51]\n" \
"ds_read_b64_tr_b16 a[172:173], v227 offset:40960\n" \
"v_mfma_f32_16x16x32_bf16 v[56:59], a[136:139], v[172:175], v[56:59]\n" \
"buffer_load_dwordx4 v[188:191], v234, s[64:67], s21 offen offset:1024 sc1\n" \
"v_mfma_f32_16x16x32_bf16 v[60:63], a[140:143], v[172:175], v[60:63]\n" \
"ds_read_b64_tr_b16 a[174:175], v227 offset:43008\n" \
"v_mfma_f32_16x16x32_bf16 v[52:55], a[140:143], v[168:171], v[52:55]\n" \
"ds_read_b64_tr_b16 a[176:177], v224 offset:45056\n" \
"v_mfma_f32_16x16x32_bf16 v[44:47], a[140:143], v[164:167], v[44:47]\n" \
"ds_read_b64_tr_b16 a[178:179], v224 offset:47104\n" \
"v_mfma_f32_16x16x32_bf16 v[36:39], a[140:143], v[160:163], v[36:39]\n" \
"buffer_load_dwordx4 v[192:195], v233, s[64:67], s22 offen offset:0 sc1\n" \
"v_mfma_f32_16x16x32_bf16 v[64:67], a[144:147], v[160:163], v[64:67]\n" \
"v_readlane_b32 s23, v241, 11\n" \
"ds_read_b64_tr_b16 a[180:181], v225 offset:45056\n" \
"v_mfma_f32_16x16x32_bf16 v[72:75], a[144:147], v[164:167], v[72:75]\n" \
"v_mfma_f32_16x16x32_bf16 v[80:83], a[144:147], v[168:171], v[80:83]\n" \
"ds_read_b64_tr_b16 a[182:183], v225 offset:47104\n" \
"v_mfma_f32_16x16x32_bf16 v[88:91], a[144:147], v[172:175], v[88:91]\n" \
"buffer_load_dwordx4 v[196:199], v233, s[64:67], s22 offen offset:1024 sc1\n" \
"v_mfma_f32_16x16x32_bf16 v[92:95], a[148:151], v[172:175], v[92:95]\n" \
"ds_read_b64_tr_b16 a[184:185], v226 offset:45056\n" \
"v_mfma_f32_16x16x32_bf16 v[84:87], a[148:151], v[168:171], v[84:87]\n" \
"ds_read_b64_tr_b16 a[186:187], v226 offset:47104\n" \
"v_mfma_f32_16x16x32_bf16 v[76:79], a[148:151], v[164:167], v[76:79]\n" \
"buffer_load_dwordx4 v[200:203], v234, s[64:67], s22 offen offset:0 sc1\n" \
"v_mfma_f32_16x16x32_bf16 v[68:71], a[148:151], v[160:163], v[68:71]\n" \
"ds_read_b64_tr_b16 a[188:189], v227 offset:45056\n" \
"v_mfma_f32_16x16x32_bf16 v[96:99], a[152:155], v[160:163], v[96:99]\n" \
"ds_read_b64_tr_b16 a[190:191], v227 offset:47104\n" \
"v_mfma_f32_16x16x32_bf16 v[104:107], a[152:155], v[164:167], v[104:107]\n" \
"v_mfma_f32_16x16x32_bf16 v[112:115], a[152:155], v[168:171], v[112:115]\n" \
"buffer_load_dwordx4 v[204:207], v234, s[64:67], s22 offen offset:1024 sc1\n" \
"v_mfma_f32_16x16x32_bf16 v[120:123], a[152:155], v[172:175], v[120:123]\n" \
"v_mfma_f32_16x16x32_bf16 v[124:127], a[156:159], v[172:175], v[124:127]\n" \
"v_mfma_f32_16x16x32_bf16 v[116:119], a[156:159], v[168:171], v[116:119]\n" \
"v_mfma_f32_16x16x32_bf16 v[108:111], a[156:159], v[164:167], v[108:111]\n" \
"v_mfma_f32_16x16x32_bf16 v[100:103], a[156:159], v[160:163], v[100:103]\n" \
"s_waitcnt vmcnt(4) lgkmcnt(0)\n" \
"v_mfma_f32_16x16x32_bf16 v[0:3], a[160:163], v[176:179], v[0:3]\n" \
"ds_read_b64_tr_b16 a[128:129], v224 offset:49152\n" \
"v_mfma_f32_16x16x32_bf16 v[8:11], a[160:163], v[180:183], v[8:11]\n" \
"buffer_load_dwordx4 v[208:211], v233, s[64:67], s23 offen offset:0 sc1\n" \
"v_mfma_f32_16x16x32_bf16 v[16:19], a[160:163], v[184:187], v[16:19]\n" \
"v_readlane_b32 s20, v241, 12\n" \
"ds_read_b64_tr_b16 a[130:131], v224 offset:51200\n" \
"v_mfma_f32_16x16x32_bf16 v[24:27], a[160:163], v[188:191], v[24:27]\n" \
"ds_read_b64_tr_b16 a[132:133], v225 offset:49152\n" \
"v_mfma_f32_16x16x32_bf16 v[28:31], a[164:167], v[188:191], v[28:31]\n" \
"buffer_load_dwordx4 v[212:215], v233, s[64:67], s23 offen offset:1024 sc1\n" \
"v_mfma_f32_16x16x32_bf16 v[20:23], a[164:167], v[184:187], v[20:23]\n" \
"ds_read_b64_tr_b16 a[134:135], v225 offset:51200\n" \
"v_mfma_f32_16x16x32_bf16 v[12:15], a[164:167], v[180:183], v[12:15]\n" \
"ds_read_b64_tr_b16 a[136:137], v226 offset:49152\n" \
"v_mfma_f32_16x16x32_bf16 v[4:7], a[164:167], v[176:179], v[4:7]\n" \
"ds_read_b64_tr_b16 a[138:139], v226 offset:51200\n" \
"v_mfma_f32_16x16x32_bf16 v[32:35], a[168:171], v[176:179], v[32:35]\n" \
"buffer_load_dwordx4 v[216:219], v234, s[64:67], s23 offen offset:0 sc1\n" \
"v_mfma_f32_16x16x32_bf16 v[40:43], a[168:171], v[180:183], v[40:43]\n" \
"v_mfma_f32_16x16x32_bf16 v[48:51], a[168:171], v[184:187], v[48:51]\n" \
"ds_read_b64_tr_b16 a[140:141], v227 offset:49152\n" \
"v_mfma_f32_16x16x32_bf16 v[56:59], a[168:171], v[188:191], v[56:59]\n" \
"buffer_load_dwordx4 v[220:223], v234, s[64:67], s23 offen offset:1024 sc1\n" \
"v_mfma_f32_16x16x32_bf16 v[60:63], a[172:175], v[188:191], v[60:63]\n" \
"ds_read_b64_tr_b16 a[142:143], v227 offset:51200\n" \
"v_mfma_f32_16x16x32_bf16 v[52:55], a[172:175], v[184:187], v[52:55]\n" \
"ds_read_b64_tr_b16 a[144:145], v224 offset:53248\n" \
"v_mfma_f32_16x16x32_bf16 v[44:47], a[172:175], v[180:183], v[44:47]\n" \
"ds_read_b64_tr_b16 a[146:147], v224 offset:55296\n" \
"v_mfma_f32_16x16x32_bf16 v[36:39], a[172:175], v[176:179], v[36:39]\n" \
"buffer_load_dwordx4 a[0:3], v233, s[64:67], s20 offen offset:0 sc1\n" \
"v_mfma_f32_16x16x32_bf16 v[64:67], a[176:179], v[176:179], v[64:67]\n" \
"v_readlane_b32 s21, v241, 13\n" \
"ds_read_b64_tr_b16 a[148:149], v225 offset:53248\n" \
"v_mfma_f32_16x16x32_bf16 v[72:75], a[176:179], v[180:183], v[72:75]\n" \
"v_mfma_f32_16x16x32_bf16 v[80:83], a[176:179], v[184:187], v[80:83]\n" \
"ds_read_b64_tr_b16 a[150:151], v225 offset:55296\n" \
"v_mfma_f32_16x16x32_bf16 v[88:91], a[176:179], v[188:191], v[88:91]\n" \
"buffer_load_dwordx4 a[4:7], v233, s[64:67], s20 offen offset:1024 sc1\n" \
"v_mfma_f32_16x16x32_bf16 v[92:95], a[180:183], v[188:191], v[92:95]\n" \
"ds_read_b64_tr_b16 a[152:153], v226 offset:53248\n" \
"v_mfma_f32_16x16x32_bf16 v[84:87], a[180:183], v[184:187], v[84:87]\n" \
"ds_read_b64_tr_b16 a[154:155], v226 offset:55296\n" \
"v_mfma_f32_16x16x32_bf16 v[76:79], a[180:183], v[180:183], v[76:79]\n" \
"buffer_load_dwordx4 a[8:11], v234, s[64:67], s20 offen offset:0 sc1\n" \
"v_mfma_f32_16x16x32_bf16 v[68:71], a[180:183], v[176:179], v[68:71]\n" \
"ds_read_b64_tr_b16 a[156:157], v227 offset:53248\n" \
"v_mfma_f32_16x16x32_bf16 v[96:99], a[184:187], v[176:179], v[96:99]\n" \
"ds_read_b64_tr_b16 a[158:159], v227 offset:55296\n" \
"v_mfma_f32_16x16x32_bf16 v[104:107], a[184:187], v[180:183], v[104:107]\n" \
"v_mfma_f32_16x16x32_bf16 v[112:115], a[184:187], v[184:187], v[112:115]\n" \
"buffer_load_dwordx4 a[12:15], v234, s[64:67], s20 offen offset:1024 sc1\n" \
"v_mfma_f32_16x16x32_bf16 v[120:123], a[184:187], v[188:191], v[120:123]\n" \
"v_mfma_f32_16x16x32_bf16 v[124:127], a[188:191], v[188:191], v[124:127]\n" \
"v_mfma_f32_16x16x32_bf16 v[116:119], a[188:191], v[184:187], v[116:119]\n" \
"v_mfma_f32_16x16x32_bf16 v[108:111], a[188:191], v[180:183], v[108:111]\n" \
"v_mfma_f32_16x16x32_bf16 v[100:103], a[188:191], v[176:179], v[100:103]\n" \
"s_waitcnt vmcnt(8) lgkmcnt(0)\n" \
"v_mfma_f32_16x16x32_bf16 v[0:3], a[128:131], v[192:195], v[0:3]\n" \
"ds_read_b64_tr_b16 a[160:161], v224 offset:57344\n" \
"v_mfma_f32_16x16x32_bf16 v[8:11], a[128:131], v[196:199], v[8:11]\n" \
"buffer_load_dwordx4 a[16:19], v233, s[64:67], s21 offen offset:0 sc1\n" \
"v_mfma_f32_16x16x32_bf16 v[16:19], a[128:131], v[200:203], v[16:19]\n" \
"v_readlane_b32 s22, v241, 14\n" \
"ds_read_b64_tr_b16 a[162:163], v224 offset:59392\n" \
"v_mfma_f32_16x16x32_bf16 v[24:27], a[128:131], v[204:207], v[24:27]\n" \
"ds_read_b64_tr_b16 a[164:165], v225 offset:57344\n" \
"v_mfma_f32_16x16x32_bf16 v[28:31], a[132:135], v[204:207], v[28:31]\n" \
"buffer_load_dwordx4 a[20:23], v233, s[64:67], s21 offen offset:1024 sc1\n" \
"v_mfma_f32_16x16x32_bf16 v[20:23], a[132:135], v[200:203], v[20:23]\n" \
"ds_read_b64_tr_b16 a[166:167], v225 offset:59392\n" \
"v_mfma_f32_16x16x32_bf16 v[12:15], a[132:135], v[196:199], v[12:15]\n" \
"ds_read_b64_tr_b16 a[168:169], v226 offset:57344\n" \
"v_mfma_f32_16x16x32_bf16 v[4:7], a[132:135], v[192:195], v[4:7]\n" \
"ds_read_b64_tr_b16 a[170:171], v226 offset:59392\n" \
"v_mfma_f32_16x16x32_bf16 v[32:35], a[136:139], v[192:195], v[32:35]\n" \
"buffer_load_dwordx4 a[24:27], v234, s[64:67], s21 offen offset:0 sc1\n" \
"v_mfma_f32_16x16x32_bf16 v[40:43], a[136:139], v[196:199], v[40:43]\n" \
"v_mfma_f32_16x16x32_bf16 v[48:51], a[136:139], v[200:203], v[48:51]\n" \
"ds_read_b64_tr_b16 a[172:173], v227 offset:57344\n" \
"v_mfma_f32_16x16x32_bf16 v[56:59], a[136:139], v[204:207], v[56:59]\n" \
"buffer_load_dwordx4 a[28:31], v234, s[64:67], s21 offen offset:1024 sc1\n" \
"v_mfma_f32_16x16x32_bf16 v[60:63], a[140:143], v[204:207], v[60:63]\n" \
"ds_read_b64_tr_b16 a[174:175], v227 offset:59392\n" \
"v_mfma_f32_16x16x32_bf16 v[52:55], a[140:143], v[200:203], v[52:55]\n" \
"ds_read_b64_tr_b16 a[176:177], v224 offset:61440\n" \
"v_mfma_f32_16x16x32_bf16 v[44:47], a[140:143], v[196:199], v[44:47]\n" \
"ds_read_b64_tr_b16 a[178:179], v224 offset:63488\n" \
"v_mfma_f32_16x16x32_bf16 v[36:39], a[140:143], v[192:195], v[36:39]\n" \
"buffer_load_dwordx4 a[32:35], v233, s[64:67], s22 offen offset:0 sc1\n" \
"v_mfma_f32_16x16x32_bf16 v[64:67], a[144:147], v[192:195], v[64:67]\n" \
"v_readlane_b32 s23, v241, 15\n" \
"ds_read_b64_tr_b16 a[180:181], v225 offset:61440\n" \
"v_mfma_f32_16x16x32_bf16 v[72:75], a[144:147], v[196:199], v[72:75]\n" \
"v_mfma_f32_16x16x32_bf16 v[80:83], a[144:147], v[200:203], v[80:83]\n" \
"ds_read_b64_tr_b16 a[182:183], v225 offset:63488\n" \
"v_mfma_f32_16x16x32_bf16 v[88:91], a[144:147], v[204:207], v[88:91]\n" \
"buffer_load_dwordx4 a[36:39], v233, s[64:67], s22 offen offset:1024 sc1\n" \
"v_mfma_f32_16x16x32_bf16 v[92:95], a[148:151], v[204:207], v[92:95]\n" \
"ds_read_b64_tr_b16 a[184:185], v226 offset:61440\n" \
"v_mfma_f32_16x16x32_bf16 v[84:87], a[148:151], v[200:203], v[84:87]\n" \
"ds_read_b64_tr_b16 a[186:187], v226 offset:63488\n" \
"v_mfma_f32_16x16x32_bf16 v[76:79], a[148:151], v[196:199], v[76:79]\n" \
"buffer_load_dwordx4 a[40:43], v234, s[64:67], s22 offen offset:0 sc1\n" \
"v_mfma_f32_16x16x32_bf16 v[68:71], a[148:151], v[192:195], v[68:71]\n" \
"ds_read_b64_tr_b16 a[188:189], v227 offset:61440\n" \
"v_mfma_f32_16x16x32_bf16 v[96:99], a[152:155], v[192:195], v[96:99]\n" \
"ds_read_b64_tr_b16 a[190:191], v227 offset:63488\n" \
"v_mfma_f32_16x16x32_bf16 v[104:107], a[152:155], v[196:199], v[104:107]\n" \
"v_mfma_f32_16x16x32_bf16 v[112:115], a[152:155], v[200:203], v[112:115]\n" \
"buffer_load_dwordx4 a[44:47], v234, s[64:67], s22 offen offset:1024 sc1\n" \
"v_mfma_f32_16x16x32_bf16 v[120:123], a[152:155], v[204:207], v[120:123]\n" \
"v_mfma_f32_16x16x32_bf16 v[124:127], a[156:159], v[204:207], v[124:127]\n" \
"v_mfma_f32_16x16x32_bf16 v[116:119], a[156:159], v[200:203], v[116:119]\n" \
"v_mfma_f32_16x16x32_bf16 v[108:111], a[156:159], v[196:199], v[108:111]\n" \
"v_mfma_f32_16x16x32_bf16 v[100:103], a[156:159], v[192:195], v[100:103]\n" \
"s_waitcnt vmcnt(12) lgkmcnt(0)\n" \
"s_barrier\n" \
"v_mfma_f32_16x16x32_bf16 v[0:3], a[160:163], v[208:211], v[0:3]\n" \
"ds_read_b64_tr_b16 a[128:129], v237 offset:0\n" \
"v_mfma_f32_16x16x32_bf16 v[8:11], a[160:163], v[212:215], v[8:11]\n" \
"buffer_load_dwordx2 v[250:251], v253, s[64:67], s84 offen sc1\n" \
"v_mfma_f32_16x16x32_bf16 v[16:19], a[160:163], v[216:219], v[16:19]\n" \
"ds_read_b64_tr_b16 a[130:131], v237 offset:2048\n" \
"v_mfma_f32_16x16x32_bf16 v[24:27], a[160:163], v[220:223], v[24:27]\n" \
"ds_read_b64_tr_b16 a[132:133], v238 offset:0\n" \
"v_mfma_f32_16x16x32_bf16 v[28:31], a[164:167], v[220:223], v[28:31]\n" \
"buffer_load_dwordx4 a[48:51], v233, s[64:67], s23 offen offset:0 sc1\n" \
"v_mfma_f32_16x16x32_bf16 v[20:23], a[164:167], v[216:219], v[20:23]\n" \
"v_readlane_b32 s20, v241, 16\n" \
"ds_read_b64_tr_b16 a[134:135], v238 offset:2048\n" \
"v_mfma_f32_16x16x32_bf16 v[12:15], a[164:167], v[212:215], v[12:15]\n" \
"ds_read_b64_tr_b16 a[136:137], v239 offset:0\n" \
"v_mfma_f32_16x16x32_bf16 v[4:7], a[164:167], v[208:211], v[4:7]\n" \
"ds_read_b64_tr_b16 a[138:139], v239 offset:2048\n" \
"v_mfma_f32_16x16x32_bf16 v[32:35], a[168:171], v[208:211], v[32:35]\n" \
"buffer_load_dwordx4 a[52:55], v233, s[64:67], s23 offen offset:1024 sc1\n" \
"v_mfma_f32_16x16x32_bf16 v[40:43], a[168:171], v[212:215], v[40:43]\n" \
"v_mfma_f32_16x16x32_bf16 v[48:51], a[168:171], v[216:219], v[48:51]\n" \
"ds_read_b64_tr_b16 a[140:141], v240 offset:0\n" \
"v_mfma_f32_16x16x32_bf16 v[56:59], a[168:171], v[220:223], v[56:59]\n" \
"buffer_load_dwordx4 a[56:59], v234, s[64:67], s23 offen offset:0 sc1\n" \
"v_mfma_f32_16x16x32_bf16 v[60:63], a[172:175], v[220:223], v[60:63]\n" \
"ds_read_b64_tr_b16 a[142:143], v240 offset:2048\n" \
"v_mfma_f32_16x16x32_bf16 v[52:55], a[172:175], v[216:219], v[52:55]\n" \
"ds_read_b64_tr_b16 a[144:145], v237 offset:4096\n" \
"v_mfma_f32_16x16x32_bf16 v[44:47], a[172:175], v[212:215], v[44:47]\n" \
"ds_read_b64_tr_b16 a[146:147], v237 offset:6144\n" \
"v_mfma_f32_16x16x32_bf16 v[36:39], a[172:175], v[208:211], v[36:39]\n" \
"buffer_load_dwordx4 a[60:63], v234, s[64:67], s23 offen offset:1024 sc1\n" \
"v_mfma_f32_16x16x32_bf16 v[64:67], a[176:179], v[208:211], v[64:67]\n" \
"ds_read_b64_tr_b16 a[148:149], v238 offset:4096\n" \
"v_mfma_f32_16x16x32_bf16 v[72:75], a[176:179], v[212:215], v[72:75]\n" \
"v_mfma_f32_16x16x32_bf16 v[80:83], a[176:179], v[216:219], v[80:83]\n" \
"ds_read_b64_tr_b16 a[150:151], v238 offset:6144\n" \
"v_mfma_f32_16x16x32_bf16 v[88:91], a[176:179], v[220:223], v[88:91]\n" \
"buffer_load_dwordx4 a[64:67], v233, s[64:67], s20 offen offset:0 sc1\n" \
"v_mfma_f32_16x16x32_bf16 v[92:95], a[180:183], v[220:223], v[92:95]\n" \
"v_readlane_b32 s21, v241, 17\n" \
"ds_read_b64_tr_b16 a[152:153], v239 offset:4096\n" \
"v_mfma_f32_16x16x32_bf16 v[84:87], a[180:183], v[216:219], v[84:87]\n" \
"ds_read_b64_tr_b16 a[154:155], v239 offset:6144\n" \
"v_mfma_f32_16x16x32_bf16 v[76:79], a[180:183], v[212:215], v[76:79]\n" \
"buffer_load_dwordx4 a[68:71], v233, s[64:67], s20 offen offset:1024 sc1\n" \
"v_mfma_f32_16x16x32_bf16 v[68:71], a[180:183], v[208:211], v[68:71]\n" \
"ds_read_b64_tr_b16 a[156:157], v240 offset:4096\n" \
"v_mfma_f32_16x16x32_bf16 v[96:99], a[184:187], v[208:211], v[96:99]\n" \
"ds_read_b64_tr_b16 a[158:159], v240 offset:6144\n" \
"v_mfma_f32_16x16x32_bf16 v[104:107], a[184:187], v[212:215], v[104:107]\n" \
"v_mfma_f32_16x16x32_bf16 v[112:115], a[184:187], v[216:219], v[112:115]\n" \
"buffer_load_dwordx4 a[72:75], v234, s[64:67], s20 offen offset:0 sc1\n" \
"v_mfma_f32_16x16x32_bf16 v[120:123], a[184:187], v[220:223], v[120:123]\n" \
"v_mfma_f32_16x16x32_bf16 v[124:127], a[188:191], v[220:223], v[124:127]\n" \
"v_mfma_f32_16x16x32_bf16 v[116:119], a[188:191], v[216:219], v[116:119]\n" \
"v_mfma_f32_16x16x32_bf16 v[108:111], a[188:191], v[212:215], v[108:111]\n" \
"v_mfma_f32_16x16x32_bf16 v[100:103], a[188:191], v[208:211], v[100:103]\n" \
"s_waitcnt vmcnt(16) lgkmcnt(0)\n" \
"v_mfma_f32_16x16x32_bf16 v[0:3], a[128:131], a[0:3], v[0:3]\n" \
"ds_read_b64_tr_b16 a[160:161], v237 offset:8192\n" \
"v_mfma_f32_16x16x32_bf16 v[8:11], a[128:131], a[4:7], v[8:11]\n" \
"buffer_load_dwordx4 a[76:79], v234, s[64:67], s20 offen offset:1024 sc1\n" \
"v_mfma_f32_16x16x32_bf16 v[16:19], a[128:131], a[8:11], v[16:19]\n" \
"ds_read_b64_tr_b16 a[162:163], v237 offset:10240\n" \
"v_mfma_f32_16x16x32_bf16 v[24:27], a[128:131], a[12:15], v[24:27]\n" \
"ds_read_b64_tr_b16 a[164:165], v238 offset:8192\n" \
"v_mfma_f32_16x16x32_bf16 v[28:31], a[132:135], a[12:15], v[28:31]\n" \
"buffer_load_dwordx4 a[80:83], v233, s[64:67], s21 offen offset:0 sc1\n" \
"v_mfma_f32_16x16x32_bf16 v[20:23], a[132:135], a[8:11], v[20:23]\n" \
"v_readlane_b32 s22, v241, 18\n" \
"ds_read_b64_tr_b16 a[166:167], v238 offset:10240\n" \
"v_mfma_f32_16x16x32_bf16 v[12:15], a[132:135], a[4:7], v[12:15]\n" \
"ds_read_b64_tr_b16 a[168:169], v239 offset:8192\n" \
"v_mfma_f32_16x16x32_bf16 v[4:7], a[132:135], a[0:3], v[4:7]\n" \
"buffer_load_dwordx4 a[84:87], v233, s[64:67], s21 offen offset:1024 sc1\n" \
"v_mfma_f32_16x16x32_bf16 v[32:35], a[136:139], a[0:3], v[32:35]\n" \
"ds_read_b64_tr_b16 a[170:171], v239 offset:10240\n" \
"v_mfma_f32_16x16x32_bf16 v[40:43], a[136:139], a[4:7], v[40:43]\n" \
"v_mfma_f32_16x16x32_bf16 v[48:51], a[136:139], a[8:11], v[48:51]\n" \
"buffer_load_dwordx4 a[88:91], v234, s[64:67], s21 offen offset:0 sc1\n" \
"v_mfma_f32_16x16x32_bf16 v[56:59], a[136:139], a[12:15], v[56:59]\n" \
"ds_read_b64_tr_b16 a[172:173], v240 offset:8192\n" \
"v_mfma_f32_16x16x32_bf16 v[60:63], a[140:143], a[12:15], v[60:63]\n" \
"ds_read_b64_tr_b16 a[174:175], v240 offset:10240\n" \
"v_mfma_f32_16x16x32_bf16 v[52:55], a[140:143], a[8:11], v[52:55]\n" \
"ds_read_b64_tr_b16 a[176:177], v237 offset:12288\n" \
"v_mfma_f32_16x16x32_bf16 v[44:47], a[140:143], a[4:7], v[44:47]\n" \
"buffer_load_dwordx4 a[92:95], v234, s[64:67], s21 offen offset:1024 sc1\n" \
"v_mfma_f32_16x16x32_bf16 v[36:39], a[140:143], a[0:3], v[36:39]\n" \
"ds_read_b64_tr_b16 a[178:179], v237 offset:14336\n" \
"v_mfma_f32_16x16x32_bf16 v[64:67], a[144:147], a[0:3], v[64:67]\n" \
"ds_read_b64_tr_b16 a[180:181], v238 offset:12288\n" \
"v_mfma_f32_16x16x32_bf16 v[72:75], a[144:147], a[4:7], v[72:75]\n" \
"buffer_load_dwordx4 a[96:99], v233, s[64:67], s22 offen offset:0 sc1\n" \
"v_mfma_f32_16x16x32_bf16 v[80:83], a[144:147], a[8:11], v[80:83]\n" \
"v_readlane_b32 s23, v241, 19\n" \
"v_mfma_f32_16x16x32_bf16 v[88:91], a[144:147], a[12:15], v[88:91]\n" \
"ds_read_b64_tr_b16 a[182:183], v238 offset:14336\n" \
"v_mfma_f32_16x16x32_bf16 v[92:95], a[148:151], a[12:15], v[92:95]\n" \
"buffer_load_dwordx4 a[100:103], v233, s[64:67], s22 offen offset:1024 sc1\n" \
"v_mfma_f32_16x16x32_bf16 v[84:87], a[148:151], a[8:11], v[84:87]\n" \
"ds_read_b64_tr_b16 a[184:185], v239 offset:12288\n" \
"v_mfma_f32_16x16x32_bf16 v[76:79], a[148:151], a[4:7], v[76:79]\n" \
"ds_read_b64_tr_b16 a[186:187], v239 offset:14336\n" \
"v_mfma_f32_16x16x32_bf16 v[68:71], a[148:151], a[0:3], v[68:71]\n" \
"buffer_load_dwordx4 a[104:107], v234, s[64:67], s22 offen offset:0 sc1\n" \
"v_mfma_f32_16x16x32_bf16 v[96:99], a[152:155], a[0:3], v[96:99]\n" \
"ds_read_b64_tr_b16 a[188:189], v240 offset:12288\n" \
"v_mfma_f32_16x16x32_bf16 v[104:107], a[152:155], a[4:7], v[104:107]\n" \
"ds_read_b64_tr_b16 a[190:191], v240 offset:14336\n" \
"v_mfma_f32_16x16x32_bf16 v[112:115], a[152:155], a[8:11], v[112:115]\n" \
"v_mfma_f32_16x16x32_bf16 v[120:123], a[152:155], a[12:15], v[120:123]\n" \
"s_waitcnt vmcnt(15)\n" \
"v_mfma_f32_16x16x32_bf16 v[124:127], a[156:159], a[12:15], v[124:127]\n" \
"v_cmp_ne_u64 vcc, s[72:73], v[250:251]\n" \
"v_mfma_f32_16x16x32_bf16 v[116:119], a[156:159], a[8:11], v[116:119]\n" \
"s_cbranch_vccnz Lsl11_%=\n" \
"Lrt11_%=:\n" \
"v_mfma_f32_16x16x32_bf16 v[108:111], a[156:159], a[4:7], v[108:111]\n" \
"v_accvgpr_read_b32 v252, a241\n" \
"s_add_u32 s61, s94, 8\n" \
"v_mfma_f32_16x16x32_bf16 v[100:103], a[156:159], a[0:3], v[100:103]\n" \
"buffer_load_dwordx2 v[242:243], v252, s[64:67], s61 offen sc1\n" \
"s_waitcnt vmcnt(21) lgkmcnt(0)\n" \
"v_mfma_f32_16x16x32_bf16 v[0:3], a[160:163], a[16:19], v[0:3]\n" \
"ds_read_b64_tr_b16 a[128:129], v237 offset:16384\n" \
"s_getpc_b64 s[28:29]\n" \
"Lct11_%=:\n" \
"s_add_u32 s28, s28, Lq_late_%=-Lct11_%=\n" \
"s_addc_u32 s29, s29, 0\n" \
"v_mfma_f32_16x16x32_bf16 v[8:11], a[160:163], a[20:23], v[8:11]\n" \
"buffer_load_dwordx4 a[108:111], v234, s[64:67], s22 offen offset:1024 sc1\n" \
"v_mfma_f32_16x16x32_bf16 v[16:19], a[160:163], a[24:27], v[16:19]\n" \
"ds_read_b64_tr_b16 a[130:131], v237 offset:18432\n" \
"v_mfma_f32_16x16x32_bf16 v[24:27], a[160:163], a[28:31], v[24:27]\n" \
"ds_read_b64_tr_b16 a[132:133], v238 offset:16384\n" \
"v_mfma_f32_16x16x32_bf16 v[28:31], a[164:167], a[28:31], v[28:31]\n" \
"ds_read_b64_tr_b16 a[134:135], v238 offset:18432\n" \
"v_mfma_f32_16x16x32_bf16 v[20:23], a[164:167], a[24:27], v[20:23]\n" \
"buffer_load_dwordx4 a[112:115], v233, s[64:67], s23 offen offset:0 sc1\n" \
"v_mfma_f32_16x16x32_bf16 v[12:15], a[164:167], a[20:23], v[12:15]\n" \
"v_readlane_b32 s20, v241, 20\n" \
"v_mfma_f32_16x16x32_bf16 v[4:7], a[164:167], a[16:19], v[4:7]\n" \
"ds_read_b64_tr_b16 a[136:137], v239 offset:16384\n" \
"v_mfma_f32_16x16x32_bf16 v[32:35], a[168:171], a[16:19], v[32:35]\n" \
"ds_read_b64_tr_b16 a[138:139], v239 offset:18432\n" \
"s_mov_b32 s30, gmk_code_end-Lq_late_%=\n" \
"s_mov_b32 s31, 0x20000\n" \
"v_mfma_f32_16x16x32_bf16 v[40:43], a[168:171], a[20:23], v[40:43]\n" \
"buffer_load_dwordx4 a[116:119], v233, s[64:67], s23 offen offset:1024 sc1\n" \
"v_mfma_f32_16x16x32_bf16 v[48:51], a[168:171], a[24:27], v[48:51]\n" \
"ds_read_b64_tr_b16 a[140:141], v240 offset:16384\n" \
"v_mfma_f32_16x16x32_bf16 v[56:59], a[168:171], a[28:31], v[56:59]\n" \
"ds_read_b64_tr_b16 a[142:143], v240 offset:18432\n" \
"v_mfma_f32_16x16x32_bf16 v[60:63], a[172:175], a[28:31], v[60:63]\n" \
"v_mfma_f32_16x16x32_bf16 v[52:55], a[172:175], a[24:27], v[52:55]\n" \
"buffer_load_dwordx4 a[120:123], v234, s[64:67], s23 offen offset:0 sc1\n" \
"v_mfma_f32_16x16x32_bf16 v[44:47], a[172:175], a[20:23], v[44:47]\n" \
"ds_read_b64_tr_b16 a[144:145], v237 offset:20480\n" \
"v_mfma_f32_16x16x32_bf16 v[36:39], a[172:175], a[16:19], v[36:39]\n" \
"ds_read_b64_tr_b16 a[146:147], v237 offset:22528\n" \
"v_mfma_f32_16x16x32_bf16 v[64:67], a[176:179], a[16:19], v[64:67]\n" \
"ds_read_b64_tr_b16 a[148:149], v238 offset:20480\n" \
"v_accvgpr_read_b32 v248, a243\n" \
"v_mfma_f32_16x16x32_bf16 v[72:75], a[176:179], a[20:23], v[72:75]\n" \
"buffer_load_dwordx4 a[124:127], v234, s[64:67], s23 offen offset:1024 sc1\n" \
"v_mfma_f32_16x16x32_bf16 v[80:83], a[176:179], a[24:27], v[80:83]\n" \
"ds_read_b64_tr_b16 a[150:151], v238 offset:22528\n" \
"v_mfma_f32_16x16x32_bf16 v[88:91], a[176:179], a[28:31], v[88:91]\n" \
"v_mfma_f32_16x16x32_bf16 v[92:95], a[180:183], a[28:31], v[92:95]\n" \
"ds_read_b64_tr_b16 a[152:153], v239 offset:20480\n" \
"v_mfma_f32_16x16x32_bf16 v[84:87], a[180:183], a[24:27], v[84:87]\n" \
"buffer_load_dwordx4 v[160:163], v233, s[64:67], s20 offen offset:0 sc1\n" \
"v_mfma_f32_16x16x32_bf16 v[76:79], a[180:183], a[20:23], v[76:79]\n" \
"v_readlane_b32 s21, v241, 21\n" \
"ds_read_b64_tr_b16 a[154:155], v239 offset:22528\n" \
"v_mfma_f32_16x16x32_bf16 v[68:71], a[180:183], a[16:19], v[68:71]\n" \
"ds_read_b64_tr_b16 a[156:157], v240 offset:20480\n" \
"v_mfma_f32_16x16x32_bf16 v[96:99], a[184:187], a[16:19], v[96:99]\n" \
"ds_read_b64_tr_b16 a[158:159], v240 offset:22528\n" \
"v_mfma_f32_16x16x32_bf16 v[104:107], a[184:187], a[20:23], v[104:107]\n" \
"buffer_load_dwordx4 v[164:167], v233, s[64:67], s20 offen offset:1024 sc1\n" \
"v_mfma_f32_16x16x32_bf16 v[112:115], a[184:187], a[24:27], v[112:115]\n" \
"buffer_load_dword a209, v248, s[28:31], 0 offen\n" \
"v_mfma_f32_16x16x32_bf16 v[120:123], a[184:187], a[28:31], v[120:123]\n" \
"v_mfma_f32_16x16x32_bf16 v[124:127], a[188:191], a[28:31], v[124:127]\n" \
"v_mfma_f32_16x16x32_bf16 v[116:119], a[188:191], a[24:27], v[116:119]\n" \
"v_mfma_f32_16x16x32_bf16 v[108:111], a[188:191], a[20:23], v[108:111]\n" \
"v_mfma_f32_16x16x32_bf16 v[100:103], a[188:191], a[16:19], v[100:103]\n" \
"s_waitcnt vmcnt(25) lgkmcnt(0)\n" \
"v_mfma_f32_16x16x32_bf16 v[0:3], a[128:131], a[32:35], v[0:3]\n" \
"ds_read_b64_tr_b16 a[160:161], v237 offset:24576\n" \
"v_mfma_f32_16x16x32_bf16 v[8:11], a[128:131], a[36:39], v[8:11]\n" \
"buffer_load_dwordx4 v[168:171], v234, s[64:67], s20 offen offset:0 sc1\n" \
"v_mfma_f32_16x16x32_bf16 v[16:19], a[128:131], a[40:43], v[16:19]\n" \
"ds_read_b64_tr_b16 a[162:163], v237 offset:26624\n" \
"v_mfma_f32_16x16x32_bf16 v[24:27], a[128:131], a[44:47], v[24:27]\n" \
"ds_read_b64_tr_b16 a[164:165], v238 offset:24576\n" \
"v_mfma_f32_16x16x32_bf16 v[28:31], a[132:135], a[44:47], v[28:31]\n" \
"buffer_load_dwordx4 v[172:175], v234, s[64:67], s20 offen offset:1024 sc1\n" \
"v_mfma_f32_16x16x32_bf16 v[20:23], a[132:135], a[40:43], v[20:23]\n" \
"ds_read_b64_tr_b16 a[166:167], v238 offset:26624\n" \
"v_mfma_f32_16x16x32_bf16 v[12:15], a[132:135], a[36:39], v[12:15]\n" \
"ds_read_b64_tr_b16 a[168:169], v239 offset:24576\n" \
"v_mfma_f32_16x16x32_bf16 v[4:7], a[132:135], a[32:35], v[4:7]\n" \
"ds_read_b64_tr_b16 a[170:171], v239 offset:26624\n" \
"v_mfma_f32_16x16x32_bf16 v[32:35], a[136:139], a[32:35], v[32:35]\n" \
"buffer_load_dwordx4 v[176:179], v233, s[64:67], s21 offen offset:0 sc1\n" \
"v_mfma_f32_16x16x32_bf16 v[40:43], a[136:139], a[36:39], v[40:43]\n" \
"v_readlane_b32 s22, v241, 22\n" \
"v_mfma_f32_16x16x32_bf16 v[48:51], a[136:139], a[40:43], v[48:51]\n" \
"ds_read_b64_tr_b16 a[172:173], v240 offset:24576\n" \
"v_mfma_f32_16x16x32_bf16 v[56:59], a[136:139], a[44:47], v[56:59]\n" \
"buffer_load_dwordx4 v[180:183], v233, s[64:67], s21 offen offset:1024 sc1\n" \
"v_mfma_f32_16x16x32_bf16 v[60:63], a[140:143], a[44:47], v[60:63]\n" \
"ds_read_b64_tr_b16 a[174:175], v240 offset:26624\n" \
"v_mfma_f32_16x16x32_bf16 v[52:55], a[140:143], a[40:43], v[52:55]\n" \
"ds_read_b64_tr_b16 a[176:177], v237 offset:28672\n" \
"v_mfma_f32_16x16x32_bf16 v[44:47], a[140:143], a[36:39], v[44:47]\n" \
"ds_read_b64_tr_b16 a[178:179], v237 offset:30720\n" \
"v_mfma_f32_16x16x32_bf16 v[36:39], a[140:143], a[32:35], v[36:39]\n" \
"buffer_load_dwordx4 v[184:187], v234, s[64:67], s21 offen offset:0 sc1\n" \
"v_mfma_f32_16x16x32_bf16 v[64:67], a[144:147], a[32:35], v[64:67]\n" \
"ds_read_b64_tr_b16 a[180:181], v238 offset:28672\n" \
"v_mfma_f32_16x16x32_bf16 v[72:75], a[144:147], a[36:39], v[72:75]\n" \
"v_mfma_f32_16x16x32_bf16 v[80:83], a[144:147], a[40:43], v[80:83]\n" \
"ds_read_b64_tr_b16 a[182:183], v238 offset:30720\n" \
"v_mfma_f32_16x16x32_bf16 v[88:91], a[144:147], a[44:47], v[88:91]\n" \
"buffer_load_dwordx4 v[188:191], v234, s[64:67], s21 offen offset:1024 sc1\n" \
"v_mfma_f32_16x16x32_bf16 v[92:95], a[148:151], a[44:47], v[92:95]\n" \
"ds_read_b64_tr_b16 a[184:185], v239 offset:28672\n" \
"v_mfma_f32_16x16x32_bf16 v[84:87], a[148:151], a[40:43], v[84:87]\n" \
"ds_read_b64_tr_b16 a[186:187], v239 offset:30720\n" \
"v_mfma_f32_16x16x32_bf16 v[76:79], a[148:151], a[36:39], v[76:79]\n" \
"buffer_load_dwordx4 v[192:195], v233, s[64:67], s22 offen offset:0 sc1\n" \
"v_mfma_f32_16x16x32_bf16 v[68:71], a[148:151], a[32:35], v[68:71]\n" \
"v_readlane_b32 s23, v241, 23\n" \
"ds_read_b64_tr_b16 a[188:189], v240 offset:28672\n" \
"v_mfma_f32_16x16x32_bf16 v[96:99], a[152:155], a[32:35], v[96:99]\n" \
"ds_read_b64_tr_b16 a[190:191], v240 offset:30720\n" \
"v_mfma_f32_16x16x32_bf16 v[104:107], a[152:155], a[36:39], v[104:107]\n" \
"v_mfma_f32_16x16x32_bf16 v[112:115], a[152:155], a[40:43], v[112:115]\n" \
"buffer_load_dwordx4 v[196:199], v233, s[64:67], s22 offen offset:1024 sc1\n" \
"v_mfma_f32_16x16x32_bf16 v[120:123], a[152:155], a[44:47], v[120:123]\n" \
"v_mfma_f32_16x16x32_bf16 v[124:127], a[156:159], a[44:47], v[124:127]\n" \
"v_mfma_f32_16x16x32_bf16 v[116:119], a[156:159], a[40:43], v[116:119]\n" \
"v_mfma_f32_16x16x32_bf16 v[108:111], a[156:159], a[36:39], v[108:111]\n" \
"v_mfma_f32_16x16x32_bf16 v[100:103], a[156:159], a[32:35], v[100:103]\n" \
"s_waitcnt vmcnt(28) lgkmcnt(0)\n" \
"s_barrier\n" \
"v_mfma_f32_16x16x32_bf16 v[0:3], a[160:163], a[48:51], v[0:3]\n" \
"ds_read_b64_tr_b16 a[128:129], v228 offset:0\n" \
"v_mfma_f32_16x16x32_bf16 v[8:11], a[160:163], a[52:55], v[8:11]\n" \
"buffer_load_dwordx4 v[200:203], v234, s[64:67], s22 offen offset:0 sc1\n" \
"v_mfma_f32_16x16x32_bf16 v[16:19], a[160:163], a[56:59], v[16:19]\n" \
"ds_read_b64_tr_b16 a[130:131], v228 offset:2048\n" \
"v_mfma_f32_16x16x32_bf16 v[24:27], a[160:163], a[60:63], v[24:27]\n" \
"ds_read_b64_tr_b16 a[132:133], v229 offset:0\n" \
"v_mfma_f32_16x16x32_bf16 v[28:31], a[164:167], a[60:63], v[28:31]\n" \
"buffer_load_dwordx4 v[204:207], v234, s[64:67], s22 offen offset:1024 sc1\n" \
"v_mfma_f32_16x16x32_bf16 v[20:23], a[164:167], a[56:59], v[20:23]\n" \
"ds_read_b64_tr_b16 a[134:135], v229 offset:2048\n" \
"v_mfma_f32_16x16x32_bf16 v[12:15], a[164:167], a[52:55], v[12:15]\n" \
"ds_read_b64_tr_b16 a[136:137], v230 offset:0\n" \
"v_mfma_f32_16x16x32_bf16 v[4:7], a[164:167], a[48:51], v[4:7]\n" \
"ds_read_b64_tr_b16 a[138:139], v230 offset:2048\n" \
"v_mfma_f32_16x16x32_bf16 v[32:35], a[168:171], a[48:51], v[32:35]\n" \
"buffer_load_dwordx4 v[208:211], v233, s[64:67], s23 offen offset:0 sc1\n" \
"v_mfma_f32_16x16x32_bf16 v[40:43], a[168:171], a[52:55], v[40:43]\n" \
"v_readlane_b32 s20, v241, 24\n" \
"v_mfma_f32_16x16x32_bf16 v[48:51], a[168:171], a[56:59], v[48:51]\n" \
"ds_read_b64_tr_b16 a[140:141], v231 offset:0\n" \
"v_mfma_f32_16x16x32_bf16 v[56:59], a[168:171], a[60:63], v[56:59]\n" \
"buffer_load_dwordx4 v[212:215], v233, s[64:67], s23 offen offset:1024 sc1\n" \
"v_mfma_f32_16x16x32_bf16 v[60:63], a[172:175], a[60:63], v[60:63]\n" \
"ds_read_b64_tr_b16 a[142:143], v231 offset:2048\n" \
"v_mfma_f32_16x16x32_bf16 v[52:55], a[172:175], a[56:59], v[52:55]\n" \
"ds_read_b64_tr_b16 a[144:145], v228 offset:4096\n" \
"v_mfma_f32_16x16x32_bf16 v[44:47], a[172:175], a[52:55], v[44:47]\n" \
"ds_read_b64_tr_b16 a[146:147], v228 offset:6144\n" \
"v_mfma_f32_16x16x32_bf16 v[36:39], a[172:175], a[48:51], v[36:39]\n" \
"v_add_u32 v234, 16384, v233\n" \
"buffer_load_dwordx4 v[216:219], v234, s[64:67], s23 offen offset:0 sc1\n" \
"v_mfma_f32_16x16x32_bf16 v[64:67], a[176:179], a[48:51], v[64:67]\n" \
"ds_read_b64_tr_b16 a[148:149], v229 offset:4096\n" \
"v_mfma_f32_16x16x32_bf16 v[72:75], a[176:179], a[52:55], v[72:75]\n" \
"v_mfma_f32_16x16x32_bf16 v[80:83], a[176:179], a[56:59], v[80:83]\n" \
"ds_read_b64_tr_b16 a[150:151], v229 offset:6144\n" \
"v_mfma_f32_16x16x32_bf16 v[88:91], a[176:179], a[60:63], v[88:91]\n" \
"buffer_load_dwordx4 v[220:223], v234, s[64:67], s23 offen offset:1024 sc1\n" \
"v_mfma_f32_16x16x32_bf16 v[92:95], a[180:183], a[60:63], v[92:95]\n" \
"ds_read_b64_tr_b16 a[152:153], v230 offset:4096\n" \
"v_mfma_f32_16x16x32_bf16 v[84:87], a[180:183], a[56:59], v[84:87]\n" \
"ds_read_b64_tr_b16 a[154:155], v230 offset:6144\n" \
"v_mfma_f32_16x16x32_bf16 v[76:79], a[180:183], a[52:55], v[76:79]\n" \
"buffer_load_dwordx4 a[0:3], v233, s[64:67], s20 offen offset:0 sc1\n" \
"v_mfma_f32_16x16x32_bf16 v[68:71], a[180:183], a[48:51], v[68:71]\n" \
"v_readlane_b32 s21, v241, 25\n" \
"ds_read_b64_tr_b16 a[156:157], v231 offset:4096\n" \
"v_mfma_f32_16x16x32_bf16 v[96:99], a[184:187], a[48:51], v[96:99]\n" \
"ds_read_b64_tr_b16 a[158:159], v231 offset:6144\n" \
"v_mfma_f32_16x16x32_bf16 v[104:107], a[184:187], a[52:55], v[104:107]\n" \
"v_mfma_f32_16x16x32_bf16 v[112:115], a[184:187], a[56:59], v[112:115]\n" \
"buffer_load_dwordx4 a[4:7], v233, s[64:67], s20 offen offset:1024 sc1\n" \
"v_mfma_f32_16x16x32_bf16 v[120:123], a[184:187], a[60:63], v[120:123]\n" \
"v_mfma_f32_16x16x32_bf16 v[124:127], a[188:191], a[60:63], v[124:127]\n" \
"v_mfma_f32_16x16x32_bf16 v[116:119], a[188:191], a[56:59], v[116:119]\n" \
"v_mfma_f32_16x16x32_bf16 v[108:111], a[188:191], a[52:55], v[108:111]\n" \
"v_mfma_f32_16x16x32_bf16 v[100:103], a[188:191], a[48:51], v[100:103]\n" \
"Lpadq2_%=:\n" \
"s_cbranch_vccnz Lprq2_%=\n" \
"s_waitcnt vmcnt(32) lgkmcnt(0)\n" \
"v_mfma_f32_16x16x32_bf16 v[0:3], a[128:131], a[64:67], v[0:3]\n" \
"ds_read_b64_tr_b16 a[160:161], v228 offset:8192\n" \
"v_mfma_f32_16x16x32_bf16 v[8:11], a[128:131], a[68:71], v[8:11]\n" \
"buffer_load_dwordx4 a[8:11], v234, s[64:67], s20 offen offset:0 sc1\n" \
"v_mfma_f32_16x16x32_bf16 v[16:19], a[128:131], a[72:75], v[16:19]\n" \
"ds_read_b64_tr_b16 a[162:163], v228 offset:10240\n" \
"v_mfma_f32_16x16x32_bf16 v[24:27], a[128:131], a[76:79], v[24:27]\n" \
"ds_read_b64_tr_b16 a[164:165], v229 offset:8192\n" \
"v_mfma_f32_16x16x32_bf16 v[28:31], a[132:135], a[76:79], v[28:31]\n" \
"buffer_load_dwordx4 a[12:15], v234, s[64:67], s20 offen offset:1024 sc1\n" \
"v_mfma_f32_16x16x32_bf16 v[20:23], a[132:135], a[72:75], v[20:23]\n" \
"ds_read_b64_tr_b16 a[166:167], v229 offset:10240\n" \
"v_mfma_f32_16x16x32_bf16 v[12:15], a[132:135], a[68:71], v[12:15]\n" \
"ds_read_b64_tr_b16 a[168:169], v230 offset:8192\n" \
"v_mfma_f32_16x16x32_bf16 v[4:7], a[132:135], a[64:67], v[4:7]\n" \
"ds_read_b64_tr_b16 a[170:171], v230 offset:10240\n" \
"v_mfma_f32_16x16x32_bf16 v[32:35], a[136:139], a[64:67], v[32:35]\n" \
"buffer_load_dwordx4 a[16:19], v233, s[64:67], s21 offen offset:0 sc1\n" \
"v_mfma_f32_16x16x32_bf16 v[40:43], a[136:139], a[68:71], v[40:43]\n" \
"v_readlane_b32 s22, v241, 26\n" \
"v_mfma_f32_16x16x32_bf16 v[48:51], a[136:139], a[72:75], v[48:51]\n" \
"ds_read_b64_tr_b16 a[172:173], v231 offset:8192\n" \
"v_mfma_f32_16x16x32_bf16 v[56:59], a[136:139], a[76:79], v[56:59]\n" \
"buffer_load_dwordx4 a[20:23], v233, s[64:67], s21 offen offset:1024 sc1\n" \
"v_mfma_f32_16x16x32_bf16 v[60:63], a[140:143], a[76:79], v[60:63]\n" \
"ds_read_b64_tr_b16 a[174:175], v231 offset:10240\n" \
"v_mfma_f32_16x16x32_bf16 v[52:55], a[140:143], a[72:75], v[52:55]\n" \
"ds_read_b64_tr_b16 a[176:177], v228 offset:12288\n" \
"v_mfma_f32_16x16x32_bf16 v[44:47], a[140:143], a[68:71], v[44:47]\n" \
"ds_read_b64_tr_b16 a[178:179], v228 offset:14336\n" \
"v_mfma_f32_16x16x32_bf16 v[36:39], a[140:143], a[64:67], v[36:39]\n" \
"buffer_load_dwordx4 a[24:27], v234, s[64:67], s21 offen offset:0 sc1\n" \
"v_mfma_f32_16x16x32_bf16 v[64:67], a[144:147], a[64:67], v[64:67]\n" \
"ds_read_b64_tr_b16 a[180:181], v229 offset:12288\n" \
"v_mfma_f32_16x16x32_bf16 v[72:75], a[144:147], a[68:71], v[72:75]\n" \
"v_mfma_f32_16x16x32_bf16 v[80:83], a[144:147], a[72:75], v[80:83]\n" \
"ds_read_b64_tr_b16 a[182:183], v229 offset:14336\n" \
"v_mfma_f32_16x16x32_bf16 v[88:91], a[144:147], a[76:79], v[88:91]\n" \
"buffer_load_dwordx4 a[28:31], v234, s[64:67], s21 offen offset:1024 sc1\n" \
"v_mfma_f32_16x16x32_bf16 v[92:95], a[148:151], a[76:79], v[92:95]\n" \
"ds_read_b64_tr_b16 a[184:185], v230 offset:12288\n" \
"v_mfma_f32_16x16x32_bf16 v[84:87], a[148:151], a[72:75], v[84:87]\n" \
"s_add_u32 m0, s80, 98304\n" \
"ds_read_b64_tr_b16 a[186:187], v230 offset:14336\n" \
"v_mfma_f32_16x16x32_bf16 v[76:79], a[148:151], a[68:71], v[76:79]\n" \
"buffer_load_dwordx4 v235, s[68:71], s24 offen lds\n" \
"v_mfma_f32_16x16x32_bf16 v[68:71], a[148:151], a[64:67], v[68:71]\n" \
"v_readlane_b32 s25, v254, 29\n" \
"ds_read_b64_tr_b16 a[188:189], v231 offset:12288\n" \
"v_mfma_f32_16x16x32_bf16 v[96:99], a[152:155], a[64:67], v[96:99]\n" \
"ds_read_b64_tr_b16 a[190:191], v231 offset:14336\n" \
"v_mfma_f32_16x16x32_bf16 v[104:107], a[152:155], a[68:71], v[104:107]\n" \
"s_add_u32 m0, s80, 102400\n" \
"v_mfma_f32_16x16x32_bf16 v[112:115], a[152:155], a[72:75], v[112:115]\n" \
"buffer_load_dwordx4 v236, s[68:71], s24 offen lds\n" \
"v_mfma_f32_16x16x32_bf16 v[120:123], a[152:155], a[76:79], v[120:123]\n" \
"v_mfma_f32_16x16x32_bf16 v[124:127], a[156:159], a[76:79], v[124:127]\n" \
"v_mfma_f32_16x16x32_bf16 v[116:119], a[156:159], a[72:75], v[116:119]\n" \
"v_mfma_f32_16x16x32_bf16 v[108:111], a[156:159], a[68:71], v[108:111]\n" \
"v_mfma_f32_16x16x32_bf16 v[100:103], a[156:159], a[64:67], v[100:103]\n" \
"s_waitcnt vmcnt(36) lgkmcnt(0)\n" \
"v_mfma_f32_16x16x32_bf16 v[0:3], a[160:163], a[80:83], v[0:3]\n" \
"s_add_u32 m0, s80, 106496\n" \
"ds_read_b64_tr_b16 a[128:129], v228 offset:16384\n" \
"v_mfma_f32_16x16x32_bf16 v[8:11], a[160:163], a[84:87], v[8:11]\n" \
"buffer_load_dwordx4 v235, s[68:71], s25 offen lds\n" \
"v_mfma_f32_16x16x32_bf16 v[16:19], a[160:163], a[88:91], v[16:19]\n" \
"v_readlane_b32 s26, v254, 30\n" \
"ds_read_b64_tr_b16 a[130:131], v228 offset:18432\n" \
"v_mfma_f32_16x16x32_bf16 v[24:27], a[160:163], a[92:95], v[24:27]\n" \
"s_add_u32 m0, s80, 110592\n" \
"ds_read_b64_tr_b16 a[132:133], v229 offset:16384\n" \
"v_mfma_f32_16x16x32_bf16 v[28:31], a[164:167], a[92:95], v[28:31]\n" \
"buffer_load_dwordx4 v236, s[68:71], s25 offen lds\n" \
"v_mfma_f32_16x16x32_bf16 v[20:23], a[164:167], a[88:91], v[20:23]\n" \
"ds_read_b64_tr_b16 a[134:135], v229 offset:18432\n" \
"v_mfma_f32_16x16x32_bf16 v[12:15], a[164:167], a[84:87], v[12:15]\n" \
"ds_read_b64_tr_b16 a[136:137], v230 offset:16384\n" \
"v_mfma_f32_16x16x32_bf16 v[4:7], a[164:167], a[80:83], v[4:7]\n" \
"s_add_u32 m0, s80, 114688\n" \
"ds_read_b64_tr_b16 a[138:139], v230 offset:18432\n" \
"v_mfma_f32_16x16x32_bf16 v[32:35], a[168:171], a[80:83], v[32:35]\n" \
"buffer_load_dwordx4 v235, s[68:71], s26 offen lds\n" \
"v_mfma_f32_16x16x32_bf16 v[40:43], a[168:171], a[84:87], v[40:43]\n" \
"v_readlane_b32 s27, v254, 31\n" \
"v_mfma_f32_16x16x32_bf16 v[48:51], a[168:171], a[88:91], v[48:51]\n" \
"s_add_u32 m0, s80, 118784\n" \
"ds_read_b64_tr_b16 a[140:141], v231 offset:16384\n" \
"v_mfma_f32_16x16x32_bf16 v[56:59], a[168:171], a[92:95], v[56:59]\n" \
"buffer_load_dwordx4 v236, s[68:71], s26 offen lds\n" \
"v_mfma_f32_16x16x32_bf16 v[60:63], a[172:175], a[92:95], v[60:63]\n" \
"ds_read_b64_tr_b16 a[142:143], v231 offset:18432\n" \
"v_mfma_f32_16x16x32_bf16 v[52:55], a[172:175], a[88:91], v[52:55]\n" \
"ds_read_b64_tr_b16 a[144:145], v228 offset:20480\n" \
"v_mfma_f32_16x16x32_bf16 v[44:47], a[172:175], a[84:87], v[44:47]\n" \
"s_add_u32 m0, s80, 122880\n" \
"ds_read_b64_tr_b16 a[146:147], v228 offset:22528\n" \
"v_mfma_f32_16x16x32_bf16 v[36:39], a[172:175], a[80:83], v[36:39]\n" \
"buffer_load_dwordx4 v235, s[68:71], s27 offen lds\n" \
"v_mfma_f32_16x16x32_bf16 v[64:67], a[176:179], a[80:83], v[64:67]\n" \
"v_readlane_b32 s24, v254, 32\n" \
"ds_read_b64_tr_b16 a[148:149], v229 offset:20480\n" \
"v_mfma_f32_16x16x32_bf16 v[72:75], a[176:179], a[84:87], v[72:75]\n" \
"v_mfma_f32_16x16x32_bf16 v[80:83], a[176:179], a[88:91], v[80:83]\n" \
"s_add_u32 m0, s80, 126976\n" \
"ds_read_b64_tr_b16 a[150:151], v229 offset:22528\n" \
"v_mfma_f32_16x16x32_bf16 v[88:91], a[176:179], a[92:95], v[88:91]\n" \
"buffer_load_dwordx4 v236, s[68:71], s27 offen lds\n" \
"v_mfma_f32_16x16x32_bf16 v[92:95], a[180:183], a[92:95], v[92:95]\n" \
"ds_read_b64_tr_b16 a[152:153], v230 offset:20480\n" \
"v_mfma_f32_16x16x32_bf16 v[84:87], a[180:183], a[88:91], v[84:87]\n" \
"ds_read_b64_tr_b16 a[154:155], v230 offset:22528\n" \
"v_mfma_f32_16x16x32_bf16 v[76:79], a[180:183], a[84:87], v[76:79]\n" \
"buffer_load_dwordx4 a[32:35], v233, s[64:67], s22 offen offset:0 sc1\n" \
"v_mfma_f32_16x16x32_bf16 v[68:71], a[180:183], a[80:83], v[68:71]\n" \
"v_readlane_b32 s23, v241, 27\n" \
"ds_read_b64_tr_b16 a[156:157], v231 offset:20480\n" \
"v_mfma_f32_16x16x32_bf16 v[96:99], a[184:187], a[80:83], v[96:99]\n" \
"ds_read_b64_tr_b16 a[158:159], v231 offset:22528\n" \
"v_mfma_f32_16x16x32_bf16 v[104:107], a[184:187], a[84:87], v[104:107]\n" \
"v_mfma_f32_16x16x32_bf16 v[112:115], a[184:187], a[88:91], v[112:115]\n" \
"buffer_load_dwordx4 a[36:39], v233, s[64:67], s22 offen offset:1024 sc1\n" \
"v_mfma_f32_16x16x32_bf16 v[120:123], a[184:187], a[92:95], v[120:123]\n" \
"v_mfma_f32_16x16x32_bf16 v[124:127], a[188:191], a[92:95], v[124:127]\n" \
"v_mfma_f32_16x16x32_bf16 v[116:119], a[188:191], a[88:91], v[116:119]\n" \
"v_mfma_f32_16x16x32_bf16 v[108:111], a[188:191], a[84:87], v[108:111]\n" \
"v_mfma_f32_16x16x32_bf16 v[100:103], a[188:191], a[80:83], v[100:103]\n" \
"s_waitcnt vmcnt(39) lgkmcnt(0)\n" \
"v_mfma_f32_16x16x32_bf16 v[0:3], a[128:131], a[96:99], v[0:3]\n" \
"ds_read_b64_tr_b16 a[160:161], v228 offset:24576\n" \
"v_mfma_f32_16x16x32_bf16 v[8:11], a[128:131], a[100:103], v[8:11]\n" \
"buffer_load_dwordx4 a[40:43], v234, s[64:67], s22 offen offset:0 sc1\n" \
"v_mfma_f32_16x16x32_bf16 v[16:19], a[128:131], a[104:107], v[16:19]\n" \
"ds_read_b64_tr_b16 a[162:163], v228 offset:26624\n" \
"v_mfma_f32_16x16x32_bf16 v[24:27], a[128:131], a[108:111], v[24:27]\n" \
"ds_read_b64_tr_b16 a[164:165], v229 offset:24576\n" \
"v_mfma_f32_16x16x32_bf16 v[28:31], a[132:135], a[108:111], v[28:31]\n" \
"buffer_load_dwordx4 a[44:47], v234, s[64:67], s22 offen offset:1024 sc1\n" \
"v_mfma_f32_16x16x32_bf16 v[20:23], a[132:135], a[104:107], v[20:23]\n" \
"ds_read_b64_tr_b16 a[166:167], v229 offset:26624\n" \
"v_mfma_f32_16x16x32_bf16 v[12:15], a[132:135], a[100:103], v[12:15]\n" \
"ds_read_b64_tr_b16 a[168:169], v230 offset:24576\n" \
"v_mfma_f32_16x16x32_bf16 v[4:7], a[132:135], a[96:99], v[4:7]\n" \
"ds_read_b64_tr_b16 a[170:171], v230 offset:26624\n" \
"v_mfma_f32_16x16x32_bf16 v[32:35], a[136:139], a[96:99], v[32:35]\n" \
"buffer_load_dwordx4 a[48:51], v233, s[64:67], s23 offen offset:0 sc1\n" \
"v_mfma_f32_16x16x32_bf16 v[40:43], a[136:139], a[100:103], v[40:43]\n" \
"v_readlane_b32 s20, v241, 28\n" \
"v_mfma_f32_16x16x32_bf16 v[48:51], a[136:139], a[104:107], v[48:51]\n" \
"ds_read_b64_tr_b16 a[172:173], v231 offset:24576\n" \
"v_mfma_f32_16x16x32_bf16 v[56:59], a[136:139], a[108:111], v[56:59]\n" \
"buffer_load_dwordx4 a[52:55], v233, s[64:67], s23 offen offset:1024 sc1\n" \
"v_mfma_f32_16x16x32_bf16 v[60:63], a[140:143], a[108:111], v[60:63]\n" \
"ds_read_b64_tr_b16 a[174:175], v231 offset:26624\n" \
"v_mfma_f32_16x16x32_bf16 v[52:55], a[140:143], a[104:107], v[52:55]\n" \
"ds_read_b64_tr_b16 a[176:177], v228 offset:28672\n" \
"v_mfma_f32_16x16x32_bf16 v[44:47], a[140:143], a[100:103], v[44:47]\n" \
"ds_read_b64_tr_b16 a[178:179], v228 offset:30720\n" \
"v_mfma_f32_16x16x32_bf16 v[36:39], a[140:143], a[96:99], v[36:39]\n" \
"buffer_load_dwordx4 a[56:59], v234, s[64:67], s23 offen offset:0 sc1\n" \
"v_mfma_f32_16x16x32_bf16 v[64:67], a[144:147], a[96:99], v[64:67]\n" \
"ds_read_b64_tr_b16 a[180:181], v229 offset:28672\n" \
"v_mfma_f32_16x16x32_bf16 v[72:75], a[144:147], a[100:103], v[72:75]\n" \
"v_mfma_f32_16x16x32_bf16 v[80:83], a[144:147], a[104:107], v[80:83]\n" \
"ds_read_b64_tr_b16 a[182:183], v229 offset:30720\n" \
"v_mfma_f32_16x16x32_bf16 v[88:91], a[144:147], a[108:111], v[88:91]\n" \
"buffer_load_dwordx4 a[60:63], v234, s[64:67], s23 offen offset:1024 sc1\n" \
"v_mfma_f32_16x16x32_bf16 v[92:95], a[148:151], a[108:111], v[92:95]\n" \
"ds_read_b64_tr_b16 a[184:185], v230 offset:28672\n" \
"v_mfma_f32_16x16x32_bf16 v[84:87], a[148:151], a[104:107], v[84:87]\n" \
"ds_read_b64_tr_b16 a[186:187], v230 offset:30720\n" \
"v_mfma_f32_16x16x32_bf16 v[76:79], a[148:151], a[100:103], v[76:79]\n" \
"buffer_load_dwordx4 a[64:67], v233, s[64:67], s20 offen offset:0 sc1\n" \
"v_mfma_f32_16x16x32_bf16 v[68:71], a[148:151], a[96:99], v[68:71]\n" \
"v_readlane_b32 s21, v241, 29\n" \
"ds_read_b64_tr_b16 a[188:189], v231 offset:28672\n" \
"v_mfma_f32_16x16x32_bf16 v[96:99], a[152:155], a[96:99], v[96:99]\n" \
"ds_read_b64_tr_b16 a[190:191], v231 offset:30720\n" \
"v_mfma_f32_16x16x32_bf16 v[104:107], a[152:155], a[100:103], v[104:107]\n" \
"v_mfma_f32_16x16x32_bf16 v[112:115], a[152:155], a[104:107], v[112:115]\n" \
"buffer_load_dwordx4 a[68:71], v233, s[64:67], s20 offen offset:1024 sc1\n" \
"v_mfma_f32_16x16x32_bf16 v[120:123], a[152:155], a[108:111], v[120:123]\n" \
"v_mfma_f32_16x16x32_bf16 v[124:127], a[156:159], a[108:111], v[124:127]\n" \
"v_mfma_f32_16x16x32_bf16 v[116:119], a[156:159], a[104:107], v[116:119]\n" \
"v_mfma_f32_16x16x32_bf16 v[108:111], a[156:159], a[100:103], v[108:111]\n" \
"v_mfma_f32_16x16x32_bf16 v[100:103], a[156:159], a[96:99], v[100:103]\n" \
"Lq_late_%=:\n" \
"s_waitcnt vmcnt(43) lgkmcnt(0)\n" \
"s_barrier\n" \
"v_mfma_f32_16x16x32_bf16 v[0:3], a[160:163], a[112:115], v[0:3]\n" \
"ds_read_b64_tr_b16 a[128:129], v237 offset:32768\n" \
"v_mfma_f32_16x16x32_bf16 v[8:11], a[160:163], a[116:119], v[8:11]\n" \
"buffer_load_dwordx4 a[72:75], v234, s[64:67], s20 offen offset:0 sc1\n" \
"v_mfma_f32_16x16x32_bf16 v[16:19], a[160:163], a[120:123], v[16:19]\n" \
"ds_read_b64_tr_b16 a[130:131], v237 offset:34816\n" \
"v_mfma_f32_16x16x32_bf16 v[24:27], a[160:163], a[124:127], v[24:27]\n" \
"ds_read_b64_tr_b16 a[132:133], v238 offset:32768\n" \
"v_mfma_f32_16x16x32_bf16 v[28:31], a[164:167], a[124:127], v[28:31]\n" \
"buffer_load_dwordx4 a[76:79], v234, s[64:67], s20 offen offset:1024 sc1\n" \
"v_mfma_f32_16x16x32_bf16 v[20:23], a[164:167], a[120:123], v[20:23]\n" \
"ds_read_b64_tr_b16 a[134:135], v238 offset:34816\n" \
"v_mfma_f32_16x16x32_bf16 v[12:15], a[164:167], a[116:119], v[12:15]\n" \
"ds_read_b64_tr_b16 a[136:137], v239 offset:32768\n" \
"v_mfma_f32_16x16x32_bf16 v[4:7], a[164:167], a[112:115], v[4:7]\n" \
"ds_read_b64_tr_b16 a[138:139], v239 offset:34816\n" \
"v_mfma_f32_16x16x32_bf16 v[32:35], a[168:171], a[112:115], v[32:35]\n" \
"buffer_load_dwordx4 a[80:83], v233, s[64:67], s21 offen offset:0 sc1\n" \
"v_mfma_f32_16x16x32_bf16 v[40:43], a[168:171], a[116:119], v[40:43]\n" \
"v_readlane_b32 s22, v241, 30\n" \
"v_mfma_f32_16x16x32_bf16 v[48:51], a[168:171], a[120:123], v[48:51]\n" \
"ds_read_b64_tr_b16 a[140:141], v240 offset:32768\n" \
"v_mfma_f32_16x16x32_bf16 v[56:59], a[168:171], a[124:127], v[56:59]\n" \
"buffer_load_dwordx4 a[84:87], v233, s[64:67], s21 offen offset:1024 sc1\n" \
"v_mfma_f32_16x16x32_bf16 v[60:63], a[172:175], a[124:127], v[60:63]\n" \
"ds_read_b64_tr_b16 a[142:143], v240 offset:34816\n" \
"v_mfma_f32_16x16x32_bf16 v[52:55], a[172:175], a[120:123], v[52:55]\n" \
"ds_read_b64_tr_b16 a[144:145], v237 offset:36864\n" \
"v_mfma_f32_16x16x32_bf16 v[44:47], a[172:175], a[116:119], v[44:47]\n" \
"ds_read_b64_tr_b16 a[146:147], v237 offset:38912\n" \
"v_mfma_f32_16x16x32_bf16 v[36:39], a[172:175], a[112:115], v[36:39]\n" \
"buffer_load_dwordx4 a[88:91], v234, s[64:67], s21 offen offset:0 sc1\n" \
"v_mfma_f32_16x16x32_bf16 v[64:67], a[176:179], a[112:115], v[64:67]\n" \
"ds_read_b64_tr_b16 a[148:149], v238 offset:36864\n" \
"v_mfma_f32_16x16x32_bf16 v[72:75], a[176:179], a[116:119], v[72:75]\n" \
"v_mfma_f32_16x16x32_bf16 v[80:83], a[176:179], a[120:123], v[80:83]\n" \
"ds_read_b64_tr_b16 a[150:151], v238 offset:38912\n" \
"v_mfma_f32_16x16x32_bf16 v[88:91], a[176:179], a[124:127], v[88:91]\n" \
"buffer_load_dwordx4 a[92:95], v234, s[64:67], s21 offen offset:1024 sc1\n" \
"v_mfma_f32_16x16x32_bf16 v[92:95], a[180:183], a[124:127], v[92:95]\n" \
"ds_read_b64_tr_b16 a[152:153], v239 offset:36864\n" \
"v_mfma_f32_16x16x32_bf16 v[84:87], a[180:183], a[120:123], v[84:87]\n" \
"s_add_u32 m0, s80, 0\n" \
"ds_read_b64_tr_b16 a[154:155], v239 offset:38912\n" \
"v_mfma_f32_16x16x32_bf16 v[76:79], a[180:183], a[116:119], v[76:79]\n" \
"buffer_load_dwordx4 v235, s[68:71], s24 offen lds\n" \
"v_mfma_f32_16x16x32_bf16 v[68:71], a[180:183], a[112:115], v[68:71]\n" \
"v_readlane_b32 s25, v254, 33\n" \
"ds_read_b64_tr_b16 a[156:157], v240 offset:36864\n" \
"v_mfma_f32_16x16x32_bf16 v[96:99], a[184:187], a[112:115], v[96:99]\n" \
"ds_read_b64_tr_b16 a[158:159], v240 offset:38912\n" \
"v_mfma_f32_16x16x32_bf16 v[104:107], a[184:187], a[116:119], v[104:107]\n" \
"s_add_u32 m0, s80, 4096\n" \
"v_mfma_f32_16x16x32_bf16 v[112:115], a[184:187], a[120:123], v[112:115]\n" \
"buffer_load_dwordx4 v236, s[68:71], s24 offen lds\n" \
"v_mfma_f32_16x16x32_bf16 v[120:123], a[184:187], a[124:127], v[120:123]\n" \
"v_mfma_f32_16x16x32_bf16 v[124:127], a[188:191], a[124:127], v[124:127]\n" \
"v_mfma_f32_16x16x32_bf16 v[116:119], a[188:191], a[120:123], v[116:119]\n" \
"v_mfma_f32_16x16x32_bf16 v[108:111], a[188:191], a[116:119], v[108:111]\n" \
"v_mfma_f32_16x16x32_bf16 v[100:103], a[188:191], a[112:115], v[100:103]\n" \
"s_waitcnt vmcnt(46) lgkmcnt(0)\n" \
"v_mfma_f32_16x16x32_bf16 v[0:3], a[128:131], v[160:163], v[0:3]\n" \
"s_add_u32 m0, s80, 8192\n" \
"ds_read_b64_tr_b16 a[160:161], v237 offset:40960\n" \
"v_mfma_f32_16x16x32_bf16 v[8:11], a[128:131], v[164:167], v[8:11]\n" \
"buffer_load_dwordx4 v235, s[68:71], s25 offen lds\n" \
"v_mfma_f32_16x16x32_bf16 v[16:19], a[128:131], v[168:171], v[16:19]\n" \
"v_readlane_b32 s26, v254, 34\n" \
"ds_read_b64_tr_b16 a[162:163], v237 offset:43008\n" \
"v_mfma_f32_16x16x32_bf16 v[24:27], a[128:131], v[172:175], v[24:27]\n" \
"s_add_u32 m0, s80, 12288\n" \
"ds_read_b64_tr_b16 a[164:165], v238 offset:40960\n" \
"v_mfma_f32_16x16x32_bf16 v[28:31], a[132:135], v[172:175], v[28:31]\n" \
"buffer_load_dwordx4 v236, s[68:71], s25 offen lds\n" \
"v_mfma_f32_16x16x32_bf16 v[20:23], a[132:135], v[168:171], v[20:23]\n" \
"ds_read_b64_tr_b16 a[166:167], v238 offset:43008\n" \
"v_mfma_f32_16x16x32_bf16 v[12:15], a[132:135], v[164:167], v[12:15]\n" \
"ds_read_b64_tr_b16 a[168:169], v239 offset:40960\n" \
"v_mfma_f32_16x16x32_bf16 v[4:7], a[132:135], v[160:163], v[4:7]\n" \
"s_add_u32 m0, s80, 16384\n" \
"ds_read_b64_tr_b16 a[170:171], v239 offset:43008\n" \
"v_mfma_f32_16x16x32_bf16 v[32:35], a[136:139], v[160:163], v[32:35]\n" \
"buffer_load_dwordx4 v235, s[68:71], s26 offen lds\n" \
"v_mfma_f32_16x16x32_bf16 v[40:43], a[136:139], v[164:167], v[40:43]\n" \
"v_readlane_b32 s27, v254, 35\n" \
"v_mfma_f32_16x16x32_bf16 v[48:51], a[136:139], v[168:171], v[48:51]\n" \
"s_add_u32 m0, s80, 20480\n" \
"ds_read_b64_tr_b16 a[172:173], v240 offset:40960\n" \
"v_mfma_f32_16x16x32_bf16 v[56:59], a[136:139], v[172:175], v[56:59]\n" \
"buffer_load_dwordx4 v236, s[68:71], s26 offen lds\n" \
"v_mfma_f32_16x16x32_bf16 v[60:63], a[140:143], v[172:175], v[60:63]\n" \
"ds_read_b64_tr_b16 a[174:175], v240 offset:43008\n" \
"v_mfma_f32_16x16x32_bf16 v[52:55], a[140:143], v[168:171], v[52:55]\n" \
"ds_read_b64_tr_b16 a[176:177], v237 offset:45056\n" \
"v_mfma_f32_16x16x32_bf16 v[44:47], a[140:143], v[164:167], v[44:47]\n" \
"s_add_u32 m0, s80, 24576\n" \
"ds_read_b64_tr_b16 a[178:179], v237 offset:47104\n" \
"v_mfma_f32_16x16x32_bf16 v[36:39], a[140:143], v[160:163], v[36:39]\n" \
"buffer_load_dwordx4 v235, s[68:71], s27 offen lds\n" \
"v_mfma_f32_16x16x32_bf16 v[64:67], a[144:147], v[160:163], v[64:67]\n" \
"v_readlane_b32 s24, v254, 36\n" \
"ds_read_b64_tr_b16 a[180:181], v238 offset:45056\n" \
"v_mfma_f32_16x16x32_bf16 v[72:75], a[144:147], v[164:167], v[72:75]\n" \
"v_mfma_f32_16x16x32_bf16 v[80:83], a[144:147], v[168:171], v[80:83]\n" \
"s_add_u32 m0, s80, 28672\n" \
"ds_read_b64_tr_b16 a[182:183], v238 offset:47104\n" \
"v_mfma_f32_16x16x32_bf16 v[88:91], a[144:147], v[172:175], v[88:91]\n" \
"buffer_load_dwordx4 v236, s[68:71], s27 offen lds\n" \
"v_mfma_f32_16x16x32_bf16 v[92:95], a[148:151], v[172:175], v[92:95]\n" \
"ds_read_b64_tr_b16 a[184:185], v239 offset:45056\n" \
"v_mfma_f32_16x16x32_bf16 v[84:87], a[148:151], v[168:171], v[84:87]\n" \
"ds_read_b64_tr_b16 a[186:187], v239 offset:47104\n" \
"v_mfma_f32_16x16x32_bf16 v[76:79], a[148:151], v[164:167], v[76:79]\n" \
"buffer_load_dwordx4 a[96:99], v233, s[64:67], s22 offen offset:0 sc1\n" \
"v_mfma_f32_16x16x32_bf16 v[68:71], a[148:151], v[160:163], v[68:71]\n" \
"v_readlane_b32 s23, v241, 31\n" \
"ds_read_b64_tr_b16 a[188:189], v240 offset:45056\n" \
"v_mfma_f32_16x16x32_bf16 v[96:99], a[152:155], v[160:163], v[96:99]\n" \
"ds_read_b64_tr_b16 a[190:191], v240 offset:47104\n" \
"v_mfma_f32_16x16x32_bf16 v[104:107], a[152:155], v[164:167], v[104:107]\n" \
"v_mfma_f32_16x16x32_bf16 v[112:115], a[152:155], v[168:171], v[112:115]\n" \
"buffer_load_dwordx4 a[100:103], v233, s[64:67], s22 offen offset:1024 sc1\n" \
"v_mfma_f32_16x16x32_bf16 v[120:123], a[152:155], v[172:175], v[120:123]\n" \
"v_mfma_f32_16x16x32_bf16 v[124:127], a[156:159], v[172:175], v[124:127]\n" \
"v_mfma_f32_16x16x32_bf16 v[116:119], a[156:159], v[168:171], v[116:119]\n" \
"v_mfma_f32_16x16x32_bf16 v[108:111], a[156:159], v[164:167], v[108:111]\n" \
"v_mfma_f32_16x16x32_bf16 v[100:103], a[156:159], v[160:163], v[100:103]\n" \
"s_waitcnt vmcnt(50) lgkmcnt(0)\n" \
"v_mfma_f32_16x16x32_bf16 v[0:3], a[160:163], v[176:179], v[0:3]\n" \
"ds_read_b64_tr_b16 a[128:129], v237 offset:49152\n" \
"v_mfma_f32_16x16x32_bf16 v[8:11], a[160:163], v[180:183], v[8:11]\n" \
"buffer_load_dwordx4 a[104:107], v234, s[64:67], s22 offen offset:0 sc1\n" \
"v_mfma_f32_16x16x32_bf16 v[16:19], a[160:163], v[184:187], v[16:19]\n" \
"ds_read_b64_tr_b16 a[130:131], v237 offset:51200\n" \
"v_mfma_f32_16x16x32_bf16 v[24:27], a[160:163], v[188:191], v[24:27]\n" \
"ds_read_b64_tr_b16 a[132:133], v238 offset:49152\n" \
"v_mfma_f32_16x16x32_bf16 v[28:31], a[164:167], v[188:191], v[28:31]\n" \
"buffer_load_dwordx4 a[108:111], v234, s[64:67], s22 offen offset:1024 sc1\n" \
"v_mfma_f32_16x16x32_bf16 v[20:23], a[164:167], v[184:187], v[20:23]\n" \
"ds_read_b64_tr_b16 a[134:135], v238 offset:51200\n" \
"v_mfma_f32_16x16x32_bf16 v[12:15], a[164:167], v[180:183], v[12:15]\n" \
"ds_read_b64_tr_b16 a[136:137], v239 offset:49152\n" \
"v_mfma_f32_16x16x32_bf16 v[4:7], a[164:167], v[176:179], v[4:7]\n" \
"ds_read_b64_tr_b16 a[138:139], v239 offset:51200\n" \
"v_mfma_f32_16x16x32_bf16 v[32:35], a[168:171], v[176:179], v[32:35]\n" \
"buffer_load_dwordx4 a[112:115], v233, s[64:67], s23 offen offset:0 sc1\n" \
"v_mfma_f32_16x16x32_bf16 v[40:43], a[168:171], v[180:183], v[40:43]\n" \
"v_readlane_b32 s20, v241, 32\n" \
"v_mfma_f32_16x16x32_bf16 v[48:51], a[168:171], v[184:187], v[48:51]\n" \
"ds_read_b64_tr_b16 a[140:141], v240 offset:49152\n" \
"v_mfma_f32_16x16x32_bf16 v[56:59], a[168:171], v[188:191], v[56:59]\n" \
"buffer_load_dwordx4 a[116:119], v233, s[64:67], s23 offen offset:1024 sc1\n" \
"v_mfma_f32_16x16x32_bf16 v[60:63], a[172:175], v[188:191], v[60:63]\n" \
"ds_read_b64_tr_b16 a[142:143], v240 offset:51200\n" \
"v_mfma_f32_16x16x32_bf16 v[52:55], a[172:175], v[184:187], v[52:55]\n" \
"ds_read_b64_tr_b16 a[144:145], v237 offset:53248\n" \
"v_mfma_f32_16x16x32_bf16 v[44:47], a[172:175], v[180:183], v[44:47]\n" \
"ds_read_b64_tr_b16 a[146:147], v237 offset:55296\n" \
"v_mfma_f32_16x16x32_bf16 v[36:39], a[172:175], v[176:179], v[36:39]\n" \
"buffer_load_dwordx4 a[120:123], v234, s[64:67], s23 offen offset:0 sc1\n" \
"v_mfma_f32_16x16x32_bf16 v[64:67], a[176:179], v[176:179], v[64:67]\n" \
"ds_read_b64_tr_b16 a[148:149], v238 offset:53248\n" \
"v_mfma_f32_16x16x32_bf16 v[72:75], a[176:179], v[180:183], v[72:75]\n" \
"v_mfma_f32_16x16x32_bf16 v[80:83], a[176:179], v[184:187], v[80:83]\n" \
"ds_read_b64_tr_b16 a[150:151], v238 offset:55296\n" \
"v_mfma_f32_16x16x32_bf16 v[88:91], a[176:179], v[188:191], v[88:91]\n" \
"buffer_load_dwordx4 a[124:127], v234, s[64:67], s23 offen offset:1024 sc1\n" \
"v_mfma_f32_16x16x32_bf16 v[92:95], a[180:183], v[188:191], v[92:95]\n" \
"ds_read_b64_tr_b16 a[152:153], v239 offset:53248\n" \
"v_mfma_f32_16x16x32_bf16 v[84:87], a[180:183], v[184:187], v[84:87]\n" \
"ds_read_b64_tr_b16 a[154:155], v239 offset:55296\n" \
"v_mfma_f32_16x16x32_bf16 v[76:79], a[180:183], v[180:183], v[76:79]\n" \
"buffer_load_dwordx4 v[160:163], v233, s[64:67], s20 offen offset:0 sc1\n" \
"v_mfma_f32_16x16x32_bf16 v[68:71], a[180:183], v[176:179], v[68:71]\n" \
"v_readlane_b32 s21, v241, 33\n" \
"ds_read_b64_tr_b16 a[156:157], v240 offset:53248\n" \
"v_mfma_f32_16x16x32_bf16 v[96:99], a[184:187], v[176:179], v[96:99]\n" \
"ds_read_b64_tr_b16 a[158:159], v240 offset:55296\n" \
"v_mfma_f32_16x16x32_bf16 v[104:107], a[184:187], v[180:183], v[104:107]\n" \
"v_mfma_f32_16x16x32_bf16 v[112:115], a[184:187], v[184:187], v[112:115]\n" \
"buffer_load_dwordx4 v[164:167], v233, s[64:67], s20 offen offset:1024 sc1\n" \
"v_mfma_f32_16x16x32_bf16 v[120:123], a[184:187], v[188:191], v[120:123]\n" \
"v_mfma_f32_16x16x32_bf16 v[124:127], a[188:191], v[188:191], v[124:127]\n" \
"v_mfma_f32_16x16x32_bf16 v[116:119], a[188:191], v[184:187], v[116:119]\n" \
"v_mfma_f32_16x16x32_bf16 v[108:111], a[188:191], v[180:183], v[108:111]\n" \
"v_mfma_f32_16x16x32_bf16 v[100:103], a[188:191], v[176:179], v[100:103]\n" \
"s_waitcnt vmcnt(54) lgkmcnt(0)\n" \
"v_mfma_f32_16x16x32_bf16 v[0:3], a[128:131], v[192:195], v[0:3]\n" \
"ds_read_b64_tr_b16 a[160:161], v237 offset:57344\n" \
"s_xor_b32 s60, s91, 2\n" \
"s_add_u32 s60, s60, s98\n" \
"v_mfma_f32_16x16x32_bf16 v[8:11], a[128:131], v[196:199], v[8:11]\n" \
"buffer_load_dwordx4 v[168:171], v234, s[64:67], s20 offen offset:0 sc1\n" \
"v_mfma_f32_16x16x32_bf16 v[16:19], a[128:131], v[200:203], v[16:19]\n" \
"ds_read_b64_tr_b16 a[162:163], v237 offset:59392\n" \
"v_mfma_f32_16x16x32_bf16 v[24:27], a[128:131], v[204:207], v[24:27]\n" \
"ds_read_b64_tr_b16 a[164:165], v238 offset:57344\n" \
"s_mul_i32 s60, s60, 49152\n" \
"s_add_u32 s41, s60, 20987904\n" \
"v_mfma_f32_16x16x32_bf16 v[28:31], a[132:135], v[204:207], v[28:31]\n" \
"buffer_load_dwordx4 v[172:175], v234, s[64:67], s20 offen offset:1024 sc1\n" \
"v_mfma_f32_16x16x32_bf16 v[20:23], a[132:135], v[200:203], v[20:23]\n" \
"ds_read_b64_tr_b16 a[166:167], v238 offset:59392\n" \
"v_mfma_f32_16x16x32_bf16 v[12:15], a[132:135], v[196:199], v[12:15]\n" \
"ds_read_b64_tr_b16 a[168:169], v239 offset:57344\n" \
"s_xor_b32 s60, s91, 1\n" \
"s_add_u32 s60, s60, s98\n" \
"v_mfma_f32_16x16x32_bf16 v[4:7], a[132:135], v[192:195], v[4:7]\n" \
"ds_read_b64_tr_b16 a[170:171], v239 offset:59392\n" \
"v_mfma_f32_16x16x32_bf16 v[32:35], a[136:139], v[192:195], v[32:35]\n" \
"buffer_load_dwordx4 v[176:179], v233, s[64:67], s21 offen offset:0 sc1\n" \
"v_mfma_f32_16x16x32_bf16 v[40:43], a[136:139], v[196:199], v[40:43]\n" \
"v_readlane_b32 s22, v241, 34\n" \
"s_mul_i32 s60, s60, 49152\n" \
"s_add_u32 s40, s60, 20971520\n" \
"v_mfma_f32_16x16x32_bf16 v[48:51], a[136:139], v[200:203], v[48:51]\n" \
"ds_read_b64_tr_b16 a[172:173], v240 offset:57344\n" \
"v_mfma_f32_16x16x32_bf16 v[56:59], a[136:139], v[204:207], v[56:59]\n" \
"buffer_load_dwordx4 v[180:183], v233, s[64:67], s21 offen offset:1024 sc1\n" \
"v_mfma_f32_16x16x32_bf16 v[60:63], a[140:143], v[204:207], v[60:63]\n" \
"ds_read_b64_tr_b16 a[174:175], v240 offset:59392\n" \
"s_xor_b32 s60, s91, 3\n" \
"s_add_u32 s60, s60, s98\n" \
"v_mfma_f32_16x16x32_bf16 v[52:55], a[140:143], v[200:203], v[52:55]\n" \
"ds_read_b64_tr_b16 a[176:177], v237 offset:61440\n" \
"v_mfma_f32_16x16x32_bf16 v[44:47], a[140:143], v[196:199], v[44:47]\n" \
"ds_read_b64_tr_b16 a[178:179], v237 offset:63488\n" \
"s_mul_i32 s60, s60, 49152\n" \
"s_add_u32 s42, s60, 21004288\n" \
"v_mfma_f32_16x16x32_bf16 v[36:39], a[140:143], v[192:195], v[36:39]\n" \
"buffer_load_dwordx4 v[184:187], v234, s[64:67], s21 offen offset:0 sc1\n" \
"v_mfma_f32_16x16x32_bf16 v[64:67], a[144:147], v[192:195], v[64:67]\n" \
"ds_read_b64_tr_b16 a[180:181], v238 offset:61440\n" \
"v_mfma_f32_16x16x32_bf16 v[72:75], a[144:147], v[196:199], v[72:75]\n" \
"s_add_u32 s60, s98, s91\n" \
"s_mul_i32 s60, s60, 49152\n" \
"v_mfma_f32_16x16x32_bf16 v[80:83], a[144:147], v[200:203], v[80:83]\n" \
"ds_read_b64_tr_b16 a[182:183], v238 offset:63488\n" \
"v_mfma_f32_16x16x32_bf16 v[88:91], a[144:147], v[204:207], v[88:91]\n" \
"buffer_load_dwordx4 v[188:191], v234, s[64:67], s21 offen offset:1024 sc1\n" \
"v_mfma_f32_16x16x32_bf16 v[92:95], a[148:151], v[204:207], v[92:95]\n" \
"ds_read_b64_tr_b16 a[184:185], v239 offset:61440\n" \
"s_add_u32 s43, s60, 20971520\n" \
"s_add_u32 s44, s43, 16384\n" \
"v_mfma_f32_16x16x32_bf16 v[84:87], a[148:151], v[200:203], v[84:87]\n" \
"s_add_u32 m0, s80, 131072\n" \
"ds_read_b64_tr_b16 a[186:187], v239 offset:63488\n" \
"v_mfma_f32_16x16x32_bf16 v[76:79], a[148:151], v[196:199], v[76:79]\n" \
"buffer_load_dwordx4 v235, s[68:71], s24 offen lds\n" \
"v_mfma_f32_16x16x32_bf16 v[68:71], a[148:151], v[192:195], v[68:71]\n" \
"v_readlane_b32 s25, v254, 37\n" \
"ds_read_b64_tr_b16 a[188:189], v240 offset:61440\n" \
"v_mfma_f32_16x16x32_bf16 v[96:99], a[152:155], v[192:195], v[96:99]\n" \
"ds_read_b64_tr_b16 a[190:191], v240 offset:63488\n" \
"v_mfma_f32_16x16x32_bf16 v[104:107], a[152:155], v[196:199], v[104:107]\n" \
"s_add_u32 m0, s80, 135168\n" \
"s_add_u32 s45, s43, 32768\n" \
"s_mov_b32 s47, 0\n" \
"v_mfma_f32_16x16x32_bf16 v[112:115], a[152:155], v[200:203], v[112:115]\n" \
"buffer_load_dwordx4 v236, s[68:71], s24 offen lds\n" \
"v_mfma_f32_16x16x32_bf16 v[120:123], a[152:155], v[204:207], v[120:123]\n" \
"v_mfma_f32_16x16x32_bf16 v[124:127], a[156:159], v[204:207], v[124:127]\n" \
"s_lshl_b32 s60, s91, 7\n" \
"s_add_u32 s46, s60, s99\n" \
"v_mfma_f32_16x16x32_bf16 v[116:119], a[156:159], v[200:203], v[116:119]\n" \
"v_mfma_f32_16x16x32_bf16 v[108:111], a[156:159], v[196:199], v[108:111]\n" \
"v_accvgpr_read_b32 v253, a240\n" \
"v_mfma_f32_16x16x32_bf16 v[100:103], a[156:159], v[192:195], v[100:103]\n" \
"s_waitcnt vmcnt(58) lgkmcnt(0)\n" \
"s_barrier\n" \
"v_mfma_f32_16x16x32_bf16 v[128:131], a[160:163], v[208:211], 0\n" \
"ds_read_b64_tr_b16 a[128:129], v224 offset:0\n" \
"ds_read_b64_tr_b16 a[130:131], v224 offset:2048\n" \
"v_cvt_pk_bf16_f32 v16, v16, v17\n" \
"v_cvt_pk_bf16_f32 v17, v18, v19\n" \
"v_mfma_f32_16x16x32_bf16 v[136:139], a[160:163], v[212:215], 0\n" \
"ds_read_b64_tr_b16 a[132:133], v225 offset:0\n" \
"v_cvt_pk_bf16_f32 v18, v20, v21\n" \
"v_mfma_f32_16x16x32_bf16 v[140:143], a[164:167], v[212:215], 0\n" \
"ds_read_b64_tr_b16 a[134:135], v225 offset:2048\n" \
"v_cvt_pk_bf16_f32 v19, v22, v23\n" \
"v_mfma_f32_16x16x32_bf16 v[132:135], a[164:167], v[208:211], 0\n" \
"ds_read_b64_tr_b16 a[136:137], v226 offset:0\n" \
"buffer_store_dwordx4 v[16:19], v232, s[64:67], s41 offen offset:0 sc1\n" \
"v_mfma_f32_16x16x32_bf16 v[144:147], a[168:171], v[208:211], 0\n" \
"ds_read_b64_tr_b16 a[138:139], v226 offset:2048\n" \
"v_cvt_pk_bf16_f32 v24, v24, v25\n" \
"v_cvt_pk_bf16_f32 v25, v26, v27\n" \
"v_mfma_f32_16x16x32_bf16 v[152:155], a[168:171], v[212:215], 0\n" \
"ds_read_b64_tr_b16 a[140:141], v227 offset:0\n" \
"v_cvt_pk_bf16_f32 v26, v28, v29\n" \
"v_mfma_f32_16x16x32_bf16 v[156:159], a[172:175], v[212:215], 0\n" \
"ds_read_b64_tr_b16 a[142:143], v227 offset:2048\n" \
"v_cvt_pk_bf16_f32 v27, v30, v31\n" \
"v_mfma_f32_16x16x32_bf16 v[148:151], a[172:175], v[208:211], 0\n" \
"ds_read_b64_tr_b16 a[144:145], v224 offset:4096\n" \
"buffer_store_dwordx4 v[24:27], v232, s[64:67], s41 offen offset:2048 sc1\n" \
"v_mfma_f32_16x16x32_bf16 v[128:131], a[176:179], v[216:219], v[128:131]\n" \
"ds_read_b64_tr_b16 a[146:147], v224 offset:6144\n" \
"v_cvt_pk_bf16_f32 v48, v48, v49\n" \
"v_cvt_pk_bf16_f32 v49, v50, v51\n" \
"v_mfma_f32_16x16x32_bf16 v[136:139], a[176:179], v[220:223], v[136:139]\n" \
"ds_read_b64_tr_b16 a[148:149], v225 offset:4096\n" \
"v_cvt_pk_bf16_f32 v50, v52, v53\n" \
"v_mfma_f32_16x16x32_bf16 v[140:143], a[180:183], v[220:223], v[140:143]\n" \
"ds_read_b64_tr_b16 a[150:151], v225 offset:6144\n" \
"v_cvt_pk_bf16_f32 v51, v54, v55\n" \
"v_mfma_f32_16x16x32_bf16 v[132:135], a[180:183], v[216:219], v[132:135]\n" \
"ds_read_b64_tr_b16 a[152:153], v226 offset:4096\n" \
"buffer_store_dwordx4 v[48:51], v232, s[64:67], s41 offen offset:1024 sc1\n" \
"v_mfma_f32_16x16x32_bf16 v[144:147], a[184:187], v[216:219], v[144:147]\n" \
"ds_read_b64_tr_b16 a[154:155], v226 offset:6144\n" \
"v_cvt_pk_bf16_f32 v56, v56, v57\n" \
"v_cvt_pk_bf16_f32 v57, v58, v59\n" \
"v_mfma_f32_16x16x32_bf16 v[152:155], a[184:187], v[220:223], v[152:155]\n" \
"ds_read_b64_tr_b16 a[156:157], v227 offset:4096\n" \
"v_cvt_pk_bf16_f32 v58, v60, v61\n" \
"v_mfma_f32_16x16x32_bf16 v[156:159], a[188:191], v[220:223], v[156:159]\n" \
"ds_read_b64_tr_b16 a[158:159], v227 offset:6144\n" \
"v_cvt_pk_bf16_f32 v59, v62, v63\n" \
"v_mfma_f32_16x16x32_bf16 v[148:151], a[188:191], v[216:219], v[148:151]\n" \
"buffer_store_dwordx4 v[56:59], v232, s[64:67], s41 offen offset:3072 sc1\n" \
"s_waitcnt vmcnt(58) lgkmcnt(8)\n" \
"v_mfma_f32_16x16x32_bf16 v[128:131], a[128:131], a[0:3], v[128:131]\n" \
"ds_read_b64_tr_b16 a[160:161], v224 offset:8192\n" \
"ds_read_b64_tr_b16 a[162:163], v224 offset:10240\n" \
"v_cvt_pk_bf16_f32 v64, v64, v65\n" \
"v_cvt_pk_bf16_f32 v65, v66, v67\n" \
"v_mfma_f32_16x16x32_bf16 v[136:139], a[128:131], a[4:7], v[136:139]\n" \
"ds_read_b64_tr_b16 a[164:165], v225 offset:8192\n" \
"v_cvt_pk_bf16_f32 v66, v68, v69\n" \
"v_mfma_f32_16x16x32_bf16 v[140:143], a[132:135], a[4:7], v[140:143]\n" \
"ds_read_b64_tr_b16 a[166:167], v225 offset:10240\n" \
"Lpadq3_%=:\n" \
"s_cbranch_vccnz Lprq3_%=\n" \
"v_cvt_pk_bf16_f32 v67, v70, v71\n" \
"v_mfma_f32_16x16x32_bf16 v[132:135], a[132:135], a[0:3], v[132:135]\n" \
"ds_read_b64_tr_b16 a[168:169], v226 offset:8192\n" \
"buffer_store_dwordx4 v[64:67], v232, s[64:67], s40 offen offset:0 sc1\n" \
"v_mfma_f32_16x16x32_bf16 v[144:147], a[136:139], a[0:3], v[144:147]\n" \
"ds_read_b64_tr_b16 a[170:171], v226 offset:10240\n" \
"v_cvt_pk_bf16_f32 v72, v72, v73\n" \
"v_cvt_pk_bf16_f32 v73, v74, v75\n" \
"v_mfma_f32_16x16x32_bf16 v[152:155], a[136:139], a[4:7], v[152:155]\n" \
"ds_read_b64_tr_b16 a[172:173], v227 offset:8192\n" \
"v_cvt_pk_bf16_f32 v74, v76, v77\n" \
"v_mfma_f32_16x16x32_bf16 v[156:159], a[140:143], a[4:7], v[156:159]\n" \
"ds_read_b64_tr_b16 a[174:175], v227 offset:10240\n" \
"v_cvt_pk_bf16_f32 v75, v78, v79\n" \
"v_mfma_f32_16x16x32_bf16 v[148:151], a[140:143], a[0:3], v[148:151]\n" \
"ds_read_b64_tr_b16 a[176:177], v224 offset:12288\n" \
"buffer_store_dwordx4 v[72:75], v232, s[64:67], s40 offen offset:2048 sc1\n" \
"s_waitcnt lgkmcnt(9)\n" \
"v_mfma_f32_16x16x32_bf16 v[128:131], a[144:147], a[8:11], v[128:131]\n" \
"ds_read_b64_tr_b16 a[178:179], v224 offset:14336\n" \
"v_cvt_pk_bf16_f32 v96, v96, v97\n" \
"v_cvt_pk_bf16_f32 v97, v98, v99\n" \
"v_mfma_f32_16x16x32_bf16 v[136:139], a[144:147], a[12:15], v[136:139]\n" \
"ds_read_b64_tr_b16 a[180:181], v225 offset:12288\n" \
"v_cvt_pk_bf16_f32 v98, v100, v101\n" \
"v_mfma_f32_16x16x32_bf16 v[140:143], a[148:151], a[12:15], v[140:143]\n" \
"ds_read_b64_tr_b16 a[182:183], v225 offset:14336\n" \
"v_cvt_pk_bf16_f32 v99, v102, v103\n" \
"v_mfma_f32_16x16x32_bf16 v[132:135], a[148:151], a[8:11], v[132:135]\n" \
"ds_read_b64_tr_b16 a[184:185], v226 offset:12288\n" \
"buffer_store_dwordx4 v[96:99], v232, s[64:67], s40 offen offset:1024 sc1\n" \
"v_mfma_f32_16x16x32_bf16 v[144:147], a[152:155], a[8:11], v[144:147]\n" \
"ds_read_b64_tr_b16 a[186:187], v226 offset:14336\n" \
"v_cvt_pk_bf16_f32 v104, v104, v105\n" \
"v_cvt_pk_bf16_f32 v105, v106, v107\n" \
"v_mfma_f32_16x16x32_bf16 v[152:155], a[152:155], a[12:15], v[152:155]\n" \
"ds_read_b64_tr_b16 a[188:189], v227 offset:12288\n" \
"v_cvt_pk_bf16_f32 v106, v108, v109\n" \
"v_mfma_f32_16x16x32_bf16 v[156:159], a[156:159], a[12:15], v[156:159]\n" \
"ds_read_b64_tr_b16 a[190:191], v227 offset:14336\n" \
"v_cvt_pk_bf16_f32 v107, v110, v111\n" \
"v_mfma_f32_16x16x32_bf16 v[148:151], a[156:159], a[8:11], v[148:151]\n" \
"buffer_store_dwordx4 v[104:107], v232, s[64:67], s40 offen offset:3072 sc1\n" \
"s_waitcnt vmcnt(58) lgkmcnt(8)\n" \
"v_mfma_f32_16x16x32_bf16 v[128:131], a[160:163], a[16:19], v[128:131]\n" \
"ds_read_b64_tr_b16 a[128:129], v224 offset:16384\n" \
"ds_read_b64_tr_b16 a[130:131], v224 offset:18432\n" \
"v_cvt_pk_bf16_f32 v80, v80, v81\n" \
"v_cvt_pk_bf16_f32 v81, v82, v83\n" \
"v_mfma_f32_16x16x32_bf16 v[136:139], a[160:163], a[20:23], v[136:139]\n" \
"ds_read_b64_tr_b16 a[132:133], v225 offset:16384\n" \
"v_cvt_pk_bf16_f32 v82, v84, v85\n" \
"v_mfma_f32_16x16x32_bf16 v[140:143], a[164:167], a[20:23], v[140:143]\n" \
"ds_read_b64_tr_b16 a[134:135], v225 offset:18432\n" \
"v_cvt_pk_bf16_f32 v83, v86, v87\n" \
"v_mfma_f32_16x16x32_bf16 v[132:135], a[164:167], a[16:19], v[132:135]\n" \
"ds_read_b64_tr_b16 a[136:137], v226 offset:16384\n" \
"buffer_store_dwordx4 v[80:83], v232, s[64:67], s42 offen offset:0 sc1\n" \
"v_mfma_f32_16x16x32_bf16 v[144:147], a[168:171], a[16:19], v[144:147]\n" \
"ds_read_b64_tr_b16 a[138:139], v226 offset:18432\n" \
"v_cvt_pk_bf16_f32 v88, v88, v89\n" \
"v_cvt_pk_bf16_f32 v89, v90, v91\n" \
"v_mfma_f32_16x16x32_bf16 v[152:155], a[168:171], a[20:23], v[152:155]\n" \
"ds_read_b64_tr_b16 a[140:141], v227 offset:16384\n" \
"v_cvt_pk_bf16_f32 v90, v92, v93\n" \
"v_mfma_f32_16x16x32_bf16 v[156:159], a[172:175], a[20:23], v[156:159]\n" \
"ds_read_b64_tr_b16 a[142:143], v227 offset:18432\n" \
"v_cvt_pk_bf16_f32 v91, v94, v95\n" \
"v_mfma_f32_16x16x32_bf16 v[148:151], a[172:175], a[16:19], v[148:151]\n" \
"ds_read_b64_tr_b16 a[144:145], v224 offset:20480\n" \
"buffer_store_dwordx4 v[88:91], v232, s[64:67], s42 offen offset:2048 sc1\n" \
"s_waitcnt lgkmcnt(9)\n" \
"v_mfma_f32_16x16x32_bf16 v[128:131], a[176:179], a[24:27], v[128:131]\n" \
"ds_read_b64_tr_b16 a[146:147], v224 offset:22528\n" \
"v_cvt_pk_bf16_f32 v112, v112, v113\n" \
"v_cvt_pk_bf16_f32 v113, v114, v115\n" \
"v_mfma_f32_16x16x32_bf16 v[136:139], a[176:179], a[28:31], v[136:139]\n" \
"ds_read_b64_tr_b16 a[148:149], v225 offset:20480\n" \
"v_cvt_pk_bf16_f32 v114, v116, v117\n" \
"v_mfma_f32_16x16x32_bf16 v[140:143], a[180:183], a[28:31], v[140:143]\n" \
"ds_read_b64_tr_b16 a[150:151], v225 offset:22528\n" \
"v_cvt_pk_bf16_f32 v115, v118, v119\n" \
"v_mfma_f32_16x16x32_bf16 v[132:135], a[180:183], a[24:27], v[132:135]\n" \
"ds_read_b64_tr_b16 a[152:153], v226 offset:20480\n" \
"buffer_store_dwordx4 v[112:115], v232, s[64:67], s42 offen offset:1024 sc1\n" \
"v_mfma_f32_16x16x32_bf16 v[144:147], a[184:187], a[24:27], v[144:147]\n" \
"ds_read_b64_tr_b16 a[154:155], v226 offset:22528\n" \
"v_cvt_pk_bf16_f32 v120, v120, v121\n" \
"v_cvt_pk_bf16_f32 v121, v122, v123\n" \
"v_mfma_f32_16x16x32_bf16 v[152:155], a[184:187], a[28:31], v[152:155]\n" \
"ds_read_b64_tr_b16 a[156:157], v227 offset:20480\n" \
"v_cvt_pk_bf16_f32 v122, v124, v125\n" \
"v_mfma_f32_16x16x32_bf16 v[156:159], a[188:191], a[28:31], v[156:159]\n" \
"ds_read_b64_tr_b16 a[158:159], v227 offset:22528\n" \
"v_cvt_pk_bf16_f32 v123, v126, v127\n" \
"v_mfma_f32_16x16x32_bf16 v[148:151], a[188:191], a[24:27], v[148:151]\n" \
"buffer_store_dwordx4 v[120:123], v232, s[64:67], s42 offen offset:3072 sc1\n" \
"s_waitcnt vmcnt(50) lgkmcnt(8)\n" \
"v_mfma_f32_16x16x32_bf16 v[128:131], a[128:131], a[32:35], v[128:131]\n" \
"s_add_u32 m0, s80, 139264\n" \
"ds_read_b64_tr_b16 a[160:161], v224 offset:24576\n" \
"v_mfma_f32_16x16x32_bf16 v[136:139], a[128:131], a[36:39], v[136:139]\n" \
"buffer_load_dwordx4 v235, s[68:71], s25 offen lds\n" \
"v_mfma_f32_16x16x32_bf16 v[140:143], a[132:135], a[36:39], v[140:143]\n" \
"v_readlane_b32 s26, v254, 38\n" \
"ds_read_b64_tr_b16 a[162:163], v224 offset:26624\n" \
"v_mfma_f32_16x16x32_bf16 v[132:135], a[132:135], a[32:35], v[132:135]\n" \
"ds_read_b64_tr_b16 a[164:165], v225 offset:24576\n" \
"v_mfma_f32_16x16x32_bf16 v[144:147], a[136:139], a[32:35], v[144:147]\n" \
"ds_read_b64_tr_b16 a[166:167], v225 offset:26624\n" \
"ds_read_b64_tr_b16 a[168:169], v226 offset:24576\n" \
"v_mfma_f32_16x16x32_bf16 v[152:155], a[136:139], a[36:39], v[152:155]\n" \
"ds_read_b64_tr_b16 a[170:171], v226 offset:26624\n" \
"v_mfma_f32_16x16x32_bf16 v[156:159], a[140:143], a[36:39], v[156:159]\n" \
"ds_read_b64_tr_b16 a[172:173], v227 offset:24576\n" \
"v_mfma_f32_16x16x32_bf16 v[148:151], a[140:143], a[32:35], v[148:151]\n" \
"s_add_u32 m0, s80, 143360\n" \
"ds_read_b64_tr_b16 a[174:175], v227 offset:26624\n" \
"s_waitcnt lgkmcnt(8)\n" \
"v_mfma_f32_16x16x32_bf16 v[128:131], a[144:147], a[40:43], v[128:131]\n" \
"buffer_load_dwordx4 v236, s[68:71], s25 offen lds\n" \
"v_mfma_f32_16x16x32_bf16 v[136:139], a[144:147], a[44:47], v[136:139]\n" \
"ds_read_b64_tr_b16 a[176:177], v224 offset:28672\n" \
"ds_read_b64_tr_b16 a[178:179], v224 offset:30720\n" \
"v_mfma_f32_16x16x32_bf16 v[140:143], a[148:151], a[44:47], v[140:143]\n" \
"ds_read_b64_tr_b16 a[180:181], v225 offset:28672\n" \
"v_mfma_f32_16x16x32_bf16 v[132:135], a[148:151], a[40:43], v[132:135]\n" \
"ds_read_b64_tr_b16 a[182:183], v225 offset:30720\n" \
"v_mfma_f32_16x16x32_bf16 v[144:147], a[152:155], a[40:43], v[144:147]\n" \
"ds_read_b64_tr_b16 a[184:185], v226 offset:28672\n" \
"ds_read_b64_tr_b16 a[186:187], v226 offset:30720\n" \
"v_mfma_f32_16x16x32_bf16 v[152:155], a[152:155], a[44:47], v[152:155]\n" \
"ds_read_b64_tr_b16 a[188:189], v227 offset:28672\n" \
"v_mfma_f32_16x16x32_bf16 v[156:159], a[156:159], a[44:47], v[156:159]\n" \
"ds_read_b64_tr_b16 a[190:191], v227 offset:30720\n" \
"v_mfma_f32_16x16x32_bf16 v[148:151], a[156:159], a[40:43], v[148:151]\n" \
"s_waitcnt vmcnt(48) lgkmcnt(0)\n" \
"s_barrier\n" \
"v_mfma_f32_16x16x32_bf16 v[128:131], a[160:163], a[48:51], v[128:131]\n" \
"s_add_u32 m0, s80, 147456\n" \
"ds_read_b64_tr_b16 a[128:129], v224 offset:32768\n" \
"v_mbcnt_lo_u32_b32 v118, -1, 0\n" \
"v_mbcnt_hi_u32_b32 v118, -1, v118\n" \
"v_mfma_f32_16x16x32_bf16 v[136:139], a[160:163], a[52:55], v[136:139]\n" \
"buffer_load_dwordx4 v235, s[68:71], s26 offen lds\n" \
"v_mov_b64 v[112:113], 0\n" \
"v_mfma_f32_16x16x32_bf16 v[140:143], a[164:167], a[52:55], v[140:143]\n" \
"ds_read_b64_tr_b16 a[130:131], v224 offset:34816\n" \
"ds_read_b64_tr_b16 a[132:133], v225 offset:32768\n" \
"v_and_b32 v119, 15, v118\n" \
"v_mfma_f32_16x16x32_bf16 v[132:135], a[164:167], a[48:51], v[132:135]\n" \
"s_add_u32 m0, s80, 151552\n" \
"ds_read_b64_tr_b16 a[134:135], v225 offset:34816\n" \
"v_lshrrev_b32 v118, 4, v118\n" \
"v_mfma_f32_16x16x32_bf16 v[144:147], a[168:171], a[48:51], v[144:147]\n" \
"buffer_load_dwordx4 v236, s[68:71], s26 offen lds\n" \
"v_lshlrev_b32 v118, 2, v118\n" \
"v_mfma_f32_16x16x32_bf16 v[152:155], a[168:171], a[52:55], v[152:155]\n" \
"ds_read_b64_tr_b16 a[136:137], v226 offset:32768\n" \
"ds_read_b64_tr_b16 a[138:139], v226 offset:34816\n" \
"v_sub_u32 v118, v119, v118\n" \
"v_mfma_f32_16x16x32_bf16 v[156:159], a[172:175], a[52:55], v[156:159]\n" \
"ds_read_b64_tr_b16 a[140:141], v227 offset:32768\n" \
"v_and_b32 v119, 1, v118\n" \
"v_mfma_f32_16x16x32_bf16 v[148:151], a[172:175], a[48:51], v[148:151]\n" \
"ds_read_b64_tr_b16 a[142:143], v227 offset:34816\n" \
"ds_read_b64_tr_b16 a[144:145], v224 offset:36864\n" \
"v_lshlrev_b32 v119, 4, v119\n" \
"v_mfma_f32_16x16x32_bf16 v[128:131], a[176:179], a[56:59], v[128:131]\n" \
"buffer_load_dwordx4 v[192:195], v233, s[64:67], s22 offen offset:0 sc1\n" \
"v_mov_b32 v116, 0x3f80\n" \
"v_lshlrev_b32 v119, v119, v116\n" \
"v_mfma_f32_16x16x32_bf16 v[136:139], a[176:179], a[60:63], v[136:139]\n" \
"v_readlane_b32 s23, v241, 35\n" \
"ds_read_b64_tr_b16 a[146:147], v224 offset:38912\n" \
"v_lshrrev_b32 v118, 1, v118\n" \
"v_mfma_f32_16x16x32_bf16 v[140:143], a[180:183], a[60:63], v[140:143]\n" \
"ds_read_b64_tr_b16 a[148:149], v225 offset:36864\n" \
"v_cmp_eq_u32 s[30:31], 0, v118\n" \
"v_mfma_f32_16x16x32_bf16 v[132:135], a[180:183], a[56:59], v[132:135]\n" \
"buffer_load_dwordx4 v[196:199], v233, s[64:67], s22 offen offset:1024 sc1\n" \
"v_cmp_eq_u32 s[28:29], 1, v118\n" \
"v_mfma_f32_16x16x32_bf16 v[144:147], a[184:187], a[56:59], v[144:147]\n" \
"ds_read_b64_tr_b16 a[150:151], v225 offset:38912\n" \
"ds_read_b64_tr_b16 a[152:153], v226 offset:36864\n" \
"v_mov_b64 v[116:117], 0\n" \
"v_mfma_f32_16x16x32_bf16 v[152:155], a[184:187], a[60:63], v[152:155]\n" \
"ds_read_b64_tr_b16 a[154:155], v226 offset:38912\n" \
"ds_read_b64_tr_b16 a[156:157], v227 offset:36864\n" \
"s_nop 0\n" \
"v_mfma_f32_16x16x32_bf16 v[156:159], a[188:191], a[60:63], v[156:159]\n" \
"ds_read_b64_tr_b16 a[158:159], v227 offset:38912\n" \
"v_cndmask_b32 v114, v112, v119, s[30:31]\n" \
"v_mfma_f32_16x16x32_bf16 v[148:151], a[188:191], a[56:59], v[148:151]\n" \
"v_cndmask_b32 v115, v112, v119, s[28:29]\n" \
"s_waitcnt vmcnt(48) lgkmcnt(8)\n" \
"v_mfma_f32_16x16x32_bf16 v[128:131], a[128:131], a[64:67], v[128:131]\n" \
"ds_read_b64_tr_b16 a[160:161], v224 offset:40960\n" \
"ds_read_b64_tr_b16 a[162:163], v224 offset:43008\n" \
"v_mfma_f32_16x16x32_bf16 v[136:139], a[128:131], a[68:71], v[136:139]\n" \
"buffer_load_dwordx4 v[200:203], v234, s[64:67], s22 offen offset:0 sc1\n" \
"v_mfma_f32_16x16x32_bf16 v[140:143], a[132:135], a[68:71], v[140:143]\n" \
"ds_read_b64_tr_b16 a[164:165], v225 offset:40960\n" \
"v_mfma_f32_16x16x32_bf16 v[132:135], a[132:135], a[64:67], v[132:135]\n" \
"ds_read_b64_tr_b16 a[166:167], v225 offset:43008\n" \
"ds_read_b64_tr_b16 a[168:169], v226 offset:40960\n" \
"v_mfma_f32_16x16x32_bf16 v[144:147], a[136:139], a[64:67], v[144:147]\n" \
"ds_read_b64_tr_b16 a[170:171], v226 offset:43008\n" \
"v_mfma_f32_16x16x32_bf16 v[152:155], a[136:139], a[68:71], v[152:155]\n" \
"buffer_load_dwordx4 v[204:207], v234, s[64:67], s22 offen offset:1024 sc1\n" \
"v_mfma_f32_16x16x32_bf16 v[156:159], a[140:143], a[68:71], v[156:159]\n" \
"ds_read_b64_tr_b16 a[172:173], v227 offset:40960\n" \
"v_mfma_f32_16x16x32_bf16 v[148:151], a[140:143], a[64:67], v[148:151]\n" \
"ds_read_b64_tr_b16 a[174:175], v227 offset:43008\n" \
"ds_read_b64_tr_b16 a[176:177], v224 offset:45056\n" \
"s_waitcnt lgkmcnt(9)\n" \
"v_mfma_f32_16x16x32_bf16 v[128:131], a[144:147], a[72:75], v[128:131]\n" \
"ds_read_b64_tr_b16 a[178:179], v224 offset:47104\n" \
"v_mfma_f32_16x16x32_bf16 v[136:139], a[144:147], a[76:79], v[136:139]\n" \
"ds_read_b64_tr_b16 a[180:181], v225 offset:45056\n" \
"ds_read_b64_tr_b16 a[182:183], v225 offset:47104\n" \
"v_mfma_f32_16x16x32_bf16 v[140:143], a[148:151], a[76:79], v[140:143]\n" \
"buffer_load_dwordx4 v[208:211], v233, s[64:67], s23 offen offset:0 sc1\n" \
"v_mfma_f32_16x16x32_bf16 v[132:135], a[148:151], a[72:75], v[132:135]\n" \
"v_readlane_b32 s20, v241, 36\n" \
"v_mfma_f32_16x16x32_bf16 v[144:147], a[152:155], a[72:75], v[144:147]\n" \
"ds_read_b64_tr_b16 a[184:185], v226 offset:45056\n" \
"ds_read_b64_tr_b16 a[186:187], v226 offset:47104\n" \
"v_mfma_f32_16x16x32_bf16 v[152:155], a[152:155], a[76:79], v[152:155]\n" \
"ds_read_b64_tr_b16 a[188:189], v227 offset:45056\n" \
"v_mfma_f32_16x16x32_bf16 v[156:159], a[156:159], a[76:79], v[156:159]\n" \
"ds_read_b64_tr_b16 a[190:191], v227 offset:47104\n" \
"v_mfma_f32_16x16x32_bf16 v[148:151], a[156:159], a[72:75], v[148:151]\n" \
"s_waitcnt vmcnt(47) lgkmcnt(8)\n" \
"v_mfma_f32_16x16x32_bf16 v[128:131], a[160:163], a[80:83], v[128:131]\n" \
"ds_read_b64_tr_b16 a[128:129], v224 offset:49152\n" \
"ds_read_b64_tr_b16 a[130:131], v224 offset:51200\n" \
"v_mfma_f32_16x16x32_bf16 v[136:139], a[160:163], a[84:87], v[136:139]\n" \
"buffer_load_dwordx4 v[212:215], v233, s[64:67], s23 offen offset:1024 sc1\n" \
"ds_read_b64_tr_b16 a[132:133], v225 offset:49152\n" \
"v_mfma_f32_16x16x32_bf16 v[140:143], a[164:167], a[84:87], v[140:143]\n" \
"ds_read_b64_tr_b16 a[134:135], v225 offset:51200\n" \
"v_mfma_f32_16x16x32_bf16 v[132:135], a[164:167], a[80:83], v[132:135]\n" \
"buffer_load_dwordx4 v[216:219], v234, s[64:67], s23 offen offset:0 sc1\n" \
"ds_read_b64_tr_b16 a[136:137], v226 offset:49152\n" \
"v_mfma_f32_16x16x32_bf16 v[144:147], a[168:171], a[80:83], v[144:147]\n" \
"ds_read_b64_tr_b16 a[138:139], v226 offset:51200\n" \
"v_mfma_f32_16x16x32_bf16 v[152:155], a[168:171], a[84:87], v[152:155]\n" \
"ds_read_b64_tr_b16 a[140:141], v227 offset:49152\n" \
"v_mfma_f32_16x16x32_bf16 v[156:159], a[172:175], a[84:87], v[156:159]\n" \
"buffer_load_dwordx4 v[220:223], v234, s[64:67], s23 offen offset:1024 sc1\n" \
"ds_read_b64_tr_b16 a[142:143], v227 offset:51200\n" \
"v_mfma_f32_16x16x32_bf16 v[148:151], a[172:175], a[80:83], v[148:151]\n" \
"ds_read_b64_tr_b16 a[144:145], v224 offset:53248\n" \
"s_waitcnt lgkmcnt(9)\n" \
"v_mfma_f32_16x16x32_bf16 v[128:131], a[176:179], a[88:91], v[128:131]\n" \
"ds_read_b64_tr_b16 a[146:147], v224 offset:55296\n" \
"v_mfma_f32_16x16x32_bf16 v[136:139], a[176:179], a[92:95], v[136:139]\n" \
"buffer_load_dwordx4 a[0:3], v233, s[64:67], s20 offen offset:0 sc1\n" \
"ds_read_b64_tr_b16 a[148:149], v225 offset:53248\n" \
"v_mfma_f32_16x16x32_bf16 v[140:143], a[180:183], a[92:95], v[140:143]\n" \
"v_readlane_b32 s21, v241, 37\n" \
"ds_read_b64_tr_b16 a[150:151], v225 offset:55296\n" \
"v_mfma_f32_16x16x32_bf16 v[132:135], a[180:183], a[88:91], v[132:135]\n" \
"ds_read_b64_tr_b16 a[152:153], v226 offset:53248\n" \
"v_mfma_f32_16x16x32_bf16 v[144:147], a[184:187], a[88:91], v[144:147]\n" \
"v_mov_b32 v246, s72\n" \
"ds_read_b64_tr_b16 a[154:155], v226 offset:55296\n" \
"v_mfma_f32_16x16x32_bf16 v[152:155], a[184:187], a[92:95], v[152:155]\n" \
"v_mov_b32 v247, s73\n" \
"ds_read_b64_tr_b16 a[156:157], v227 offset:53248\n" \
"v_mfma_f32_16x16x32_bf16 v[156:159], a[188:191], a[92:95], v[156:159]\n" \
"s_waitcnt vmcnt(13)\n" \
"ds_read_b64_tr_b16 a[158:159], v227 offset:55296\n" \
"v_mfma_f32_16x16x32_bf16 v[148:151], a[188:191], a[88:91], v[148:151]\n" \
"s_mov_b64 exec, 1\n" \
"buffer_store_dwordx2 v[246:247], off, s[64:67], s46 sc1\n" \
"s_mov_b64 exec, -1\n" \
"s_waitcnt vmcnt(40) lgkmcnt(8)\n" \
"v_mfma_f32_16x16x32_bf16 v[128:131], a[128:131], a[96:99], v[128:131]\n" \
"ds_read_b64_tr_b16 a[160:161], v224 offset:57344\n" \
"ds_read_b64_tr_b16 a[162:163], v224 offset:59392\n" \
"v_mfma_f32_16x16x32_bf16 v[136:139], a[128:131], a[100:103], v[136:139]\n" \
"buffer_load_dwordx4 a[4:7], v233, s[64:67], s20 offen offset:1024 sc1\n" \
"v_mfma_f32_16x16x32_bf16 v[140:143], a[132:135], a[100:103], v[140:143]\n" \
"ds_read_b64_tr_b16 a[164:165], v225 offset:57344\n" \
"v_mfma_f32_16x16x32_bf16 v[132:135], a[132:135], a[96:99], v[132:135]\n" \
"ds_read_b64_tr_b16 a[166:167], v225 offset:59392\n" \
"v_mfma_f32_16x16x32_bf16 v[144:147], a[136:139], a[96:99], v[144:147]\n" \
"ds_read_b64_tr_b16 a[168:169], v226 offset:57344\n" \
"ds_read_b64_tr_b16 a[170:171], v226 offset:59392\n" \
"v_mfma_f32_16x16x32_bf16 v[152:155], a[136:139], a[100:103], v[152:155]\n" \
"buffer_load_dwordx4 a[8:11], v234, s[64:67], s20 offen offset:0 sc1\n" \
"v_mfma_f32_16x16x32_bf16 v[156:159], a[140:143], a[100:103], v[156:159]\n" \
"ds_read_b64_tr_b16 a[172:173], v227 offset:57344\n" \
"v_mfma_f32_16x16x32_bf16 v[148:151], a[140:143], a[96:99], v[148:151]\n" \
"ds_read_b64_tr_b16 a[174:175], v227 offset:59392\n" \
"s_waitcnt lgkmcnt(8)\n" \
"v_mfma_f32_16x16x32_bf16 v[128:131], a[144:147], a[104:107], v[128:131]\n" \
"ds_read_b64_tr_b16 a[176:177], v224 offset:61440\n" \
"ds_read_b64_tr_b16 a[178:179], v224 offset:63488\n" \
"v_mfma_f32_16x16x32_bf16 v[136:139], a[144:147], a[108:111], v[136:139]\n" \
"ds_read_b64_tr_b16 a[180:181], v225 offset:61440\n" \
"v_mfma_f32_16x16x32_bf16 v[140:143], a[148:151], a[108:111], v[140:143]\n" \
"buffer_load_dwordx4 a[12:15], v234, s[64:67], s20 offen offset:1024 sc1\n" \
"v_mfma_f32_16x16x32_bf16 v[132:135], a[148:151], a[104:107], v[132:135]\n" \
"ds_read_b64_tr_b16 a[182:183], v225 offset:63488\n" \
"v_mfma_f32_16x16x32_bf16 v[144:147], a[152:155], a[104:107], v[144:147]\n" \
"ds_read_b64_tr_b16 a[184:185], v226 offset:61440\n" \
"ds_read_b64_tr_b16 a[186:187], v226 offset:63488\n" \
"v_mfma_f32_16x16x32_bf16 v[152:155], a[152:155], a[108:111], v[152:155]\n" \
"ds_read_b64_tr_b16 a[188:189], v227 offset:61440\n" \
"v_mfma_f32_16x16x32_bf16 v[156:159], a[156:159], a[108:111], v[156:159]\n" \
"ds_read_b64_tr_b16 a[190:191], v227 offset:63488\n" \
"v_mfma_f32_16x16x32_bf16 v[148:151], a[156:159], a[104:107], v[148:151]\n" \
"s_waitcnt vmcnt(39) lgkmcnt(0)\n" \
"s_barrier\n" \
"v_mfma_f32_16x16x32_bf16 v[128:131], a[160:163], a[112:115], v[128:131]\n" \
"ds_read_b64_tr_b16 a[128:129], v237 offset:0\n" \
"ds_read_b64_tr_b16 a[130:131], v237 offset:2048\n" \
"v_mfma_f32_16x16x32_bf16 v[136:139], a[160:163], a[116:119], v[136:139]\n" \
"buffer_load_dwordx2 v[250:251], v253, s[64:67], s99 offen sc1\n" \
"v_mfma_f32_16x16x32_bf16 v[140:143], a[164:167], a[116:119], v[140:143]\n" \
"ds_read_b64_tr_b16 a[132:133], v238 offset:0\n" \
"ds_read_b64_tr_b16 a[134:135], v238 offset:2048\n" \
"v_mfma_f32_16x16x32_bf16 v[132:135], a[164:167], a[112:115], v[132:135]\n" \
"ds_read_b64_tr_b16 a[136:137], v239 offset:0\n" \
"v_mfma_f32_16x16x32_bf16 v[144:147], a[168:171], a[112:115], v[144:147]\n" \
"buffer_load_dwordx4 a[16:19], v233, s[64:67], s21 offen offset:0 sc1\n" \
"v_mfma_f32_16x16x32_bf16 v[152:155], a[168:171], a[116:119], v[152:155]\n" \
"v_readlane_b32 s22, v241, 38\n" \
"ds_read_b64_tr_b16 a[138:139], v239 offset:2048\n" \
"v_mfma_f32_16x16x32_bf16 v[156:159], a[172:175], a[116:119], v[156:159]\n" \
"ds_read_b64_tr_b16 a[140:141], v240 offset:0\n" \
"v_mfma_f32_16x16x32_bf16 v[148:151], a[172:175], a[112:115], v[148:151]\n" \
"ds_read_b64_tr_b16 a[142:143], v240 offset:2048\n" \
"ds_read_b64_tr_b16 a[144:145], v237 offset:4096\n" \
"v_mfma_f32_16x16x32_bf16 v[128:131], a[176:179], a[120:123], v[128:131]\n" \
"buffer_load_dwordx4 a[20:23], v233, s[64:67], s21 offen offset:1024 sc1\n" \
"v_mfma_f32_16x16x32_bf16 v[136:139], a[176:179], a[124:127], v[136:139]\n" \
"ds_read_b64_tr_b16 a[146:147], v237 offset:6144\n" \
"v_mfma_f32_16x16x32_bf16 v[140:143], a[180:183], a[124:127], v[140:143]\n" \
"ds_read_b64_tr_b16 a[148:149], v238 offset:4096\n" \
"ds_read_b64_tr_b16 a[150:151], v238 offset:6144\n" \
"v_mfma_f32_16x16x32_bf16 v[132:135], a[180:183], a[120:123], v[132:135]\n" \
"buffer_load_dwordx4 a[24:27], v234, s[64:67], s21 offen offset:0 sc1\n" \
"v_mfma_f32_16x16x32_bf16 v[144:147], a[184:187], a[120:123], v[144:147]\n" \
"ds_read_b64_tr_b16 a[152:153], v239 offset:4096\n" \
"v_mfma_f32_16x16x32_bf16 v[152:155], a[184:187], a[124:127], v[152:155]\n" \
"ds_read_b64_tr_b16 a[154:155], v239 offset:6144\n" \
"ds_read_b64_tr_b16 a[156:157], v240 offset:4096\n" \
"v_mfma_f32_16x16x32_bf16 v[156:159], a[188:191], a[124:127], v[156:159]\n" \
"ds_read_b64_tr_b16 a[158:159], v240 offset:6144\n" \
"v_mfma_f32_16x16x32_bf16 v[148:151], a[188:191], a[120:123], v[148:151]\n" \
"s_waitcnt vmcnt(39) lgkmcnt(8)\n" \
"v_mfma_f32_16x16x32_bf16 v[128:131], a[128:131], v[160:163], v[128:131]\n" \
"ds_read_b64_tr_b16 a[160:161], v237 offset:8192\n" \
"ds_read_b64_tr_b16 a[162:163], v237 offset:10240\n" \
"v_mfma_f32_16x16x32_bf16 v[136:139], a[128:131], v[164:167], v[136:139]\n" \
"buffer_load_dwordx4 a[28:31], v234, s[64:67], s21 offen offset:1024 sc1\n" \
"v_mfma_f32_16x16x32_bf16 v[140:143], a[132:135], v[164:167], v[140:143]\n" \
"ds_read_b64_tr_b16 a[164:165], v238 offset:8192\n" \
"ds_read_b64_tr_b16 a[166:167], v238 offset:10240\n" \
"v_mfma_f32_16x16x32_bf16 v[132:135], a[132:135], v[160:163], v[132:135]\n" \
"ds_read_b64_tr_b16 a[168:169], v239 offset:8192\n" \
"ds_read_b64_tr_b16 a[170:171], v239 offset:10240\n" \
"v_mfma_f32_16x16x32_bf16 v[144:147], a[136:139], v[160:163], v[144:147]\n" \
"buffer_load_dwordx4 a[32:35], v233, s[64:67], s22 offen offset:0 sc1\n" \
"v_mfma_f32_16x16x32_bf16 v[152:155], a[136:139], v[164:167], v[152:155]\n" \
"ds_read_b64_tr_b16 a[172:173], v240 offset:8192\n" \
"ds_read_b64_tr_b16 a[174:175], v240 offset:10240\n" \
"v_mfma_f32_16x16x32_bf16 v[156:159], a[140:143], v[164:167], v[156:159]\n" \
"ds_read_b64_tr_b16 a[176:177], v237 offset:12288\n" \
"v_mfma_f32_16x16x32_bf16 v[148:151], a[140:143], v[160:163], v[148:151]\n" \
"ds_read_b64_tr_b16 a[178:179], v237 offset:14336\n" \
"ds_read_b64_tr_b16 a[180:181], v238 offset:12288\n" \
"s_waitcnt lgkmcnt(11)\n" \
"v_mfma_f32_16x16x32_bf16 v[128:131], a[144:147], v[168:171], v[128:131]\n" \
"buffer_load_dwordx4 a[36:39], v233, s[64:67], s22 offen offset:1024 sc1\n" \
"v_mfma_f32_16x16x32_bf16 v[136:139], a[144:147], v[172:175], v[136:139]\n" \
"ds_read_b64_tr_b16 a[182:183], v238 offset:14336\n" \
"ds_read_b64_tr_b16 a[184:185], v239 offset:12288\n" \
"v_mfma_f32_16x16x32_bf16 v[140:143], a[148:151], v[172:175], v[140:143]\n" \
"ds_read_b64_tr_b16 a[186:187], v239 offset:14336\n" \
"ds_read_b64_tr_b16 a[188:189], v240 offset:12288\n" \
"v_mfma_f32_16x16x32_bf16 v[132:135], a[148:151], v[168:171], v[132:135]\n" \
"ds_read_b64_tr_b16 a[190:191], v240 offset:14336\n" \
"v_mfma_f32_16x16x32_bf16 v[144:147], a[152:155], v[168:171], v[144:147]\n" \
"s_waitcnt vmcnt(6)\n" \
"v_mfma_f32_16x16x32_bf16 v[152:155], a[152:155], v[172:175], v[152:155]\n" \
"v_cmp_ne_u64 vcc, s[72:73], v[250:251]\n" \
"v_mfma_f32_16x16x32_bf16 v[156:159], a[156:159], v[172:175], v[156:159]\n" \
"s_nop 0\n" \
"v_mfma_f32_16x16x32_bf16 v[148:151], a[156:159], v[168:171], v[148:151]\n" \
"s_cbranch_vccnz Lqc_nf_%=\n" \
"s_waitcnt vmcnt(38) lgkmcnt(8)\n" \
"v_mfma_f32_16x16x32_bf16 v[128:131], a[160:163], v[176:179], v[128:131]\n" \
"ds_read_b64_tr_b16 a[128:129], v237 offset:16384\n" \
"ds_read_b64_tr_b16 a[130:131], v237 offset:18432\n" \
"v_mfma_f32_16x16x32_bf16 v[136:139], a[160:163], v[180:183], v[136:139]\n" \
"buffer_load_dwordx4 v[64:67], v232, s[64:67], s43 offen offset:0 sc1\n" \
"v_mfma_f32_16x16x32_bf16 v[140:143], a[164:167], v[180:183], v[140:143]\n" \
"ds_read_b64_tr_b16 a[132:133], v238 offset:16384\n" \
"ds_read_b64_tr_b16 a[134:135], v238 offset:18432\n" \
"v_mfma_f32_16x16x32_bf16 v[132:135], a[164:167], v[176:179], v[132:135]\n" \
"buffer_load_dwordx4 v[68:71], v232, s[64:67], s43 offen offset:1024 sc1\n" \
"v_mfma_f32_16x16x32_bf16 v[144:147], a[168:171], v[176:179], v[144:147]\n" \
"ds_read_b64_tr_b16 a[136:137], v239 offset:16384\n" \
"ds_read_b64_tr_b16 a[138:139], v239 offset:18432\n" \
"v_mfma_f32_16x16x32_bf16 v[152:155], a[168:171], v[180:183], v[152:155]\n" \
"buffer_load_dwordx4 v[72:75], v232, s[64:67], s43 offen offset:2048 sc1\n" \
"v_mfma_f32_16x16x32_bf16 v[156:159], a[172:175], v[180:183], v[156:159]\n" \
"ds_read_b64_tr_b16 a[140:141], v240 offset:16384\n" \
"ds_read_b64_tr_b16 a[142:143], v240 offset:18432\n" \
"v_mfma_f32_16x16x32_bf16 v[148:151], a[172:175], v[176:179], v[148:151]\n" \
"ds_read_b64_tr_b16 a[144:145], v237 offset:20480\n" \
"s_waitcnt lgkmcnt(9)\n" \
"v_mfma_f32_16x16x32_bf16 v[128:131], a[176:179], v[184:187], v[128:131]\n" \
"buffer_load_dwordx4 v[76:79], v232, s[64:67], s43 offen offset:3072 sc1\n" \
"v_mfma_f32_16x16x32_bf16 v[136:139], a[176:179], v[188:191], v[136:139]\n" \
"ds_read_b64_tr_b16 a[146:147], v237 offset:22528\n" \
"ds_read_b64_tr_b16 a[148:149], v238 offset:20480\n" \
"v_mfma_f32_16x16x32_bf16 v[140:143], a[180:183], v[188:191], v[140:143]\n" \
"buffer_load_dwordx4 v[80:83], v232, s[64:67], s44 offen offset:0 sc1\n" \
"v_mfma_f32_16x16x32_bf16 v[132:135], a[180:183], v[184:187], v[132:135]\n" \
"ds_read_b64_tr_b16 a[150:151], v238 offset:22528\n" \
"ds_read_b64_tr_b16 a[152:153], v239 offset:20480\n" \
"v_mfma_f32_16x16x32_bf16 v[144:147], a[184:187], v[184:187], v[144:147]\n" \
"buffer_load_dwordx4 v[84:87], v232, s[64:67], s44 offen offset:1024 sc1\n" \
"v_mfma_f32_16x16x32_bf16 v[152:155], a[184:187], v[188:191], v[152:155]\n" \
"ds_read_b64_tr_b16 a[154:155], v239 offset:22528\n" \
"ds_read_b64_tr_b16 a[156:157], v240 offset:20480\n" \
"v_mfma_f32_16x16x32_bf16 v[156:159], a[188:191], v[188:191], v[156:159]\n" \
"ds_read_b64_tr_b16 a[158:159], v240 offset:22528\n" \
"v_mfma_f32_16x16x32_bf16 v[148:151], a[188:191], v[184:187], v[148:151]\n" \
"s_waitcnt vmcnt(22) lgkmcnt(8)\n" \
"v_mfma_f32_16x16x32_bf16 v[128:131], a[128:131], v[192:195], v[128:131]\n" \
"ds_read_b64_tr_b16 a[160:161], v237 offset:24576\n" \
"ds_read_b64_tr_b16 a[162:163], v237 offset:26624\n" \
"v_mfma_f32_16x16x32_bf16 v[136:139], a[128:131], v[196:199], v[136:139]\n" \
"buffer_load_dwordx4 v[88:91], v232, s[64:67], s44 offen offset:2048 sc1\n" \
"ds_read_b64_tr_b16 a[164:165], v238 offset:24576\n" \
"v_mfma_f32_16x16x32_bf16 v[140:143], a[132:135], v[196:199], v[140:143]\n" \
"buffer_load_dwordx4 v[92:95], v232, s[64:67], s44 offen offset:3072 sc1\n" \
"ds_read_b64_tr_b16 a[166:167], v238 offset:26624\n" \
"v_mfma_f32_16x16x32_bf16 v[132:135], a[132:135], v[192:195], v[132:135]\n" \
"ds_read_b64_tr_b16 a[168:169], v239 offset:24576\n" \
"v_mfma_f32_16x16x32_bf16 v[144:147], a[136:139], v[192:195], v[144:147]\n" \
"buffer_load_dwordx4 v[96:99], v232, s[64:67], s45 offen offset:0 sc1\n" \
"ds_read_b64_tr_b16 a[170:171], v239 offset:26624\n" \
"v_mfma_f32_16x16x32_bf16 v[152:155], a[136:139], v[196:199], v[152:155]\n" \
"ds_read_b64_tr_b16 a[172:173], v240 offset:24576\n" \
"v_mfma_f32_16x16x32_bf16 v[156:159], a[140:143], v[196:199], v[156:159]\n" \
"buffer_load_dwordx4 v[100:103], v232, s[64:67], s45 offen offset:1024 sc1\n" \
"ds_read_b64_tr_b16 a[174:175], v240 offset:26624\n" \
"v_mfma_f32_16x16x32_bf16 v[148:151], a[140:143], v[192:195], v[148:151]\n" \
"ds_read_b64_tr_b16 a[176:177], v237 offset:28672\n" \
"s_waitcnt lgkmcnt(9)\n" \
"v_mfma_f32_16x16x32_bf16 v[128:131], a[144:147], v[200:203], v[128:131]\n" \
"buffer_load_dwordx4 v[104:107], v232, s[64:67], s45 offen offset:2048 sc1\n" \
"ds_read_b64_tr_b16 a[178:179], v237 offset:30720\n" \
"v_mfma_f32_16x16x32_bf16 v[136:139], a[144:147], v[204:207], v[136:139]\n" \
"buffer_load_dwordx4 v[108:111], v232, s[64:67], s45 offen offset:3072 sc1\n" \
"ds_read_b64_tr_b16 a[180:181], v238 offset:28672\n" \
"v_mfma_f32_16x16x32_bf16 v[140:143], a[148:151], v[204:207], v[140:143]\n" \
"ds_read_b64_tr_b16 a[182:183], v238 offset:30720\n" \
"v_mfma_f32_16x16x32_bf16 v[132:135], a[148:151], v[200:203], v[132:135]\n" \
"buffer_load_dwordx4 a[40:43], v234, s[64:67], s22 offen offset:0 sc1\n" \
"ds_read_b64_tr_b16 a[184:185], v239 offset:28672\n" \
"v_mfma_f32_16x16x32_bf16 v[144:147], a[152:155], v[200:203], v[144:147]\n" \
"ds_read_b64_tr_b16 a[186:187], v239 offset:30720\n" \
"v_mfma_f32_16x16x32_bf16 v[152:155], a[152:155], v[204:207], v[152:155]\n" \
"buffer_load_dwordx4 a[44:47], v234, s[64:67], s22 offen offset:1024 sc1\n" \
"ds_read_b64_tr_b16 a[188:189], v240 offset:28672\n" \
"v_mfma_f32_16x16x32_bf16 v[156:159], a[156:159], v[204:207], v[156:159]\n" \
"ds_read_b64_tr_b16 a[190:191], v240 offset:30720\n" \
"v_mfma_f32_16x16x32_bf16 v[148:151], a[156:159], v[200:203], v[148:151]\n" \
"s_waitcnt vmcnt(26) lgkmcnt(0)\n" \
"s_barrier\n" \
"v_mfma_f32_16x16x32_bf16 v[128:131], a[160:163], v[208:211], v[128:131]\n" \
"ds_read_b64_tr_b16 a[128:129], v228 offset:0\n" \
"ds_read_b64_tr_b16 a[130:131], v228 offset:2048\n" \
"v_mfma_f32_16x16x32_bf16 v[136:139], a[160:163], v[212:215], v[136:139]\n" \
"ds_read_b64_tr_b16 a[132:133], v229 offset:0\n" \
"v_mfma_f32_16x16x32_bf16 v[140:143], a[164:167], v[212:215], v[140:143]\n" \
"ds_read_b64_tr_b16 a[134:135], v229 offset:2048\n" \
"v_mfma_f32_16x16x32_bf16 v[132:135], a[164:167], v[208:211], v[132:135]\n" \
"ds_read_b64_tr_b16 a[136:137], v230 offset:0\n" \
"v_mfma_f32_16x16x32_bf16 v[144:147], a[168:171], v[208:211], v[144:147]\n" \
"ds_read_b64_tr_b16 a[138:139], v230 offset:2048\n" \
"v_mfma_f32_16x16x32_bf16 v[152:155], a[168:171], v[212:215], v[152:155]\n" \
"ds_read_b64_tr_b16 a[140:141], v231 offset:0\n" \
"v_mfma_f32_16x16x32_bf16 v[156:159], a[172:175], v[212:215], v[156:159]\n" \
"ds_read_b64_tr_b16 a[142:143], v231 offset:2048\n" \
"v_mfma_f32_16x16x32_bf16 v[148:151], a[172:175], v[208:211], v[148:151]\n" \
"ds_read_b64_tr_b16 a[144:145], v228 offset:4096\n" \
"v_mfma_f32_16x16x32_bf16 v[128:131], a[176:179], v[216:219], v[128:131]\n" \
"ds_read_b64_tr_b16 a[146:147], v228 offset:6144\n" \
"v_mfma_f32_16x16x32_bf16 v[136:139], a[176:179], v[220:223], v[136:139]\n" \
"ds_read_b64_tr_b16 a[148:149], v229 offset:4096\n" \
"v_mfma_f32_16x16x32_bf16 v[140:143], a[180:183], v[220:223], v[140:143]\n" \
"ds_read_b64_tr_b16 a[150:151], v229 offset:6144\n" \
"v_mfma_f32_16x16x32_bf16 v[132:135], a[180:183], v[216:219], v[132:135]\n" \
"ds_read_b64_tr_b16 a[152:153], v230 offset:4096\n" \
"v_mfma_f32_16x16x32_bf16 v[144:147], a[184:187], v[216:219], v[144:147]\n" \
"ds_read_b64_tr_b16 a[154:155], v230 offset:6144\n" \
"v_mfma_f32_16x16x32_bf16 v[152:155], a[184:187], v[220:223], v[152:155]\n" \
"ds_read_b64_tr_b16 a[156:157], v231 offset:4096\n" \
"v_mfma_f32_16x16x32_bf16 v[156:159], a[188:191], v[220:223], v[156:159]\n" \
"ds_read_b64_tr_b16 a[158:159], v231 offset:6144\n" \
"v_mfma_f32_16x16x32_bf16 v[148:151], a[188:191], v[216:219], v[148:151]\n" \
"s_waitcnt vmcnt(10) lgkmcnt(8)\n" \
"v_mfma_f32_16x16x32_bf16 v[128:131], a[128:131], a[0:3], v[128:131]\n" \
"ds_read_b64_tr_b16 a[160:161], v228 offset:8192\n" \
"v_mfma_f32_16x16x32_bf16 v[136:139], a[128:131], a[4:7], v[136:139]\n" \
"ds_read_b64_tr_b16 a[162:163], v228 offset:10240\n" \
"v_mfma_f32_16x16x32_bf16 v[140:143], a[132:135], a[4:7], v[140:143]\n" \
"ds_read_b64_tr_b16 a[164:165], v229 offset:8192\n" \
"v_mfma_f32_16x16x32_bf16 v[132:135], a[132:135], a[0:3], v[132:135]\n" \
"v_mfma_f32_16x16x32_bf16 v[144:147], a[136:139], a[0:3], v[144:147]\n" \
"ds_read_b64_tr_b16 a[166:167], v229 offset:10240\n" \
"v_mfma_f32_16x16x32_bf16 v[152:155], a[136:139], a[4:7], v[152:155]\n" \
"ds_read_b64_tr_b16 a[168:169], v230 offset:8192\n" \
"v_mfma_f32_16x16x32_bf16 v[156:159], a[140:143], a[4:7], v[156:159]\n" \
"v_mfma_f32_16x16x32_bf16 v[148:151], a[140:143], a[0:3], v[148:151]\n" \
"ds_read_b64_tr_b16 a[170:171], v230 offset:10240\n" \
"s_waitcnt lgkmcnt(6)\n" \
"v_mfma_f32_16x16x32_bf16 v[128:131], a[144:147], a[8:11], v[128:131]\n" \
"ds_read_b64_tr_b16 a[172:173], v231 offset:8192\n" \
"v_mfma_f32_16x16x32_bf16 v[136:139], a[144:147], a[12:15], v[136:139]\n" \
"v_mfma_f32_16x16x32_bf16 v[140:143], a[148:151], a[12:15], v[140:143]\n" \
"ds_read_b64_tr_b16 a[174:175], v231 offset:10240\n" \
"v_mfma_f32_16x16x32_bf16 v[132:135], a[148:151], a[8:11], v[132:135]\n" \
"ds_read_b64_tr_b16 a[176:177], v228 offset:12288\n" \
"Lpadq4_%=:\n" \
"s_cbranch_vccnz Lprq0_%=\n" \
"v_mfma_f32_16x16x32_bf16 v[144:147], a[152:155], a[8:11], v[144:147]\n" \
"ds_read_b64_tr_b16 a[178:179], v228 offset:14336\n" \
"v_mfma_f32_16x16x32_bf16 v[152:155], a[152:155], a[12:15], v[152:155]\n" \
"v_mfma_f32_16x16x32_bf16 v[156:159], a[156:159], a[12:15], v[156:159]\n" \
"ds_read_b64_tr_b16 a[180:181], v229 offset:12288\n" \
"v_mfma_f32_16x16x32_bf16 v[148:151], a[156:159], a[8:11], v[148:151]\n" \
"ds_read_b64_tr_b16 a[182:183], v229 offset:14336\n" \
"v_mfma_f32_16x16x32_bf16 v[0:3], v[114:117], v[64:67], v[0:3]\n" \
"v_mfma_f32_16x16x32_bf16 v[4:7], v[112:115], v[64:67], v[4:7]\n" \
"ds_read_b64_tr_b16 a[184:185], v230 offset:12288\n" \
"v_mfma_f32_16x16x32_bf16 v[32:35], v[114:117], v[68:71], v[32:35]\n" \
"ds_read_b64_tr_b16 a[186:187], v230 offset:14336\n" \
"v_mfma_f32_16x16x32_bf16 v[36:39], v[112:115], v[68:71], v[36:39]\n" \
"v_mfma_f32_16x16x32_bf16 v[8:11], v[114:117], v[72:75], v[8:11]\n" \
"ds_read_b64_tr_b16 a[188:189], v231 offset:12288\n" \
"v_mfma_f32_16x16x32_bf16 v[12:15], v[112:115], v[72:75], v[12:15]\n" \
"ds_read_b64_tr_b16 a[190:191], v231 offset:14336\n" \
"v_mfma_f32_16x16x32_bf16 v[40:43], v[114:117], v[76:79], v[40:43]\n" \
"v_mfma_f32_16x16x32_bf16 v[44:47], v[112:115], v[76:79], v[44:47]\n" \
"s_waitcnt vmcnt(6) lgkmcnt(8)\n" \
"v_mfma_f32_16x16x32_bf16 v[128:131], a[160:163], a[16:19], v[128:131]\n" \
"ds_read_b64_tr_b16 a[128:129], v228 offset:16384\n" \
"v_mfma_f32_16x16x32_bf16 v[136:139], a[160:163], a[20:23], v[136:139]\n" \
"ds_read_b64_tr_b16 a[130:131], v228 offset:18432\n" \
"v_mfma_f32_16x16x32_bf16 v[140:143], a[164:167], a[20:23], v[140:143]\n" \
"ds_read_b64_tr_b16 a[132:133], v229 offset:16384\n" \
"v_mfma_f32_16x16x32_bf16 v[132:135], a[164:167], a[16:19], v[132:135]\n" \
"v_mfma_f32_16x16x32_bf16 v[144:147], a[168:171], a[16:19], v[144:147]\n" \
"ds_read_b64_tr_b16 a[134:135], v229 offset:18432\n" \
"v_mfma_f32_16x16x32_bf16 v[152:155], a[168:171], a[20:23], v[152:155]\n" \
"ds_read_b64_tr_b16 a[136:137], v230 offset:16384\n" \
"v_mfma_f32_16x16x32_bf16 v[156:159], a[172:175], a[20:23], v[156:159]\n" \
"v_mfma_f32_16x16x32_bf16 v[148:151], a[172:175], a[16:19], v[148:151]\n" \
"ds_read_b64_tr_b16 a[138:139], v230 offset:18432\n" \
"s_waitcnt lgkmcnt(6)\n" \
"v_mfma_f32_16x16x32_bf16 v[128:131], a[176:179], a[24:27], v[128:131]\n" \
"ds_read_b64_tr_b16 a[140:141], v231 offset:16384\n" \
"v_mfma_f32_16x16x32_bf16 v[136:139], a[176:179], a[28:31], v[136:139]\n" \
"v_mfma_f32_16x16x32_bf16 v[140:143], a[180:183], a[28:31], v[140:143]\n" \
"ds_read_b64_tr_b16 a[142:143], v231 offset:18432\n" \
"v_mfma_f32_16x16x32_bf16 v[132:135], a[180:183], a[24:27], v[132:135]\n" \
"ds_read_b64_tr_b16 a[144:145], v228 offset:20480\n" \
"v_mfma_f32_16x16x32_bf16 v[144:147], a[184:187], a[24:27], v[144:147]\n" \
"ds_read_b64_tr_b16 a[146:147], v228 offset:22528\n" \
"v_mfma_f32_16x16x32_bf16 v[152:155], a[184:187], a[28:31], v[152:155]\n" \
"v_mfma_f32_16x16x32_bf16 v[156:159], a[188:191], a[28:31], v[156:159]\n" \
"ds_read_b64_tr_b16 a[148:149], v229 offset:20480\n" \
"v_mfma_f32_16x16x32_bf16 v[148:151], a[188:191], a[24:27], v[148:151]\n" \
"ds_read_b64_tr_b16 a[150:151], v229 offset:22528\n" \
"v_mfma_f32_16x16x32_bf16 v[0:3], v[114:117], v[80:83], v[0:3]\n" \
"v_mfma_f32_16x16x32_bf16 v[4:7], v[112:115], v[80:83], v[4:7]\n" \
"ds_read_b64_tr_b16 a[152:153], v230 offset:20480\n" \
"v_mfma_f32_16x16x32_bf16 v[32:35], v[114:117], v[84:87], v[32:35]\n" \
"ds_read_b64_tr_b16 a[154:155], v230 offset:22528\n" \
"v_mfma_f32_16x16x32_bf16 v[36:39], v[112:115], v[84:87], v[36:39]\n" \
"v_mfma_f32_16x16x32_bf16 v[8:11], v[114:117], v[88:91], v[8:11]\n" \
"ds_read_b64_tr_b16 a[156:157], v231 offset:20480\n" \
"v_mfma_f32_16x16x32_bf16 v[12:15], v[112:115], v[88:91], v[12:15]\n" \
"ds_read_b64_tr_b16 a[158:159], v231 offset:22528\n" \
"v_mfma_f32_16x16x32_bf16 v[40:43], v[114:117], v[92:95], v[40:43]\n" \
"v_mfma_f32_16x16x32_bf16 v[44:47], v[112:115], v[92:95], v[44:47]\n" \
"s_waitcnt vmcnt(0) lgkmcnt(8)\n" \
"v_mfma_f32_16x16x32_bf16 v[128:131], a[128:131], a[32:35], v[128:131]\n" \
"v_mfma_f32_16x16x32_bf16 v[136:139], a[128:131], a[36:39], v[136:139]\n" \
"v_mfma_f32_16x16x32_bf16 v[140:143], a[132:135], a[36:39], v[140:143]\n" \
"v_mfma_f32_16x16x32_bf16 v[132:135], a[132:135], a[32:35], v[132:135]\n" \
"v_mfma_f32_16x16x32_bf16 v[144:147], a[136:139], a[32:35], v[144:147]\n" \
"v_mfma_f32_16x16x32_bf16 v[152:155], a[136:139], a[36:39], v[152:155]\n" \
"v_mfma_f32_16x16x32_bf16 v[156:159], a[140:143], a[36:39], v[156:159]\n" \
"v_mfma_f32_16x16x32_bf16 v[148:151], a[140:143], a[32:35], v[148:151]\n" \
"s_waitcnt lgkmcnt(0)\n" \
"v_mfma_f32_16x16x32_bf16 v[128:131], a[144:147], a[40:43], v[128:131]\n" \
"v_mfma_f32_16x16x32_bf16 v[136:139], a[144:147], a[44:47], v[136:139]\n" \
"v_mfma_f32_16x16x32_bf16 v[140:143], a[148:151], a[44:47], v[140:143]\n" \
"v_mfma_f32_16x16x32_bf16 v[132:135], a[148:151], a[40:43], v[132:135]\n" \
"v_mfma_f32_16x16x32_bf16 v[144:147], a[152:155], a[40:43], v[144:147]\n" \
"v_mfma_f32_16x16x32_bf16 v[152:155], a[152:155], a[44:47], v[152:155]\n" \
"v_mfma_f32_16x16x32_bf16 v[156:159], a[156:159], a[44:47], v[156:159]\n" \
"v_mfma_f32_16x16x32_bf16 v[148:151], a[156:159], a[40:43], v[148:151]\n" \
"v_mfma_f32_16x16x32_bf16 v[0:3], v[114:117], v[96:99], v[0:3]\n" \
"v_mfma_f32_16x16x32_bf16 v[4:7], v[112:115], v[96:99], v[4:7]\n" \
"v_mfma_f32_16x16x32_bf16 v[32:35], v[114:117], v[100:103], v[32:35]\n" \
"v_mfma_f32_16x16x32_bf16 v[36:39], v[112:115], v[100:103], v[36:39]\n" \
"v_mfma_f32_16x16x32_bf16 v[8:11], v[114:117], v[104:107], v[8:11]\n" \
"v_mfma_f32_16x16x32_bf16 v[12:15], v[112:115], v[104:107], v[12:15]\n" \
"v_mfma_f32_16x16x32_bf16 v[40:43], v[114:117], v[108:111], v[40:43]\n" \
"v_mfma_f32_16x16x32_bf16 v[44:47], v[112:115], v[108:111], v[44:47]\n" \
"s_branch Lfin2_%=\n" \
"Lqc_nf_%=:\n" \
"s_waitcnt vmcnt(38) lgkmcnt(8)\n" \
"v_mfma_f32_16x16x32_bf16 v[128:131], a[160:163], v[176:179], v[128:131]\n" \
"ds_read_b64_tr_b16 a[128:129], v237 offset:16384\n" \
"ds_read_b64_tr_b16 a[130:131], v237 offset:18432\n" \
"v_mfma_f32_16x16x32_bf16 v[136:139], a[160:163], v[180:183], v[136:139]\n" \
"buffer_load_dwordx4 a[40:43], v234, s[64:67], s22 offen offset:0 sc1\n" \
"v_mfma_f32_16x16x32_bf16 v[140:143], a[164:167], v[180:183], v[140:143]\n" \
"ds_read_b64_tr_b16 a[132:133], v238 offset:16384\n" \
"v_mfma_f32_16x16x32_bf16 v[132:135], a[164:167], v[176:179], v[132:135]\n" \
"ds_read_b64_tr_b16 a[134:135], v238 offset:18432\n" \
"v_mfma_f32_16x16x32_bf16 v[144:147], a[168:171], v[176:179], v[144:147]\n" \
"ds_read_b64_tr_b16 a[136:137], v239 offset:16384\n" \
"v_mfma_f32_16x16x32_bf16 v[152:155], a[168:171], v[180:183], v[152:155]\n" \
"ds_read_b64_tr_b16 a[138:139], v239 offset:18432\n" \
"ds_read_b64_tr_b16 a[140:141], v240 offset:16384\n" \
"v_mfma_f32_16x16x32_bf16 v[156:159], a[172:175], v[180:183], v[156:159]\n" \
"ds_read_b64_tr_b16 a[142:143], v240 offset:18432\n" \
"v_mfma_f32_16x16x32_bf16 v[148:151], a[172:175], v[176:179], v[148:151]\n" \
"ds_read_b64_tr_b16 a[144:145], v237 offset:20480\n" \
"s_waitcnt lgkmcnt(9)\n" \
"v_mfma_f32_16x16x32_bf16 v[128:131], a[176:179], v[184:187], v[128:131]\n" \
"buffer_load_dwordx4 a[44:47], v234, s[64:67], s22 offen offset:1024 sc1\n" \
"v_mfma_f32_16x16x32_bf16 v[136:139], a[176:179], v[188:191], v[136:139]\n" \
"ds_read_b64_tr_b16 a[146:147], v237 offset:22528\n" \
"v_mfma_f32_16x16x32_bf16 v[140:143], a[180:183], v[188:191], v[140:143]\n" \
"ds_read_b64_tr_b16 a[148:149], v238 offset:20480\n" \
"ds_read_b64_tr_b16 a[150:151], v238 offset:22528\n" \
"v_mfma_f32_16x16x32_bf16 v[132:135], a[180:183], v[184:187], v[132:135]\n" \
"ds_read_b64_tr_b16 a[152:153], v239 offset:20480\n" \
"v_mfma_f32_16x16x32_bf16 v[144:147], a[184:187], v[184:187], v[144:147]\n" \
"ds_read_b64_tr_b16 a[154:155], v239 offset:22528\n" \
"v_mfma_f32_16x16x32_bf16 v[152:155], a[184:187], v[188:191], v[152:155]\n" \
"ds_read_b64_tr_b16 a[156:157], v240 offset:20480\n" \
"v_mfma_f32_16x16x32_bf16 v[156:159], a[188:191], v[188:191], v[156:159]\n" \
"ds_read_b64_tr_b16 a[158:159], v240 offset:22528\n" \
"v_mfma_f32_16x16x32_bf16 v[148:151], a[188:191], v[184:187], v[148:151]\n" \
"s_waitcnt vmcnt(18) lgkmcnt(8)\n" \
"v_mfma_f32_16x16x32_bf16 v[128:131], a[128:131], v[192:195], v[128:131]\n" \
"ds_read_b64_tr_b16 a[160:161], v237 offset:24576\n" \
"ds_read_b64_tr_b16 a[162:163], v237 offset:26624\n" \
"v_mfma_f32_16x16x32_bf16 v[136:139], a[128:131], v[196:199], v[136:139]\n" \
"ds_read_b64_tr_b16 a[164:165], v238 offset:24576\n" \
"v_mfma_f32_16x16x32_bf16 v[140:143], a[132:135], v[196:199], v[140:143]\n" \
"ds_read_b64_tr_b16 a[166:167], v238 offset:26624\n" \
"v_mfma_f32_16x16x32_bf16 v[132:135], a[132:135], v[192:195], v[132:135]\n" \
"ds_read_b64_tr_b16 a[168:169], v239 offset:24576\n" \
"v_mfma_f32_16x16x32_bf16 v[144:147], a[136:139], v[192:195], v[144:147]\n" \
"ds_read_b64_tr_b16 a[170:171], v239 offset:26624\n" \
"v_mfma_f32_16x16x32_bf16 v[152:155], a[136:139], v[196:199], v[152:155]\n" \
"ds_read_b64_tr_b16 a[172:173], v240 offset:24576\n" \
"v_mfma_f32_16x16x32_bf16 v[156:159], a[140:143], v[196:199], v[156:159]\n" \
"ds_read_b64_tr_b16 a[174:175], v240 offset:26624\n" \
"v_mfma_f32_16x16x32_bf16 v[148:151], a[140:143], v[192:195], v[148:151]\n" \
"ds_read_b64_tr_b16 a[176:177], v237 offset:28672\n" \
"s_waitcnt lgkmcnt(9)\n" \
"v_mfma_f32_16x16x32_bf16 v[128:131], a[144:147], v[200:203], v[128:131]\n" \
"ds_read_b64_tr_b16 a[178:179], v237 offset:30720\n" \
"v_mfma_f32_16x16x32_bf16 v[136:139], a[144:147], v[204:207], v[136:139]\n" \
"ds_read_b64_tr_b16 a[180:181], v238 offset:28672\n" \
"v_mfma_f32_16x16x32_bf16 v[140:143], a[148:151], v[204:207], v[140:143]\n" \
"ds_read_b64_tr_b16 a[182:183], v238 offset:30720\n" \
"v_mfma_f32_16x16x32_bf16 v[132:135], a[148:151], v[200:203], v[132:135]\n" \
"ds_read_b64_tr_b16 a[184:185], v239 offset:28672\n" \
"v_mfma_f32_16x16x32_bf16 v[144:147], a[152:155], v[200:203], v[144:147]\n" \
"ds_read_b64_tr_b16 a[186:187], v239 offset:30720\n" \
"v_mfma_f32_16x16x32_bf16 v[152:155], a[152:155], v[204:207], v[152:155]\n" \
"ds_read_b64_tr_b16 a[188:189], v240 offset:28672\n" \
"v_mfma_f32_16x16x32_bf16 v[156:159], a[156:159], v[204:207], v[156:159]\n" \
"ds_read_b64_tr_b16 a[190:191], v240 offset:30720\n" \
"v_mfma_f32_16x16x32_bf16 v[148:151], a[156:159], v[200:203], v[148:151]\n" \
"s_waitcnt vmcnt(14) lgkmcnt(0)\n" \
"s_barrier\n" \
"v_mfma_f32_16x16x32_bf16 v[128:131], a[160:163], v[208:211], v[128:131]\n" \
"ds_read_b64_tr_b16 a[128:129], v228 offset:0\n" \
"ds_read_b64_tr_b16 a[130:131], v228 offset:2048\n" \
"v_mfma_f32_16x16x32_bf16 v[136:139], a[160:163], v[212:215], v[136:139]\n" \
"ds_read_b64_tr_b16 a[132:133], v229 offset:0\n" \
"v_mfma_f32_16x16x32_bf16 v[140:143], a[164:167], v[212:215], v[140:143]\n" \
"ds_read_b64_tr_b16 a[134:135], v229 offset:2048\n" \
"v_mfma_f32_16x16x32_bf16 v[132:135], a[164:167], v[208:211], v[132:135]\n" \
"ds_read_b64_tr_b16 a[136:137], v230 offset:0\n" \
"v_mfma_f32_16x16x32_bf16 v[144:147], a[168:171], v[208:211], v[144:147]\n" \
"ds_read_b64_tr_b16 a[138:139], v230 offset:2048\n" \
"v_mfma_f32_16x16x32_bf16 v[152:155], a[168:171], v[212:215], v[152:155]\n" \
"ds_read_b64_tr_b16 a[140:141], v231 offset:0\n" \
"v_mfma_f32_16x16x32_bf16 v[156:159], a[172:175], v[212:215], v[156:159]\n" \
"ds_read_b64_tr_b16 a[142:143], v231 offset:2048\n" \
"v_mfma_f32_16x16x32_bf16 v[148:151], a[172:175], v[208:211], v[148:151]\n" \
"ds_read_b64_tr_b16 a[144:145], v228 offset:4096\n" \
"v_mfma_f32_16x16x32_bf16 v[128:131], a[176:179], v[216:219], v[128:131]\n" \
"ds_read_b64_tr_b16 a[146:147], v228 offset:6144\n" \
"v_mfma_f32_16x16x32_bf16 v[136:139], a[176:179], v[220:223], v[136:139]\n" \
"ds_read_b64_tr_b16 a[148:149], v229 offset:4096\n" \
"v_mfma_f32_16x16x32_bf16 v[140:143], a[180:183], v[220:223], v[140:143]\n" \
"ds_read_b64_tr_b16 a[150:151], v229 offset:6144\n" \
"v_mfma_f32_16x16x32_bf16 v[132:135], a[180:183], v[216:219], v[132:135]\n" \
"ds_read_b64_tr_b16 a[152:153], v230 offset:4096\n" \
"v_mfma_f32_16x16x32_bf16 v[144:147], a[184:187], v[216:219], v[144:147]\n" \
"ds_read_b64_tr_b16 a[154:155], v230 offset:6144\n" \
"v_mfma_f32_16x16x32_bf16 v[152:155], a[184:187], v[220:223], v[152:155]\n" \
"ds_read_b64_tr_b16 a[156:157], v231 offset:4096\n" \
"v_mfma_f32_16x16x32_bf16 v[156:159], a[188:191], v[220:223], v[156:159]\n" \
"ds_read_b64_tr_b16 a[158:159], v231 offset:6144\n" \
"v_mfma_f32_16x16x32_bf16 v[148:151], a[188:191], v[216:219], v[148:151]\n" \
"s_waitcnt vmcnt(9) lgkmcnt(8)\n" \
"v_mfma_f32_16x16x32_bf16 v[128:131], a[128:131], a[0:3], v[128:131]\n" \
"ds_read_b64_tr_b16 a[160:161], v228 offset:8192\n" \
"ds_read_b64_tr_b16 a[162:163], v228 offset:10240\n" \
"v_mfma_f32_16x16x32_bf16 v[136:139], a[128:131], a[4:7], v[136:139]\n" \
"ds_read_b64_tr_b16 a[164:165], v229 offset:8192\n" \
"v_mfma_f32_16x16x32_bf16 v[140:143], a[132:135], a[4:7], v[140:143]\n" \
"ds_read_b64_tr_b16 a[166:167], v229 offset:10240\n" \
"v_mfma_f32_16x16x32_bf16 v[132:135], a[132:135], a[0:3], v[132:135]\n" \
"ds_read_b64_tr_b16 a[168:169], v230 offset:8192\n" \
"v_mfma_f32_16x16x32_bf16 v[144:147], a[136:139], a[0:3], v[144:147]\n" \
"ds_read_b64_tr_b16 a[170:171], v230 offset:10240\n" \
"v_mfma_f32_16x16x32_bf16 v[152:155], a[136:139], a[4:7], v[152:155]\n" \
"ds_read_b64_tr_b16 a[172:173], v231 offset:8192\n" \
"v_mfma_f32_16x16x32_bf16 v[156:159], a[140:143], a[4:7], v[156:159]\n" \
"ds_read_b64_tr_b16 a[174:175], v231 offset:10240\n" \
"v_mfma_f32_16x16x32_bf16 v[148:151], a[140:143], a[0:3], v[148:151]\n" \
"ds_read_b64_tr_b16 a[176:177], v228 offset:12288\n" \
"s_waitcnt lgkmcnt(9)\n" \
"v_mfma_f32_16x16x32_bf16 v[128:131], a[144:147], a[8:11], v[128:131]\n" \
"ds_read_b64_tr_b16 a[178:179], v228 offset:14336\n" \
"v_mfma_f32_16x16x32_bf16 v[136:139], a[144:147], a[12:15], v[136:139]\n" \
"ds_read_b64_tr_b16 a[180:181], v229 offset:12288\n" \
"v_mfma_f32_16x16x32_bf16 v[140:143], a[148:151], a[12:15], v[140:143]\n" \
"ds_read_b64_tr_b16 a[182:183], v229 offset:14336\n" \
"v_mfma_f32_16x16x32_bf16 v[132:135], a[148:151], a[8:11], v[132:135]\n" \
"ds_read_b64_tr_b16 a[184:185], v230 offset:12288\n" \
"v_mfma_f32_16x16x32_bf16 v[144:147], a[152:155], a[8:11], v[144:147]\n" \
"ds_read_b64_tr_b16 a[186:187], v230 offset:14336\n" \
"v_mfma_f32_16x16x32_bf16 v[152:155], a[152:155], a[12:15], v[152:155]\n" \
"ds_read_b64_tr_b16 a[188:189], v231 offset:12288\n" \
"v_mfma_f32_16x16x32_bf16 v[156:159], a[156:159], a[12:15], v[156:159]\n" \
"ds_read_b64_tr_b16 a[190:191], v231 offset:14336\n" \
"v_mfma_f32_16x16x32_bf16 v[148:151], a[156:159], a[8:11], v[148:151]\n" \
"s_waitcnt vmcnt(4) lgkmcnt(8)\n" \
"v_mfma_f32_16x16x32_bf16 v[128:131], a[160:163], a[16:19], v[128:131]\n" \
"ds_read_b64_tr_b16 a[128:129], v228 offset:16384\n" \
"ds_read_b64_tr_b16 a[130:131], v228 offset:18432\n" \
"v_mfma_f32_16x16x32_bf16 v[136:139], a[160:163], a[20:23], v[136:139]\n" \
"ds_read_b64_tr_b16 a[132:133], v229 offset:16384\n" \
"v_mfma_f32_16x16x32_bf16 v[140:143], a[164:167], a[20:23], v[140:143]\n" \
"ds_read_b64_tr_b16 a[134:135], v229 offset:18432\n" \
"v_mfma_f32_16x16x32_bf16 v[132:135], a[164:167], a[16:19], v[132:135]\n" \
"ds_read_b64_tr_b16 a[136:137], v230 offset:16384\n" \
"v_mfma_f32_16x16x32_bf16 v[144:147], a[168:171], a[16:19], v[144:147]\n" \
"ds_read_b64_tr_b16 a[138:139], v230 offset:18432\n" \
"v_mfma_f32_16x16x32_bf16 v[152:155], a[168:171], a[20:23], v[152:155]\n" \
"ds_read_b64_tr_b16 a[140:141], v231 offset:16384\n" \
"v_mfma_f32_16x16x32_bf16 v[156:159], a[172:175], a[20:23], v[156:159]\n" \
"ds_read_b64_tr_b16 a[142:143], v231 offset:18432\n" \
"v_mfma_f32_16x16x32_bf16 v[148:151], a[172:175], a[16:19], v[148:151]\n" \
"ds_read_b64_tr_b16 a[144:145], v228 offset:20480\n" \
"s_waitcnt lgkmcnt(9)\n" \
"v_mfma_f32_16x16x32_bf16 v[128:131], a[176:179], a[24:27], v[128:131]\n" \
"ds_read_b64_tr_b16 a[146:147], v228 offset:22528\n" \
"v_mfma_f32_16x16x32_bf16 v[136:139], a[176:179], a[28:31], v[136:139]\n" \
"ds_read_b64_tr_b16 a[148:149], v229 offset:20480\n" \
"v_mfma_f32_16x16x32_bf16 v[140:143], a[180:183], a[28:31], v[140:143]\n" \
"ds_read_b64_tr_b16 a[150:151], v229 offset:22528\n" \
"v_mfma_f32_16x16x32_bf16 v[132:135], a[180:183], a[24:27], v[132:135]\n" \
"ds_read_b64_tr_b16 a[152:153], v230 offset:20480\n" \
"v_mfma_f32_16x16x32_bf16 v[144:147], a[184:187], a[24:27], v[144:147]\n" \
"ds_read_b64_tr_b16 a[154:155], v230 offset:22528\n" \
"v_mfma_f32_16x16x32_bf16 v[152:155], a[184:187], a[28:31], v[152:155]\n" \
"ds_read_b64_tr_b16 a[156:157], v231 offset:20480\n" \
"v_mfma_f32_16x16x32_bf16 v[156:159], a[188:191], a[28:31], v[156:159]\n" \
"ds_read_b64_tr_b16 a[158:159], v231 offset:22528\n" \
"v_mfma_f32_16x16x32_bf16 v[148:151], a[188:191], a[24:27], v[148:151]\n" \
"s_waitcnt vmcnt(0) lgkmcnt(8)\n" \
"v_mfma_f32_16x16x32_bf16 v[128:131], a[128:131], a[32:35], v[128:131]\n" \
"v_mfma_f32_16x16x32_bf16 v[136:139], a[128:131], a[36:39], v[136:139]\n" \
"v_mfma_f32_16x16x32_bf16 v[140:143], a[132:135], a[36:39], v[140:143]\n" \
"v_mfma_f32_16x16x32_bf16 v[132:135], a[132:135], a[32:35], v[132:135]\n" \
"v_mfma_f32_16x16x32_bf16 v[144:147], a[136:139], a[32:35], v[144:147]\n" \
"v_mfma_f32_16x16x32_bf16 v[152:155], a[136:139], a[36:39], v[152:155]\n" \
"v_mfma_f32_16x16x32_bf16 v[156:159], a[140:143], a[36:39], v[156:159]\n" \
"v_mfma_f32_16x16x32_bf16 v[148:151], a[140:143], a[32:35], v[148:151]\n" \
"s_waitcnt lgkmcnt(0)\n" \
"v_mfma_f32_16x16x32_bf16 v[128:131], a[144:147], a[40:43], v[128:131]\n" \
"v_mfma_f32_16x16x32_bf16 v[136:139], a[144:147], a[44:47], v[136:139]\n" \
"v_mfma_f32_16x16x32_bf16 v[140:143], a[148:151], a[44:47], v[140:143]\n" \
"v_mfma_f32_16x16x32_bf16 v[132:135], a[148:151], a[40:43], v[132:135]\n" \
"v_mfma_f32_16x16x32_bf16 v[144:147], a[152:155], a[40:43], v[144:147]\n" \
"v_mfma_f32_16x16x32_bf16 v[152:155], a[152:155], a[44:47], v[152:155]\n" \
"v_mfma_f32_16x16x32_bf16 v[156:159], a[156:159], a[44:47], v[156:159]\n" \
"v_mfma_f32_16x16x32_bf16 v[148:151], a[156:159], a[40:43], v[148:151]\n" \
"s_mov_b32 s61, 0\n" \
"Lp12_%=:\n" \
"buffer_load_dwordx2 v[250:251], v253, s[64:67], s99 offen sc1\n" \
"s_waitcnt vmcnt(0)\n" \
"v_cmp_ne_u64 vcc, s[72:73], v[250:251]\n" \
"s_nop 0\n" \
"s_cbranch_vccz Lpd12_%=\n" \
"s_add_u32 s61, s61, 1\n" \
"s_cmp_lt_u32 s61, 0x40000\n" \
"s_sleep 1\n" \
"s_cbranch_scc1 Lp12_%=\n" \
"s_mov_b64 vcc, 0\n" \
"Lpd12_%=:\n" \
"buffer_load_dwordx4 v[64:67], v232, s[64:67], s43 offen offset:0 sc1\n" \
"buffer_load_dwordx4 v[68:71], v232, s[64:67], s43 offen offset:1024 sc1\n" \
"buffer_load_dwordx4 v[72:75], v232, s[64:67], s43 offen offset:2048 sc1\n" \
"buffer_load_dwordx4 v[76:79], v232, s[64:67], s43 offen offset:3072 sc1\n" \
"buffer_load_dwordx4 v[80:83], v232, s[64:67], s44 offen offset:0 sc1\n" \
"buffer_load_dwordx4 v[84:87], v232, s[64:67], s44 offen offset:1024 sc1\n" \
"buffer_load_dwordx4 v[88:91], v232, s[64:67], s44 offen offset:2048 sc1\n" \
"buffer_load_dwordx4 v[92:95], v232, s[64:67], s44 offen offset:3072 sc1\n" \
"buffer_load_dwordx4 v[96:99], v232, s[64:67], s45 offen offset:0 sc1\n" \
"buffer_load_dwordx4 v[100:103], v232, s[64:67], s45 offen offset:1024 sc1\n" \
"buffer_load_dwordx4 v[104:107], v232, s[64:67], s45 offen offset:2048 sc1\n" \
"buffer_load_dwordx4 v[108:111], v232, s[64:67], s45 offen offset:3072 sc1\n" \
"s_branch Lfin_%=\n" \
"Lsl10_%=:\n" \
"s_mov_b32 s61, 0\n" \
"Lp13_%=:\n" \
"buffer_load_dwordx2 v[250:251], v253, s[64:67], s84 offen sc1\n" \
"s_waitcnt vmcnt(0)\n" \
"v_cmp_ne_u64 vcc, s[72:73], v[250:251]\n" \
"s_nop 0\n" \
"s_cbranch_vccz Lpd13_%=\n" \
"s_add_u32 s61, s61, 1\n" \
"s_cmp_lt_u32 s61, 0x40000\n" \
"s_sleep 1\n" \
"s_cbranch_scc1 Lp13_%=\n" \
"s_mov_b64 vcc, 0\n" \
"Lpd13_%=:\n" \
"s_branch Lrt10_%=\n" \
"Lsl11_%=:\n" \
"s_mov_b32 s61, 0\n" \
"Lp14_%=:\n" \
"buffer_load_dwordx2 v[250:251], v253, s[64:67], s84 offen sc1\n" \
"s_waitcnt vmcnt(0)\n" \
"v_cmp_ne_u64 vcc, s[72:73], v[250:251]\n" \
"s_nop 0\n" \
"s_cbranch_vccz Lpd14_%=\n" \
"s_add_u32 s61, s61, 1\n" \
"s_cmp_lt_u32 s61, 0x40000\n" \
"s_sleep 1\n" \
"s_cbranch_scc1 Lp14_%=\n" \
"s_mov_b64 vcc, 0\n" \
"Lpd14_%=:\n" \
"s_branch Lrt11_%=\n" \
"Lfin_%=:\n" \
"s_waitcnt vmcnt(10)\n" \
"v_mfma_f32_16x16x32_bf16 v[0:3], v[114:117], v[64:67], v[0:3]\n" \
"v_mfma_f32_16x16x32_bf16 v[4:7], v[112:115], v[64:67], v[4:7]\n" \
"v_mfma_f32_16x16x32_bf16 v[32:35], v[114:117], v[68:71], v[32:35]\n" \
"v_mfma_f32_16x16x32_bf16 v[36:39], v[112:115], v[68:71], v[36:39]\n" \
"s_waitcnt vmcnt(8)\n" \
"v_mfma_f32_16x16x32_bf16 v[8:11], v[114:117], v[72:75], v[8:11]\n" \
"v_mfma_f32_16x16x32_bf16 v[12:15], v[112:115], v[72:75], v[12:15]\n" \
"v_mfma_f32_16x16x32_bf16 v[40:43], v[114:117], v[76:79], v[40:43]\n" \
"v_mfma_f32_16x16x32_bf16 v[44:47], v[112:115], v[76:79], v[44:47]\n" \
"s_waitcnt vmcnt(6)\n" \
"v_mfma_f32_16x16x32_bf16 v[0:3], v[114:117], v[80:83], v[0:3]\n" \
"v_mfma_f32_16x16x32_bf16 v[4:7], v[112:115], v[80:83], v[4:7]\n" \
"v_mfma_f32_16x16x32_bf16 v[32:35], v[114:117], v[84:87], v[32:35]\n" \
"v_mfma_f32_16x16x32_bf16 v[36:39], v[112:115], v[84:87], v[36:39]\n" \
"s_waitcnt vmcnt(4)\n" \
"v_mfma_f32_16x16x32_bf16 v[8:11], v[114:117], v[88:91], v[8:11]\n" \
"v_mfma_f32_16x16x32_bf16 v[12:15], v[112:115], v[88:91], v[12:15]\n" \
"v_mfma_f32_16x16x32_bf16 v[40:43], v[114:117], v[92:95], v[40:43]\n" \
"v_mfma_f32_16x16x32_bf16 v[44:47], v[112:115], v[92:95], v[44:47]\n" \
"s_waitcnt vmcnt(2)\n" \
"v_mfma_f32_16x16x32_bf16 v[0:3], v[114:117], v[96:99], v[0:3]\n" \
"v_mfma_f32_16x16x32_bf16 v[4:7], v[112:115], v[96:99], v[4:7]\n" \
"v_mfma_f32_16x16x32_bf16 v[32:35], v[114:117], v[100:103], v[32:35]\n" \
"v_mfma_f32_16x16x32_bf16 v[36:39], v[112:115], v[100:103], v[36:39]\n" \
"s_waitcnt vmcnt(0)\n" \
"v_mfma_f32_16x16x32_bf16 v[8:11], v[114:117], v[104:107], v[8:11]\n" \
"v_mfma_f32_16x16x32_bf16 v[12:15], v[112:115], v[104:107], v[12:15]\n" \
"v_mfma_f32_16x16x32_bf16 v[40:43], v[114:117], v[108:111], v[40:43]\n" \
"v_mfma_f32_16x16x32_bf16 v[44:47], v[112:115], v[108:111], v[44:47]\n" \
"Lfin2_%=:\n" \
"Lpadfq_%=:\n" \
"s_cbranch_vccnz Lprq0_%=\n" \
"Lpadfh_%=:\n" \
"s_cbranch_vccnz Lprh0_%=\n" \
"s_mov_b64 s[48:49], s[16:17]\n" \
"s_mov_b32 s50, 4194304\n" \
"s_mov_b32 s51, 0x20000\n" \
"v_sub_f32 v248, 1.0, v242\n" \
"v_sub_f32 v249, 1.0, v243\n" \
"v_mul_f32 v0, v0, v248\n" \
"v_mul_f32 v1, v1, v248\n" \
"v_mul_f32 v32, v32, v248\n" \
"v_mul_f32 v33, v33, v248\n" \
"v_mul_f32 v2, v2, v248\n" \
"v_mul_f32 v3, v3, v248\n" \
"v_fmac_f32 v0, v128, v242\n" \
"v_fmac_f32 v1, v129, v242\n" \
"v_mul_f32 v34, v34, v248\n" \
"v_mul_f32 v35, v35, v248\n" \
"v_mul_f32 v4, v4, v248\n" \
"v_mul_f32 v5, v5, v248\n" \
"v_mul_f32 v6, v6, v248\n" \
"v_mul_f32 v7, v7, v248\n" \
"v_fmac_f32 v32, v144, v242\n" \
"v_fmac_f32 v33, v145, v242\n" \
"v_fmac_f32 v2, v130, v242\n" \
"v_fmac_f32 v3, v131, v242\n" \
"v_mul_f32 v36, v36, v248\n" \
"v_mul_f32 v37, v37, v248\n" \
"v_mul_f32 v38, v38, v248\n" \
"v_mul_f32 v39, v39, v248\n" \
"v_cvt_pk_bf16_f32 v0, v0, v1\n" \
"v_fmac_f32 v34, v146, v242\n" \
"v_fmac_f32 v35, v147, v242\n" \
"v_mul_f32 v8, v8, v249\n" \
"v_mul_f32 v9, v9, v249\n" \
"v_fmac_f32 v4, v132, v242\n" \
"v_fmac_f32 v5, v133, v242\n" \
"v_fmac_f32 v6, v134, v242\n" \
"v_fmac_f32 v7, v135, v242\n" \
"v_cvt_pk_bf16_f32 v32, v32, v33\n" \
"v_mul_f32 v40, v40, v249\n" \
"v_mul_f32 v41, v41, v249\n" \
"v_cvt_pk_bf16_f32 v1, v2, v3\n" \
"v_fmac_f32 v36, v148, v242\n" \
"v_fmac_f32 v37, v149, v242\n" \
"v_fmac_f32 v38, v150, v242\n" \
"v_fmac_f32 v39, v151, v242\n" \
"v_mul_f32 v10, v10, v249\n" \
"v_mul_f32 v11, v11, v249\n" \
"v_cvt_pk_bf16_f32 v33, v34, v35\n" \
"v_fmac_f32 v8, v136, v243\n" \
"v_fmac_f32 v9, v137, v243\n" \
"v_mul_f32 v42, v42, v249\n" \
"v_mul_f32 v43, v43, v249\n" \
"v_cvt_pk_bf16_f32 v2, v4, v5\n" \
"v_cvt_pk_bf16_f32 v3, v6, v7\n" \
"v_mul_f32 v12, v12, v249\n" \
"v_mul_f32 v13, v13, v249\n" \
"v_mul_f32 v14, v14, v249\n" \
"v_mul_f32 v15, v15, v249\n" \
"v_fmac_f32 v40, v152, v243\n" \
"v_fmac_f32 v41, v153, v243\n" \
"v_cvt_pk_bf16_f32 v34, v36, v37\n" \
"v_cvt_pk_bf16_f32 v35, v38, v39\n" \
"v_fmac_f32 v10, v138, v243\n" \
"v_fmac_f32 v11, v139, v243\n" \
"v_mul_f32 v44, v44, v249\n" \
"v_mul_f32 v45, v45, v249\n" \
"v_mul_f32 v46, v46, v249\n" \
"v_mul_f32 v47, v47, v249\n" \
"v_cvt_pk_bf16_f32 v8, v8, v9\n" \
"v_fmac_f32 v42, v154, v243\n" \
"v_fmac_f32 v43, v155, v243\n" \
"v_fmac_f32 v12, v140, v243\n" \
"v_fmac_f32 v13, v141, v243\n" \
"v_fmac_f32 v14, v142, v243\n" \
"v_fmac_f32 v15, v143, v243\n" \
"v_cvt_pk_bf16_f32 v40, v40, v41\n" \
"v_permlane16_swap_b32 v0, v2\n" \
"v_permlane16_swap_b32 v1, v3\n" \
"v_cvt_pk_bf16_f32 v9, v10, v11\n" \
"v_fmac_f32 v44, v156, v243\n" \
"v_fmac_f32 v45, v157, v243\n" \
"v_fmac_f32 v46, v158, v243\n" \
"v_fmac_f32 v47, v159, v243\n" \
"v_permlane16_swap_b32 v32, v34\n" \
"v_permlane16_swap_b32 v33, v35\n" \
"v_cvt_pk_bf16_f32 v41, v42, v43\n" \
"v_cvt_pk_bf16_f32 v10, v12, v13\n" \
"v_cvt_pk_bf16_f32 v11, v14, v15\n" \
"v_mov_b32 v4, v0\n" \
"v_mov_b32 v5, v1\n" \
"v_mov_b32 v6, v2\n" \
"v_mov_b32 v7, v3\n" \
"v_cvt_pk_bf16_f32 v42, v44, v45\n" \
"v_cvt_pk_bf16_f32 v43, v46, v47\n" \
"v_accvgpr_read_b32 v241, a242\n" \
"v_mov_b32_dpp v0, v32 row_ror:8 row_mask:0xf bank_mask:0xc\n" \
"v_mov_b32_dpp v1, v33 row_ror:8 row_mask:0xf bank_mask:0xc\n" \
"v_mov_b32_dpp v2, v34 row_ror:8 row_mask:0xf bank_mask:0xc\n" \
"v_mov_b32_dpp v3, v35 row_ror:8 row_mask:0xf bank_mask:0xc\n" \
"v_permlane16_swap_b32 v8, v10\n" \
"v_permlane16_swap_b32 v9, v11\n" \
"v_permlane16_swap_b32 v40, v42\n" \
"v_permlane16_swap_b32 v41, v43\n" \
"buffer_store_dwordx4 v[0:3], v241, s[48:51], 0 offen offset:0 sc1\n" \
"v_mov_b32_dpp v32, v4 row_ror:8 row_mask:0xf bank_mask:0x3\n" \
"v_mov_b32_dpp v33, v5 row_ror:8 row_mask:0xf bank_mask:0x3\n" \
"v_mov_b32_dpp v34, v6 row_ror:8 row_mask:0xf bank_mask:0x3\n" \
"v_mov_b32_dpp v35, v7 row_ror:8 row_mask:0xf bank_mask:0x3\n" \
"s_mov_b32 s52, 32768\n" \
"v_mov_b32 v12, v8\n" \
"v_mov_b32 v13, v9\n" \
"v_mov_b32 v14, v10\n" \
"v_mov_b32 v15, v11\n" \
"buffer_store_dwordx4 v[32:35], v241, s[48:51], s52 offen offset:0 sc1\n" \
"v_mov_b32_dpp v8, v40 row_ror:8 row_mask:0xf bank_mask:0xc\n" \
"v_mov_b32_dpp v9, v41 row_ror:8 row_mask:0xf bank_mask:0xc\n" \
"v_mov_b32_dpp v10, v42 row_ror:8 row_mask:0xf bank_mask:0xc\n" \
"v_mov_b32_dpp v11, v43 row_ror:8 row_mask:0xf bank_mask:0xc\n" \
"v_mov_b32_dpp v40, v12 row_ror:8 row_mask:0xf bank_mask:0x3\n" \
"buffer_store_dwordx4 v[8:11], v241, s[48:51], 0 offen offset:2048 sc1\n" \
"v_mov_b32_dpp v41, v13 row_ror:8 row_mask:0xf bank_mask:0x3\n" \
"v_mov_b32_dpp v42, v14 row_ror:8 row_mask:0xf bank_mask:0x3\n" \
"v_mov_b32_dpp v43, v15 row_ror:8 row_mask:0xf bank_mask:0x3\n" \
"buffer_store_dwordx4 v[40:43], v241, s[48:51], s52 offen offset:2048 sc1\n" \
"s_cmp_lg_u32 s92, 0\n" \
"s_cbranch_scc1 Lno_e_%=\n" \
"v_mov_b32 v246, s72\n" \
"v_mov_b32 v247, s73\n" \
"s_mov_b64 exec, 1\n" \
"buffer_store_dwordx2 v[246:247], off, s[64:67], s93 sc1\n" \
"s_mov_b64 exec, -1\n" \
"Lno_e_%=:\n" \
"gmk_code_end:\n"

__global__ __launch_bounds__(256, 1) void gated_mix_kernel(const void* x, const void* wqkv, const void* wo, const void* w1,
                                                           const void* w2, const void* gw, void* out, void* ws) {
  __shared__ __attribute__((aligned(16))) char sm[163840];

  typedef unsigned u32x16 __attribute__((ext_vector_type(16)));
  u32x16 ka;
  asm volatile("s_load_dwordx16 %0, %1, 0x0" : "={s[4:19]}"(ka) : "s"(__builtin_amdgcn_kernarg_segment_ptr()));
  const unsigned wg = blockIdx.x, xcd = wg & 7, idx = wg >> 3;
  const unsigned eta = (xcd & 1) ^ 0u, v = idx & 7;
  const unsigned rb = 2 * (xcd >> 1) + (idx >> 4), type = (idx >> 3) & 1, t = 8 * eta + v, cb = v, j = eta + 2 * type;
  const unsigned n = threadIdx.x & 63, wv = threadIdx.x >> 6;
  {
    const unsigned co = ((idx * 4u + wv) * 6u + (n < 5u ? n : 5u)) * 128u;
    asm volatile("s_getpc_b64 s[20:21]\ngmk_pc0:\n\ts_mov_b32 s22, gmk_code_end-gmk_pc0\n\ts_mov_b32 s23, 0x20000\n\tbuffer_load_dword a200, %0, s[20:23], 0 offen"
                 : : "v"(co) : "a200", "s20", "s21", "s22", "s23");
  }
  const unsigned info = rb | (t << 4) | (type << 8) | (cb << 12) | (j << 16) | ((8u * eta) << 20) | (8u << 26);
  const unsigned NFH = 12u, NBH = 33u, NBQ = 23u, QB = 4u, NB = type ? NBQ : NBH, SWI = 8u - 2u * QB;
  auto step_of = [&](unsigned l, unsigned& sb) -> unsigned {
    const unsigned jj = type ? l : l - 6u, nf = type ? 21u - NFH : NFH, hf = jj >= nf ? 1u : 0u;
    const unsigned r0 = hf ? jj - nf + (type ? NBH - QB - 6u - NFH : 0u) : jj + (type ? NFH : 0u), r = r0 < 21u ? r0 : 0u;
    const unsigned q3 = (r * 43u) >> 7, tl = q3 >= v ? q3 + 1u : q3;
    const unsigned s1 = (!type && l < 6u) ? 6u * t + l : 48u * eta + 6u * tl + 3u * hf + (r - 3u * q3);
    const unsigned s2 = 96u + 8u * eta + QB * type + (l - (NB - QB));
    const unsigned c = l - NB < 16u + SWI ? l - NB : 0u, hs = SWI >> 1;
    const unsigned s3 = c < SWI ? 96u + 2u * QB + (c >= hs ? 8u + 2u * (c - hs) : 2u * c) : 112u + 2u * (c - SWI);
    const unsigned sa = l < NB - QB ? s1 : (l < NB ? s2 : s3);
    sb = (l >= NB && c >= SWI) ? 2u * (c - SWI) : sa;
    return sa;
  };
  unsigned sb;
  const unsigned sa = step_of(n, sb);
  const unsigned ta = (rb * 144u + sa) * 16384u, tb = sb * 65536u + cb * 256u;
  const unsigned q4 = n < NFH / 3u - 1u ? n : NFH / 3u - 1u;
  const unsigned p1a = (8u * eta + (q4 >= v ? q4 + 1u : q4)) * 128u + 0x20000u;
  const unsigned q2 = n < 2u - 1u ? n : 2u - 1u, p1f = (8u * eta + (q2 >= v ? q2 + 1u : q2)) * 128u + 0x20000u;
  asm volatile(KASM
               :
               : "{s[4:19]}"(ka),
                 [info] "s"(info),
                 "{v249}"(threadIdx.x), "{v250}"(ta), "{v251}"(tb), "{v252}"(p1a), "{v253}"(p1f), [sm] "s"((unsigned)(size_t)sm)
               : "v0","v1","v2","v3","v4","v5","v6","v7","v8","v9","v10","v11","v12","v13","v14","v15","v16","v17","v18","v19","v20","v21","v22","v23","v24","v25","v26","v27","v28","v29","v30","v31","v32","v33","v34","v35","v36","v37","v38","v39","v40","v41","v42","v43","v44","v45","v46","v47","v48","v49","v50","v51","v52","v53","v54","v55","v56","v57","v58","v59","v60","v61","v62","v63","v64","v65","v66","v67","v68","v69","v70","v71","v72","v73","v74","v75","v76","v77","v78","v79","v80","v81","v82","v83","v84","v85","v86","v87","v88","v89","v90","v91","v92","v93","v94","v95","v96","v97","v98","v99","v100","v101","v102","v103","v104","v105","v106","v107","v108","v109","v110","v111","v112","v113","v114","v115","v116","v117","v118","v119","v120","v121","v122","v123","v124","v125","v126","v127","v128","v129","v130","v131","v132","v133","v134","v135","v136","v137","v138","v139","v140","v141","v142","v143","v144","v145","v146","v147","v148","v149","v150","v151","v152","v153","v154","v155","v156","v157","v158","v159","v160","v161","v162","v163","v164","v165","v166","v167","v168","v169","v170","v171","v172","v173","v174","v175","v176","v177","v178","v179","v180","v181","v182","v183","v184","v185","v186","v187","v188","v189","v190","v191","v192","v193","v194","v195","v196","v197","v198","v199","v200","v201","v202","v203","v204","v205","v206","v207","v208","v209","v210","v211","v212","v213","v214","v215","v216","v217","v218","v219","v220","v221","v222","v223","v224","v225","v226","v227","v228","v229","v230","v231","v232","v233","v234","v235","v236","v237","v238","v239","v240","v241","v242","v243","v244","v245","v246","v247","v248","v254","a0","a1","a2","a3","a4","a5","a6","a7","a8","a9","a10","a11","a12","a13","a14","a15","a16","a17","a18","a19","a20","a21","a22","a23","a24","a25","a26","a27","a28","a29","a30","a31","a32","a33","a34","a35","a36","a37","a38","a39","a40","a41","a42","a43","a44","a45","a46","a47","a48","a49","a50","a51","a52","a53","a54","a55","a56","a57","a58","a59","a60","a61","a62","a63","a64","a65","a66","a67","a68","a69","a70","a71","a72","a73","a74","a75","a76","a77","a78","a79","a80","a81","a82","a83","a84","a85","a86","a87","a88","a89","a90","a91","a92","a93","a94","a95","a96","a97","a98","a99","a100","a101","a102","a103","a104","a105","a106","a107","a108","a109","a110","a111","a112","a113","a114","a115","a116","a117","a118","a119","a120","a121","a122","a123","a124","a125","a126","a127","a128","a129","a130","a131","a132","a133","a134","a135","a136","a137","a138","a139","a140","a141","a142","a143","a144","a145","a146","a147","a148","a149","a150","a151","a152","a153","a154","a155","a156","a157","a158","a159","a160","a161","a162","a163","a164","a165","a166","a167","a168","a169","a170","a171","a172","a173","a174","a175","a176","a177","a178","a179","a180","a181","a182","a183","a184","a185","a186","a187","a188","a189","a190","a191","a192","a193","a194","a195","a196","a197","a198","a199","a200","a201","a202","a203","a204","a205","a206","a207","a208","a209","a210","a211","a212","a213","a214","a215","a216","a217","a218","a219","a220","a221","a222","a223","a224","a225","a226","a227","a228","a229","a230","a231","a232","a233","a234","a235","a236","a237","a238","a239","a240","a241","a242","a243","a244","a245","a246","a247","a248","a249","a250","a251","a252","a253","a254","a255","s0","s1","s20","s21","s22","s23","s24","s25","s26","s27","s28","s29","s30","s31","s34","s35","s36","s37","s38","s39","s40","s41","s42","s43","s44","s45","s46","s47","s48","s49","s50","s51","s52","s53","s54","s55","s56","s57","s58","s59","s60","s61","s62","s63","s64","s65","s66","s67","s68","s69","s70","s71","s72","s73","s74","s75","s76","s77","s78","s79","s80","s81","s82","s83","s84","s85","s86","s87","s88","s89","s90","s91","s92","s93","s94","s95","s96","s97","s98","s99","vcc","scc","memory");
}

extern "C" void kernel_launch(void* const* d_in, const int* in_sizes, int n_in, void* d_out, int out_size, void* d_ws,
                              size_t ws_size, hipStream_t stream) {
  gated_mix_kernel<<<256, 256, 0, stream>>>(d_in[0], d_in[1], d_in[2], d_in[4], d_in[5], d_in[6], d_out, d_ws);
}
```
